```python
import torch

def _softmax(x, axis=-1):
    return torch.softmax(x, dim=axis)
def _gelu(x):
    return torch.nn.functional.gelu(x, approximate="tanh")
def _silu(x):
    return torch.nn.functional.silu(x)
def _sigmoid(x):
    return torch.sigmoid(x)
def _layernorm(x, axis=-1, eps=1e-5):
    m = x.mean(dim=axis, keepdim=True)
    v = x.var(dim=axis, keepdim=True, unbiased=False)
    return (x - m) / torch.sqrt(v + eps)
def _scatter_add(tgt, idx, src, axis=0):
    return tgt.clone().index_add_(axis, idx, src)
def _segment_sum(src, seg, n):
    out = torch.zeros((n,) + src.shape[1:], dtype=src.dtype)
    return out.index_add_(0, seg, src)
def _pad(x, pw):
    flat = tuple(v for pair in reversed(pw) for v in pair)
    return torch.nn.functional.pad(x, flat)

kernel_name = 'recurrent_scan__sliding_block_cached__mixer_token_mlp__prompt'

def setup_inputs():

    _w = (lambda a: (lambda t: t.to(torch.bfloat16) if t.is_floating_point() else t)(torch.as_tensor(a)))
    def _A_setup():
        B, Sq, D = 1, 2048, 1024
        Hq, Hkv, Sk, Dh = 32, 4, 2048, 128
        assert Hq % Hkv == 0, (Hq, Hkv)
        return {
            "x": _w((lambda *s: torch.randn(*s, dtype=torch.bfloat16) * 0.05)(B, Sq, D)),
            "k_cache": _w((lambda *s: torch.randn(*s, dtype=torch.bfloat16) * 1.6)(B, Hkv, Sk, Dh)),
            "v_cache": _w((lambda *s: torch.randn(*s, dtype=torch.bfloat16) * 0.8)(B, Hkv, Sk, Dh)),
            "wq": _w((lambda *s: torch.randn(*s, dtype=torch.bfloat16) * 1.6)(D, Hq * Dh) * (D ** -0.5)),
            "wo": _w((lambda *s: torch.randn(*s, dtype=torch.bfloat16) * 0.8)(Hq * Dh, D) * ((Hq * Dh) ** -0.5)),
        }
    _A_in = _A_setup()

    def _B_setup():
        B, S, D, F = 1, 2048, 1024, 3584
        return {
            "x": _w((lambda *s: torch.randn(*s, dtype=torch.bfloat16) * 0.05)(B, S, D)),
            "w1": _w((lambda *s: torch.randn(*s, dtype=torch.bfloat16) * 1.6)(S, F) * (S ** -0.5)),
            "b1": _w((lambda *s: torch.randn(*s, dtype=torch.bfloat16) * 1.6)(F) * 0.02),
            "w2": _w((lambda *s: torch.randn(*s, dtype=torch.bfloat16) * 0.2)(F, S) * (F ** -0.5)),
            "b2": _w((lambda *s: torch.randn(*s, dtype=torch.bfloat16) * 0.2)(S) * 0.02),
        }
    _B_in = _B_setup()

    return {
        "A_x": _A_in["x"],
        "A_k_cache": _A_in["k_cache"],
        "A_v_cache": _A_in["v_cache"],
        "A_wq": _A_in["wq"],
        "A_wo": _A_in["wo"],
        "B_x": _B_in["x"],
        "B_w1": _B_in["w1"],
        "B_b1": _B_in["b1"],
        "B_w2": _B_in["w2"],
        "B_b2": _B_in["b2"],
    }

def reference(A_x, A_k_cache, A_v_cache, A_wq, A_wo, B_x, B_w1, B_b1, B_w2, B_b2):
    def _A_ref(x, k_cache, v_cache, wq, wo):
        B, Sq, D = x.shape
        Hq, Dh = 32, 128
        Hkv, Sk = k_cache.shape[1], k_cache.shape[2]
        W = min(4096, max(256, Sk // 4))
        rep = Hq // Hkv
        q = (lambda x, *d: x.permute(*d))((x @ wq).reshape(B, Sq, Hq, Dh), 0, 2, 1, 3)
        k = (lambda x, n, axis: torch.repeat_interleave(x, n, dim=axis))(k_cache[:, :, Sk - W:, :], rep, axis=1)
        v = (lambda x, n, axis: torch.repeat_interleave(x, n, dim=axis))(v_cache[:, :, Sk - W:, :], rep, axis=1)
        scores = torch.einsum("bhqd,bhkd->bhqk", q, k) * (Dh ** -0.5)
        p = _softmax(scores, axis=-1)
        o = torch.einsum("bhqk,bhkd->bhqd", p, v)
        return (lambda x, *d: x.permute(*d))(o, 0, 2, 1, 3).reshape(B, Sq, Hq * Dh) @ wo

    def _B_ref(x, w1, b1, w2, b2):
        B, S, D = x.shape
        h = _layernorm(x, axis=-1)
        u = torch.einsum("bsd,sf->bdf", h, w1) + b1
        u = _gelu(u)
        y = torch.einsum("bdf,fs->bds", u, w2) + b2
        return (lambda x, *d: x.permute(*d))(y, 0, 2, 1)

    x = A_x
    xn = _layernorm(x, axis=-1)
    s = _A_ref(xn, A_k_cache, A_v_cache, A_wq, A_wo)
    g = _silu(_B_ref(xn, B_w1, B_b1, B_w2, B_b2))
    return x + s * g
```

```cpp
#include <hip/hip_runtime.h>
#include <hip/hip_bf16.h>

namespace {
typedef short bf16x8 __attribute__((ext_vector_type(8)));
typedef short s16x4 __attribute__((ext_vector_type(4)));
typedef float f32x2 __attribute__((ext_vector_type(2)));
typedef float f32x4 __attribute__((ext_vector_type(4)));
typedef float f32x16 __attribute__((ext_vector_type(16)));
typedef unsigned u32x4 __attribute__((ext_vector_type(4)));
typedef unsigned u32x2 __attribute__((ext_vector_type(2)));
typedef __bf16 bf2_t __attribute__((ext_vector_type(2)));
typedef unsigned short u16;
typedef __amdgpu_buffer_rsrc_t rsrc_t;
#define LAS __attribute__((address_space(3)))
#define DI __device__ __forceinline__
#define BAR() asm volatile("s_barrier" ::: "memory")
#define WAITL0() do { asm volatile("" ::: "memory"); __builtin_amdgcn_s_waitcnt(0xC07F); asm volatile("" ::: "memory"); } while (0)
#define SCHED() __builtin_amdgcn_sched_barrier(0)
#define SGB(mask, n) __builtin_amdgcn_sched_group_barrier(mask, n, 0)

constexpr int SEQ = 2048, DM = 1024, NH = 32, DH = 128, SK = 2048, WIN = 512, FF = 3584;
constexpr unsigned WS_XN = 0u, WS_H = 4u << 20, WS_O = 8u << 20, WS_U = 24u << 20, WS_G = 32u << 20, WS_FLAG = 40u << 20;
constexpr unsigned LDS_BYTES = 163840;

template <int N> DI void waitv() { asm volatile("s_waitcnt vmcnt(%0)" :: "i"(N) : "memory"); }
DI unsigned rfl(unsigned x) { return __builtin_amdgcn_readfirstlane(x); }
DI rsrc_t mkrsrc(const void* p, unsigned bytes) { return __builtin_amdgcn_make_buffer_rsrc((void*)p, (short)0, (int)bytes, 0x00020000); }
DI void dma16(unsigned m0v, unsigned voff, rsrc_t r, unsigned soff) {
  asm volatile("s_mov_b32 m0, %0\n\ts_nop 0\n\tbuffer_load_dwordx4 %1, %2, %3 offen lds" :: "s"(m0v), "v"(voff), "s"(r), "s"(soff) : "memory");
}
DI bf16x8 ldsR(unsigned a) { return *(const LAS bf16x8*)(size_t)a; }
DI s16x4 ldsT(unsigned a) { return __builtin_amdgcn_ds_read_tr16_b64_v4i16((LAS s16x4*)(size_t)a); }
DI bf16x8 cat(s16x4 a, s16x4 b) { return __builtin_shufflevector(a, b, 0, 1, 2, 3, 4, 5, 6, 7); }
DI unsigned pk2(float lo, float hi) { f32x2 v = {lo, hi}; return __builtin_bit_cast(unsigned, __builtin_convertvector(v, bf2_t)); }
DI float bflo(unsigned w) { return __uint_as_float(w << 16); }
DI float bfhi(unsigned w) { return __uint_as_float(w & 0xffff0000u); }
DI float rbf(float x) { return bflo(pk2(x, x) & 0xffffu); }
DI float wsum(float v) {
#pragma unroll
  for (int o = 32; o > 0; o >>= 1) v += __shfl_xor(v, o);
  return v;
}

DI void ln16(float (&v)[16]) {
  float s = 0.f;
#pragma unroll
  for (int i = 0; i < 16; ++i) s += v[i];
  const float mean = wsum(s) * (1.0f / DM);
  float q = 0.f;
#pragma unroll
  for (int i = 0; i < 16; ++i) { const float d = v[i] - mean; q += d * d; }
  const float var = rbf(wsum(q) * (1.0f / DM));
  const float mb = rbf(mean), sd = rbf(sqrtf(rbf(var + 1e-5f)));
#pragma unroll
  for (int i = 0; i < 16; ++i) v[i] = rbf(rbf(v[i] - mb) / sd);
}
__global__ __launch_bounds__(512) void k_ln(const u16* __restrict__ x, u16* __restrict__ xn, u16* __restrict__ h, unsigned* __restrict__ flags) {
  const unsigned lane = threadIdx.x & 63u, row = blockIdx.x * 8u + (threadIdx.x >> 6);
  if (blockIdx.x == 0 && threadIdx.x < 128) flags[threadIdx.x * 16] = 0u;
  const u32x4 a = *(const u32x4*)(x + row * DM + lane * 8), b = *(const u32x4*)(x + row * DM + 512 + lane * 8);
  float v[16];
#pragma unroll
  for (int i = 0; i < 4; ++i) { v[2 * i] = bflo(a[i]); v[2 * i + 1] = bfhi(a[i]); v[8 + 2 * i] = bflo(b[i]); v[9 + 2 * i] = bfhi(b[i]); }
  ln16(v);
  u16* xr = xn + row * DM + (lane >> 1) * 16 + (lane & 1u) * 4;
#pragma unroll
  for (int p = 0; p < 2; ++p) {
    *(u32x2*)(xr + 512 * p) = (u32x2){pk2(v[8 * p], v[8 * p + 1]), pk2(v[8 * p + 2], v[8 * p + 3])};
    *(u32x2*)(xr + 512 * p + 8) = (u32x2){pk2(v[8 * p + 4], v[8 * p + 5]), pk2(v[8 * p + 6], v[8 * p + 7])};
  }
  ln16(v);
#pragma unroll
  for (int p = 0; p < 2; ++p)
    *(u32x4*)(h + row * DM + 512 * p + lane * 8) = (u32x4){pk2(v[8 * p], v[8 * p + 1]), pk2(v[8 * p + 2], v[8 * p + 3]), pk2(v[8 * p + 4], v[8 * p + 5]), pk2(v[8 * p + 6], v[8 * p + 7])};
}

enum { RROW = 0, RCHK = 1, T1 = 2, T2 = 3 };
template <int KIND> struct Opnd {
  static constexpr int NA = KIND == RROW ? 2 : KIND == RCHK ? 1 : 4;
  static constexpr int RPB = KIND >= T1 ? 2 : 1;
  unsigned a[NA];
  DI void init(unsigned base, unsigned off, unsigned lane) {
    const unsigned fr = lane & 15u, g = lane >> 4, q = fr >> 2, p = lane & 3u;
    if constexpr (KIND == RROW) {
#pragma unroll
      for (int kk = 0; kk < 2; ++kk) a[kk] = base + (off + fr) * 128u + 16u * ((4u * kk + g) ^ (fr >> 1));
    } else if constexpr (KIND == RCHK) {
      a[0] = base + g * 2048u + (off + fr) * 16u;
    } else {
      const unsigned x = ((q >> 1) << 1) | (g & 1u);
#pragma unroll
      for (int n = 0; n < 4; ++n)
        a[n] = base + (KIND == T1 ? 4096u : 2048u) * (g >> 1) + 1024u * (off >> 6) + 128u * (4u * (g & 1u) + q) + 32u * ((unsigned)n ^ x) + 8u * p;
    }
  }
  DI bf16x8 ld(int b, int kk) const {
    if constexpr (KIND == RROW) return ldsR(a[kk] + 2048u * b);
    else if constexpr (KIND == RCHK) return ldsR(a[0] + 8192u * kk + 256u * b);
    else { const unsigned ad = a[b & 3] + 1024u * (b >> 2) + 8192u * kk; return cat(ldsT(ad), ldsT(ad + (KIND == T1 ? 2048u : 4096u))); }
  }
  DI void adv(unsigned delta) {
#pragma unroll
    for (int i = 0; i < NA; ++i) { a[i] += delta; asm volatile("" : "+v"(a[i])); }
  }
};

template <int PK, int QK, int PM, int QM, int NS, unsigned STG>
DI void gemm_compute(f32x4 (&acc)[PM][QM], Opnd<PK>& P, Opnd<QK>& Q, const int N) {
  bf16x8 pf[2][PM], qf[2][QM];
  constexpr int NR = PM * Opnd<PK>::RPB + QM * Opnd<QK>::RPB, NM = PM * QM, NA = Opnd<PK>::NA + Opnd<QK>::NA;
  auto load = [&](int kk) {
    pf[kk][0] = P.ld(0, kk);
#pragma unroll
    for (int q = 0; q < QM; ++q) qf[kk][q] = Q.ld(q, kk);
#pragma unroll
    for (int p = 1; p < PM; ++p) pf[kk][p] = P.ld(p, kk);
  };
  auto mma = [&](int kk) {
#pragma unroll
    for (int p = 0; p < PM; ++p)
#pragma unroll
      for (int q = 0; q < QM; ++q) acc[p][q] = __builtin_amdgcn_mfma_f32_16x16x32_bf16(pf[kk][p], qf[kk][q], acc[p][q], 0, 0, 0);
  };
  constexpr int ND = (NR + NA + 2 > NM) ? (NR + NA + 2 - NM) : 0;
  BAR();
  load(0);
  int stg = 0;
  for (int t = 0; t < N; ++t) {
    SCHED();
    load(1); mma(0);
    stg = (stg == NS - 1) ? 0 : stg + 1;
    const unsigned delta = stg == 0 ? (unsigned)(-(int)((NS - 1) * STG)) : STG;
    P.adv(delta); Q.adv(delta);
#pragma unroll
    for (int i = 0; i < ND; ++i) { SGB(0x8, 1); SGB(0x100, 2); }
#pragma unroll
    for (int i = 0; i < NR - 2 * ND; ++i) { SGB(0x8, 1); SGB(0x100, 1); }
#pragma unroll
    for (int i = 0; i < NA; ++i) { SGB(0x8, 1); SGB(0x2, 1); }
    SGB(0x8, NM);
    SCHED();
    WAITL0(); BAR();
    SCHED();
    load(0); mma(1);
#pragma unroll
    for (int i = 0; i < ND; ++i) { SGB(0x8, 1); SGB(0x100, 2); }
#pragma unroll
    for (int i = 0; i < NR - 2 * ND; ++i) { SGB(0x8, 1); SGB(0x100, 1); }
    SGB(0x8, NM);
  }
  SCHED();
}

DI unsigned voffRow(unsigned lane, unsigned ldb, unsigned par) { return (lane >> 3) * ldb + 16u * ((lane & 7u) ^ (4u * par + (lane >> 4))); }
DI unsigned voffT(unsigned lane, unsigned ldb) { return (lane >> 3) * ldb + 16u * ((lane & 7u) ^ (2u * ((((lane >> 4) & 1u) << 1) | (lane >> 5)))); }
DI void dmaT4(unsigned dst, unsigned lw, unsigned vo, rsrc_t r, unsigned so, unsigned ldb) {
#pragma unroll
  for (int q = 0; q < 4; ++q) dma16(dst + lw * 4096u + 1024u * q, vo, r, so + (lw * 16u + (q >> 1) * 8u) * ldb + (q & 1) * 128u);
}
DI void dmaLin4(unsigned dst, unsigned lw, unsigned vo, rsrc_t r, unsigned so) {
#pragma unroll
  for (int q = 0; q < 4; ++q) dma16(dst + lw * 4096u + 1024u * q, vo, r, so + lw * 4096u + 1024u * q);
}

DI float gelu_t(float v) {
  const float z = v * (1.0f + 0.044715f * v * v) * (-2.3022081986f);
  return v * __builtin_amdgcn_rcpf(1.0f + __builtin_amdgcn_exp2f(z));
}

constexpr unsigned QSTG = 49152u;
constexpr unsigned KBASE = 114688u, VBASE = 65536u;
constexpr unsigned KSLOT[3] = {32768u, 0u, 16384u}, VSLOT[3] = {0u, 16384u, 32768u};
constexpr float QSCALE = 0.12751743082f;
constexpr float THR = 8.0f;

struct Att {
  bf16x8 qf[8], kv[16], pf[4];
  f32x16 o[4], s[2], negm;
  float l, fct;
  unsigned aK[4], aV[2];
};
DI void att_loadK(Att& a, unsigned slot) {
#pragma unroll
  for (int ks = 0; ks < 8; ++ks)
#pragma unroll
    for (int kb = 0; kb < 2; ++kb) a.kv[2 * ks + kb] = ldsR(a.aK[ks & 3] + slot + 8192u * (ks >> 2) + 4096u * kb);
}
DI void att_loadV(Att& a, unsigned slot) {
#pragma unroll
  for (int s = 0; s < 4; ++s)
#pragma unroll
    for (int db = 0; db < 4; ++db) { const unsigned ad = a.aV[db & 1] + slot + 4096u * s + 1024u * (db >> 1); a.kv[4 * s + db] = cat(ldsT(ad), ldsT(ad + 2048u)); }
}
DI unsigned lane_id() { unsigned l; asm volatile("v_mbcnt_lo_u32_b32 %0, -1, 0\n\tv_mbcnt_hi_u32_b32 %0, -1, %0" : "=v"(l)); return l; }
DI void add_bcast3(f32x16& t0, f32x16& t1, f32x16& t2, float v) {
  const float one = lane_id() < 32u ? 1.0f : 0.0f;
  asm volatile("s_nop 7\n\ts_nop 7\n\tv_mfma_f32_32x32x2_f32 %0, %3, %4, %0\n\tv_mfma_f32_32x32x2_f32 %1, %3, %4, %1\n\tv_mfma_f32_32x32x2_f32 %2, %3, %4, %2\n\t"
               "s_nop 7\n\ts_nop 7\n\ts_nop 7" : "+v"(t0), "+v"(t1), "+v"(t2) : "v"(one), "v"(v));
}
template <bool FIRST> DI void att_qk(Att& a) {
  __builtin_amdgcn_s_setprio(1);
#pragma unroll
  for (int ks = 0; ks < 8; ++ks)
#pragma unroll
    for (int kb = 0; kb < 2; ++kb) {
      if (ks == 0) a.s[kb] = __builtin_amdgcn_mfma_f32_32x32x16_bf16(a.kv[kb], a.qf[0], FIRST ? (f32x16){} : a.negm, 0, 0, 0);
      else a.s[kb] = __builtin_amdgcn_mfma_f32_32x32x16_bf16(a.kv[2 * ks + kb], a.qf[ks], a.s[kb], 0, 0, 0);
    }
  __builtin_amdgcn_s_setprio(0);
}
DI void att_pv(Att& a) {
  __builtin_amdgcn_s_setprio(1);
#pragma unroll
  for (int s = 0; s < 4; ++s)
#pragma unroll
    for (int db = 0; db < 4; ++db) a.o[db] = __builtin_amdgcn_mfma_f32_32x32x16_bf16(a.kv[4 * s + db], a.pf[s], a.o[db], 0, 0, 0);
  __builtin_amdgcn_s_setprio(0);
}
template <bool FIRST> DI void att_sm1(Att& a) {
  float mx = fmaxf(a.s[0][0], a.s[1][0]);
#pragma unroll
  for (int r = 1; r < 16; ++r) mx = fmaxf(fmaxf(mx, a.s[0][r]), a.s[1][r]);
  { auto rr = __builtin_amdgcn_permlane32_swap(__float_as_uint(mx), __float_as_uint(mx), false, false);
    mx = fmaxf(__uint_as_float(rr[0]), __uint_as_float(rr[1])); }
  a.fct = 1.0f;
  if (FIRST) {
#pragma unroll
    for (int r = 0; r < 16; ++r) { a.s[0][r] -= mx; a.s[1][r] -= mx; a.negm[r] = -mx; }
  } else if (__builtin_expect(__any(mx > THR), 0)) {
    const float dl = fmaxf(mx, 0.f);
    a.fct = __builtin_amdgcn_exp2f(-dl);
    add_bcast3(a.s[0], a.s[1], a.negm, -dl);
  }
#pragma unroll
  for (int r = 0; r < 16; ++r) a.s[0][r] = __builtin_amdgcn_exp2f(a.s[0][r]);
  asm volatile("" : "+v"(a.s[0]), "+v"(a.s[1]), "+v"(a.negm), "+v"(a.fct));
}
template <bool FIRST> DI void att_sm2(Att& a) {
  if (!FIRST) {
    if (__builtin_expect(__any(a.fct != 1.0f), 0)) {
#pragma unroll
      for (int db = 0; db < 4; ++db) a.o[db] *= a.fct;
      a.l *= a.fct;
    }
  }
#pragma unroll
  for (int r = 0; r < 16; ++r) a.s[1][r] = __builtin_amdgcn_exp2f(a.s[1][r]);
  float s0 = 0.f, s1 = 0.f;
#pragma unroll
  for (int r = 0; r < 16; ++r) { s0 += a.s[0][r]; s1 += a.s[1][r]; }
  a.l += s0 + s1;
#pragma unroll
  for (int s = 0; s < 4; ++s) {
    const f32x16& v = a.s[s >> 1]; const int b = 8 * (s & 1);
    u32x4 w = {pk2(v[b], v[b + 1]), pk2(v[b + 2], v[b + 3]), pk2(v[b + 4], v[b + 5]), pk2(v[b + 6], v[b + 7])};
    a.pf[s] = __builtin_bit_cast(bf16x8, w);
  }
  asm volatile("" : "+v"(a.pf[0]), "+v"(a.pf[1]), "+v"(a.pf[2]), "+v"(a.pf[3]), "+v"(a.l));
}

__global__ __launch_bounds__(512, 2) void k_mid(const u16* __restrict__ xn, const u16* __restrict__ hh, const u16* __restrict__ kc, const u16* __restrict__ vc,
                                                const u16* __restrict__ wq, const u16* __restrict__ w1, const u16* __restrict__ b1,
                                                u16* __restrict__ Og, u16* __restrict__ Ug) {
  extern __shared__ __attribute__((aligned(16))) char lds[];
  const unsigned L = (unsigned)(size_t)lds;
  const unsigned tid = threadIdx.x, lane = tid & 63u, wid = rfl(tid >> 6), grp = wid >> 2, lw = wid & 3u;
  const unsigned xcd = blockIdx.x & 7u, idx = blockIdx.x >> 3;
  const unsigned qt = (xcd & 1u) * 4u + (idx & 3u), kvh = xcd >> 1, head = kvh * 8u + (idx >> 2);
  const bool hasU = idx < 28u;
  const unsigned udt = (xcd & 1u) * 4u + (idx & 3u), uft = (xcd >> 1) * 7u + (idx >> 2);
  const rsrc_t sK = mkrsrc(kc + ((size_t)kvh * SK + (SK - WIN)) * DH, WIN * DH * 2), sV = mkrsrc(vc + ((size_t)kvh * SK + (SK - WIN)) * DH, WIN * DH * 2);
  const rsrc_t sH = mkrsrc(hh, SEQ * DM * 2), sW1 = mkrsrc(w1, SEQ * FF * 2);
  const unsigned voK0 = voffRow(lane, 256u, 0), voV = voffT(lane, 256u);
  auto dmaK2 = [&](unsigned slot, unsigned t, unsigned p0) {
    unsigned voK1; asm volatile("v_xor_b32 %0, 64, %1" : "=v"(voK1) : "v"(voK0));
    dma16(L + KBASE + slot + 1024u * p0, voK0, sK, t * 16384u + (p0 & 7u) * 2048u + (p0 >> 3) * 128u);
    dma16(L + KBASE + slot + 1024u * p0 + 1024u, voK1, sK, t * 16384u + (p0 & 7u) * 2048u + 2048u + (p0 >> 3) * 128u); };
  auto dmaV2 = [&](unsigned slot, unsigned t, unsigned p0) {
    dma16(L + VBASE + slot + 1024u * p0, voV, sV, t * 16384u + (p0 >> 1) * 2048u);
    dma16(L + VBASE + slot + 1024u * p0 + 1024u, voV, sV, t * 16384u + (p0 >> 1) * 2048u + 128u); };

  if (grp == 1) {
    const rsrc_t sX = mkrsrc(xn, SEQ * DM * 2), sWq = mkrsrc(wq, DM * NH * DH * 2);
    const unsigned voX0 = voffRow(lane, 2048u, 0), voX1 = voffRow(lane, 2048u, 1), voWq = voffT(lane, 8192u);
    auto step = [&](unsigned j, unsigned stg) {
      const unsigned dst = L + stg * QSTG;
      dmaT4(dst, lw, voWq, sWq, j * 64u * 8192u + head * 256u, 8192u);
#pragma unroll
      for (int q = 0; q < 8; ++q) dma16(dst + 16384u + lw * 8192u + 1024u * q, (q & 1) ? voX1 : voX0, sX, (qt * 256u + lw * 64u + q * 8u) * 2048u + j * 128u);
    };
    step(0, 0); step(1, 1); step(2, 2);
    dmaK2(KSLOT[0], 0, lw * 4u); dmaK2(KSLOT[0], 0, lw * 4u + 2u);
    waitv<28>(); BAR();
    waitv<16>(); BAR(); step(3, 0);
    waitv<16>(); BAR(); step(4, 1);
    unsigned stg = 2;
    for (unsigned t = 2; t < 13; ++t) { waitv<12>(); BAR(); step(t + 3, stg); stg = stg == 2 ? 0 : stg + 1; }
    waitv<12>(); BAR();
    dmaV2(VSLOT[0], 0, lw * 4u); dmaV2(VSLOT[0], 0, lw * 4u + 2u); dmaV2(VSLOT[1], 1, lw * 4u); dmaV2(VSLOT[1], 1, lw * 4u + 2u);
    waitv<8>(); BAR();
    dmaK2(KSLOT[1], 1, lw * 4u); dmaK2(KSLOT[1], 1, lw * 4u + 2u); dmaK2(KSLOT[2], 2, lw * 4u); dmaK2(KSLOT[2], 2, lw * 4u + 2u);
    BAR();
  } else {
    Opnd<T1> P; Opnd<RROW> Q;
    P.init(L, 0u, lane); Q.init(L + 16384u, lw * 64u, lane);
    f32x4 acc[8][4] = {};
    gemm_compute<T1, RROW, 8, 4, 3, QSTG>(acc, P, Q, 16);
    const unsigned wa = L + (lane >> 5) * 4096u + (lw * 64u + (lane & 15u)) * 16u + ((lane >> 4) & 1u) * 8u;
#pragma unroll
    for (int n = 0; n < 8; ++n)
#pragma unroll
      for (int m = 0; m < 4; ++m) {
        const f32x4 v = acc[n][m] * QSCALE;
        *(LAS u32x2*)(size_t)(wa + 8192u * n + 256u * m) = (u32x2){pk2(v[0], v[1]), pk2(v[2], v[3])};
      }
  }
  WAITL0(); BAR();

  {
    Att a;
    const unsigned r32 = lane & 31u, hi = lane >> 5, blk = (lane >> 4) & 1u, q4 = (lane & 15u) >> 2, p4 = lane & 3u;
#pragma unroll
    for (int ks = 0; ks < 8; ++ks) a.qf[ks] = ldsR(L + (2u * ks) * 4096u + hi * 4096u + (wid * 32u + r32) * 16u);
#pragma unroll
    for (int j = 0; j < 4; ++j) a.aK[j] = L + KBASE + r32 * 128u + 16u * ((2u * j + hi) ^ ((r32 >> 1) & 7u));
#pragma unroll
    for (int n = 0; n < 2; ++n) a.aV[n] = L + VBASE + 128u * (4u * hi + q4) + 32u * ((2u * n + blk) ^ (((q4 >> 1) << 1) | hi)) + 8u * p4;
#pragma unroll
    for (int db = 0; db < 4; ++db) a.o[db] = (f32x16){};
    a.l = 0.f;
    auto ustepP_ = [&](unsigned j, unsigned stg, unsigned vo) { dmaT4(L + stg * 32768u, lw, vo, sW1, j * 64u * (FF * 2u) + uft * 256u, FF * 2u); };
    auto ustepQ_ = [&](unsigned j, unsigned stg, unsigned vo) { dmaT4(L + stg * 32768u + 16384u, lw, vo, sH, j * 64u * 2048u + udt * 256u, 2048u); };
    const bool pre = hasU && grp == 1;
    att_loadK(a, KSLOT[0]);
    WAITL0(); BAR();
    if (grp == 1) BAR();
#define ATT_TILE(i, FIRST)                                                                                                    \
    SCHED(); att_qk<FIRST>(a); SCHED(); BAR();                                                                                \
    if (!(FIRST)) att_loadV(a, VSLOT[((i) + 2) % 3]);                                                                         \
    if ((i) <= 4) dmaK2(KSLOT[(i) % 3], (i) + 3, wid * 2u);                                                                   \
    else if (pre) { if ((i) >= 6) ustepP_((i) - 6, (i) - 6, voffT(lane_id(), FF * 2u)); }                                            \
    att_sm1<FIRST>(a);                                                                                                        \
    if ((i) <= 4) waitv<4>(); else if ((i) == 6) { if (pre) waitv<4>(); else waitv<0>(); }                                    \
    WAITL0(); SCHED(); BAR();                                                                                                 \
    if (!(FIRST)) { SCHED(); att_pv(a); SCHED(); }                                                                            \
    BAR();                                                                                                                    \
    if ((i) < 7) att_loadK(a, KSLOT[((i) + 1) % 3]); else att_loadV(a, VSLOT[7 % 3]);                                         \
    if ((i) <= 5) dmaV2(VSLOT[((i) + 2) % 3], (i) + 2, wid * 2u);                                                             \
    else if (pre) { ustepQ_((i) - 6, (i) - 6, voffT(lane_id(), 2048u)); }                                                          \
    att_sm2<FIRST>(a);                                                                                                        \
    if ((i) <= 4) waitv<4>(); else if ((i) == 5) waitv<2>();                                                                  \
    WAITL0(); SCHED(); BAR();
    ATT_TILE(0, true) ATT_TILE(1, false) ATT_TILE(2, false) ATT_TILE(3, false)
    ATT_TILE(4, false) ATT_TILE(5, false) ATT_TILE(6, false) ATT_TILE(7, false)
#undef ATT_TILE
    SCHED(); att_pv(a); SCHED();
    if (grp == 0) BAR();
    const unsigned ln = lane_id(), voH = voffT(ln, 2048u), voW1 = voffT(ln, FF * 2u);
    auto ustepP = [&](unsigned j, unsigned stg) { ustepP_(j, stg, voW1); };
    auto ustepQ = [&](unsigned j, unsigned stg) { ustepQ_(j, stg, voH); };
    if (pre) { ustepP(2, 2); ustepQ(2, 2); ustepP(3, 3); ustepQ(3, 3); ustepP(4, 4); ustepQ(4, 4); }
    { auto rr = __builtin_amdgcn_permlane32_swap(__float_as_uint(a.l), __float_as_uint(a.l), false, false);
      a.l = __uint_as_float(rr[0]) + __uint_as_float(rr[1]); }
    const float inv = 1.0f / a.l;
    const unsigned row = qt * 256u + wid * 32u + (ln & 31u);
    char* op = (char*)Og + ((size_t)((row >> 7) * 64u + head * 2u) * 8u + (ln >> 5)) * 2048u + (row & 127u) * 16u;
#pragma unroll
    for (int db = 0; db < 4; ++db)
#pragma unroll
      for (int i = 0; i < 2; ++i) {
        const f32x16& v = a.o[db]; const int b = 8 * i;
        *(u32x4*)(op + (db * 4 + 2 * i) * 2048) = (u32x4){pk2(v[b] * inv, v[b + 1] * inv), pk2(v[b + 2] * inv, v[b + 3] * inv), pk2(v[b + 4] * inv, v[b + 5] * inv), pk2(v[b + 6] * inv, v[b + 7] * inv)};
      }
    if (!hasU) return;
    if (grp == 1) {
      waitv<40>(); BAR();
      waitv<32>(); BAR(); ustepP(5, 0); ustepQ(5, 0);
      waitv<32>(); BAR(); ustepP(6, 1); ustepQ(6, 1);
      waitv<32>(); BAR(); ustepP(7, 2); ustepQ(7, 2);
      waitv<32>(); BAR(); ustepP(8, 3); ustepQ(8, 3);
      unsigned stg = 4;
      for (unsigned t = 4; t < 27; ++t) { waitv<24>(); BAR(); ustepP(t + 5, stg); ustepQ(t + 5, stg); stg = stg == 4 ? 0 : stg + 1; }
      waitv<24>(); BAR(); waitv<16>(); BAR(); waitv<8>(); BAR(); waitv<0>(); BAR(); BAR();
      return;
    }
  }
  {
    Opnd<T2> P, Q;
    const unsigned ln = lane_id(), wp = lw >> 1, wqd = lw & 1u, fr = ln & 15u, fq = ln >> 4;
    P.init(L, wp * 64u, ln); Q.init(L + 16384u, wqd * 64u, ln);
    f32x4 acc[4][4] = {};
    gemm_compute<T2, T2, 4, 4, 5, 32768u>(acc, P, Q, 32);
    char* up = (char*)Ug + ((size_t)(udt * 56u + uft * 2u + wp) * 8u + fq) * 2048u + (wqd * 64u + fr) * 16u;
#pragma unroll
    for (int pp = 0; pp < 2; ++pp) {
      const u32x2 ba = *(const u32x2*)(b1 + uft * 128u + wp * 64u + pp * 32u + fq * 4u), bb = *(const u32x2*)(b1 + uft * 128u + wp * 64u + pp * 32u + 16u + fq * 4u);
      const float bias[8] = {bflo(ba[0]), bfhi(ba[0]), bflo(ba[1]), bfhi(ba[1]), bflo(bb[0]), bfhi(bb[0]), bflo(bb[1]), bfhi(bb[1])};
#pragma unroll
      for (int m = 0; m < 4; ++m) {
        float v[8];
#pragma unroll
        for (int j = 0; j < 8; ++j) v[j] = gelu_t(acc[2 * pp + (j >> 2)][m][j & 3] + bias[j]);
        *(u32x4*)(up + pp * 8192 + m * 256) = (u32x4){pk2(v[0], v[1]), pk2(v[2], v[3]), pk2(v[4], v[5]), pk2(v[6], v[7])};
      }
    }
  }
}

__global__ __launch_bounds__(512, 2) void k_fin(const u16* __restrict__ x, const u16* __restrict__ Og, const u16* __restrict__ wo, const u16* __restrict__ Ug,
                                                const u16* __restrict__ w2, const u16* __restrict__ b2, char* __restrict__ Gx, unsigned* __restrict__ flags,
                                                u16* __restrict__ out) {
  extern __shared__ __attribute__((aligned(16))) char lds[];
  const unsigned L = (unsigned)(size_t)lds;
  const unsigned tid = threadIdx.x, lane = tid & 63u, wid = rfl(tid >> 6), lw = wid & 3u;
  const unsigned xcd = blockIdx.x & 7u, idx = blockIdx.x >> 3;
  const unsigned st = (xcd & 1u) * 8u + (idx & 7u), dt = ((xcd >> 1) & 1u) * 4u + (idx >> 3);
  const bool isY = xcd >= 4u;
  const unsigned N = isY ? 56u : 64u;
  if (wid >= 4) {
    const rsrc_t sT = isY ? mkrsrc(w2, FF * SEQ * 2) : mkrsrc(wo, NH * DH * DM * 2);
    const rsrc_t sR = isY ? mkrsrc(Ug, DM * FF * 2) : mkrsrc(Og, SEQ * NH * DH * 2);
    const unsigned ldT = isY ? 4096u : 2048u, voT = voffT(lane, ldT), voL = lane * 16u;
    const unsigned tcol = (isY ? st : dt) * 256u, rbase = (isY ? dt : st) * N * 16384u;
    const unsigned toff = isY ? 16384u : 0u, roff = 16384u - toff;
    auto step = [&](unsigned j, unsigned stg) {
      dmaT4(L + stg * 32768u + toff, lw, voT, sT, j * 64u * ldT + tcol, ldT);
      dmaLin4(L + stg * 32768u + roff, lw, voL, sR, rbase + j * 16384u);
    };
#pragma unroll
    for (int j = 0; j < 5; ++j) step(j, j);
    waitv<32>(); BAR();
    unsigned stg = 0;
    for (unsigned t = 0; t + 5 < N; ++t) { waitv<24>(); BAR(); step(t + 5, stg); stg = stg == 4 ? 0 : stg + 1; }
    waitv<24>(); BAR(); waitv<16>(); BAR(); waitv<8>(); BAR(); waitv<0>(); BAR(); BAR();
    return;
  }
  const unsigned wp = lw >> 1, wq = lw & 1u, fr = lane & 15u, fq = lane >> 4;
  f32x4 acc[4][4] = {};
  if (isY) {
    Opnd<RCHK> P; Opnd<T2> Q;
    P.init(L, wp * 64u, lane); Q.init(L + 16384u, wq * 64u, lane);
    gemm_compute<RCHK, T2, 4, 4, 5, 32768u>(acc, P, Q, 56);
  } else {
    Opnd<T1> P; Opnd<RCHK> Q;
    P.init(L, wp * 64u, lane); Q.init(L + 16384u, wq * 64u, lane);
    gemm_compute<T1, RCHK, 4, 4, 5, 32768u>(acc, P, Q, 64);
  }
  unsigned own[4][4][2];
  const unsigned srow = st * 128u + wq * 64u + fr, dcol = dt * 128u + wp * 64u + fq * 4u;
#pragma unroll
  for (int q = 0; q < 4; ++q) {
    float bias = 0.f;
    if (isY) bias = bflo(b2[srow + q * 16u]);
#pragma unroll
    for (int p = 0; p < 4; ++p) {
      f32x4 v = acc[p][q];
      if (isY) {
#pragma unroll
        for (int i = 0; i < 4; ++i) { const float y = rbf(rbf(v[i]) + bias); v[i] = y * __builtin_amdgcn_rcpf(1.0f + __builtin_amdgcn_exp2f(y * -1.4426950408889634f)); }
      }
      own[p][q][0] = pk2(v[0], v[1]); own[p][q][1] = pk2(v[2], v[3]);
    }
  }
  const unsigned tile = st * 8u + dt;
  unsigned* flag = flags + tile * 16u;
  const rsrc_t sG = mkrsrc(Gx + (size_t)tile * 65536u, 65536u);
  const unsigned mine = (isY ? 32768u : 0u) + tid * 16u, theirs = (isY ? 0u : 32768u) + tid * 16u;
  LAS unsigned* word = (LAS unsigned*)(size_t)L;
  BAR();
  if (tid == 0) word[0] = __hip_atomic_load(flag, __ATOMIC_RELAXED, __HIP_MEMORY_SCOPE_AGENT);
  asm volatile("s_waitcnt vmcnt(0) lgkmcnt(0)" ::: "memory"); BAR();
  unsigned seen = rfl(word[0]);
  if (seen == 0u) {
#pragma unroll
    for (int p = 0; p < 4; ++p)
#pragma unroll
      for (int h2 = 0; h2 < 2; ++h2)
        __builtin_amdgcn_raw_buffer_store_b128((u32x4){own[p][2 * h2][0], own[p][2 * h2][1], own[p][2 * h2 + 1][0], own[p][2 * h2 + 1][1]}, sG, mine + (p * 2 + h2) * 4096u, 0, 16);
    asm volatile("s_waitcnt vmcnt(0)" ::: "memory"); BAR();
    if (tid == 0) word[1] = __hip_atomic_fetch_add(flag, 1u, __ATOMIC_RELAXED, __HIP_MEMORY_SCOPE_AGENT);
    asm volatile("s_waitcnt vmcnt(0) lgkmcnt(0)" ::: "memory"); BAR();
    seen = rfl(word[1]);
    if (seen == 0u) return;
  }
#pragma unroll
  for (int p = 0; p < 4; ++p)
#pragma unroll
    for (int h2 = 0; h2 < 2; ++h2) {
      const u32x4 o = __builtin_amdgcn_raw_buffer_load_b128(sG, theirs + (p * 2 + h2) * 4096u, 0, 16);
#pragma unroll
      for (int e = 0; e < 2; ++e) {
        const int q = 2 * h2 + e;
        const size_t off = (size_t)(srow + q * 16u) * DM + dcol + p * 16u;
        const u32x2 xv = *(const u32x2*)(x + off);
        const unsigned a0 = own[p][q][0], a1 = own[p][q][1], c0 = o[2 * e], c1 = o[2 * e + 1];
        const float r0 = bflo(xv[0]) + rbf(bflo(a0) * bflo(c0)), r1 = bfhi(xv[0]) + rbf(bfhi(a0) * bfhi(c0));
        const float r2 = bflo(xv[1]) + rbf(bflo(a1) * bflo(c1)), r3 = bfhi(xv[1]) + rbf(bfhi(a1) * bfhi(c1));
        *(u32x2*)(out + off) = (u32x2){pk2(r0, r1), pk2(r2, r3)};
      }
    }
}
}

extern "C" void kernel_launch(void* const* d_in, const int* in_sizes, int n_in, void* d_out, int out_size, void* d_ws, size_t ws_size, hipStream_t stream) {
  const u16* x = (const u16*)d_in[0]; const u16* kc = (const u16*)d_in[1]; const u16* vc = (const u16*)d_in[2];
  const u16* wq = (const u16*)d_in[3]; const u16* wo = (const u16*)d_in[4];
  const u16* w1 = (const u16*)d_in[6]; const u16* b1 = (const u16*)d_in[7]; const u16* w2 = (const u16*)d_in[8]; const u16* b2 = (const u16*)d_in[9];
  char* ws = (char*)d_ws;
  (void)hipFuncSetAttribute((const void*)k_mid, hipFuncAttributeMaxDynamicSharedMemorySize, LDS_BYTES);
  (void)hipFuncSetAttribute((const void*)k_fin, hipFuncAttributeMaxDynamicSharedMemorySize, LDS_BYTES);
  k_ln<<<256, 512, 0, stream>>>(x, (u16*)(ws + WS_XN), (u16*)(ws + WS_H), (unsigned*)(ws + WS_FLAG));
  k_mid<<<256, 512, LDS_BYTES, stream>>>((const u16*)(ws + WS_XN), (const u16*)(ws + WS_H), kc, vc, wq, w1, b1, (u16*)(ws + WS_O), (u16*)(ws + WS_U));
  k_fin<<<256, 512, LDS_BYTES, stream>>>(x, (const u16*)(ws + WS_O), wo, (const u16*)(ws + WS_U), w2, b2, ws + WS_G, (unsigned*)(ws + WS_FLAG), (u16*)d_out);
}
```

```cpp
#include <hip/hip_runtime.h>
#include <hip/hip_bf16.h>

namespace {
typedef short bf16x8 __attribute__((ext_vector_type(8)));
typedef short s16x4 __attribute__((ext_vector_type(4)));
typedef float f32x2 __attribute__((ext_vector_type(2)));
typedef float f32x4 __attribute__((ext_vector_type(4)));
typedef float f32x16 __attribute__((ext_vector_type(16)));
typedef unsigned u32x4 __attribute__((ext_vector_type(4)));
typedef unsigned u32x2 __attribute__((ext_vector_type(2)));
typedef __bf16 bf2_t __attribute__((ext_vector_type(2)));
typedef unsigned short u16;
typedef __amdgpu_buffer_rsrc_t rsrc_t;
#define LAS __attribute__((address_space(3)))
#define DI __device__ __forceinline__
#define BAR() asm volatile("s_barrier" ::: "memory")
#define WAITL0() do { asm volatile("" ::: "memory"); __builtin_amdgcn_s_waitcnt(0xC07F); asm volatile("" ::: "memory"); } while (0)
#define SCHED() __builtin_amdgcn_sched_barrier(0)
#define SGB(mask, n) __builtin_amdgcn_sched_group_barrier(mask, n, 0)

constexpr int SEQ = 2048, DM = 1024, NH = 32, DH = 128, SK = 2048, WIN = 512, FF = 3584;
constexpr unsigned WS_XN = 0u, WS_H = 4u << 20, WS_O = 8u << 20, WS_U = 24u << 20, WS_G = 32u << 20, WS_FLAG = 48u << 20;
constexpr unsigned LDS_BYTES = 163840;
#define LNPF 1
#define PFSLEEP 40

#define ST_DECL
#define STAMP(i) do {} while (0)
#define ST_OUT(k)
template <int N> DI void waitv() { asm volatile("s_waitcnt vmcnt(%0)" :: "i"(N) : "memory"); }
DI unsigned rfl(unsigned x) { return __builtin_amdgcn_readfirstlane(x); }
DI rsrc_t mkrsrc(const void* p, unsigned bytes) { return __builtin_amdgcn_make_buffer_rsrc((void*)p, (short)0, (int)bytes, 0x00020000); }
DI void dma16(unsigned m0v, unsigned voff, rsrc_t r, unsigned soff) {
  asm volatile("s_mov_b32 m0, %0\n\ts_nop 0\n\tbuffer_load_dwordx4 %1, %2, %3 offen lds" :: "s"(m0v), "v"(voff), "s"(r), "s"(soff) : "memory");
}
DI bf16x8 ldsR(unsigned a) { return *(const LAS bf16x8*)(size_t)a; }
DI s16x4 ldsT(unsigned a) { return __builtin_amdgcn_ds_read_tr16_b64_v4i16((LAS s16x4*)(size_t)a); }
DI bf16x8 cat(s16x4 a, s16x4 b) { return __builtin_shufflevector(a, b, 0, 1, 2, 3, 4, 5, 6, 7); }
DI unsigned pk2(float lo, float hi) { f32x2 v = {lo, hi}; return __builtin_bit_cast(unsigned, __builtin_convertvector(v, bf2_t)); }
DI float bflo(unsigned w) { return __uint_as_float(w << 16); }
DI float bfhi(unsigned w) { return __uint_as_float(w & 0xffff0000u); }
DI float rbf(float x) { return bflo(pk2(x, x) & 0xffffu); }
template <int CTRL, int RM> DI float dpp_add(float v) {
  return v + __builtin_bit_cast(float, __builtin_amdgcn_update_dpp(0, __builtin_bit_cast(int, v), CTRL, RM, 0xf, false));
}
DI float wsum(float v) {
  v = dpp_add<0x111, 0xf>(v); v = dpp_add<0x112, 0xf>(v); v = dpp_add<0x114, 0xf>(v); v = dpp_add<0x118, 0xf>(v);
  v = dpp_add<0x142, 0xa>(v); v = dpp_add<0x143, 0xc>(v);
  return __builtin_bit_cast(float, __builtin_amdgcn_readlane(__builtin_bit_cast(int, v), 63));
}

DI void ln16(float (&v)[16]) {
  float s = 0.f;
#pragma unroll
  for (int i = 0; i < 16; ++i) s += v[i];
  const float mean = wsum(s) * (1.0f / DM);
  float q = 0.f;
#pragma unroll
  for (int i = 0; i < 16; ++i) { const float d = v[i] - mean; q += d * d; }
  const float var = rbf(wsum(q) * (1.0f / DM));
  const float mb = rbf(mean), inv = 1.0f / rbf(sqrtf(rbf(var + 1e-5f)));
#pragma unroll
  for (int i = 0; i < 16; i += 2) {
    const unsigned d = pk2(v[i] - mb, v[i + 1] - mb), r = pk2(bflo(d) * inv, bfhi(d) * inv);
    v[i] = bflo(r); v[i + 1] = bfhi(r);
  }
}
DI unsigned pkx(float lo, float hi) { return (__float_as_uint(lo) >> 16) | (__float_as_uint(hi) & 0xffff0000u); }
__global__ __launch_bounds__(512) void k_ln(const u16* __restrict__ x, const u16* __restrict__ wq, u16* __restrict__ xn, u16* __restrict__ h, unsigned* __restrict__ flags) {
  const unsigned lane = threadIdx.x & 63u, row = blockIdx.x * 8u + (threadIdx.x >> 6);
  ST_DECL; STAMP(0);
  const u32x4 a = *(const u32x4*)(x + row * DM + lane * 8), b = *(const u32x4*)(x + row * DM + 512 + lane * 8);
  unsigned warm = 0;
  if (threadIdx.x < 64 * LNPF) warm = *(const unsigned*)(wq + ((threadIdx.x >> 6) * 256u + blockIdx.x) * (NH * DH) + lane * 64u);
  if (blockIdx.x == 0 && threadIdx.x < 128) flags[threadIdx.x * 16] = 0u;
  float v[16];
#pragma unroll
  for (int i = 0; i < 4; ++i) { v[2 * i] = bflo(a[i]); v[2 * i + 1] = bfhi(a[i]); v[8 + 2 * i] = bflo(b[i]); v[9 + 2 * i] = bfhi(b[i]); }
  ln16(v);
  u16* xr = xn + row * DM + (lane >> 1) * 16 + (lane & 1u) * 4;
#pragma unroll
  for (int p = 0; p < 2; ++p) {
    *(u32x2*)(xr + 512 * p) = (u32x2){pkx(v[8 * p], v[8 * p + 1]), pkx(v[8 * p + 2], v[8 * p + 3])};
    *(u32x2*)(xr + 512 * p + 8) = (u32x2){pkx(v[8 * p + 4], v[8 * p + 5]), pkx(v[8 * p + 6], v[8 * p + 7])};
  }
  ln16(v);
#pragma unroll
  for (int p = 0; p < 2; ++p)
    *(u32x4*)(h + row * DM + 512 * p + lane * 8) = (u32x4){pkx(v[8 * p], v[8 * p + 1]), pkx(v[8 * p + 2], v[8 * p + 3]), pkx(v[8 * p + 4], v[8 * p + 5]), pkx(v[8 * p + 6], v[8 * p + 7])};
  asm volatile("" :: "v"(warm));
  STAMP(1); ST_OUT(0);
}

enum { RROW = 0, RCHK = 1, T1 = 2, T2 = 3 };
template <int KIND> struct Opnd {
  static constexpr int NA = KIND == RROW ? 2 : KIND == RCHK ? 1 : 4;
  static constexpr int RPB = KIND >= T1 ? 2 : 1;
  unsigned a[NA];
  DI void init(unsigned base, unsigned off, unsigned lane) {
    const unsigned fr = lane & 15u, g = lane >> 4, q = fr >> 2, p = lane & 3u;
    if constexpr (KIND == RROW) {
#pragma unroll
      for (int kk = 0; kk < 2; ++kk) a[kk] = base + (off + fr) * 128u + 16u * ((4u * kk + g) ^ (fr >> 1));
    } else if constexpr (KIND == RCHK) {
      a[0] = base + g * 2048u + (off + fr) * 16u;
    } else {
      const unsigned x = ((q >> 1) << 1) | (g & 1u);
#pragma unroll
      for (int n = 0; n < 4; ++n)
        a[n] = base + (KIND == T1 ? 4096u : 2048u) * (g >> 1) + 1024u * (off >> 6) + 128u * (4u * (g & 1u) + q) + 32u * ((unsigned)n ^ x) + 8u * p;
    }
  }
  DI bf16x8 ld(int b, int kk) const {
    if constexpr (KIND == RROW) return ldsR(a[kk] + 2048u * b);
    else if constexpr (KIND == RCHK) return ldsR(a[0] + 8192u * kk + 256u * b);
    else { const unsigned ad = a[b & 3] + 1024u * (b >> 2) + 8192u * kk; return cat(ldsT(ad), ldsT(ad + (KIND == T1 ? 2048u : 4096u))); }
  }
  DI void adv(unsigned delta) {
#pragma unroll
    for (int i = 0; i < NA; ++i) { a[i] += delta; asm volatile("" : "+v"(a[i])); }
  }
};

template <int PK, int QK, int PM, int QM, int NS, unsigned STG, int TAIL>
DI void gemm_compute(f32x4 (&acc)[PM][QM], Opnd<PK>& P, Opnd<QK>& Q, const int N) {
  bf16x8 pf[2][PM], qf[2][QM];
  constexpr int NR = PM * Opnd<PK>::RPB + QM * Opnd<QK>::RPB, NM = PM * QM, NA = Opnd<PK>::NA + Opnd<QK>::NA;
  auto load = [&](int kk) {
    pf[kk][0] = P.ld(0, kk);
#pragma unroll
    for (int q = 0; q < QM; ++q) qf[kk][q] = Q.ld(q, kk);
#pragma unroll
    for (int p = 1; p < PM; ++p) pf[kk][p] = P.ld(p, kk);
  };
  auto mma = [&](int kk) {
#pragma unroll
    for (int p = 0; p < PM; ++p)
#pragma unroll
      for (int q = 0; q < QM; ++q) acc[p][q] = __builtin_amdgcn_mfma_f32_16x16x32_bf16(pf[kk][p], qf[kk][q], acc[p][q], 0, 0, 0);
  };
  constexpr int ND = (NR + NA + 2 > NM) ? (NR + NA + 2 - NM) : 0;
  BAR();
  load(0);
  int stg = 0;
  auto step = [&](bool sync) {
    SCHED();
    load(1); mma(0);
    stg = (stg == NS - 1) ? 0 : stg + 1;
    const unsigned delta = stg == 0 ? (unsigned)(-(int)((NS - 1) * STG)) : STG;
    P.adv(delta); Q.adv(delta);
#pragma unroll
    for (int i = 0; i < ND; ++i) { SGB(0x8, 1); SGB(0x100, 2); }
#pragma unroll
    for (int i = 0; i < NR - 2 * ND; ++i) { SGB(0x8, 1); SGB(0x100, 1); }
#pragma unroll
    for (int i = 0; i < NA; ++i) { SGB(0x8, 1); SGB(0x2, 1); }
    SGB(0x8, NM);
    SCHED();
    if (sync) { WAITL0(); BAR(); }
    SCHED();
    load(0); mma(1);
#pragma unroll
    for (int i = 0; i < ND; ++i) { SGB(0x8, 1); SGB(0x100, 2); }
#pragma unroll
    for (int i = 0; i < NR - 2 * ND; ++i) { SGB(0x8, 1); SGB(0x100, 1); }
    SGB(0x8, NM);
  };
  for (int t = 0; t < N - TAIL; ++t) step(true);
  for (int t = 0; t < TAIL; ++t) step(false);
  SCHED();
}

DI unsigned voffRow(unsigned lane, unsigned ldb, unsigned par) { return (lane >> 3) * ldb + 16u * ((lane & 7u) ^ (4u * par + (lane >> 4))); }
DI unsigned voffT(unsigned lane, unsigned ldb) { return (lane >> 3) * ldb + 16u * ((lane & 7u) ^ (2u * ((((lane >> 4) & 1u) << 1) | (lane >> 5)))); }
DI void dmaT4(unsigned dst, unsigned lw, unsigned vo, rsrc_t r, unsigned so, unsigned ldb) {
#pragma unroll
  for (int q = 0; q < 4; ++q) dma16(dst + lw * 4096u + 1024u * q, vo, r, so + (lw * 16u + (q >> 1) * 8u) * ldb + (q & 1) * 128u);
}
DI void dmaLin4(unsigned dst, unsigned lw, unsigned vo, rsrc_t r, unsigned so) {
#pragma unroll
  for (int q = 0; q < 4; ++q) dma16(dst + lw * 4096u + 1024u * q, vo, r, so + lw * 4096u + 1024u * q);
}

DI float gelu_t(float v) {
  const float z = v * (1.0f + 0.044715f * v * v) * (-2.3022081986f);
  return v * __builtin_amdgcn_rcpf(1.0f + __builtin_amdgcn_exp2f(z));
}

constexpr unsigned QSTG = 49152u;
constexpr unsigned KBASE = 65536u, VBASE = 114688u;
constexpr unsigned KSLOT[3] = {0u, 32768u, 16384u}, VSLOT[3] = {32768u, 16384u, 0u};
DI void touch8k(rsrc_t r, unsigned vo128, unsigned soff) {
  asm volatile("buffer_load_dword v255, %0, %1, %2 offen" :: "v"(vo128), "s"(r), "s"(soff) : "memory", "v255");
}
constexpr float QSCALE = 0.12751743082f;
#define BIGSUM 4096.0f

#define ATTPRIO 0
#define PRIO(x) do { if (ATTPRIO == 1) __builtin_amdgcn_s_setprio(x); else if (ATTPRIO == 2) __builtin_amdgcn_s_setprio(1 - (x)); } while (0)
struct Att {
  bf16x8 qf[8], kv[16], pf[4], pn[2];
  f32x16 o[4], s[2], negm;
  float l, ts;
  unsigned aK[4], aV[2];
};
template <unsigned SLOT, int J> DI void att_k(Att& a) {
  asm volatile("ds_read_b128 %0, %1 offset:%2" : "+v"(a.kv[J]) : "v"(a.aK[(J >> 1) & 3]), "i"(SLOT + 8192u * (J >> 3) + 4096u * (J & 1)) : "memory");
}
template <unsigned SLOT, int J> DI void att_v(Att& a) {
  s16x4 lo = __builtin_shufflevector(a.kv[J], a.kv[J], 0, 1, 2, 3), hi = __builtin_shufflevector(a.kv[J], a.kv[J], 4, 5, 6, 7);
  constexpr unsigned off = SLOT + 4096u * (J >> 2) + 1024u * ((J >> 1) & 1);
  asm volatile("ds_read_b64_tr_b16 %0, %2 offset:%3\n\tds_read_b64_tr_b16 %1, %2 offset:%4" : "+v"(lo), "+v"(hi) : "v"(a.aV[J & 1]), "i"(off), "i"(off + 2048u) : "memory");
  a.kv[J] = cat(lo, hi);
}
DI void att_wait(Att& a) {
  asm volatile("s_waitcnt lgkmcnt(0)" : "+v"(a.kv[0]), "+v"(a.kv[1]), "+v"(a.kv[2]), "+v"(a.kv[3]), "+v"(a.kv[4]), "+v"(a.kv[5]), "+v"(a.kv[6]), "+v"(a.kv[7]),
               "+v"(a.kv[8]), "+v"(a.kv[9]), "+v"(a.kv[10]), "+v"(a.kv[11]), "+v"(a.kv[12]), "+v"(a.kv[13]), "+v"(a.kv[14]), "+v"(a.kv[15]) :: "memory");
}
DI void att_loadK(Att& a, unsigned slot) {
#pragma unroll
  for (int j = 0; j < 16; ++j) a.kv[j] = ldsR(a.aK[(j >> 1) & 3] + slot + 8192u * (j >> 3) + 4096u * (j & 1));
}
DI unsigned lane_id() { unsigned l; asm volatile("v_mbcnt_lo_u32_b32 %0, -1, 0\n\tv_mbcnt_hi_u32_b32 %0, -1, %0" : "=v"(l)); return l; }
DI void add_bcast3(f32x16& t0, f32x16& t1, f32x16& t2, float v) {
  const float one = lane_id() < 32u ? 1.0f : 0.0f;
  asm volatile("s_nop 7\n\ts_nop 7\n\tv_mfma_f32_32x32x2_f32 %0, %3, %4, %0\n\tv_mfma_f32_32x32x2_f32 %1, %3, %4, %1\n\tv_mfma_f32_32x32x2_f32 %2, %3, %4, %2\n\t"
               "s_nop 7\n\ts_nop 7\n\ts_nop 7" : "+v"(t0), "+v"(t1), "+v"(t2) : "v"(one), "v"(v));
}
template <bool FIRST, unsigned SLOT, int J> DI void att_qk1(Att& a) {
  a.s[J & 1] = __builtin_amdgcn_mfma_f32_32x32x16_bf16(a.kv[J], a.qf[J >> 1], J < 2 ? (FIRST ? (f32x16){} : a.negm) : a.s[J & 1], 0, 0, 0);
  SCHED();
  if constexpr (!FIRST) att_v<SLOT, J>(a);
  SCHED();
  if constexpr (J < 15) att_qk1<FIRST, SLOT, J + 1>(a);
}
template <int NEXT, unsigned SLOT, int J> DI void att_pv1(Att& a) {
  a.o[J & 3] = __builtin_amdgcn_mfma_f32_32x32x16_bf16(a.kv[J], a.pf[J >> 2], a.o[J & 3], 0, 0, 0);
  SCHED();
  if constexpr (NEXT == 1) att_k<SLOT, J>(a); else if constexpr (NEXT == 2) att_v<SLOT, J>(a);
  SCHED();
  if constexpr (J < 15) att_pv1<NEXT, SLOT, J + 1>(a);
}
DI float max3(float x, float y, float z) { return __builtin_fmaxf(__builtin_fmaxf(x, y), z); }
DI float att_rowmax(const Att& a) {
  float m0 = max3(a.s[0][0], a.s[0][1], a.s[0][2]), m1 = max3(a.s[1][0], a.s[1][1], a.s[1][2]);
#pragma unroll
  for (int r = 3; r < 15; r += 2) { m0 = max3(m0, a.s[0][r], a.s[0][r + 1]); m1 = max3(m1, a.s[1][r], a.s[1][r + 1]); }
  const float mx = __builtin_fmaxf(max3(m0, m1, a.s[0][15]), a.s[1][15]);
  auto rr = __builtin_amdgcn_permlane32_swap(__float_as_uint(mx), __float_as_uint(mx), false, false);
  return __builtin_fmaxf(__uint_as_float(rr[0]), __uint_as_float(rr[1]));
}
DI float att_exp16(const f32x16& v, bf16x8& p0, bf16x8& p1) {
  float s0 = 0.f, s1 = 0.f;
  u32x4 w[2];
#pragma unroll
  for (int h = 0; h < 2; ++h) {
    float e[8];
#pragma unroll
    for (int r = 0; r < 8; ++r) e[r] = __builtin_amdgcn_exp2f(v[8 * h + r]);
#pragma unroll
    for (int r = 0; r < 8; r += 2) {
      if (h == 0 && r == 0) { s0 = e[0]; s1 = e[1]; } else { s0 += e[r]; asm("" : "+v"(s0)); s1 += e[r + 1]; asm("" : "+v"(s1)); }
      w[h][r >> 1] = pk2(e[r], e[r + 1]);
    }
    asm volatile("" : "+v"(w[h]));
    SCHED();
  }
  p0 = __builtin_bit_cast(bf16x8, w[0]); p1 = __builtin_bit_cast(bf16x8, w[1]);
  return s0 + s1;
}
template <bool FIRST> DI void att_sm1(Att& a) {
  if (FIRST) {
    const float mx = att_rowmax(a);
#pragma unroll
    for (int r = 0; r < 16; ++r) { a.s[0][r] -= mx; a.s[1][r] -= mx; a.negm[r] = -mx; }
  }
  a.ts = att_exp16(a.s[0], a.pn[0], a.pn[1]);
  asm volatile("" : "+v"(a.pn[0]), "+v"(a.pn[1]), "+v"(a.ts), "+v"(a.s[1]), "+v"(a.negm));
}
template <bool FIRST> DI void att_sm2(Att& a) {
  a.ts += att_exp16(a.s[1], a.pf[2], a.pf[3]);
  if (!FIRST) {
    if (__builtin_expect(!__all(a.ts <= BIGSUM), 0)) {
      const float dl = fmaxf(att_rowmax(a), 0.f), fct = __builtin_amdgcn_exp2f(-dl);
#pragma unroll
      for (int db = 0; db < 4; ++db) a.o[db] *= fct;
      a.l *= fct;
      add_bcast3(a.s[0], a.s[1], a.negm, -dl);
      a.ts = att_exp16(a.s[0], a.pn[0], a.pn[1]) + att_exp16(a.s[1], a.pf[2], a.pf[3]);
    }
  }
  a.l += a.ts;
  a.pf[0] = a.pn[0]; a.pf[1] = a.pn[1];
  asm volatile("" : "+v"(a.pf[0]), "+v"(a.pf[1]), "+v"(a.pf[2]), "+v"(a.pf[3]), "+v"(a.l));
}

__global__ __launch_bounds__(512, 2) __attribute__((amdgpu_num_vgpr(127)))
void k_mid(const u16* __restrict__ xn, const u16* __restrict__ hh, const u16* __restrict__ kc, const u16* __restrict__ vc,
           const u16* __restrict__ wq, const u16* __restrict__ w1, const u16* __restrict__ b1, const u16* __restrict__ wo, const u16* __restrict__ w2,
           u16* __restrict__ Og, u16* __restrict__ Ug) {
  extern __shared__ __attribute__((aligned(16))) char lds[];
  const unsigned L = (unsigned)(size_t)lds;
  const unsigned tid = threadIdx.x, lane = tid & 63u, wid = rfl(tid >> 6), grp = wid >> 2, lw = wid & 3u;
  const unsigned bid = blockIdx.x, xcd = bid & 7u, idx = bid >> 3;
  const unsigned qt = (xcd & 1u) * 4u + (idx & 3u), kvh = xcd >> 1, head = kvh * 8u + (idx >> 2);
  const bool hasU = idx < 28u;
  const unsigned udt = (xcd & 1u) * 4u + (idx & 3u), uft = (xcd >> 1) * 7u + (idx >> 2);
  const rsrc_t sK = mkrsrc(kc + ((size_t)kvh * SK + (SK - WIN)) * DH, WIN * DH * 2), sV = mkrsrc(vc + ((size_t)kvh * SK + (SK - WIN)) * DH, WIN * DH * 2);
  const rsrc_t sH = mkrsrc(hh, SEQ * DM * 2), sW1 = mkrsrc(w1, SEQ * FF * 2);
  const unsigned voK0 = voffRow(lane, 256u, 0), voV = voffT(lane, 256u);
  auto dmaK2 = [&](unsigned slot, unsigned t, unsigned p0) {
    unsigned voK1; asm volatile("v_xor_b32 %0, 64, %1" : "=v"(voK1) : "v"(voK0));
    dma16(L + KBASE + slot + 1024u * p0, voK0, sK, t * 16384u + (p0 & 7u) * 2048u + (p0 >> 3) * 128u);
    dma16(L + KBASE + slot + 1024u * p0 + 1024u, voK1, sK, t * 16384u + (p0 & 7u) * 2048u + 2048u + (p0 >> 3) * 128u); };
  auto dmaV2 = [&](unsigned slot, unsigned t, unsigned p0) {
    dma16(L + VBASE + slot + 1024u * p0, voV, sV, t * 16384u + (p0 >> 1) * 2048u);
    dma16(L + VBASE + slot + 1024u * p0 + 1024u, voV, sV, t * 16384u + (p0 >> 1) * 2048u + 128u); };
  auto tileK = [&](unsigned t) {
#pragma unroll
    for (int p = 0; p < 16; p += 2) dmaK2(KSLOT[t % 3], t, p); };
  auto tileV = [&](unsigned t) {
#pragma unroll
    for (int p = 0; p < 16; p += 2) dmaV2(VSLOT[t % 3], t, p); };
  Att a;
  { const unsigned r32 = lane & 31u, hi = lane >> 5;
#pragma unroll
    for (int j = 0; j < 4; ++j) a.aK[j] = L + KBASE + r32 * 128u + 16u * ((2u * j + hi) ^ ((r32 >> 1) & 7u)); }

  ST_DECL; STAMP(0);
  if (wid == 7) {
    const rsrc_t sWq = mkrsrc(wq, DM * NH * DH * 2);
    const unsigned vo = lane * 128u;
    __builtin_amdgcn_s_sleep(PFSLEEP);
#pragma unroll
    for (int j = LNPF; j < 4; ++j) touch8k(sWq, vo, (j * 256u + bid) * 8192u);
    if (idx < 16u) touch8k(sK, vo, idx * 8192u); else touch8k(sV, vo, (idx - 16u) * 8192u);
    for (unsigned t = 0; t < 17; ++t) BAR();
#pragma unroll
    for (int j = 0; j < 3; ++j) touch8k(sW1, vo, (j * 256u + bid) * 8192u);
    att_loadK(a, KSLOT[0]);
  } else if (grp == 1) {
    const rsrc_t sS = lw == 0 ? mkrsrc(wq, DM * NH * DH * 2) : mkrsrc(xn, SEQ * DM * 2);
    const unsigned vo0 = lw == 0 ? voffT(lane, 8192u) : voffRow(lane, 2048u, 0), vo1 = lw == 0 ? vo0 : vo0 ^ 64u;
    const unsigned s0 = lw == 0 ? head * 256u : (qt * 256u + (lw - 1u) * 128u) * 2048u;
    const unsigned sj = lw == 0 ? 64u * 8192u : 128u, sp = lw == 0 ? 8u * 8192u : 16u * 2048u, so = lw == 0 ? 128u : 8u * 2048u;
    auto step = [&](unsigned j, unsigned stg) {
      const unsigned dst = L + stg * QSTG + lw * 16384u, src = s0 + j * sj;
#pragma unroll
      for (int q = 0; q < 8; ++q) { dma16(dst + 2048u * q, vo0, sS, src + q * sp); dma16(dst + 2048u * q + 1024u, vo1, sS, src + q * sp + so); }
    };
    step(0, 0); step(1, 1); step(2, 2);
    waitv<32>(); BAR(); STAMP(1);
    unsigned stg = 0;
    for (unsigned t = 0; t < 13; ++t) { waitv<16>(); BAR(); step(t + 3, stg); stg = stg == 2 ? 0 : stg + 1; }
    waitv<16>(); BAR();
    if (lw == 0) tileK(0); else if (lw == 1) tileK(2); else tileV(0);
    waitv<16>(); BAR();
    if (lw == 1) tileK(1); else if (lw == 2) tileV(1);
    if (lw == 0) waitv<0>();
    BAR();
    att_loadK(a, KSLOT[0]);
  } else {
    Opnd<T1> P; Opnd<RROW> Q;
    P.init(L, 0u, lane); Q.init(L + 16384u, lw * 64u, lane);
    f32x4 acc[8][4] = {};
    gemm_compute<T1, RROW, 8, 4, 3, QSTG, 0>(acc, P, Q, 16);
    STAMP(2);
    att_loadK(a, KSLOT[0]);
    const unsigned wa = L + (lane >> 5) * 4096u + (lw * 64u + (lane & 15u)) * 16u + ((lane >> 4) & 1u) * 8u;
#pragma unroll
    for (int n = 0; n < 8; ++n)
#pragma unroll
      for (int m = 0; m < 4; ++m) {
        const f32x4 v = acc[n][m] * QSCALE;
        *(LAS u32x2*)(size_t)(wa + 8192u * n + 256u * m) = (u32x2){pk2(v[0], v[1]), pk2(v[2], v[3])};
      }
  }
  WAITL0(); BAR(); STAMP(3);

  {
    const unsigned l2 = lane_id(), hi = l2 >> 5, blk = (l2 >> 4) & 1u, q4 = (l2 & 15u) >> 2, p4 = l2 & 3u;
#pragma unroll
    for (int ks = 0; ks < 8; ++ks) a.qf[ks] = ldsR(L + (2u * ks) * 4096u + hi * 4096u + (wid * 32u + (l2 & 31u)) * 16u);
#pragma unroll
    for (int n = 0; n < 2; ++n) a.aV[n] = L + VBASE + 128u * (4u * hi + q4) + 32u * ((2u * n + blk) ^ (((q4 >> 1) << 1) | hi)) + 8u * p4;
#pragma unroll
    for (int db = 0; db < 4; ++db) a.o[db] = (f32x16){};
    a.l = 0.f;
    auto ustepP_ = [&](unsigned j, unsigned stg, unsigned vo) { dmaT4(L + stg * 32768u, lw, vo, sW1, j * 64u * (FF * 2u) + uft * 256u, FF * 2u); };
    auto ustepQ_ = [&](unsigned j, unsigned stg, unsigned vo) { dmaT4(L + stg * 32768u + 16384u, lw, vo, sH, j * 64u * 2048u + udt * 256u, 2048u); };
    const bool pre = hasU && grp == 1;
    if (grp == 1) { if (wid != 7) waitv<0>(); WAITL0(); BAR(); }
    STAMP(4);
    const rsrc_t sWo = mkrsrc(wo, NH * DH * DM * 2), sW2 = mkrsrc(w2, FF * SEQ * 2);
    const unsigned lk = bid * 8192u + (wid >> 1) * 2048u + (wid & 1u) * 128u;
#define LOOK(v) do { if ((v) < 4) touch8k(sW1, voV, lk + ((v) + 3) * 0x200000u); else if ((v) < 8) touch8k(sWo, voV, lk + ((v) - 4) * 0x200000u); \
                     else if ((v) < 14) touch8k(sW2, voV, lk + ((v) - 8) * 0x200000u); } while (0)
#define USTEP(i) ((i) < 5 ? (i) - 3 : (i) - 4)
#define ATT_TILE(i, FIRST, WB, WBP, WD, WDP)                                                                                  \
    if ((i) == 2) STAMP(11);                                                                                                  \
    SCHED(); att_qk1<FIRST, VSLOT[((i) + 2) % 3], 0>(a); if ((i) == 2) STAMP(12); BAR(); SCHED(); if ((i) == 2) STAMP(13);    \
    if ((i) <= 4) dmaK2(KSLOT[(i) % 3], (i) + 3, wid * 2u);                                                                   \
    LOOK(2 * (i));                                                                                                            \
    att_sm1<FIRST>(a);                                                                                                        \
    if ((i) >= 3 && pre) { waitv<WBP>(); if ((i) != 5) ustepP_(USTEP(i), USTEP(i), voffT(lane_id(), FF * 2u)); } else waitv<WB>(); \
    if (!(FIRST)) att_wait(a); SCHED(); if ((i) == 2) STAMP(14); BAR(); if ((i) == 2) STAMP(15);                              \
    if (FIRST) att_loadK(a, KSLOT[1]); else { SCHED(); att_pv1<((i) < 7 ? 1 : 2), ((i) < 7 ? KSLOT[((i) + 1) % 3] : VSLOT[7 % 3]), 0>(a); } \
    if ((i) == 2) STAMP(16); BAR(); SCHED(); if ((i) == 2) STAMP(17);                                                         \
    if ((i) <= 5) dmaV2(VSLOT[((i) + 2) % 3], (i) + 2, wid * 2u);                                                             \
    LOOK(2 * (i) + 1);                                                                                                        \
    att_sm2<FIRST>(a);                                                                                                        \
    if ((i) >= 3 && pre) { waitv<WDP>(); if ((i) != 5) ustepQ_(USTEP(i), USTEP(i), voffT(lane_id(), 2048u)); } else waitv<WD>(); \
    att_wait(a); SCHED(); if ((i) == 2) STAMP(18); BAR(); if ((i) == 2) STAMP(19);
    ATT_TILE(0, true, 63, 63, 63, 63) ATT_TILE(1, false, 63, 63, 10, 10) ATT_TILE(2, false, 10, 10, 10, 10) ATT_TILE(3, false, 10, 10, 10, 14)
    ATT_TILE(4, false, 10, 18, 10, 22) ATT_TILE(5, false, 8, 20, 8, 16) ATT_TILE(6, false, 6, 10, 3, 7) ATT_TILE(7, false, 63, 63, 63, 63)
#undef ATT_TILE
#undef USTEP
#undef LOOK
    SCHED(); att_pv1<0, 0u, 0>(a); STAMP(5);
    if (grp == 0) BAR();
    const unsigned ln = lane_id();
    { auto rr = __builtin_amdgcn_permlane32_swap(__float_as_uint(a.l), __float_as_uint(a.l), false, false);
      a.l = __uint_as_float(rr[0]) + __uint_as_float(rr[1]); }
    const float inv = 1.0f / a.l;
    const unsigned row = qt * 256u + wid * 32u + (ln & 31u);
    char* op = (char*)Og + ((size_t)((row >> 7) * 64u + head * 2u) * 8u + (ln >> 5)) * 2048u + (row & 127u) * 16u;
    auto ostore = [&](int db) {
#pragma unroll
      for (int i = 0; i < 2; ++i) {
        const f32x16& v = a.o[db]; const int b = 8 * i;
        *(u32x4*)(op + (db * 4 + 2 * i) * 2048) = (u32x4){pk2(v[b] * inv, v[b + 1] * inv), pk2(v[b + 2] * inv, v[b + 3] * inv), pk2(v[b + 4] * inv, v[b + 5] * inv), pk2(v[b + 6] * inv, v[b + 7] * inv)};
      }
    };
    if (pre) {
      const unsigned voH = voffT(ln, 2048u), voW1 = voffT(ln, FF * 2u);
      auto ustep = [&](unsigned j, unsigned stg) { ustepP_(j, stg, voW1); ustepQ_(j, stg, voH); };
      waitv<24>(); BAR(); STAMP(7);
      ustep(4, 4);
      ostore(0); waitv<26>(); BAR(); ustep(5, 0);
      ostore(1); waitv<28>(); BAR(); ustep(6, 1);
      ostore(2); waitv<30>(); BAR(); ustep(7, 2);
      ostore(3); waitv<32>(); BAR(); ustep(8, 3);
      unsigned stg = 4;
      for (unsigned t = 4; t < 27; ++t) { waitv<24>(); BAR(); ustep(t + 5, stg); stg = stg == 4 ? 0 : stg + 1; }
      waitv<24>(); BAR(); waitv<16>(); BAR(); waitv<8>(); BAR(); waitv<0>(); BAR(); BAR();
      STAMP(8); ST_OUT(1); return;
    }
    ostore(0); ostore(1); ostore(2); ostore(3);
    STAMP(6);
    if (!hasU) {
      const unsigned w = ((idx - 28u) * 8u + xcd) * 8u + wid, vo = ln * 128u;
      touch8k(sW2, vo, (6u * 256u + w) * 8192u);
      waitv<0>(); STAMP(7);
      ST_OUT(1); return;
    }
  }
  {
    Opnd<T2> P, Q;
    const unsigned ln = lane_id(), wp = lw >> 1, wqd = lw & 1u, fr = ln & 15u, fq = ln >> 4;
    P.init(L, wp * 64u, ln); Q.init(L + 16384u, wqd * 64u, ln);
    u32x2 bv[4];
#pragma unroll
    for (int p = 0; p < 4; ++p) bv[p] = *(const u32x2*)(b1 + uft * 128u + wp * 64u + p * 16u + fq * 4u);
    f32x4 acc[4][4] = {};
    gemm_compute<T2, T2, 4, 4, 5, 32768u, 0>(acc, P, Q, 32);
    STAMP(8);
    char* up = (char*)Ug + ((size_t)(udt * 56u + uft * 2u + wp) * 8u + fq) * 2048u + (wqd * 64u + fr) * 16u;
#pragma unroll
    for (int pp = 0; pp < 2; ++pp) {
      const u32x2 ba = bv[2 * pp], bb = bv[2 * pp + 1];
      const float bias[8] = {bflo(ba[0]), bfhi(ba[0]), bflo(ba[1]), bfhi(ba[1]), bflo(bb[0]), bfhi(bb[0]), bflo(bb[1]), bfhi(bb[1])};
#pragma unroll
      for (int m = 0; m < 4; ++m) {
        float v[8];
#pragma unroll
        for (int j = 0; j < 8; ++j) v[j] = gelu_t(acc[2 * pp + (j >> 2)][m][j & 3] + bias[j]);
        *(u32x4*)(up + pp * 8192 + m * 256) = (u32x4){pk2(v[0], v[1]), pk2(v[2], v[3]), pk2(v[4], v[5]), pk2(v[6], v[7])};
      }
    }
    STAMP(9); waitv<0>(); STAMP(10); ST_OUT(1);
  }
}

constexpr unsigned GSLOT = 98304u;
__global__ __launch_bounds__(512, 2) void k_fin(const u16* __restrict__ x, const u16* __restrict__ Og, const u16* __restrict__ wo, const u16* __restrict__ Ug,
                                                const u16* __restrict__ w2, const u16* __restrict__ b2, char* __restrict__ Gx, unsigned* __restrict__ flags,
                                                u16* __restrict__ out) {
  extern __shared__ __attribute__((aligned(16))) char lds[];
  const unsigned L = (unsigned)(size_t)lds;
  const unsigned tid = threadIdx.x, lane = tid & 63u, wid = rfl(tid >> 6), lw = wid & 3u;
  const unsigned xcd = blockIdx.x & 7u, idx = blockIdx.x >> 3;
  const unsigned st = (xcd & 1u) * 8u + (idx & 7u), dt = ((xcd >> 1) & 1u) * 4u + (idx >> 3);
  const bool isY = xcd >= 4u;
  const unsigned N = isY ? 56u : 64u;
  const unsigned XL = L + (isY ? 1u : 4u) * 32768u, GL = L + (isY ? 2u : 0u) * 32768u, WL = L + (isY ? 3u : 1u) * 32768u;
  const unsigned tile = st * 8u + dt;
  unsigned* flag = flags + tile * 16u;
  const rsrc_t sG = mkrsrc(Gx + (size_t)tile * GSLOT, GSLOT);
  ST_DECL; STAMP(0);
  f32x4 acc[4][4] = {};
  u32x4 gp[8];
  const unsigned wp = lw >> 1, wq = lw & 1u, fr = lane & 15u, fq = lane >> 4;
  if (wid >= 4) {
    const rsrc_t sT = isY ? mkrsrc(w2, FF * SEQ * 2) : mkrsrc(wo, NH * DH * DM * 2);
    const rsrc_t sR = isY ? mkrsrc(Ug, DM * FF * 2) : mkrsrc(Og, SEQ * NH * DH * 2);
    const unsigned ldT = isY ? 4096u : 2048u, voT = voffT(lane, ldT), voL = lane * 16u;
    const unsigned tcol = (isY ? st : dt) * 256u, rbase = (isY ? dt : st) * N * 16384u;
    const unsigned toff = isY ? 16384u : 0u, roff = 16384u - toff;
    auto step = [&](unsigned j, unsigned stg) {
      dmaT4(L + stg * 32768u + toff, lw, voT, sT, j * 64u * ldT + tcol, ldT);
      dmaLin4(L + stg * 32768u + roff, lw, voL, sR, rbase + j * 16384u);
    };
#pragma unroll
    for (int j = 0; j < 5; ++j) step(j, j);
    waitv<32>(); BAR(); STAMP(1);
    unsigned stg = 0;
    for (unsigned t = 0; t + 5 < N; ++t) { waitv<24>(); BAR(); step(t + 5, stg); stg = stg == 4 ? 0 : stg + 1; }
    waitv<24>(); BAR();
    {
      const rsrc_t sX = mkrsrc(x, SEQ * DM * 2);
#pragma unroll
      for (int q = 0; q < 8; ++q)
        dma16(XL + (lw * 8u + q) * 1024u, (lane >> 4) * 2048u + 16u * ((lane & 15u) ^ ((4u * q + (lane >> 4)) & 15u)), sX, (st * 128u + lw * 32u + q * 4u) * 2048u + dt * 256u);
    }
    waitv<8>(); BAR();
    STAMP(2);
    if (!isY) {
      const unsigned long long t0 = __builtin_amdgcn_s_memrealtime();
      bool ok;
      for (;;) {
        ok = rfl(__hip_atomic_load(flag, __ATOMIC_RELAXED, __HIP_MEMORY_SCOPE_AGENT)) != 0u;
        if (ok || __builtin_amdgcn_s_memrealtime() - t0 > 500ull) break;
        __builtin_amdgcn_s_sleep(2);
      }
      if (ok) {
        asm volatile("" ::: "memory");
        u32x4 g[8];
#pragma unroll
        for (int q = 0; q < 8; ++q) g[q] = __builtin_amdgcn_raw_buffer_load_b128(sG, voL + (lw * 8u + q) * 1024u, 0, 16);
#pragma unroll
        for (int q = 0; q < 8; ++q) *(LAS u32x4*)(size_t)(GL + voL + (lw * 8u + q) * 1024u) = g[q];
      } else if (lane == 0) *(LAS unsigned*)(size_t)(GL + lw * 8192u) = 0xffffffffu;
    }
    waitv<0>(); STAMP(3);
  } else {
    if (isY) {
      float bias[4];
#pragma unroll
      for (int q = 0; q < 4; ++q) bias[q] = bflo(b2[st * 128u + wq * 64u + q * 16u + fr]);
      Opnd<RCHK> P; Opnd<T2> Q;
      P.init(L, wp * 64u, lane); Q.init(L + 16384u, wq * 64u, lane);
      gemm_compute<RCHK, T2, 4, 4, 5, 32768u, 3>(acc, P, Q, 56);
      STAMP(2);
#pragma unroll
      for (int j = 0; j < 8; ++j) {
        float g[8];
#pragma unroll
        for (int c = 0; c < 8; ++c) {
          const int q = 2 * (j & 1) + (c >> 2); const float y = acc[j >> 1][q][c & 3] + bias[q];
          g[c] = y * __builtin_amdgcn_rcpf(1.0f + __builtin_amdgcn_exp2f(y * -1.4426950408889634f));
        }
        gp[j] = (u32x4){pk2(g[0], g[1]), pk2(g[2], g[3]), pk2(g[4], g[5]), pk2(g[6], g[7])};
        __builtin_amdgcn_raw_buffer_store_b128(gp[j], sG, tid * 16u + j * 4096u, 0, 16);
      }
      waitv<0>();
    } else {
      Opnd<T1> P; Opnd<RCHK> Q;
      P.init(L, wp * 64u, lane); Q.init(L + 16384u, wq * 64u, lane);
      gemm_compute<T1, RCHK, 4, 4, 5, 32768u, 3>(acc, P, Q, 64);
      STAMP(2);
    }
  }
  WAITL0(); BAR();
  bool fast = false;
  if (!isY) fast = !__any(*(const LAS unsigned*)(size_t)(GL + (lane & 3u) * 8192u) == 0xffffffffu);
  if (!fast) {
    if (!isY) {
      if (wid < 4) {
#pragma unroll
        for (int j = 0; j < 16; ++j) __builtin_amdgcn_raw_buffer_store_b128(__builtin_bit_cast(u32x4, acc[j >> 2][j & 3]), sG, 32768u + tid * 16u + j * 4096u, 0, 16);
      }
      waitv<0>(); BAR();
    }
    if (tid == 0) *(LAS unsigned*)(size_t)WL = __hip_atomic_fetch_add(flag, 1u, __ATOMIC_RELAXED, __HIP_MEMORY_SCOPE_AGENT);
    asm volatile("s_waitcnt vmcnt(0) lgkmcnt(0)" ::: "memory"); BAR();
    if (rfl(*(const LAS unsigned*)(size_t)WL) == 0u) { STAMP(5); ST_OUT(2); return; }
  }
  if (wid < 4) {
    STAMP(3);
    if (fast) {
#pragma unroll
      for (int j = 0; j < 8; ++j) gp[j] = *(const LAS u32x4*)(size_t)(GL + tid * 16u + j * 4096u);
    } else if (isY) {
#pragma unroll
      for (int j = 0; j < 16; ++j) acc[j >> 2][j & 3] = __builtin_bit_cast(f32x4, __builtin_amdgcn_raw_buffer_load_b128(sG, 32768u + tid * 16u + j * 4096u, 0, 16));
    } else {
#pragma unroll
      for (int j = 0; j < 8; ++j) gp[j] = __builtin_amdgcn_raw_buffer_load_b128(sG, tid * 16u + j * 4096u, 0, 16);
    }
    const unsigned xa = XL + (wq * 64u + fr) * 256u + (fq & 1u) * 8u;
    u32x2 xv[4][4];
#pragma unroll
    for (int p = 0; p < 4; ++p)
#pragma unroll
      for (int q = 0; q < 4; ++q) xv[p][q] = *(const LAS u32x2*)(size_t)(xa + 16u * ((wp * 8u + 2u * p + (fq >> 1)) ^ fr) + q * 4096u);
#pragma unroll
    for (int p = 0; p < 4; ++p)
#pragma unroll
      for (int q = 0; q < 4; ++q) {
        const f32x4 v = acc[p][q]; const unsigned g0 = gp[2 * p + (q >> 1)][2 * (q & 1)], g1 = gp[2 * p + (q >> 1)][2 * (q & 1) + 1];
        *(LAS u32x2*)(size_t)(xa + 16u * ((wp * 8u + 2u * p + (fq >> 1)) ^ fr) + q * 4096u) =
            (u32x2){pk2(fmaf(v[0], bflo(g0), bflo(xv[p][q][0])), fmaf(v[1], bfhi(g0), bfhi(xv[p][q][0]))), pk2(fmaf(v[2], bflo(g1), bflo(xv[p][q][1])), fmaf(v[3], bfhi(g1), bfhi(xv[p][q][1])))};
      }
  }
  WAITL0(); BAR();
#pragma unroll
  for (int q = 0; q < 4; ++q) {
    const unsigned row = wid * 16u + q * 4u + (lane >> 4);
    const u32x4 v = *(const LAS u32x4*)(size_t)(XL + row * 256u + 16u * ((lane & 15u) ^ (row & 15u)));
    *(u32x4*)(out + (size_t)(st * 128u + row) * DM + dt * 128u + (lane & 15u) * 8u) = v;
  }
  STAMP(4); waitv<0>(); STAMP(5); ST_OUT(2);
}
}

extern "C" void kernel_launch(void* const* d_in, const int* in_sizes, int n_in, void* d_out, int out_size, void* d_ws, size_t ws_size, hipStream_t stream) {
  const u16* x = (const u16*)d_in[0]; const u16* kc = (const u16*)d_in[1]; const u16* vc = (const u16*)d_in[2];
  const u16* wq = (const u16*)d_in[3]; const u16* wo = (const u16*)d_in[4];
  const u16* w1 = (const u16*)d_in[6]; const u16* b1 = (const u16*)d_in[7]; const u16* w2 = (const u16*)d_in[8]; const u16* b2 = (const u16*)d_in[9];
  char* ws = (char*)d_ws;
  (void)hipFuncSetAttribute((const void*)k_mid, hipFuncAttributeMaxDynamicSharedMemorySize, LDS_BYTES);
  (void)hipFuncSetAttribute((const void*)k_fin, hipFuncAttributeMaxDynamicSharedMemorySize, LDS_BYTES);
  k_ln<<<256, 512, 0, stream>>>(x, wq, (u16*)(ws + WS_XN), (u16*)(ws + WS_H), (unsigned*)(ws + WS_FLAG));
  k_mid<<<256, 512, LDS_BYTES, stream>>>((const u16*)(ws + WS_XN), (const u16*)(ws + WS_H), kc, vc, wq, w1, b1, wo, w2, (u16*)(ws + WS_O), (u16*)(ws + WS_U));
  k_fin<<<256, 512, LDS_BYTES, stream>>>(x, (const u16*)(ws + WS_O), wo, (const u16*)(ws + WS_U), w2, b2, ws + WS_G, (unsigned*)(ws + WS_FLAG), (u16*)d_out);
}
```

```cpp
#include <hip/hip_runtime.h>
#include <hip/hip_bf16.h>

namespace {
typedef short bf16x8 __attribute__((ext_vector_type(8)));
typedef short s16x4 __attribute__((ext_vector_type(4)));
typedef float f32x2 __attribute__((ext_vector_type(2)));
typedef float f32x4 __attribute__((ext_vector_type(4)));
typedef float f32x16 __attribute__((ext_vector_type(16)));
typedef unsigned u32x4 __attribute__((ext_vector_type(4)));
typedef unsigned u32x2 __attribute__((ext_vector_type(2)));
typedef __bf16 bf2_t __attribute__((ext_vector_type(2)));
typedef unsigned short u16;
typedef __amdgpu_buffer_rsrc_t rsrc_t;
#define LAS __attribute__((address_space(3)))
#define DI __device__ __forceinline__
#define BAR() asm volatile("s_barrier" ::: "memory")
#define WAITL0() do { asm volatile("" ::: "memory"); __builtin_amdgcn_s_waitcnt(0xC07F); asm volatile("" ::: "memory"); } while (0)
#define SCHED() __builtin_amdgcn_sched_barrier(0)
#define SGB(mask, n) __builtin_amdgcn_sched_group_barrier(mask, n, 0)

constexpr int SEQ = 2048, DM = 1024, NH = 32, DH = 128, SK = 2048, WIN = 512, FF = 3584;
constexpr unsigned WS_XN = 0u, WS_H = 4u << 20, WS_O = 8u << 20, WS_U = 24u << 20, WS_G = 32u << 20, WS_FLAG = 36u << 20, WS_TOTAL = 37u << 20;
constexpr unsigned LDS_BYTES = 163840;
constexpr unsigned long long SPIN_LIMIT = 2000000ull;
#define LNPF 1
#define LNAUX 16
#define PFSLEEP 40

#define ST_DECL
#define STAMP(i) do {} while (0)
#define ST_OUT(k)
template <int N> DI void waitv() { asm volatile("s_waitcnt vmcnt(%0)" :: "i"(N) : "memory"); }
DI unsigned rfl(unsigned x) { return __builtin_amdgcn_readfirstlane(x); }
DI rsrc_t mkrsrc(const void* p, unsigned bytes) { return __builtin_amdgcn_make_buffer_rsrc((void*)p, (short)0, (int)bytes, 0x00020000); }
DI void dma16(unsigned m0v, unsigned voff, rsrc_t r, unsigned soff) {
  asm volatile("s_mov_b32 m0, %0\n\ts_nop 0\n\tbuffer_load_dwordx4 %1, %2, %3 offen lds" :: "s"(m0v), "v"(voff), "s"(r), "s"(soff) : "memory");
}
DI rsrc_t code_rsrc() {
  unsigned long long b, e;
  asm volatile("s_getpc_b64 s[40:41]\n\ts_add_u32 s40, s40, rsk_main@rel32@lo+4\n\ts_addc_u32 s41, s41, rsk_main@rel32@hi+12\n\ts_mov_b64 %0, s[40:41]\n\t"
               "s_getpc_b64 s[40:41]\n\ts_add_u32 s40, s40, rsk_ln@rel32@lo+4\n\ts_addc_u32 s41, s41, rsk_ln@rel32@hi+12\n\ts_mov_b64 %1, s[40:41]"
               : "=&s"(b), "=&s"(e) :: "s40", "s41", "scc");
  return mkrsrc((const void*)b, (e > b && e - b < 0x40000ull) ? (unsigned)(e - b) : 0u);
}
DI void dma64(unsigned m0v, unsigned va, unsigned vb, rsrc_t r, unsigned s0, unsigned s1, unsigned s2, unsigned s3) {
  asm volatile("s_mov_b32 m0, %0\n\ts_nop 0\n\tbuffer_load_dwordx4 %1, %3, %4 offen lds\n\tbuffer_load_dwordx4 %2, %3, %5 offen offset:1024 lds\n\t"
               "buffer_load_dwordx4 %1, %3, %6 offen offset:2048 lds\n\tbuffer_load_dwordx4 %2, %3, %7 offen offset:3072 lds"
               :: "s"(m0v), "v"(va), "v"(vb), "s"(r), "s"(s0), "s"(s1), "s"(s2), "s"(s3) : "memory");
}
DI void dma64d(unsigned m0v, unsigned va, rsrc_t r, unsigned s0) {
  asm volatile("s_mov_b32 m0, %0\n\ts_nop 0\n\tbuffer_load_dwordx4 %1, %2, %3 offen sc1 lds\n\tbuffer_load_dwordx4 %1, %2, %3 offen offset:1024 sc1 lds\n\t"
               "buffer_load_dwordx4 %1, %2, %3 offen offset:2048 sc1 lds\n\tbuffer_load_dwordx4 %1, %2, %3 offen offset:3072 sc1 lds"
               :: "s"(m0v), "v"(va), "s"(r), "s"(s0) : "memory");
}
DI rsrc_t mkrsrcB(const void* p, unsigned bytes) { return mkrsrc((const char*)p - 4096, bytes + 4096u); }
#define PINS(x) asm volatile("" : "+s"(x))
DI bf16x8 ldsR(unsigned a) { return *(const LAS bf16x8*)(size_t)a; }
DI s16x4 ldsT(unsigned a) { return __builtin_amdgcn_ds_read_tr16_b64_v4i16((LAS s16x4*)(size_t)a); }
DI bf16x8 cat(s16x4 a, s16x4 b) { return __builtin_shufflevector(a, b, 0, 1, 2, 3, 4, 5, 6, 7); }
DI unsigned pk2(float lo, float hi) { f32x2 v = {lo, hi}; return __builtin_bit_cast(unsigned, __builtin_convertvector(v, bf2_t)); }
DI float bflo(unsigned w) { return __uint_as_float(w << 16); }
DI float bfhi(unsigned w) { return __uint_as_float(w & 0xffff0000u); }
DI float rbf(float x) { return bflo(pk2(x, x) & 0xffffu); }
template <int CTRL, int RM> DI float dpp_add(float v) {
  return v + __builtin_bit_cast(float, __builtin_amdgcn_update_dpp(0, __builtin_bit_cast(int, v), CTRL, RM, 0xf, false));
}
DI float wsum(float v) {
  v = dpp_add<0x111, 0xf>(v); v = dpp_add<0x112, 0xf>(v); v = dpp_add<0x114, 0xf>(v); v = dpp_add<0x118, 0xf>(v);
  v = dpp_add<0x142, 0xa>(v); v = dpp_add<0x143, 0xc>(v);
  return __builtin_bit_cast(float, __builtin_amdgcn_readlane(__builtin_bit_cast(int, v), 63));
}

DI void ln16(float (&v)[16]) {
  float s = 0.f;
#pragma unroll
  for (int i = 0; i < 16; ++i) s += v[i];
  const float mean = wsum(s) * (1.0f / DM);
  float q = 0.f;
#pragma unroll
  for (int i = 0; i < 16; ++i) { const float d = v[i] - mean; q += d * d; }
  const float var = rbf(wsum(q) * (1.0f / DM));
  const float mb = rbf(mean), inv = 1.0f / rbf(sqrtf(rbf(var + 1e-5f)));
#pragma unroll
  for (int i = 0; i < 16; i += 2) {
    const unsigned d = pk2(v[i] - mb, v[i + 1] - mb), r = pk2(bflo(d) * inv, bfhi(d) * inv);
    v[i] = bflo(r); v[i + 1] = bfhi(r);
  }
}
DI unsigned pkx(float lo, float hi) { return (__float_as_uint(lo) >> 16) | (__float_as_uint(hi) & 0xffff0000u); }
enum { RROW = 0, RCHK = 1, T1 = 2, T2 = 3 };
template <int KIND> struct Opnd {
  static constexpr int NA = KIND == RROW ? 2 : KIND == RCHK ? 1 : 4;
  static constexpr int RPB = KIND >= T1 ? 2 : 1;
  unsigned a[NA];
  DI void init(unsigned base, unsigned off, unsigned lane) {
    const unsigned fr = lane & 15u, g = lane >> 4, q = fr >> 2, p = lane & 3u;
    if constexpr (KIND == RROW) {
#pragma unroll
      for (int kk = 0; kk < 2; ++kk) a[kk] = base + (off + fr) * 128u + 16u * ((4u * kk + g) ^ (fr >> 1));
    } else if constexpr (KIND == RCHK) {
      a[0] = base + g * 2048u + (off + fr) * 16u;
    } else {
      const unsigned x = ((q >> 1) << 1) | (g & 1u);
#pragma unroll
      for (int n = 0; n < 4; ++n)
        a[n] = base + (KIND == T1 ? 4096u : 2048u) * (g >> 1) + 1024u * (off >> 6) + 128u * (4u * (g & 1u) + q) + 32u * ((unsigned)n ^ x) + 8u * p;
    }
  }
  DI bf16x8 ld(int b, int kk) const {
    if constexpr (KIND == RROW) return ldsR(a[kk] + 2048u * b);
    else if constexpr (KIND == RCHK) return ldsR(a[0] + 8192u * kk + 256u * b);
    else { const unsigned ad = a[b & 3] + 1024u * (b >> 2) + 8192u * kk; return cat(ldsT(ad), ldsT(ad + (KIND == T1 ? 2048u : 4096u))); }
  }
  DI void adv(unsigned delta) {
#pragma unroll
    for (int i = 0; i < NA; ++i) { a[i] += delta; asm volatile("" : "+v"(a[i])); }
  }
};

template <int PK, int QK, int PM, int QM, int NS, unsigned STG, int TAIL, int S0, int DRAIN>
DI void gemm_compute(f32x4 (&acc)[PM][QM], Opnd<PK>& P, Opnd<QK>& Q, const int N) {
  bf16x8 pf[2][PM], qf[2][QM];
  constexpr int NR = PM * Opnd<PK>::RPB + QM * Opnd<QK>::RPB, NM = PM * QM, NA = Opnd<PK>::NA + Opnd<QK>::NA;
  auto load = [&](int kk) {
    pf[kk][0] = P.ld(0, kk);
#pragma unroll
    for (int q = 0; q < QM; ++q) qf[kk][q] = Q.ld(q, kk);
#pragma unroll
    for (int p = 1; p < PM; ++p) pf[kk][p] = P.ld(p, kk);
  };
  auto mma = [&](int kk) {
#pragma unroll
    for (int p = 0; p < PM; ++p)
#pragma unroll
      for (int q = 0; q < QM; ++q) acc[p][q] = __builtin_amdgcn_mfma_f32_16x16x32_bf16(pf[kk][p], qf[kk][q], acc[p][q], 0, 0, 0);
  };
  constexpr int ND = (NR + NA + 2 > NM) ? (NR + NA + 2 - NM) : 0;
  BAR();
  load(0);
  int stg = S0;
  auto step = [&](bool sync) {
    SCHED();
    load(1); mma(0);
    stg = (stg == NS - 1) ? 0 : stg + 1;
    const unsigned delta = stg == 0 ? (unsigned)(-(int)((NS - 1) * STG)) : STG;
    P.adv(delta); Q.adv(delta);
#pragma unroll
    for (int i = 0; i < ND; ++i) { SGB(0x8, 1); SGB(0x100, 2); }
#pragma unroll
    for (int i = 0; i < NR - 2 * ND; ++i) { SGB(0x8, 1); SGB(0x100, 1); }
#pragma unroll
    for (int i = 0; i < NA; ++i) { SGB(0x8, 1); SGB(0x2, 1); }
    SGB(0x8, NM);
    SCHED();
    if (sync) { WAITL0(); BAR(); }
    SCHED();
    load(0); mma(1);
#pragma unroll
    for (int i = 0; i < ND; ++i) { SGB(0x8, 1); SGB(0x100, 2); }
#pragma unroll
    for (int i = 0; i < NR - 2 * ND; ++i) { SGB(0x8, 1); SGB(0x100, 1); }
    SGB(0x8, NM);
  };
  if (DRAIN > 0) {
#pragma nounroll
    for (int t = 0; t < DRAIN; ++t) step(true);
    waitv<0>();
  }
#pragma nounroll
  for (int t = DRAIN; t < N - TAIL; ++t) step(true);
#pragma nounroll
  for (int t = 0; t < TAIL; ++t) step(false);
  SCHED();
}

DI unsigned voffRow(unsigned lane, unsigned ldb, unsigned par) { return (lane >> 3) * ldb + 16u * ((lane & 7u) ^ (4u * par + (lane >> 4))); }
DI unsigned voffT(unsigned lane, unsigned ldb) { return (lane >> 3) * ldb + 16u * ((lane & 7u) ^ (2u * ((((lane >> 4) & 1u) << 1) | (lane >> 5)))); }
struct T4 { unsigned s0, s1, s2, s3;
  DI void set(unsigned so, unsigned lw, unsigned ldb) { s0 = so + 4096u + lw * 16u * ldb; s1 = s0 + 128u - 1024u; s2 = s0 + 8u * ldb - 2048u; s3 = s2 + 128u - 1024u; }
  DI void pin() { PINS(s0); PINS(s1); PINS(s2); PINS(s3); }
  DI void go(unsigned dst, unsigned vo, rsrc_t r) const { dma64(dst, vo, vo, r, s0, s1, s2, s3); } };
DI void dmaT4(unsigned dst, unsigned lw, unsigned vo, rsrc_t r, unsigned so, unsigned ldb) { T4 t; t.set(so, lw, ldb); t.go(dst + lw * 4096u, vo, r); }

DI float gelu_t(float v) {
  const float z = v * (1.0f + 0.044715f * v * v) * (-2.3022081986f);
  return v * __builtin_amdgcn_rcpf(1.0f + __builtin_amdgcn_exp2f(z));
}

constexpr unsigned QSTG = 49152u;
constexpr unsigned KBASE = 65536u, VBASE = 114688u;
constexpr unsigned KSLOT[3] = {0u, 32768u, 16384u}, VSLOT[3] = {32768u, 16384u, 0u};
DI void touch8k(rsrc_t r, unsigned vo128, unsigned soff) {
  asm volatile("buffer_load_dword v255, %0, %1, %2 offen" :: "v"(vo128), "s"(r), "s"(soff) : "memory", "v255");
}
constexpr float QSCALE = 0.12751743082f;
#define BIGSUM 4096.0f

#define ATTPRIO 0
#define PRIO(x) do { if (ATTPRIO == 1) __builtin_amdgcn_s_setprio(x); else if (ATTPRIO == 2) __builtin_amdgcn_s_setprio(1 - (x)); } while (0)
struct Att {
  bf16x8 qf[8], kv[16], pf[4], pn[2];
  f32x16 o[4], s[2], negm;
  float l, ts;
  unsigned aK[4], aV[2];
};
template <unsigned SLOT, int J> DI void att_k(Att& a) {
  asm volatile("ds_read_b128 %0, %1 offset:%2" : "+v"(a.kv[J]) : "v"(a.aK[(J >> 1) & 3]), "i"(SLOT + 8192u * (J >> 3) + 4096u * (J & 1)) : "memory");
}
template <unsigned SLOT, int J> DI void att_v(Att& a) {
  s16x4 lo = __builtin_shufflevector(a.kv[J], a.kv[J], 0, 1, 2, 3), hi = __builtin_shufflevector(a.kv[J], a.kv[J], 4, 5, 6, 7);
  constexpr unsigned off = SLOT + 4096u * (J >> 2) + 1024u * ((J >> 1) & 1);
  asm volatile("ds_read_b64_tr_b16 %0, %2 offset:%3\n\tds_read_b64_tr_b16 %1, %2 offset:%4" : "+v"(lo), "+v"(hi) : "v"(a.aV[J & 1]), "i"(off), "i"(off + 2048u) : "memory");
  a.kv[J] = cat(lo, hi);
}
DI void att_wait(Att& a) {
  asm volatile("s_waitcnt lgkmcnt(0)" : "+v"(a.kv[0]), "+v"(a.kv[1]), "+v"(a.kv[2]), "+v"(a.kv[3]), "+v"(a.kv[4]), "+v"(a.kv[5]), "+v"(a.kv[6]), "+v"(a.kv[7]),
               "+v"(a.kv[8]), "+v"(a.kv[9]), "+v"(a.kv[10]), "+v"(a.kv[11]), "+v"(a.kv[12]), "+v"(a.kv[13]), "+v"(a.kv[14]), "+v"(a.kv[15]) :: "memory");
}
DI void att_loadK(Att& a, unsigned slot) {
#pragma unroll
  for (int j = 0; j < 16; ++j) a.kv[j] = ldsR(a.aK[(j >> 1) & 3] + slot + 8192u * (j >> 3) + 4096u * (j & 1));
}
DI unsigned lane_id() { unsigned l; asm volatile("v_mbcnt_lo_u32_b32 %0, -1, 0\n\tv_mbcnt_hi_u32_b32 %0, -1, %0" : "=v"(l)); return l; }
DI void add_bcast3(f32x16& t0, f32x16& t1, f32x16& t2, float v) {
  const float one = lane_id() < 32u ? 1.0f : 0.0f;
  asm volatile("s_nop 7\n\ts_nop 7\n\tv_mfma_f32_32x32x2_f32 %0, %3, %4, %0\n\tv_mfma_f32_32x32x2_f32 %1, %3, %4, %1\n\tv_mfma_f32_32x32x2_f32 %2, %3, %4, %2\n\t"
               "s_nop 7\n\ts_nop 7\n\ts_nop 7" : "+v"(t0), "+v"(t1), "+v"(t2) : "v"(one), "v"(v));
}
template <bool FIRST, unsigned SLOT, int J> DI void att_qk1(Att& a) {
  a.s[J & 1] = __builtin_amdgcn_mfma_f32_32x32x16_bf16(a.kv[J], a.qf[J >> 1], J < 2 ? (FIRST ? (f32x16){} : a.negm) : a.s[J & 1], 0, 0, 0);
  SCHED();
  if constexpr (!FIRST) att_v<SLOT, J>(a);
  SCHED();
  if constexpr (J < 15) att_qk1<FIRST, SLOT, J + 1>(a);
}
template <int NEXT, unsigned SLOT, int J> DI void att_pv1(Att& a) {
  a.o[J & 3] = __builtin_amdgcn_mfma_f32_32x32x16_bf16(a.kv[J], a.pf[J >> 2], a.o[J & 3], 0, 0, 0);
  SCHED();
  if constexpr (NEXT == 1) att_k<SLOT, J>(a); else if constexpr (NEXT == 2) att_v<SLOT, J>(a);
  SCHED();
  if constexpr (J < 15) att_pv1<NEXT, SLOT, J + 1>(a);
}
DI float max3(float x, float y, float z) { return __builtin_fmaxf(__builtin_fmaxf(x, y), z); }
DI float att_rowmax(const Att& a) {
  float m0 = max3(a.s[0][0], a.s[0][1], a.s[0][2]), m1 = max3(a.s[1][0], a.s[1][1], a.s[1][2]);
#pragma unroll
  for (int r = 3; r < 15; r += 2) { m0 = max3(m0, a.s[0][r], a.s[0][r + 1]); m1 = max3(m1, a.s[1][r], a.s[1][r + 1]); }
  const float mx = __builtin_fmaxf(max3(m0, m1, a.s[0][15]), a.s[1][15]);
  auto rr = __builtin_amdgcn_permlane32_swap(__float_as_uint(mx), __float_as_uint(mx), false, false);
  return __builtin_fmaxf(__uint_as_float(rr[0]), __uint_as_float(rr[1]));
}
DI float att_exp16(const f32x16& v, bf16x8& p0, bf16x8& p1) {
  float s0 = 0.f, s1 = 0.f;
  u32x4 w[2];
#pragma unroll
  for (int h = 0; h < 2; ++h) {
    float e[8];
#pragma unroll
    for (int r = 0; r < 8; ++r) e[r] = __builtin_amdgcn_exp2f(v[8 * h + r]);
#pragma unroll
    for (int r = 0; r < 8; r += 2) {
      if (h == 0 && r == 0) { s0 = e[0]; s1 = e[1]; } else { s0 += e[r]; asm("" : "+v"(s0)); s1 += e[r + 1]; asm("" : "+v"(s1)); }
      w[h][r >> 1] = pk2(e[r], e[r + 1]);
    }
    asm volatile("" : "+v"(w[h]));
    SCHED();
  }
  p0 = __builtin_bit_cast(bf16x8, w[0]); p1 = __builtin_bit_cast(bf16x8, w[1]);
  return s0 + s1;
}
template <bool FIRST> DI void att_sm1(Att& a) {
  if (FIRST) {
    const float mx = att_rowmax(a);
#pragma unroll
    for (int r = 0; r < 16; ++r) { a.s[0][r] -= mx; a.s[1][r] -= mx; a.negm[r] = -mx; }
  }
  a.ts = att_exp16(a.s[0], a.pn[0], a.pn[1]);
  asm volatile("" : "+v"(a.pn[0]), "+v"(a.pn[1]), "+v"(a.ts), "+v"(a.s[1]), "+v"(a.negm));
}
template <bool FIRST> DI void att_sm2(Att& a) {
  a.ts += att_exp16(a.s[1], a.pf[2], a.pf[3]);
  if (!FIRST) {
    if (__builtin_expect(!__all(a.ts <= BIGSUM), 0)) {
      const float dl = fmaxf(att_rowmax(a), 0.f), fct = __builtin_amdgcn_exp2f(-dl);
#pragma unroll
      for (int db = 0; db < 4; ++db) a.o[db] *= fct;
      a.l *= fct;
      add_bcast3(a.s[0], a.s[1], a.negm, -dl);
      a.ts = att_exp16(a.s[0], a.pn[0], a.pn[1]) + att_exp16(a.s[1], a.pf[2], a.pf[3]);
    }
  }
  a.l += a.ts;
  a.pf[0] = a.pn[0]; a.pf[1] = a.pn[1];
  asm volatile("" : "+v"(a.pf[0]), "+v"(a.pf[1]), "+v"(a.pf[2]), "+v"(a.pf[3]), "+v"(a.l));
}

extern "C" __global__ __launch_bounds__(512, 2) __attribute__((amdgpu_num_vgpr(127)))
void rsk_main(const u16* __restrict__ kc, const u16* __restrict__ vc,
           const u16* __restrict__ wq, const u16* __restrict__ w1, const u16* __restrict__ b1, const u16* __restrict__ wo, const u16* __restrict__ w2,
           const u16* __restrict__ b2, const u16* __restrict__ x, char* __restrict__ ws, u16* __restrict__ out) {
  extern __shared__ __attribute__((aligned(16))) char lds[];
  const unsigned L = (unsigned)(size_t)lds;
  const char* xn = ws + WS_XN; const char* hh = ws + WS_H;
  const unsigned tid = threadIdx.x, lane = tid & 63u, wid = rfl(tid >> 6), grp = wid >> 2, lw = wid & 3u;
  const unsigned bid = blockIdx.x, xcd = bid & 7u, idx = bid >> 3;
  const unsigned qt = (xcd & 1u) * 4u + (idx & 3u), kvh = xcd >> 1, head = kvh * 8u + (idx >> 2);
  const bool hasU = idx < 28u;
  const unsigned udt = (xcd & 1u) * 4u + (idx & 3u), uft = (xcd >> 1) * 7u + (idx >> 2);
  const rsrc_t sK = mkrsrc(kc + ((size_t)kvh * SK + (SK - WIN)) * DH, WIN * DH * 2), sV = mkrsrc(vc + ((size_t)kvh * SK + (SK - WIN)) * DH, WIN * DH * 2);
  const rsrc_t sH = mkrsrcB(hh, SEQ * DM * 2), sW1 = mkrsrcB(w1, SEQ * FF * 2);
  const unsigned voK0 = voffRow(lane, 256u, 0), voV = voffT(lane, 256u);
  auto dmaK2 = [&](unsigned slot, unsigned t, unsigned p0) {
    unsigned voK1; asm volatile("v_xor_b32 %0, 64, %1" : "=v"(voK1) : "v"(voK0));
    dma16(L + KBASE + slot + 1024u * p0, voK0, sK, t * 16384u + (p0 & 7u) * 2048u + (p0 >> 3) * 128u);
    dma16(L + KBASE + slot + 1024u * p0 + 1024u, voK1, sK, t * 16384u + (p0 & 7u) * 2048u + 2048u + (p0 >> 3) * 128u); };
  auto dmaV2 = [&](unsigned slot, unsigned t, unsigned p0) {
    dma16(L + VBASE + slot + 1024u * p0, voV, sV, t * 16384u + (p0 >> 1) * 2048u);
    dma16(L + VBASE + slot + 1024u * p0 + 1024u, voV, sV, t * 16384u + (p0 >> 1) * 2048u + 128u); };
  auto tileK = [&](unsigned t) {
#pragma unroll
    for (int p = 0; p < 16; p += 2) dmaK2(KSLOT[t % 3], t, p); };
  auto tileV = [&](unsigned t) {
#pragma unroll
    for (int p = 0; p < 16; p += 2) dmaV2(VSLOT[t % 3], t, p); };
  Att a;
  { const unsigned r32 = lane & 31u, hi = lane >> 5;
#pragma unroll
    for (int j = 0; j < 4; ++j) a.aK[j] = L + KBASE + r32 * 128u + 16u * ((2u * j + hi) ^ ((r32 >> 1) & 7u)); }

  ST_DECL; STAMP(0);
  if (wid == 7) {
    const rsrc_t sWq = mkrsrc(wq, DM * NH * DH * 2);
    const unsigned vo = lane * 128u;
    __builtin_amdgcn_s_sleep(PFSLEEP);
#pragma unroll
    for (int j = LNPF; j < 4; ++j) touch8k(sWq, vo, (j * 256u + bid) * 8192u);
    if (idx < 16u) touch8k(sK, vo, idx * 8192u); else touch8k(sV, vo, (idx - 16u) * 8192u);
    for (unsigned t = 0; t < 13; ++t) BAR();
    touch8k(code_rsrc(), lane < 16u ? vo : 0x80000000u, idx * 2048u);
    BAR(); BAR(); BAR(); BAR();
#pragma unroll
    for (int j = 0; j < 3; ++j) touch8k(sW1, vo, (j * 256u + bid) * 8192u + 4096u);
    att_loadK(a, KSLOT[0]);
  } else if (grp == 1) {
    const rsrc_t sS = lw == 0 ? mkrsrcB(wq, DM * NH * DH * 2) : mkrsrcB(xn, SEQ * DM * 2);
    const unsigned vo0 = lw == 0 ? voffT(lane, 8192u) : voffRow(lane, 2048u, 0), vo1 = lw == 0 ? vo0 : vo0 ^ 64u;
    const unsigned sj = lw == 0 ? 64u * 8192u : 128u, sp = lw == 0 ? 8u * 8192u : 16u * 2048u, so = (lw == 0 ? 128u : 8u * 2048u) - 1024u;
    unsigned src = 4096u + (lw == 0 ? head * 256u : (qt * 256u + (lw - 1u) * 128u) * 2048u), dst = L + lw * 16384u;
    auto step = [&]() {
#pragma unroll
      for (int q = 0; q < 4; ++q) { const unsigned e = src + 2u * q * sp; dma64(dst + 4096u * q, vo0, vo1, sS, e, e + so, e + sp - 2048u, e + sp - 2048u + so); }
      src += sj; dst = dst >= L + 2u * QSTG ? dst - 2u * QSTG : dst + QSTG;
    };
#pragma nounroll
    for (unsigned t = 0; t < 3; ++t) step();
    waitv<32>(); BAR(); STAMP(1);
#pragma nounroll
    for (unsigned t = 0; t < 13; ++t) { waitv<16>(); BAR(); step(); }
    waitv<16>(); BAR();
    if (lw == 0) tileK(0); else if (lw == 1) tileK(2); else tileV(0);
    waitv<16>(); BAR();
    if (lw == 1) tileK(1); else if (lw == 2) tileV(1);
    if (lw == 0) waitv<0>();
    BAR();
    att_loadK(a, KSLOT[0]);
  } else {
    Opnd<T1> P; Opnd<RROW> Q;
    P.init(L, 0u, lane); Q.init(L + 16384u, lw * 64u, lane);
    f32x4 acc[8][4] = {};
    gemm_compute<T1, RROW, 8, 4, 3, QSTG, 0, 0, 0>(acc, P, Q, 16);
    STAMP(2);
    att_loadK(a, KSLOT[0]);
    const unsigned wa = L + (lane >> 5) * 4096u + (lw * 64u + (lane & 15u)) * 16u + ((lane >> 4) & 1u) * 8u;
#pragma unroll
    for (int n = 0; n < 8; ++n)
#pragma unroll
      for (int m = 0; m < 4; ++m) {
        const f32x4 v = acc[n][m] * QSCALE;
        *(LAS u32x2*)(size_t)(wa + 8192u * n + 256u * m) = (u32x2){pk2(v[0], v[1]), pk2(v[2], v[3])};
      }
  }
  WAITL0(); BAR(); STAMP(3);

  u32x4 opk[8]; unsigned oo;
  const rsrc_t sO = mkrsrc(ws + WS_O, SEQ * NH * DH * 2);
  {
    const unsigned l2 = lane_id(), hi = l2 >> 5, blk = (l2 >> 4) & 1u, q4 = (l2 & 15u) >> 2, p4 = l2 & 3u;
#pragma unroll
    for (int ks = 0; ks < 8; ++ks) a.qf[ks] = ldsR(L + (2u * ks) * 4096u + hi * 4096u + (wid * 32u + (l2 & 31u)) * 16u);
#pragma unroll
    for (int n = 0; n < 2; ++n) a.aV[n] = L + VBASE + 128u * (4u * hi + q4) + 32u * ((2u * n + blk) ^ (((q4 >> 1) << 1) | hi)) + 8u * p4;
#pragma unroll
    for (int db = 0; db < 4; ++db) a.o[db] = (f32x16){};
    a.l = 0.f;
    auto ustepP_ = [&](unsigned j, unsigned stg, unsigned vo) { dmaT4(L + stg * 32768u, lw, vo, sW1, j * 64u * (FF * 2u) + uft * 256u, FF * 2u); };
    auto ustepQ_ = [&](unsigned j, unsigned stg, unsigned vo) { dmaT4(L + stg * 32768u + 16384u, lw, vo, sH, j * 64u * 2048u + udt * 256u, 2048u); };
    const bool pre = hasU && grp == 1;
    if (grp == 1) { if (wid != 7) waitv<0>(); WAITL0(); BAR(); }
    STAMP(4);
    const rsrc_t sWo = mkrsrc(wo, NH * DH * DM * 2), sW2 = mkrsrc(w2, FF * SEQ * 2);
    const unsigned lk = bid * 8192u + (wid >> 1) * 2048u + (wid & 1u) * 128u;
#define LOOK(v) do { if ((v) < 4) touch8k(sW1, voV, lk + ((v) + 3) * 0x200000u + 4096u); else if ((v) < 8) touch8k(sWo, voV, lk + ((v) - 4) * 0x200000u); \
                     else if ((v) < 11) touch8k(sW2, voV, lk + ((v) - 4) * 0x200000u); } while (0)
#define USTEP(i) ((i) < 5 ? (i) - 3 : (i) - 4)
#define ATT_TILE(i, FIRST, WB, WBP, WD, WDP)                                                                                  \
    SCHED(); att_qk1<FIRST, VSLOT[((i) + 2) % 3], 0>(a); BAR(); SCHED();    \
    if ((i) <= 4) dmaK2(KSLOT[(i) % 3], (i) + 3, wid * 2u);                                                                   \
    LOOK(2 * (i));                                                                                                            \
    att_sm1<FIRST>(a);                                                                                                        \
    if ((i) >= 3 && pre) { waitv<WBP>(); if ((i) != 5) ustepP_(USTEP(i), USTEP(i), voffT(lane_id(), FF * 2u)); } else waitv<WB>(); \
    if (!(FIRST)) att_wait(a); SCHED(); BAR();                              \
    if (FIRST) att_loadK(a, KSLOT[1]); else { SCHED(); att_pv1<((i) < 7 ? 1 : 2), ((i) < 7 ? KSLOT[((i) + 1) % 3] : VSLOT[7 % 3]), 0>(a); } \
    BAR(); SCHED();                                                         \
    if ((i) <= 5) dmaV2(VSLOT[((i) + 2) % 3], (i) + 2, wid * 2u);                                                             \
    LOOK(2 * (i) + 1);                                                                                                        \
    att_sm2<FIRST>(a);                                                                                                        \
    if ((i) >= 3 && pre) { waitv<WDP>(); if ((i) != 5) ustepQ_(USTEP(i), USTEP(i), voffT(lane_id(), 2048u)); } else waitv<WD>(); \
    att_wait(a); SCHED(); BAR();
    ATT_TILE(0, true, 63, 63, 63, 63) ATT_TILE(1, false, 63, 63, 10, 10) ATT_TILE(2, false, 10, 10, 10, 10) ATT_TILE(3, false, 10, 10, 10, 14)
    ATT_TILE(4, false, 10, 18, 10, 22) ATT_TILE(5, false, 8, 20, 7, 15) ATT_TILE(6, false, 4, 8, 0, 4) ATT_TILE(7, false, 63, 63, 63, 63)
#undef ATT_TILE
#undef USTEP
#undef LOOK
    SCHED(); att_pv1<0, 0u, 0>(a); STAMP(5);
    if (grp == 0) BAR();
    const unsigned ln = lane_id();
    { auto rr = __builtin_amdgcn_permlane32_swap(__float_as_uint(a.l), __float_as_uint(a.l), false, false);
      a.l = __uint_as_float(rr[0]) + __uint_as_float(rr[1]); }
    const float inv = 1.0f / a.l;
    const unsigned row = qt * 256u + wid * 32u + (ln & 31u);
    oo = (((row >> 7) * 64u + head * 2u) * 8u + (ln >> 5)) * 2048u + (row & 127u) * 16u;
#pragma unroll
    for (int j = 0; j < 8; ++j) {
      const f32x16& v = a.o[j >> 1]; const int b = 8 * (j & 1);
      opk[j] = (u32x4){pk2(v[b] * inv, v[b + 1] * inv), pk2(v[b + 2] * inv, v[b + 3] * inv), pk2(v[b + 4] * inv, v[b + 5] * inv), pk2(v[b + 6] * inv, v[b + 7] * inv)};
      if (!pre) __builtin_amdgcn_raw_buffer_store_b128(opk[j], sO, oo + j * 4096u, 0, 16);
    }
    if (pre) asm volatile("" : "+v"(opk[0]), "+v"(opk[1]), "+v"(opk[2]), "+v"(opk[3]), "+v"(opk[4]), "+v"(opk[5]), "+v"(opk[6]), "+v"(opk[7]));
    STAMP(6);
  }
  const unsigned ln = lane_id();
  const bool isY = xcd >= 4u;
  const unsigned dt = udt, st = ((xcd >> 1) & 1u) * 8u + (idx >> 2), tile = st * 8u + dt;
  unsigned* flags = (unsigned*)(ws + WS_FLAG);
  unsigned* fG = flags + tile * 32u; unsigned* fO = flags + 4096u; unsigned* fU = flags + 4352u + dt * 32u;
  const rsrc_t sG = mkrsrc(ws + WS_G + (size_t)tile * 32768u, 32768u), sU = mkrsrc(ws + WS_U, DM * FF * 2);
  constexpr unsigned XO = 32768u, GO = 65536u;
  if (!hasU) {
    waitv<0>(); BAR();
    if (tid == 256u) __hip_atomic_fetch_add(fO + qt * 32u, 1u, __ATOMIC_RELAXED, __HIP_MEMORY_SCOPE_AGENT);
    const rsrc_t sW2 = mkrsrc(w2, FF * SEQ * 2);
    const unsigned w = ((idx - 28u) * 4u + (xcd & 3u)) * 8u + wid;
    if (!isY) { touch8k(sW2, ln * 128u, (768u + w) * 8192u); touch8k(sW2, ln * 128u, (896u + w) * 8192u); }
    else {
#pragma unroll
      for (int j = 0; j < 6; ++j) touch8k(sW2, ln * 128u, (j * 128u + w) * 8192u);
    }
  }
  if (grp == 1) {
    const unsigned N = isY ? 56u : 64u;
    const rsrc_t sT = isY ? mkrsrcB(w2, FF * SEQ * 2) : mkrsrcB(wo, NH * DH * DM * 2);
    const rsrc_t sR = isY ? sU : sO;
    const unsigned ldT = isY ? 4096u : 2048u, voT = voffT(ln, ldT), voL = ln * 16u;
    const unsigned tcol = (isY ? st : dt) * 256u, rbase = (isY ? dt : st) * N * 16384u + lw * 4096u;
    const unsigned toff = (isY ? 16384u : 0u) + lw * 4096u, roff = (isY ? 0u : 16384u) + lw * 4096u, g0 = isY ? (xcd >> 1) * 14u : 0u;
    const unsigned* dep = isY ? fU + (ln < 28u ? ln : 0u) : fO + (st >> 1) * 32u;
    const unsigned need = isY ? 1u : 32u;
    auto peek = [&]() { asm volatile("global_load_dword v254, %0, off sc1" :: "v"(dep) : "memory", "v254"); };
    auto await = [&](unsigned long long mask, bool peeked) {
      if (peeked) { unsigned v; asm volatile("v_mov_b32 %0, v254" : "=v"(v) :: "memory"); if ((~__ballot(v >= need) & mask) == 0ull) return; }
      const unsigned long long t0 = __builtin_amdgcn_s_memrealtime();
      while ((~__ballot(__hip_atomic_load(dep, __ATOMIC_RELAXED, __HIP_MEMORY_SCOPE_AGENT) >= need) & mask) != 0ull) {
        if (__builtin_amdgcn_s_memrealtime() - t0 > SPIN_LIMIT) break;
        __builtin_amdgcn_s_sleep(4);
      }
      asm volatile("" ::: "memory");
    };
    unsigned sb = L + 4u * 32768u;
    auto nexts = [&]() { sb = sb == L + 4u * 32768u ? L : sb + 32768u; };
    unsigned kT = g0;
    auto dT = [&]() { T4 p; p.set(kT * 64u * ldT + tcol, lw, ldT); p.go(sb + toff, voT, sT); ++kT; nexts(); };
    if (hasU) {
      const unsigned voH = voffT(ln, 2048u), voW1 = voffT(ln, FF * 2u);
      unsigned j = 4;
      auto urun = [&](unsigned n) {
#pragma nounroll
        for (unsigned i = 0; i < n; ++i) {
          T4 p, q; p.set(j * 64u * (FF * 2u) + uft * 256u, lw, FF * 2u); q.set(j * 64u * 2048u + udt * 256u, lw, 2048u);
          unsigned d0 = sb + lw * 4096u, d1 = d0 + 16384u; p.pin(); q.pin(); PINS(d0); PINS(d1);
          waitv<24>(); BAR();
          p.go(d0, voW1, sW1); q.go(d1, voH, sH);
          ++j; nexts();
        }
      };
      urun(7);
      STAMP(7);
#pragma unroll
      for (int i = 0; i < 8; ++i) { __builtin_amdgcn_raw_buffer_store_b128(opk[i], sO, oo + i * 4096u, 0, 16); urun(2); }
      touch8k(code_rsrc(), ln < 5u ? ln * 128u : 0x80000000u, (idx * 4u + lw) * 640u);
      urun(3);
      if (tid == 256u) __hip_atomic_fetch_add(fO + qt * 32u, 1u, __ATOMIC_RELAXED, __HIP_MEMORY_SCOPE_AGENT);
      urun(2);
      waitv<24>(); BAR(); dT(); waitv<20>(); BAR(); dT(); peek(); waitv<16>(); BAR(); dT(); waitv<12>(); BAR(); dT(); BAR(); dT();
      if (isY) {
        BAR();
        if (tid == 256u) __hip_atomic_store(fU + uft, 1u, __ATOMIC_RELAXED, __HIP_MEMORY_SCOPE_AGENT);
      } else waitv<12>();
      STAMP(8);
    } else {
      sb = L + 2u * 32768u;
#pragma nounroll
      for (unsigned j = 0; j < 5; ++j) dT();
    }
    await(isY ? 0x7full << (g0 >> 1) : 1ull, hasU && !isY);
#pragma nounroll
    for (unsigned j = 0; j < 5; ++j) { dma64d(sb + roff, voL, sR, rbase + (g0 + j) * 16384u); nexts(); }
    auto dstep = [&](auto wait) {
      T4 p; p.set(kT * 64u * ldT + tcol, lw, ldT);
      unsigned d0 = sb + toff, d1 = sb + roff, r0 = rbase + kT * 16384u; p.pin(); PINS(d0); PINS(d1); PINS(r0);
      wait(); BAR();
      p.go(d0, voT, sT); dma64d(d1, voL, sR, r0);
      ++kT; nexts();
    };
    waitv<16>(); BAR(); STAMP(11);
    dstep([] { waitv<12>(); }); dstep([] { waitv<16>(); }); dstep([] { waitv<20>(); });
    if (!isY && hasU && tid == 256u) __hip_atomic_store(fU + uft, 1u, __ATOMIC_RELAXED, __HIP_MEMORY_SCOPE_AGENT);
#pragma nounroll
    for (unsigned seg = isY ? 0u : 4u; seg < 5u; ++seg) {
      const unsigned n = !isY ? 56u : seg == 0u ? 2u : seg == 2u ? 10u : seg == 4u ? 28u : 4u;
#pragma nounroll
      for (unsigned i = 0; i < n; ++i) dstep([] { waitv<24>(); });
      if (seg == 0u || seg == 2u) peek();
      else if (seg < 4u) { waitv<32>(); await(seg == 1u ? 0x3fffull << 14 : 0x3fffull, true); kT = seg == 1u ? 70u - g0 : 0u; }
    }
    waitv<24>(); BAR();
    if (isY) { waitv<0>(); BAR(); STAMP(12); ST_OUT(1); return; }
    {
      const rsrc_t sX = mkrsrc(x, SEQ * DM * 2);
#pragma unroll
      for (int q = 0; q < 8; ++q)
        dma16(L + XO + (lw * 8u + q) * 1024u, (ln >> 4) * 2048u + 16u * ((ln & 15u) ^ ((4u * q + (ln >> 4)) & 15u)), sX, (st * 128u + lw * 32u + q * 4u) * 2048u + dt * 256u);
    }
    waitv<8>(); BAR();
    STAMP(12);
    {
      const unsigned long long t0 = __builtin_amdgcn_s_memrealtime();
      while (rfl(__hip_atomic_load(fG, __ATOMIC_RELAXED, __HIP_MEMORY_SCOPE_AGENT)) == 0u) {
        if (__builtin_amdgcn_s_memrealtime() - t0 > SPIN_LIMIT) break;
        __builtin_amdgcn_s_sleep(2);
      }
      asm volatile("" ::: "memory");
      u32x4 g[8];
#pragma unroll
      for (int q = 0; q < 8; ++q) g[q] = __builtin_amdgcn_raw_buffer_load_b128(sG, voL + (lw * 8u + q) * 1024u, 0, 16);
#pragma unroll
      for (int q = 0; q < 8; ++q) *(LAS u32x4*)(size_t)(L + GO + voL + (lw * 8u + q) * 1024u) = g[q];
    }
    waitv<0>(); WAITL0(); STAMP(13); BAR();
  } else {
    const unsigned wp = lw >> 1, wq2 = lw & 1u, fr = ln & 15u, fq = ln >> 4;
    if (hasU) {
      Opnd<T2> P, Q;
      P.init(L, wp * 64u, ln); Q.init(L + 16384u, wq2 * 64u, ln);
      u32x2 bv[4];
#pragma unroll
      for (int p = 0; p < 4; ++p) bv[p] = *(const u32x2*)(b1 + uft * 128u + wp * 64u + p * 16u + fq * 4u);
      f32x4 acc[4][4] = {};
      gemm_compute<T2, T2, 4, 4, 5, 32768u, 0, 0, 16>(acc, P, Q, 32);
      STAMP(8);
      const unsigned uo = ((udt * 56u + uft * 2u + wp) * 8u + fq) * 2048u + (wq2 * 64u + fr) * 16u;
#pragma unroll
      for (int pp = 0; pp < 2; ++pp) {
        const u32x2 ba = bv[2 * pp], bb = bv[2 * pp + 1];
        const float bias[8] = {bflo(ba[0]), bfhi(ba[0]), bflo(ba[1]), bfhi(ba[1]), bflo(bb[0]), bfhi(bb[0]), bflo(bb[1]), bfhi(bb[1])};
#pragma unroll
        for (int m = 0; m < 4; ++m) {
          float v[8];
#pragma unroll
          for (int j = 0; j < 8; ++j) v[j] = gelu_t(acc[2 * pp + (j >> 2)][m][j & 3] + bias[j]);
          __builtin_amdgcn_raw_buffer_store_b128((u32x4){pk2(v[0], v[1]), pk2(v[2], v[3]), pk2(v[4], v[5]), pk2(v[6], v[7])}, sU, uo + pp * 8192u + m * 256u, 0, 16);
        }
      }
      STAMP(9);
      if (isY) { waitv<0>(); BAR(); }
    }
    f32x4 acc[4][4] = {};
    const unsigned t4 = (wid * 64u + ln) * 16u;
    if (isY) {
      float bias[4];
#pragma unroll
      for (int q = 0; q < 4; ++q) bias[q] = bflo(b2[st * 128u + wq2 * 64u + q * 16u + fr]);
      Opnd<RCHK> P; Opnd<T2> Q;
      P.init(L + 65536u, wp * 64u, ln); Q.init(L + 65536u + 16384u, wq2 * 64u, ln);
      gemm_compute<RCHK, T2, 4, 4, 5, 32768u, 3, 2, 0>(acc, P, Q, 56);
      STAMP(12);
#pragma unroll
      for (int j = 0; j < 8; ++j) {
        float g[8];
#pragma unroll
        for (int c = 0; c < 8; ++c) {
          const int q = 2 * (j & 1) + (c >> 2); const float y = acc[j >> 1][q][c & 3] + bias[q];
          g[c] = y * __builtin_amdgcn_rcpf(1.0f + __builtin_amdgcn_exp2f(y * -1.4426950408889634f));
        }
        __builtin_amdgcn_raw_buffer_store_b128((u32x4){pk2(g[0], g[1]), pk2(g[2], g[3]), pk2(g[4], g[5]), pk2(g[6], g[7])}, sG, t4 + j * 4096u, 0, 16);
      }
      waitv<0>(); BAR();
      if (tid == 0u) __hip_atomic_store(fG, 1u, __ATOMIC_RELAXED, __HIP_MEMORY_SCOPE_AGENT);
      STAMP(15); ST_OUT(1); return;
    }
    Opnd<T1> P; Opnd<RCHK> Q;
    P.init(L + 65536u, wp * 64u, ln); Q.init(L + 65536u + 16384u, wq2 * 64u, ln);
    gemm_compute<T1, RCHK, 4, 4, 5, 32768u, 3, 2, 2>(acc, P, Q, 64);
    STAMP(12);
    BAR();
    STAMP(13);
    u32x4 gp[8];
#pragma unroll
    for (int j = 0; j < 8; ++j) gp[j] = *(const LAS u32x4*)(size_t)(L + GO + t4 + j * 4096u);
    const unsigned xa = L + XO + (wq2 * 64u + fr) * 256u + (fq & 1u) * 8u;
    u32x2 xv[4][4];
#pragma unroll
    for (int p = 0; p < 4; ++p)
#pragma unroll
      for (int q = 0; q < 4; ++q) xv[p][q] = *(const LAS u32x2*)(size_t)(xa + 16u * ((wp * 8u + 2u * p + (fq >> 1)) ^ fr) + q * 4096u);
#pragma unroll
    for (int p = 0; p < 4; ++p)
#pragma unroll
      for (int q = 0; q < 4; ++q) {
        const f32x4 v = acc[p][q]; const unsigned g0 = gp[2 * p + (q >> 1)][2 * (q & 1)], g1 = gp[2 * p + (q >> 1)][2 * (q & 1) + 1];
        *(LAS u32x2*)(size_t)(xa + 16u * ((wp * 8u + 2u * p + (fq >> 1)) ^ fr) + q * 4096u) =
            (u32x2){pk2(fmaf(v[0], bflo(g0), bflo(xv[p][q][0])), fmaf(v[1], bfhi(g0), bfhi(xv[p][q][0]))), pk2(fmaf(v[2], bflo(g1), bflo(xv[p][q][1])), fmaf(v[3], bfhi(g1), bfhi(xv[p][q][1])))};
      }
  }
  WAITL0(); BAR();
#pragma unroll
  for (int q = 0; q < 4; ++q) {
    const unsigned row = wid * 16u + q * 4u + (ln >> 4);
    const u32x4 v = *(const LAS u32x4*)(size_t)(L + XO + row * 256u + 16u * ((ln & 15u) ^ (row & 15u)));
    *(u32x4*)(out + (size_t)(st * 128u + row) * DM + dt * 128u + (ln & 15u) * 8u) = v;
  }
  STAMP(14); waitv<0>(); STAMP(15); ST_OUT(1);
}

extern "C" __global__ __launch_bounds__(512) void rsk_ln(const u16* __restrict__ x, const u16* __restrict__ wq, u16* __restrict__ xn, u16* __restrict__ h, unsigned* __restrict__ flags) {
  const unsigned lane = threadIdx.x & 63u, row = blockIdx.x * 8u + (threadIdx.x >> 6);
  ST_DECL; STAMP(0);
  const u32x4 a = *(const u32x4*)(x + row * DM + lane * 8), b = *(const u32x4*)(x + row * DM + 512 + lane * 8);
  unsigned warm = 0;
  if (threadIdx.x < 64 * LNPF) warm = *(const unsigned*)(wq + ((threadIdx.x >> 6) * 256u + blockIdx.x) * (NH * DH) + lane * 64u);
  if (threadIdx.x >= 448 && threadIdx.x < 464) warm = __builtin_amdgcn_raw_buffer_load_b32(code_rsrc(), (blockIdx.x >> 3) * 2048u + (lane & 15u) * 128u, 0, 0);
  if (blockIdx.x < 9) flags[blockIdx.x * 512u + threadIdx.x] = 0u;
  float v[16];
#pragma unroll
  for (int i = 0; i < 4; ++i) { v[2 * i] = bflo(a[i]); v[2 * i + 1] = bfhi(a[i]); v[8 + 2 * i] = bflo(b[i]); v[9 + 2 * i] = bfhi(b[i]); }
  ln16(v);
  const rsrc_t sXn = mkrsrc(xn, SEQ * DM * 2), sHh = mkrsrc(h, SEQ * DM * 2);
  const unsigned xo = row * 2048u + (lane >> 1) * 32u + (lane & 1u) * 16u;
#pragma unroll
  for (int p = 0; p < 2; ++p) {
    const unsigned lo0 = pkx(v[8 * p], v[8 * p + 1]), lo1 = pkx(v[8 * p + 2], v[8 * p + 3]), hi0 = pkx(v[8 * p + 4], v[8 * p + 5]), hi1 = pkx(v[8 * p + 6], v[8 * p + 7]);
    const bool odd = lane & 1u;
    const unsigned s0 = __builtin_amdgcn_update_dpp(0, (int)(odd ? lo0 : hi0), 0xB1, 0xf, 0xf, false), s1 = __builtin_amdgcn_update_dpp(0, (int)(odd ? lo1 : hi1), 0xB1, 0xf, 0xf, false);
    __builtin_amdgcn_raw_buffer_store_b128(odd ? (u32x4){s0, s1, hi0, hi1} : (u32x4){lo0, lo1, s0, s1}, sXn, xo + 1024u * p, 0, LNAUX);
  }
  ln16(v);
#pragma unroll
  for (int p = 0; p < 2; ++p)
    __builtin_amdgcn_raw_buffer_store_b128((u32x4){pkx(v[8 * p], v[8 * p + 1]), pkx(v[8 * p + 2], v[8 * p + 3]), pkx(v[8 * p + 4], v[8 * p + 5]), pkx(v[8 * p + 6], v[8 * p + 7])},
                                           sHh, row * 2048u + 1024u * p + lane * 16u, 0, LNAUX);
  asm volatile("" :: "v"(warm));
  STAMP(1); waitv<0>(); STAMP(2); ST_OUT(0);
}

}

extern "C" void kernel_launch(void* const* d_in, const int* in_sizes, int n_in, void* d_out, int out_size, void* d_ws, size_t ws_size, hipStream_t stream) {
  const u16* x = (const u16*)d_in[0]; const u16* kc = (const u16*)d_in[1]; const u16* vc = (const u16*)d_in[2];
  const u16* wq = (const u16*)d_in[3]; const u16* wo = (const u16*)d_in[4];
  const u16* w1 = (const u16*)d_in[6]; const u16* b1 = (const u16*)d_in[7]; const u16* w2 = (const u16*)d_in[8]; const u16* b2 = (const u16*)d_in[9];
#define WSEND 0
  char* ws = (char*)d_ws + (WSEND ? ((ws_size - WS_TOTAL) & ~(size_t)0xfffff) : 0);
  (void)hipFuncSetAttribute((const void*)rsk_main, hipFuncAttributeMaxDynamicSharedMemorySize, LDS_BYTES);
  rsk_ln<<<256, 512, 0, stream>>>(x, wq, (u16*)(ws + WS_XN), (u16*)(ws + WS_H), (unsigned*)(ws + WS_FLAG));
  rsk_main<<<256, 512, LDS_BYTES, stream>>>(kc, vc, wq, w1, b1, wo, w2, b2, x, ws, (u16*)d_out);
}
```

```cpp
#include <hip/hip_runtime.h>
#include <hip/hip_bf16.h>

namespace {
typedef short bf16x8 __attribute__((ext_vector_type(8)));
typedef short s16x4 __attribute__((ext_vector_type(4)));
typedef float f32x2 __attribute__((ext_vector_type(2)));
typedef float f32x4 __attribute__((ext_vector_type(4)));
typedef float f32x16 __attribute__((ext_vector_type(16)));
typedef unsigned u32x4 __attribute__((ext_vector_type(4)));
typedef unsigned u32x2 __attribute__((ext_vector_type(2)));
typedef __bf16 bf2_t __attribute__((ext_vector_type(2)));
typedef unsigned short u16;
typedef __amdgpu_buffer_rsrc_t rsrc_t;
#define LAS __attribute__((address_space(3)))
#define DI __device__ __forceinline__
#define BAR() asm volatile("s_barrier" ::: "memory")
#define WAITL0() do { asm volatile("" ::: "memory"); __builtin_amdgcn_s_waitcnt(0xC07F); asm volatile("" ::: "memory"); } while (0)
#define SCHED() __builtin_amdgcn_sched_barrier(0)
#define SGB(mask, n) __builtin_amdgcn_sched_group_barrier(mask, n, 0)

constexpr int SEQ = 2048, DM = 1024, NH = 32, DH = 128, SK = 2048, WIN = 512, FF = 3584;
constexpr unsigned WS_XN = 0u, WS_H = 4u << 20, WS_O = 8u << 20, WS_U = 24u << 20, WS_G = 32u << 20, WS_FLAG = 36u << 20, WS_TOTAL = 37u << 20;
constexpr unsigned LDS_BYTES = 163840;
constexpr unsigned long long SPIN_LIMIT = 2000000ull;
#define LNPF 1
#define LNAUX 16
#define PFDIV 2
#define PFGAP 16
#define PFSLEEP 40

#define ST_DECL
#define STAMP(i) do {} while (0)
#define ST_OUT(k)
template <int N> DI void waitv() { asm volatile("s_waitcnt vmcnt(%0)" :: "i"(N) : "memory"); }
DI unsigned rfl(unsigned x) { return __builtin_amdgcn_readfirstlane(x); }
DI rsrc_t mkrsrc(const void* p, unsigned bytes) { return __builtin_amdgcn_make_buffer_rsrc((void*)p, (short)0, (int)bytes, 0x00020000); }
DI void dma16(unsigned m0v, unsigned voff, rsrc_t r, unsigned soff) {
  asm volatile("s_mov_b32 m0, %0\n\ts_nop 0\n\tbuffer_load_dwordx4 %1, %2, %3 offen lds" :: "s"(m0v), "v"(voff), "s"(r), "s"(soff) : "memory");
}
DI rsrc_t code_rsrc() {
  unsigned long long b, e;
  asm volatile("s_getpc_b64 s[40:41]\n\ts_add_u32 s40, s40, rsk_main@rel32@lo+4\n\ts_addc_u32 s41, s41, rsk_main@rel32@hi+12\n\ts_mov_b64 %0, s[40:41]\n\t"
               "s_getpc_b64 s[40:41]\n\ts_add_u32 s40, s40, rsk_end@rel32@lo+4\n\ts_addc_u32 s41, s41, rsk_end@rel32@hi+12\n\ts_mov_b64 %1, s[40:41]"
               : "=&s"(b), "=&s"(e) :: "s40", "s41", "scc");
  return mkrsrc((const void*)b, (e > b && e - b < 0x40000ull) ? (unsigned)(e - b) : 0u);
}
template <bool DEV>
DI void dma64(unsigned m0v, unsigned va, unsigned vb, rsrc_t r, unsigned s0, unsigned s1, unsigned s2, unsigned s3) {
  if constexpr (DEV)
    asm volatile("s_mov_b32 m0, %0\n\ts_nop 0\n\tbuffer_load_dwordx4 %1, %3, %4 offen sc1 lds\n\tbuffer_load_dwordx4 %2, %3, %5 offen offset:1024 sc1 lds\n\t"
                 "buffer_load_dwordx4 %1, %3, %6 offen offset:2048 sc1 lds\n\tbuffer_load_dwordx4 %2, %3, %7 offen offset:3072 sc1 lds"
                 :: "s"(m0v), "v"(va), "v"(vb), "s"(r), "s"(s0), "s"(s1), "s"(s2), "s"(s3) : "memory");
  else
    asm volatile("s_mov_b32 m0, %0\n\ts_nop 0\n\tbuffer_load_dwordx4 %1, %3, %4 offen lds\n\tbuffer_load_dwordx4 %2, %3, %5 offen offset:1024 lds\n\t"
                 "buffer_load_dwordx4 %1, %3, %6 offen offset:2048 lds\n\tbuffer_load_dwordx4 %2, %3, %7 offen offset:3072 lds"
                 :: "s"(m0v), "v"(va), "v"(vb), "s"(r), "s"(s0), "s"(s1), "s"(s2), "s"(s3) : "memory");
}
DI void dma64d(unsigned m0v, unsigned va, rsrc_t r, unsigned s0) {
  asm volatile("s_mov_b32 m0, %0\n\ts_nop 0\n\tbuffer_load_dwordx4 %1, %2, %3 offen sc1 lds\n\tbuffer_load_dwordx4 %1, %2, %3 offen offset:1024 sc1 lds\n\t"
               "buffer_load_dwordx4 %1, %2, %3 offen offset:2048 sc1 lds\n\tbuffer_load_dwordx4 %1, %2, %3 offen offset:3072 sc1 lds"
               :: "s"(m0v), "v"(va), "s"(r), "s"(s0) : "memory");
}
DI rsrc_t mkrsrcB(const void* p, unsigned bytes) { return mkrsrc((const char*)p - 4096, bytes + 4096u); }
#define PINS(x) asm volatile("" : "+s"(x))
DI bf16x8 ldsR(unsigned a) { return *(const LAS bf16x8*)(size_t)a; }
DI s16x4 ldsT(unsigned a) { return __builtin_amdgcn_ds_read_tr16_b64_v4i16((LAS s16x4*)(size_t)a); }
DI bf16x8 cat(s16x4 a, s16x4 b) { return __builtin_shufflevector(a, b, 0, 1, 2, 3, 4, 5, 6, 7); }
DI unsigned pk2(float lo, float hi) { f32x2 v = {lo, hi}; return __builtin_bit_cast(unsigned, __builtin_convertvector(v, bf2_t)); }
DI float bflo(unsigned w) { return __uint_as_float(w << 16); }
DI float bfhi(unsigned w) { return __uint_as_float(w & 0xffff0000u); }
DI float rbf(float x) { return bflo(pk2(x, x) & 0xffffu); }
template <int CTRL, int RM> DI float dpp_add(float v) {
  return v + __builtin_bit_cast(float, __builtin_amdgcn_update_dpp(0, __builtin_bit_cast(int, v), CTRL, RM, 0xf, false));
}
DI float wsum(float v) {
  v = dpp_add<0x111, 0xf>(v); v = dpp_add<0x112, 0xf>(v); v = dpp_add<0x114, 0xf>(v); v = dpp_add<0x118, 0xf>(v);
  v = dpp_add<0x142, 0xa>(v); v = dpp_add<0x143, 0xc>(v);
  return __builtin_bit_cast(float, __builtin_amdgcn_readlane(__builtin_bit_cast(int, v), 63));
}

DI void ln16(float (&v)[16]) {
  float s = 0.f;
#pragma unroll
  for (int i = 0; i < 16; ++i) s += v[i];
  const float mean = wsum(s) * (1.0f / DM);
  float q = 0.f;
#pragma unroll
  for (int i = 0; i < 16; ++i) { const float d = v[i] - mean; q += d * d; }
  const float var = rbf(wsum(q) * (1.0f / DM));
  const float mb = rbf(mean), inv = 1.0f / rbf(sqrtf(rbf(var + 1e-5f)));
#pragma unroll
  for (int i = 0; i < 16; i += 2) {
    const unsigned d = pk2(v[i] - mb, v[i + 1] - mb), r = pk2(bflo(d) * inv, bfhi(d) * inv);
    v[i] = bflo(r); v[i + 1] = bfhi(r);
  }
}
DI unsigned pkx(float lo, float hi) { return (__float_as_uint(lo) >> 16) | (__float_as_uint(hi) & 0xffff0000u); }
enum { RROW = 0, RCHK = 1, T1 = 2, T2 = 3 };
template <int KIND> struct Opnd {
  static constexpr int NA = KIND == RROW ? 2 : KIND == RCHK ? 1 : 4;
  static constexpr int RPB = KIND >= T1 ? 2 : 1;
  unsigned a[NA];
  DI void init(unsigned base, unsigned off, unsigned lane) {
    const unsigned fr = lane & 15u, g = lane >> 4, q = fr >> 2, p = lane & 3u;
    if constexpr (KIND == RROW) {
#pragma unroll
      for (int kk = 0; kk < 2; ++kk) a[kk] = base + (off + fr) * 128u + 16u * ((4u * kk + g) ^ (fr >> 1));
    } else if constexpr (KIND == RCHK) {
      a[0] = base + g * 2048u + (off + fr) * 16u;
    } else {
      const unsigned x = ((q >> 1) << 1) | (g & 1u);
#pragma unroll
      for (int n = 0; n < 4; ++n)
        a[n] = base + (KIND == T1 ? 4096u : 2048u) * (g >> 1) + 1024u * (off >> 6) + 128u * (4u * (g & 1u) + q) + 32u * ((unsigned)n ^ x) + 8u * p;
    }
  }
  DI bf16x8 ld(int b, int kk) const {
    if constexpr (KIND == RROW) return ldsR(a[kk] + 2048u * b);
    else if constexpr (KIND == RCHK) return ldsR(a[0] + 8192u * kk + 256u * b);
    else { const unsigned ad = a[b & 3] + 1024u * (b >> 2) + 8192u * kk; return cat(ldsT(ad), ldsT(ad + (KIND == T1 ? 2048u : 4096u))); }
  }
  DI void adv(unsigned delta) {
#pragma unroll
    for (int i = 0; i < NA; ++i) { a[i] += delta; asm volatile("" : "+v"(a[i])); }
  }
};

template <int PK, int QK, int PM, int QM, int NS, unsigned STG, int TAIL, int S0, int DRAIN>
DI void gemm_compute(f32x4 (&acc)[PM][QM], Opnd<PK>& P, Opnd<QK>& Q, const int N) {
  bf16x8 pf[2][PM], qf[2][QM];
  constexpr int NR = PM * Opnd<PK>::RPB + QM * Opnd<QK>::RPB, NM = PM * QM, NA = Opnd<PK>::NA + Opnd<QK>::NA;
  auto load = [&](int kk) {
    pf[kk][0] = P.ld(0, kk);
#pragma unroll
    for (int q = 0; q < QM; ++q) qf[kk][q] = Q.ld(q, kk);
#pragma unroll
    for (int p = 1; p < PM; ++p) pf[kk][p] = P.ld(p, kk);
  };
  auto mma = [&](int kk) {
#pragma unroll
    for (int p = 0; p < PM; ++p)
#pragma unroll
      for (int q = 0; q < QM; ++q) acc[p][q] = __builtin_amdgcn_mfma_f32_16x16x32_bf16(pf[kk][p], qf[kk][q], acc[p][q], 0, 0, 0);
  };
  constexpr int ND = (NR + NA + 2 > NM) ? (NR + NA + 2 - NM) : 0;
  BAR();
  load(0);
  int stg = S0;
  auto step = [&](bool sync) {
    SCHED();
    load(1); mma(0);
    stg = (stg == NS - 1) ? 0 : stg + 1;
    const unsigned delta = stg == 0 ? (unsigned)(-(int)((NS - 1) * STG)) : STG;
    P.adv(delta); Q.adv(delta);
#pragma unroll
    for (int i = 0; i < ND; ++i) { SGB(0x8, 1); SGB(0x100, 2); }
#pragma unroll
    for (int i = 0; i < NR - 2 * ND; ++i) { SGB(0x8, 1); SGB(0x100, 1); }
#pragma unroll
    for (int i = 0; i < NA; ++i) { SGB(0x8, 1); SGB(0x2, 1); }
    SGB(0x8, NM);
    SCHED();
    if (sync) { WAITL0(); BAR(); }
    SCHED();
    load(0); mma(1);
#pragma unroll
    for (int i = 0; i < ND; ++i) { SGB(0x8, 1); SGB(0x100, 2); }
#pragma unroll
    for (int i = 0; i < NR - 2 * ND; ++i) { SGB(0x8, 1); SGB(0x100, 1); }
    SGB(0x8, NM);
  };
  if (DRAIN > 0) {
#pragma nounroll
    for (int t = 0; t < DRAIN; ++t) step(true);
    waitv<0>();
  }
#pragma nounroll
  for (int t = DRAIN; t < N - TAIL; ++t) step(true);
#pragma nounroll
  for (int t = 0; t < TAIL; ++t) step(false);
  SCHED();
}

DI unsigned voffRow(unsigned lane, unsigned ldb, unsigned par) { return (lane >> 3) * ldb + 16u * ((lane & 7u) ^ (4u * par + (lane >> 4))); }
DI unsigned voffT(unsigned lane, unsigned ldb) { return (lane >> 3) * ldb + 16u * ((lane & 7u) ^ (2u * ((((lane >> 4) & 1u) << 1) | (lane >> 5)))); }
struct T4 { unsigned s0, s1, s2, s3;
  DI void set(unsigned so, unsigned lw, unsigned ldb) { s0 = so + 4096u + lw * 16u * ldb; s1 = s0 + 128u - 1024u; s2 = s0 + 8u * ldb - 2048u; s3 = s2 + 128u - 1024u; }
  DI void pin() { PINS(s0); PINS(s1); PINS(s2); PINS(s3); }
  template <bool DEV = false> DI void go(unsigned dst, unsigned vo, rsrc_t r) const { dma64<DEV>(dst, vo, vo, r, s0, s1, s2, s3); } };
template <bool DEV> DI void dmaT4(unsigned dst, unsigned lw, unsigned vo, rsrc_t r, unsigned so, unsigned ldb) { T4 t; t.set(so, lw, ldb); t.go<DEV>(dst + lw * 4096u, vo, r); }

typedef unsigned long long u64;
constexpr unsigned FL_X = 64, FL_H = 320, FL_O = 576, FL_U = 832, FL_G = 1088;
DI void flag_set(u64* p, u64 tag) { __hip_atomic_store(p, tag, __ATOMIC_RELAXED, __HIP_MEMORY_SCOPE_AGENT); }
DI u64 flag_get(const u64* p) { return __hip_atomic_load(p, __ATOMIC_RELAXED, __HIP_MEMORY_SCOPE_AGENT); }
DI void flag_wait(const u64* p, u64 tag, u64 mask) {
  const u64 t0 = __builtin_amdgcn_s_memrealtime();
  while ((__ballot(flag_get(p) != tag) & mask) != 0ull) {
    if (__builtin_amdgcn_s_memrealtime() - t0 > SPIN_LIMIT) break;
    __builtin_amdgcn_s_sleep(2);
  }
  asm volatile("" ::: "memory");
}

DI float gelu_t(float v) {
  const float z = v * (1.0f + 0.044715f * v * v) * (-2.3022081986f);
  return v * __builtin_amdgcn_rcpf(1.0f + __builtin_amdgcn_exp2f(z));
}

constexpr unsigned QSTG = 49152u;
constexpr unsigned KBASE = 65536u, VBASE = 114688u;
constexpr unsigned KSLOT[3] = {0u, 32768u, 16384u}, VSLOT[3] = {32768u, 16384u, 0u};
DI void touch8k(rsrc_t r, unsigned vo128, unsigned soff) {
  asm volatile("buffer_load_dword v255, %0, %1, %2 offen" :: "v"(vo128), "s"(r), "s"(soff) : "memory", "v255");
}
constexpr float QSCALE = 0.12751743082f;
#define BIGSUM 4096.0f

#define ATTPRIO 0
#define PRIO(x) do { if (ATTPRIO == 1) __builtin_amdgcn_s_setprio(x); else if (ATTPRIO == 2) __builtin_amdgcn_s_setprio(1 - (x)); } while (0)
struct Att {
  bf16x8 qf[8], kv[16], pf[4], pn[2];
  f32x16 o[4], s[2], negm;
  float l, ts;
  unsigned aK[4], aV[2];
};
template <unsigned SLOT, int J> DI void att_k(Att& a) {
  asm volatile("ds_read_b128 %0, %1 offset:%2" : "+v"(a.kv[J]) : "v"(a.aK[(J >> 1) & 3]), "i"(SLOT + 8192u * (J >> 3) + 4096u * (J & 1)) : "memory");
}
template <unsigned SLOT, int J> DI void att_v(Att& a) {
  s16x4 lo = __builtin_shufflevector(a.kv[J], a.kv[J], 0, 1, 2, 3), hi = __builtin_shufflevector(a.kv[J], a.kv[J], 4, 5, 6, 7);
  constexpr unsigned off = SLOT + 4096u * (J >> 2) + 1024u * ((J >> 1) & 1);
  asm volatile("ds_read_b64_tr_b16 %0, %2 offset:%3\n\tds_read_b64_tr_b16 %1, %2 offset:%4" : "+v"(lo), "+v"(hi) : "v"(a.aV[J & 1]), "i"(off), "i"(off + 2048u) : "memory");
  a.kv[J] = cat(lo, hi);
}
DI void att_wait(Att& a) {
  asm volatile("s_waitcnt lgkmcnt(0)" : "+v"(a.kv[0]), "+v"(a.kv[1]), "+v"(a.kv[2]), "+v"(a.kv[3]), "+v"(a.kv[4]), "+v"(a.kv[5]), "+v"(a.kv[6]), "+v"(a.kv[7]),
               "+v"(a.kv[8]), "+v"(a.kv[9]), "+v"(a.kv[10]), "+v"(a.kv[11]), "+v"(a.kv[12]), "+v"(a.kv[13]), "+v"(a.kv[14]), "+v"(a.kv[15]) :: "memory");
}
DI void att_loadK(Att& a, unsigned slot) {
#pragma unroll
  for (int j = 0; j < 16; ++j) a.kv[j] = ldsR(a.aK[(j >> 1) & 3] + slot + 8192u * (j >> 3) + 4096u * (j & 1));
}
DI unsigned lane_id() { unsigned l; asm volatile("v_mbcnt_lo_u32_b32 %0, -1, 0\n\tv_mbcnt_hi_u32_b32 %0, -1, %0" : "=v"(l)); return l; }
DI void add_bcast3(f32x16& t0, f32x16& t1, f32x16& t2, float v) {
  const float one = lane_id() < 32u ? 1.0f : 0.0f;
  asm volatile("s_nop 7\n\ts_nop 7\n\tv_mfma_f32_32x32x2_f32 %0, %3, %4, %0\n\tv_mfma_f32_32x32x2_f32 %1, %3, %4, %1\n\tv_mfma_f32_32x32x2_f32 %2, %3, %4, %2\n\t"
               "s_nop 7\n\ts_nop 7\n\ts_nop 7" : "+v"(t0), "+v"(t1), "+v"(t2) : "v"(one), "v"(v));
}
template <bool FIRST, unsigned SLOT, int J> DI void att_qk1(Att& a) {
  a.s[J & 1] = __builtin_amdgcn_mfma_f32_32x32x16_bf16(a.kv[J], a.qf[J >> 1], J < 2 ? (FIRST ? (f32x16){} : a.negm) : a.s[J & 1], 0, 0, 0);
  SCHED();
  if constexpr (!FIRST) att_v<SLOT, J>(a);
  SCHED();
  if constexpr (J < 15) att_qk1<FIRST, SLOT, J + 1>(a);
}
template <int NEXT, unsigned SLOT, int J> DI void att_pv1(Att& a) {
  a.o[J & 3] = __builtin_amdgcn_mfma_f32_32x32x16_bf16(a.kv[J], a.pf[J >> 2], a.o[J & 3], 0, 0, 0);
  SCHED();
  if constexpr (NEXT == 1) att_k<SLOT, J>(a); else if constexpr (NEXT == 2) att_v<SLOT, J>(a);
  SCHED();
  if constexpr (J < 15) att_pv1<NEXT, SLOT, J + 1>(a);
}
DI float max3(float x, float y, float z) { return __builtin_fmaxf(__builtin_fmaxf(x, y), z); }
DI float att_rowmax(const Att& a) {
  float m0 = max3(a.s[0][0], a.s[0][1], a.s[0][2]), m1 = max3(a.s[1][0], a.s[1][1], a.s[1][2]);
#pragma unroll
  for (int r = 3; r < 15; r += 2) { m0 = max3(m0, a.s[0][r], a.s[0][r + 1]); m1 = max3(m1, a.s[1][r], a.s[1][r + 1]); }
  const float mx = __builtin_fmaxf(max3(m0, m1, a.s[0][15]), a.s[1][15]);
  auto rr = __builtin_amdgcn_permlane32_swap(__float_as_uint(mx), __float_as_uint(mx), false, false);
  return __builtin_fmaxf(__uint_as_float(rr[0]), __uint_as_float(rr[1]));
}
DI float att_exp16(const f32x16& v, bf16x8& p0, bf16x8& p1) {
  float s0 = 0.f, s1 = 0.f;
  u32x4 w[2];
#pragma unroll
  for (int h = 0; h < 2; ++h) {
    float e[8];
#pragma unroll
    for (int r = 0; r < 8; ++r) e[r] = __builtin_amdgcn_exp2f(v[8 * h + r]);
#pragma unroll
    for (int r = 0; r < 8; r += 2) {
      if (h == 0 && r == 0) { s0 = e[0]; s1 = e[1]; } else { s0 += e[r]; asm("" : "+v"(s0)); s1 += e[r + 1]; asm("" : "+v"(s1)); }
      w[h][r >> 1] = pk2(e[r], e[r + 1]);
    }
    asm volatile("" : "+v"(w[h]));
    SCHED();
  }
  p0 = __builtin_bit_cast(bf16x8, w[0]); p1 = __builtin_bit_cast(bf16x8, w[1]);
  return s0 + s1;
}
template <bool FIRST> DI void att_sm1(Att& a) {
  if (FIRST) {
    const float mx = att_rowmax(a);
#pragma unroll
    for (int r = 0; r < 16; ++r) { a.s[0][r] -= mx; a.s[1][r] -= mx; a.negm[r] = -mx; }
  }
  a.ts = att_exp16(a.s[0], a.pn[0], a.pn[1]);
  asm volatile("" : "+v"(a.pn[0]), "+v"(a.pn[1]), "+v"(a.ts), "+v"(a.s[1]), "+v"(a.negm));
}
template <bool FIRST> DI void att_sm2(Att& a) {
  a.ts += att_exp16(a.s[1], a.pf[2], a.pf[3]);
  if (!FIRST) {
    if (__builtin_expect(!__all(a.ts <= BIGSUM), 0)) {
      const float dl = fmaxf(att_rowmax(a), 0.f), fct = __builtin_amdgcn_exp2f(-dl);
#pragma unroll
      for (int db = 0; db < 4; ++db) a.o[db] *= fct;
      a.l *= fct;
      add_bcast3(a.s[0], a.s[1], a.negm, -dl);
      a.ts = att_exp16(a.s[0], a.pn[0], a.pn[1]) + att_exp16(a.s[1], a.pf[2], a.pf[3]);
    }
  }
  a.l += a.ts;
  a.pf[0] = a.pn[0]; a.pf[1] = a.pn[1];
  asm volatile("" : "+v"(a.pf[0]), "+v"(a.pf[1]), "+v"(a.pf[2]), "+v"(a.pf[3]), "+v"(a.l));
}

extern "C" __global__ __launch_bounds__(512, 2) __attribute__((amdgpu_num_vgpr(127)))
void rsk_main(const u16* __restrict__ kc, const u16* __restrict__ vc,
           const u16* __restrict__ wq, const u16* __restrict__ w1, const u16* __restrict__ b1, const u16* __restrict__ wo, const u16* __restrict__ w2,
           const u16* __restrict__ b2, const u16* __restrict__ x, char* __restrict__ ws, u16* __restrict__ out) {
  extern __shared__ __attribute__((aligned(16))) char lds[];
  const unsigned L = (unsigned)(size_t)lds;
  const char* xn = ws + WS_XN; const char* hh = ws + WS_H;
  const unsigned tid = threadIdx.x, lane = tid & 63u, wid = rfl(tid >> 6), grp = wid >> 2, lw = wid & 3u;
  const unsigned bid = blockIdx.x, xcd = bid & 7u, idx = bid >> 3;
  const unsigned qt = (xcd & 1u) * 4u + (idx & 3u), kvh = xcd >> 1, head = kvh * 8u + (idx >> 2);
  const bool hasU = idx < 28u;
  const unsigned udt = (xcd & 1u) * 4u + (idx & 3u), uft = (xcd >> 1) * 7u + (idx >> 2);
  const rsrc_t sK = mkrsrc(kc + ((size_t)kvh * SK + (SK - WIN)) * DH, WIN * DH * 2), sV = mkrsrc(vc + ((size_t)kvh * SK + (SK - WIN)) * DH, WIN * DH * 2);
  const rsrc_t sH = mkrsrcB(hh, SEQ * DM * 2), sW1 = mkrsrcB(w1, SEQ * FF * 2);
  const unsigned voK0 = voffRow(lane, 256u, 0), voV = voffT(lane, 256u);
  auto dmaK2 = [&](unsigned slot, unsigned t, unsigned p0) {
    unsigned voK1; asm volatile("v_xor_b32 %0, 64, %1" : "=v"(voK1) : "v"(voK0));
    dma16(L + KBASE + slot + 1024u * p0, voK0, sK, t * 16384u + (p0 & 7u) * 2048u + (p0 >> 3) * 128u);
    dma16(L + KBASE + slot + 1024u * p0 + 1024u, voK1, sK, t * 16384u + (p0 & 7u) * 2048u + 2048u + (p0 >> 3) * 128u); };
  auto dmaV2 = [&](unsigned slot, unsigned t, unsigned p0) {
    dma16(L + VBASE + slot + 1024u * p0, voV, sV, t * 16384u + (p0 >> 1) * 2048u);
    dma16(L + VBASE + slot + 1024u * p0 + 1024u, voV, sV, t * 16384u + (p0 >> 1) * 2048u + 128u); };
  auto tileK = [&](unsigned t) {
#pragma unroll
    for (int p = 0; p < 16; p += 2) dmaK2(KSLOT[t % 3], t, p); };
  auto tileV = [&](unsigned t) {
#pragma unroll
    for (int p = 0; p < 16; p += 2) dmaV2(VSLOT[t % 3], t, p); };
  Att a;
  { const unsigned r32 = lane & 31u, hi = lane >> 5;
#pragma unroll
    for (int j = 0; j < 4; ++j) a.aK[j] = L + KBASE + r32 * 128u + 16u * ((2u * j + hi) ^ ((r32 >> 1) & 7u)); }

  ST_DECL; STAMP(0);
  u64* flags = (u64*)(ws + WS_FLAG);
  const rsrc_t sS = lw == 0 ? mkrsrcB(wq, DM * NH * DH * 2) : mkrsrcB(xn, SEQ * DM * 2);
  unsigned asrc = 4096u + (lw == 0 ? head * 256u : (qt * 256u + (lw - 1u) * 128u) * 2048u), adst = L + lw * 16384u;
  auto astep = [&](unsigned vo0, unsigned vo1) {
    const unsigned sp = lw == 0 ? 8u * 8192u : 16u * 2048u, so = (lw == 0 ? 128u : 8u * 2048u) - 1024u;
#pragma unroll
    for (int q = 0; q < 4; ++q) { const unsigned e = asrc + 2u * q * sp; dma64<true>(adst + 4096u * q, vo0, vo1, sS, e, e + so, e + sp - 2048u, e + sp - 2048u + so); }
    asrc += lw == 0 ? 64u * 8192u : 128u; adst = adst >= L + 2u * QSTG ? adst - 2u * QSTG : adst + QSTG;
  };
  u64 tag;
  {
    const unsigned row = bid * 8u + wid;
    u32x4 xa, xb; u32x2 ep;
    asm volatile("buffer_load_dwordx4 %0, %3, %4, %5 offen\n\tbuffer_load_dwordx4 %1, %3, %4, %5 offen offset:1024\n\tglobal_load_dwordx2 %2, %6, %7 sc1"
                 : "=&v"(xa), "=&v"(xb), "=&v"(ep) : "v"(lane * 16u), "s"(mkrsrc(x, SEQ * DM * 2)), "s"(row * 2048u), "v"(0u), "s"(flags) : "memory");
    if (wid == 4) {
      const unsigned vo = voffT(lane, 8192u);
#pragma nounroll
      for (unsigned t = 0; t < 3; ++t) astep(vo, vo);
      waitv<48>();
    } else if (wid == 7) {
      touch8k(code_rsrc(), lane < 16u ? lane * 128u : 0x80000000u, idx * 2048u);
      touch8k(mkrsrc(wq, DM * NH * DH * 2), lane * 128u, (192u + bid) * 8192u);
      waitv<2>();
    } else waitv<0>();
    asm volatile("" : "+v"(xa), "+v"(xb), "+v"(ep));
    tag = (((u64)rfl(ep[1]) << 32) | rfl(ep[0])) * 0x9E3779B97F4A7C15ull + 0x632BE59BD9B4E019ull;
    float v[16];
#pragma unroll
    for (int i = 0; i < 4; ++i) { v[2 * i] = bflo(xa[i]); v[2 * i + 1] = bfhi(xa[i]); v[8 + 2 * i] = bflo(xb[i]); v[9 + 2 * i] = bfhi(xb[i]); }
    ln16(v);
    const rsrc_t sW = mkrsrc(ws, WS_O);
#pragma unroll
    for (int p = 0; p < 2; ++p) {
      const unsigned lo0 = pkx(v[8 * p], v[8 * p + 1]), lo1 = pkx(v[8 * p + 2], v[8 * p + 3]), hi0 = pkx(v[8 * p + 4], v[8 * p + 5]), hi1 = pkx(v[8 * p + 6], v[8 * p + 7]);
      const bool odd = lane & 1u;
      const unsigned s0 = __builtin_amdgcn_update_dpp(0, (int)(odd ? lo0 : hi0), 0xB1, 0xf, 0xf, false), s1 = __builtin_amdgcn_update_dpp(0, (int)(odd ? lo1 : hi1), 0xB1, 0xf, 0xf, false);
      __builtin_amdgcn_raw_buffer_store_b128(odd ? (u32x4){s0, s1, hi0, hi1} : (u32x4){lo0, lo1, s0, s1}, sW, WS_XN + row * 2048u + lane * 16u + 1024u * p, 0, 16);
    }
    ln16(v);
    u32x4 h0 = {pkx(v[0], v[1]), pkx(v[2], v[3]), pkx(v[4], v[5]), pkx(v[6], v[7])}, h1 = {pkx(v[8], v[9]), pkx(v[10], v[11]), pkx(v[12], v[13]), pkx(v[14], v[15])};
    asm volatile("s_waitcnt vmcnt(0)" : "+v"(h0), "+v"(h1) :: "memory");
    BAR();
    if (tid == 0u) flag_set(flags + FL_X + bid, tag);
    __builtin_amdgcn_raw_buffer_store_b128(h0, sW, WS_H + row * 2048u + lane * 16u, 0, 16);
    __builtin_amdgcn_raw_buffer_store_b128(h1, sW, WS_H + row * 2048u + lane * 16u + 1024u, 0, 16);
    STAMP(10);
  }
  if (wid == 7) {
    const unsigned vo = lane * 128u;
    waitv<0>(); BAR();
    if (lane == 0u) flag_set(flags + FL_H + bid, tag);
    const rsrc_t sWq = mkrsrc(wq, DM * NH * DH * 2);
#pragma unroll
    for (int j = 0; j < 3; ++j) touch8k(sWq, vo, (448u + j * 256u + bid) * 8192u);
    if (idx < 16u) touch8k(sK, vo, idx * 8192u); else touch8k(sV, vo, (idx - 16u) * 8192u);
    for (unsigned t = 0; t < 9; ++t) BAR();
    const u64 f0 = flag_get(flags + FL_H + lane), f1 = flag_get(flags + FL_H + 64u + lane), f2 = flag_get(flags + FL_H + 128u + lane), f3 = flag_get(flags + FL_H + 192u + lane);
    touch8k(code_rsrc(), lane < 16u ? vo : 0x80000000u, idx * 2048u);
    BAR(); BAR(); BAR();
    if (__any(f0 != tag || f1 != tag || f2 != tag || f3 != tag)) {
#pragma nounroll
      for (unsigned j = 0; j < 256u; j += 64u) flag_wait(flags + FL_H + j + lane, tag, ~0ull);
    }
    if (bid == 0u && lane == 0u) flag_set(flags, tag);
    BAR(); BAR(); BAR(); BAR();
#pragma unroll
    for (int j = 0; j < 3; ++j) touch8k(sW1, vo, (j * 256u + bid) * 8192u + 4096u);
    att_loadK(a, KSLOT[0]);
  } else if (grp == 1) {
    const unsigned vo0 = lw == 0 ? voffT(lane, 8192u) : voffRow(lane, 2048u, 0), vo1 = lw == 0 ? vo0 : vo0 ^ 64u;
    if (lw == 0) waitv<0>();
    else {
      flag_wait(flags + FL_X + qt * 32u + (lw - 1u) * 16u + (lane & 15u), tag, ~0ull);
#pragma nounroll
      for (unsigned t = 0; t < 3; ++t) astep(vo0, vo1);
      waitv<32>();
    }
    BAR(); STAMP(1);
#pragma nounroll
    for (unsigned t = 0; t < 13; ++t) { waitv<16>(); BAR(); astep(vo0, vo1); }
    waitv<16>(); BAR();
    if (lw == 0) tileK(0); else if (lw == 1) tileK(2); else tileV(0);
    waitv<16>(); BAR();
    if (lw == 1) tileK(1); else if (lw == 2) tileV(1);
    if (lw == 0) waitv<0>();
    BAR();
    att_loadK(a, KSLOT[0]);
  } else {
    Opnd<T1> P; Opnd<RROW> Q;
    P.init(L, 0u, lane); Q.init(L + 16384u, lw * 64u, lane);
    f32x4 acc[8][4] = {};
    waitv<0>();
    gemm_compute<T1, RROW, 8, 4, 3, QSTG, 0, 0, 0>(acc, P, Q, 16);
    STAMP(2);
    att_loadK(a, KSLOT[0]);
    const unsigned wa = L + (lane >> 5) * 4096u + (lw * 64u + (lane & 15u)) * 16u + ((lane >> 4) & 1u) * 8u;
#pragma unroll
    for (int n = 0; n < 8; ++n)
#pragma unroll
      for (int m = 0; m < 4; ++m) {
        const f32x4 v = acc[n][m] * QSCALE;
        *(LAS u32x2*)(size_t)(wa + 8192u * n + 256u * m) = (u32x2){pk2(v[0], v[1]), pk2(v[2], v[3])};
      }
  }
  WAITL0(); BAR(); STAMP(3);

  u32x4 opk[8]; unsigned oo;
  const rsrc_t sO = mkrsrc(ws + WS_O, SEQ * NH * DH * 2);
  {
    const unsigned l2 = lane_id(), hi = l2 >> 5, blk = (l2 >> 4) & 1u, q4 = (l2 & 15u) >> 2, p4 = l2 & 3u;
#pragma unroll
    for (int ks = 0; ks < 8; ++ks) a.qf[ks] = ldsR(L + (2u * ks) * 4096u + hi * 4096u + (wid * 32u + (l2 & 31u)) * 16u);
#pragma unroll
    for (int n = 0; n < 2; ++n) a.aV[n] = L + VBASE + 128u * (4u * hi + q4) + 32u * ((2u * n + blk) ^ (((q4 >> 1) << 1) | hi)) + 8u * p4;
#pragma unroll
    for (int db = 0; db < 4; ++db) a.o[db] = (f32x16){};
    a.l = 0.f;
    auto ustepP_ = [&](unsigned j, unsigned stg, unsigned vo) { dmaT4<false>(L + stg * 32768u, lw, vo, sW1, j * 64u * (FF * 2u) + uft * 256u, FF * 2u); };
    auto ustepQ_ = [&](unsigned j, unsigned stg, unsigned vo) { dmaT4<true>(L + stg * 32768u + 16384u, lw, vo, sH, j * 64u * 2048u + udt * 256u, 2048u); };
    const bool pre = hasU && grp == 1;
    if (grp == 1) { if (wid != 7) waitv<0>(); WAITL0(); BAR(); }
    STAMP(4);
    const rsrc_t sWo = mkrsrc(wo, NH * DH * DM * 2), sW2 = mkrsrc(w2, FF * SEQ * 2);
    const unsigned lk = bid * 8192u + (wid >> 1) * 2048u + (wid & 1u) * 128u;
#define LOOK(v) do { if ((v) < 4) touch8k(sW1, voV, lk + ((v) + 3) * 0x200000u + 4096u); else if ((v) < 8) touch8k(sWo, voV, lk + ((v) - 4) * 0x200000u); \
                     else if ((v) < 11) touch8k(sW2, voV, lk + ((v) - 4) * 0x200000u); } while (0)
#define USTEP(i) ((i) < 5 ? (i) - 3 : (i) - 4)
#define ATT_TILE(i, FIRST, WB, WBP, WD, WDP)                                                                                  \
    SCHED(); att_qk1<FIRST, VSLOT[((i) + 2) % 3], 0>(a); BAR(); SCHED();    \
    if ((i) <= 4) dmaK2(KSLOT[(i) % 3], (i) + 3, wid * 2u);                                                                   \
    LOOK(2 * (i));                                                                                                            \
    att_sm1<FIRST>(a);                                                                                                        \
    if ((i) >= 3 && pre) { waitv<WBP>(); if ((i) != 5) ustepP_(USTEP(i), USTEP(i), voffT(lane_id(), FF * 2u)); } else waitv<WB>(); \
    if (!(FIRST)) att_wait(a); SCHED(); BAR();                              \
    if (FIRST) att_loadK(a, KSLOT[1]); else { SCHED(); att_pv1<((i) < 7 ? 1 : 2), ((i) < 7 ? KSLOT[((i) + 1) % 3] : VSLOT[7 % 3]), 0>(a); } \
    BAR(); SCHED();                                                         \
    if ((i) <= 5) dmaV2(VSLOT[((i) + 2) % 3], (i) + 2, wid * 2u);                                                             \
    LOOK(2 * (i) + 1);                                                                                                        \
    att_sm2<FIRST>(a);                                                                                                        \
    if ((i) >= 3 && pre) { waitv<WDP>(); if ((i) != 5) ustepQ_(USTEP(i), USTEP(i), voffT(lane_id(), 2048u)); } else waitv<WD>(); \
    att_wait(a); SCHED(); BAR();
    ATT_TILE(0, true, 63, 63, 63, 63) ATT_TILE(1, false, 63, 63, 10, 10) ATT_TILE(2, false, 10, 10, 10, 10) ATT_TILE(3, false, 10, 10, 10, 14)
    ATT_TILE(4, false, 10, 18, 10, 22) ATT_TILE(5, false, 8, 20, 7, 15) ATT_TILE(6, false, 4, 8, 0, 4) ATT_TILE(7, false, 63, 63, 63, 63)
#undef ATT_TILE
#undef USTEP
#undef LOOK
    SCHED(); att_pv1<0, 0u, 0>(a); STAMP(5);
    if (grp == 0) BAR();
    const unsigned ln = lane_id();
    { auto rr = __builtin_amdgcn_permlane32_swap(__float_as_uint(a.l), __float_as_uint(a.l), false, false);
      a.l = __uint_as_float(rr[0]) + __uint_as_float(rr[1]); }
    const float inv = 1.0f / a.l;
    const unsigned row = qt * 256u + wid * 32u + (ln & 31u);
    oo = (((row >> 7) * 64u + head * 2u) * 8u + (ln >> 5)) * 2048u + (row & 127u) * 16u;
#pragma unroll
    for (int j = 0; j < 8; ++j) {
      const f32x16& v = a.o[j >> 1]; const int b = 8 * (j & 1);
      opk[j] = (u32x4){pk2(v[b] * inv, v[b + 1] * inv), pk2(v[b + 2] * inv, v[b + 3] * inv), pk2(v[b + 4] * inv, v[b + 5] * inv), pk2(v[b + 6] * inv, v[b + 7] * inv)};
      if (!pre) __builtin_amdgcn_raw_buffer_store_b128(opk[j], sO, oo + j * 4096u, 0, 16);
    }
    if (pre) asm volatile("" : "+v"(opk[0]), "+v"(opk[1]), "+v"(opk[2]), "+v"(opk[3]), "+v"(opk[4]), "+v"(opk[5]), "+v"(opk[6]), "+v"(opk[7]));
    STAMP(6);
  }
  const unsigned ln = lane_id();
  const bool isY = xcd >= 4u;
  const unsigned dt = udt, st = ((xcd >> 1) & 1u) * 8u + (idx >> 2), tile = st * 8u + dt;
  u64* fG = flags + FL_G + tile * 16u; u64* fO = flags + FL_O; u64* fU = flags + FL_U + dt * 32u;
  const rsrc_t sG = mkrsrc(ws + WS_G + (size_t)tile * 32768u, 32768u), sU = mkrsrc(ws + WS_U, DM * FF * 2);
  constexpr unsigned XO = 32768u, GO = 65536u;
  if (!hasU) {
    waitv<0>(); BAR();
    if (tid == 256u) flag_set(fO + qt * 32u + head, tag);
    const rsrc_t sW2 = mkrsrc(w2, FF * SEQ * 2);
    const unsigned w = ((idx - 28u) * 8u + xcd) * 8u + wid;
#pragma nounroll
    for (unsigned j = 0; j < 4u * PFDIV; ++j) {
      touch8k(sW2, ln < 64u / PFDIV ? ln * 128u : 0x80000000u, ((((j / PFDIV) + 3u) & 3u) * 256u + w) * 8192u + (j % PFDIV) * (8192u / PFDIV));
      waitv<0>(); __builtin_amdgcn_s_sleep(PFGAP);
    }
    STAMP(7);
  }
  if (grp == 1) {
    const unsigned N = isY ? 56u : 64u;
    const rsrc_t sT = isY ? mkrsrcB(w2, FF * SEQ * 2) : mkrsrcB(wo, NH * DH * DM * 2);
    const rsrc_t sR = isY ? sU : sO;
    const unsigned ldT = isY ? 4096u : 2048u, voT = voffT(ln, ldT), voL = ln * 16u;
    const unsigned tcol = (isY ? st : dt) * 256u, rbase = (isY ? dt : st) * N * 16384u + lw * 4096u;
    const unsigned toff = (isY ? 16384u : 0u) + lw * 4096u, roff = (isY ? 0u : 16384u) + lw * 4096u, g0 = isY ? (xcd >> 1) * 14u : 0u;
    const u64* dep = (isY ? fU : fO + (st >> 1) * 32u) + (ln & 31u);
    auto peek = [&]() { asm volatile("global_load_dwordx2 v[254:255], %0, off sc1" :: "v"(dep) : "memory", "v254", "v255"); };
    auto await = [&](u64 mask, bool peeked) {
      if (peeked) { u64 v; asm volatile("v_mov_b64 %0, v[254:255]" : "=v"(v) :: "memory"); if ((__ballot(v != tag) & mask) == 0ull) return; }
      flag_wait(dep, tag, mask);
    };
    unsigned sb = L + 4u * 32768u;
    auto nexts = [&]() { sb = sb == L + 4u * 32768u ? L : sb + 32768u; };
    unsigned kT = g0;
    auto dT = [&]() { T4 p; p.set(kT * 64u * ldT + tcol, lw, ldT); p.go(sb + toff, voT, sT); ++kT; nexts(); };
    if (hasU) {
      const unsigned voH = voffT(ln, 2048u), voW1 = voffT(ln, FF * 2u);
      unsigned j = 4;
      auto urun = [&](unsigned n) {
#pragma nounroll
        for (unsigned i = 0; i < n; ++i) {
          T4 p, q; p.set(j * 64u * (FF * 2u) + uft * 256u, lw, FF * 2u); q.set(j * 64u * 2048u + udt * 256u, lw, 2048u);
          unsigned d0 = sb + lw * 4096u, d1 = d0 + 16384u; p.pin(); q.pin(); PINS(d0); PINS(d1);
          waitv<24>(); BAR();
          p.go(d0, voW1, sW1); q.go<true>(d1, voH, sH);
          ++j; nexts();
        }
      };
      urun(7);
      STAMP(7);
#pragma unroll
      for (int i = 0; i < 8; ++i) { __builtin_amdgcn_raw_buffer_store_b128(opk[i], sO, oo + i * 4096u, 0, 16); urun(2); }
      touch8k(code_rsrc(), ln < 5u ? ln * 128u : 0x80000000u, (idx * 4u + lw) * 640u);
      urun(3);
      if (tid == 256u) flag_set(fO + qt * 32u + head, tag);
      urun(2);
      waitv<24>(); BAR(); dT(); waitv<20>(); BAR(); dT(); peek(); waitv<16>(); BAR(); dT(); waitv<12>(); BAR(); dT(); BAR(); dT();
      if (isY) {
        BAR();
        if (tid == 256u) flag_set(fU + uft, tag);
      } else waitv<12>();
      STAMP(8);
    } else {
      sb = L + 2u * 32768u;
#pragma nounroll
      for (unsigned j = 0; j < 5; ++j) dT();
    }
    await(isY ? 0x7full << (g0 >> 1) : 0xffffffffull, hasU && !isY);
#pragma nounroll
    for (unsigned j = 0; j < 5; ++j) { dma64d(sb + roff, voL, sR, rbase + (g0 + j) * 16384u); nexts(); }
    auto dstep = [&](auto wait) {
      T4 p; p.set(kT * 64u * ldT + tcol, lw, ldT);
      unsigned d0 = sb + toff, d1 = sb + roff, r0 = rbase + kT * 16384u; p.pin(); PINS(d0); PINS(d1); PINS(r0);
      wait(); BAR();
      p.go(d0, voT, sT); dma64d(d1, voL, sR, r0);
      ++kT; nexts();
    };
    waitv<16>(); BAR(); STAMP(11);
    dstep([] { waitv<12>(); }); dstep([] { waitv<16>(); }); dstep([] { waitv<20>(); });
    if (!isY && hasU && tid == 256u) flag_set(fU + uft, tag);
#pragma nounroll
    for (unsigned seg = isY ? 0u : 4u; seg < 5u; ++seg) {
      const unsigned n = !isY ? 56u : seg == 0u ? 2u : seg == 2u ? 10u : seg == 4u ? 28u : 4u;
#pragma nounroll
      for (unsigned i = 0; i < n; ++i) dstep([] { waitv<24>(); });
      if (seg == 0u || seg == 2u) peek();
      else if (seg < 4u) { waitv<32>(); await(seg == 1u ? 0x3fffull << 14 : 0x3fffull, true); kT = seg == 1u ? 70u - g0 : 0u; }
    }
    waitv<24>(); BAR();
    if (isY) { waitv<0>(); BAR(); STAMP(12); ST_OUT(1); return; }
    {
      const rsrc_t sX = mkrsrc(x, SEQ * DM * 2);
#pragma unroll
      for (int q = 0; q < 8; ++q)
        dma16(L + XO + (lw * 8u + q) * 1024u, (ln >> 4) * 2048u + 16u * ((ln & 15u) ^ ((4u * q + (ln >> 4)) & 15u)), sX, (st * 128u + lw * 32u + q * 4u) * 2048u + dt * 256u);
    }
    waitv<8>(); BAR();
    STAMP(12);
    {
      flag_wait(fG, tag, 1ull);
      u32x4 g[8];
#pragma unroll
      for (int q = 0; q < 8; ++q) g[q] = __builtin_amdgcn_raw_buffer_load_b128(sG, voL + (lw * 8u + q) * 1024u, 0, 16);
#pragma unroll
      for (int q = 0; q < 8; ++q) *(LAS u32x4*)(size_t)(L + GO + voL + (lw * 8u + q) * 1024u) = g[q];
    }
    waitv<0>(); WAITL0(); STAMP(13); BAR();
  } else {
    const unsigned wp = lw >> 1, wq2 = lw & 1u, fr = ln & 15u, fq = ln >> 4;
    if (hasU) {
      Opnd<T2> P, Q;
      P.init(L, wp * 64u, ln); Q.init(L + 16384u, wq2 * 64u, ln);
      u32x2 bv[4];
#pragma unroll
      for (int p = 0; p < 4; ++p) bv[p] = *(const u32x2*)(b1 + uft * 128u + wp * 64u + p * 16u + fq * 4u);
      f32x4 acc[4][4] = {};
      gemm_compute<T2, T2, 4, 4, 5, 32768u, 0, 0, 16>(acc, P, Q, 32);
      STAMP(8);
      const unsigned uo = ((udt * 56u + uft * 2u + wp) * 8u + fq) * 2048u + (wq2 * 64u + fr) * 16u;
#pragma unroll
      for (int pp = 0; pp < 2; ++pp) {
        const u32x2 ba = bv[2 * pp], bb = bv[2 * pp + 1];
        const float bias[8] = {bflo(ba[0]), bfhi(ba[0]), bflo(ba[1]), bfhi(ba[1]), bflo(bb[0]), bfhi(bb[0]), bflo(bb[1]), bfhi(bb[1])};
#pragma unroll
        for (int m = 0; m < 4; ++m) {
          float v[8];
#pragma unroll
          for (int j = 0; j < 8; ++j) v[j] = gelu_t(acc[2 * pp + (j >> 2)][m][j & 3] + bias[j]);
          __builtin_amdgcn_raw_buffer_store_b128((u32x4){pk2(v[0], v[1]), pk2(v[2], v[3]), pk2(v[4], v[5]), pk2(v[6], v[7])}, sU, uo + pp * 8192u + m * 256u, 0, 16);
        }
      }
      STAMP(9);
      if (isY) { waitv<0>(); BAR(); }
    }
    f32x4 acc[4][4] = {};
    const unsigned t4 = (wid * 64u + ln) * 16u;
    if (isY) {
      float bias[4];
#pragma unroll
      for (int q = 0; q < 4; ++q) bias[q] = bflo(b2[st * 128u + wq2 * 64u + q * 16u + fr]);
      Opnd<RCHK> P; Opnd<T2> Q;
      P.init(L + 65536u, wp * 64u, ln); Q.init(L + 65536u + 16384u, wq2 * 64u, ln);
      gemm_compute<RCHK, T2, 4, 4, 5, 32768u, 3, 2, 0>(acc, P, Q, 56);
      STAMP(12);
#pragma unroll
      for (int j = 0; j < 8; ++j) {
        float g[8];
#pragma unroll
        for (int c = 0; c < 8; ++c) {
          const int q = 2 * (j & 1) + (c >> 2); const float y = acc[j >> 1][q][c & 3] + bias[q];
          g[c] = y * __builtin_amdgcn_rcpf(1.0f + __builtin_amdgcn_exp2f(y * -1.4426950408889634f));
        }
        __builtin_amdgcn_raw_buffer_store_b128((u32x4){pk2(g[0], g[1]), pk2(g[2], g[3]), pk2(g[4], g[5]), pk2(g[6], g[7])}, sG, t4 + j * 4096u, 0, 16);
      }
      waitv<0>(); BAR();
      if (tid == 0u) flag_set(fG, tag);
      STAMP(15); ST_OUT(1); return;
    }
    Opnd<T1> P; Opnd<RCHK> Q;
    P.init(L + 65536u, wp * 64u, ln); Q.init(L + 65536u + 16384u, wq2 * 64u, ln);
    gemm_compute<T1, RCHK, 4, 4, 5, 32768u, 3, 2, 2>(acc, P, Q, 64);
    STAMP(12);
    BAR();
    STAMP(13);
    u32x4 gp[8];
#pragma unroll
    for (int j = 0; j < 8; ++j) gp[j] = *(const LAS u32x4*)(size_t)(L + GO + t4 + j * 4096u);
    const unsigned xa = L + XO + (wq2 * 64u + fr) * 256u + (fq & 1u) * 8u;
    u32x2 xv[4][4];
#pragma unroll
    for (int p = 0; p < 4; ++p)
#pragma unroll
      for (int q = 0; q < 4; ++q) xv[p][q] = *(const LAS u32x2*)(size_t)(xa + 16u * ((wp * 8u + 2u * p + (fq >> 1)) ^ fr) + q * 4096u);
#pragma unroll
    for (int p = 0; p < 4; ++p)
#pragma unroll
      for (int q = 0; q < 4; ++q) {
        const f32x4 v = acc[p][q]; const unsigned g0 = gp[2 * p + (q >> 1)][2 * (q & 1)], g1 = gp[2 * p + (q >> 1)][2 * (q & 1) + 1];
        *(LAS u32x2*)(size_t)(xa + 16u * ((wp * 8u + 2u * p + (fq >> 1)) ^ fr) + q * 4096u) =
            (u32x2){pk2(fmaf(v[0], bflo(g0), bflo(xv[p][q][0])), fmaf(v[1], bfhi(g0), bfhi(xv[p][q][0]))), pk2(fmaf(v[2], bflo(g1), bflo(xv[p][q][1])), fmaf(v[3], bfhi(g1), bfhi(xv[p][q][1])))};
      }
  }
  WAITL0(); BAR();
#pragma unroll
  for (int q = 0; q < 4; ++q) {
    const unsigned row = wid * 16u + q * 4u + (ln >> 4);
    const u32x4 v = *(const LAS u32x4*)(size_t)(L + XO + row * 256u + 16u * ((ln & 15u) ^ (row & 15u)));
    *(u32x4*)(out + (size_t)(st * 128u + row) * DM + dt * 128u + (ln & 15u) * 8u) = v;
  }
  STAMP(14); waitv<0>(); STAMP(15); ST_OUT(1);
}

extern "C" __global__ void rsk_end() {}
}

extern "C" void kernel_launch(void* const* d_in, const int* in_sizes, int n_in, void* d_out, int out_size, void* d_ws, size_t ws_size, hipStream_t stream) {
  const u16* x = (const u16*)d_in[0]; const u16* kc = (const u16*)d_in[1]; const u16* vc = (const u16*)d_in[2];
  const u16* wq = (const u16*)d_in[3]; const u16* wo = (const u16*)d_in[4];
  const u16* w1 = (const u16*)d_in[6]; const u16* b1 = (const u16*)d_in[7]; const u16* w2 = (const u16*)d_in[8]; const u16* b2 = (const u16*)d_in[9];
#define WSEND 0
  char* ws = (char*)d_ws + (WSEND ? ((ws_size - WS_TOTAL) & ~(size_t)0xfffff) : 0);
  (void)hipFuncSetAttribute((const void*)rsk_main, hipFuncAttributeMaxDynamicSharedMemorySize, LDS_BYTES);
  rsk_main<<<256, 512, LDS_BYTES, stream>>>(kc, vc, wq, w1, b1, wo, w2, b2, x, ws, (u16*)d_out);
}
```

```cpp
#include <hip/hip_runtime.h>
#include <hip/hip_bf16.h>

namespace {
typedef short bf16x8 __attribute__((ext_vector_type(8)));
typedef short s16x4 __attribute__((ext_vector_type(4)));
typedef float f32x2 __attribute__((ext_vector_type(2)));
typedef float f32x4 __attribute__((ext_vector_type(4)));
typedef float f32x16 __attribute__((ext_vector_type(16)));
typedef unsigned u32x4 __attribute__((ext_vector_type(4)));
typedef unsigned u32x2 __attribute__((ext_vector_type(2)));
typedef __bf16 bf2_t __attribute__((ext_vector_type(2)));
typedef unsigned short u16;
typedef __amdgpu_buffer_rsrc_t rsrc_t;
#define LAS __attribute__((address_space(3)))
#define DI __device__ __forceinline__
#define BAR() asm volatile("s_barrier" ::: "memory")
#define WAITL0() do { asm volatile("" ::: "memory"); __builtin_amdgcn_s_waitcnt(0xC07F); asm volatile("" ::: "memory"); } while (0)
#define SCHED() __builtin_amdgcn_sched_barrier(0)
#define SGB(mask, n) __builtin_amdgcn_sched_group_barrier(mask, n, 0)

constexpr int SEQ = 2048, DM = 1024, NH = 32, DH = 128, SK = 2048, WIN = 512, FF = 3584;
constexpr unsigned WS_XN = 0u, WS_H = 4u << 20, WS_O = 8u << 20, WS_U = 24u << 20, WS_G = 32u << 20, WS_FLAG = 36u << 20, WS_TOTAL = 37u << 20;
constexpr unsigned LDS_BYTES = 163840;
constexpr unsigned long long SPIN_LIMIT = 2000000ull;
#define LNPF 1
#define LNAUX 16
#define PFDIV 2
#define PFGAP 16
#define XP 0
#define XSLEEP 2
#define PFSLEEP 40

#define ST_DECL
#define STAMP(i) do {} while (0)
#define ST_OUT(k)
template <int N> DI void waitv() { asm volatile("s_waitcnt vmcnt(%0)" :: "i"(N) : "memory"); }
DI unsigned rfl(unsigned x) { return __builtin_amdgcn_readfirstlane(x); }
DI rsrc_t mkrsrc(const void* p, unsigned bytes) { return __builtin_amdgcn_make_buffer_rsrc((void*)p, (short)0, (int)bytes, 0x00020000); }
DI void dma16(unsigned m0v, unsigned voff, rsrc_t r, unsigned soff) {
  asm volatile("s_mov_b32 m0, %0\n\ts_nop 0\n\tbuffer_load_dwordx4 %1, %2, %3 offen lds" :: "s"(m0v), "v"(voff), "s"(r), "s"(soff) : "memory");
}
DI rsrc_t code_rsrc() {
  unsigned long long b, e;
  asm volatile("s_getpc_b64 s[40:41]\n\ts_add_u32 s40, s40, rsk_main@rel32@lo+4\n\ts_addc_u32 s41, s41, rsk_main@rel32@hi+12\n\ts_mov_b64 %0, s[40:41]\n\t"
               "s_getpc_b64 s[40:41]\n\ts_add_u32 s40, s40, rsk_end@rel32@lo+4\n\ts_addc_u32 s41, s41, rsk_end@rel32@hi+12\n\ts_mov_b64 %1, s[40:41]"
               : "=&s"(b), "=&s"(e) :: "s40", "s41", "scc");
  return mkrsrc((const void*)b, (e > b && e - b < 0x40000ull) ? (unsigned)(e - b) : 0u);
}
template <bool DEV>
DI void dma64(unsigned m0v, unsigned va, unsigned vb, rsrc_t r, unsigned s0, unsigned s1, unsigned s2, unsigned s3) {
  if constexpr (DEV)
    asm volatile("s_mov_b32 m0, %0\n\ts_nop 0\n\tbuffer_load_dwordx4 %1, %3, %4 offen sc1 lds\n\tbuffer_load_dwordx4 %2, %3, %5 offen offset:1024 sc1 lds\n\t"
                 "buffer_load_dwordx4 %1, %3, %6 offen offset:2048 sc1 lds\n\tbuffer_load_dwordx4 %2, %3, %7 offen offset:3072 sc1 lds"
                 :: "s"(m0v), "v"(va), "v"(vb), "s"(r), "s"(s0), "s"(s1), "s"(s2), "s"(s3) : "memory");
  else
    asm volatile("s_mov_b32 m0, %0\n\ts_nop 0\n\tbuffer_load_dwordx4 %1, %3, %4 offen lds\n\tbuffer_load_dwordx4 %2, %3, %5 offen offset:1024 lds\n\t"
                 "buffer_load_dwordx4 %1, %3, %6 offen offset:2048 lds\n\tbuffer_load_dwordx4 %2, %3, %7 offen offset:3072 lds"
                 :: "s"(m0v), "v"(va), "v"(vb), "s"(r), "s"(s0), "s"(s1), "s"(s2), "s"(s3) : "memory");
}
DI void dma64d(unsigned m0v, unsigned va, rsrc_t r, unsigned s0) {
  asm volatile("s_mov_b32 m0, %0\n\ts_nop 0\n\tbuffer_load_dwordx4 %1, %2, %3 offen sc1 lds\n\tbuffer_load_dwordx4 %1, %2, %3 offen offset:1024 sc1 lds\n\t"
               "buffer_load_dwordx4 %1, %2, %3 offen offset:2048 sc1 lds\n\tbuffer_load_dwordx4 %1, %2, %3 offen offset:3072 sc1 lds"
               :: "s"(m0v), "v"(va), "s"(r), "s"(s0) : "memory");
}
DI rsrc_t mkrsrcB(const void* p, unsigned bytes) { return mkrsrc((const char*)p - 4096, bytes + 4096u); }
#define PINS(x) asm volatile("" : "+s"(x))
DI bf16x8 ldsR(unsigned a) { return *(const LAS bf16x8*)(size_t)a; }
DI s16x4 ldsT(unsigned a) { return __builtin_amdgcn_ds_read_tr16_b64_v4i16((LAS s16x4*)(size_t)a); }
DI bf16x8 cat(s16x4 a, s16x4 b) { return __builtin_shufflevector(a, b, 0, 1, 2, 3, 4, 5, 6, 7); }
DI unsigned pk2(float lo, float hi) { f32x2 v = {lo, hi}; return __builtin_bit_cast(unsigned, __builtin_convertvector(v, bf2_t)); }
DI float bflo(unsigned w) { return __uint_as_float(w << 16); }
DI float bfhi(unsigned w) { return __uint_as_float(w & 0xffff0000u); }
DI float rbf(float x) { return bflo(pk2(x, x) & 0xffffu); }
template <int CTRL, int RM> DI float dpp_add(float v) {
  return v + __builtin_bit_cast(float, __builtin_amdgcn_update_dpp(0, __builtin_bit_cast(int, v), CTRL, RM, 0xf, false));
}
DI void wsum2(float& u, float& v) {
#define WS2(C, M) u = dpp_add<C, M>(u); v = dpp_add<C, M>(v);
  WS2(0x111, 0xf) WS2(0x112, 0xf) WS2(0x114, 0xf) WS2(0x118, 0xf) WS2(0x142, 0xa) WS2(0x143, 0xc)
#undef WS2
  u = __builtin_bit_cast(float, __builtin_amdgcn_readlane(__builtin_bit_cast(int, u), 63));
  v = __builtin_bit_cast(float, __builtin_amdgcn_readlane(__builtin_bit_cast(int, v), 63));
}

DI void ln16(float (&v)[16]) {
  const float x0 = __builtin_bit_cast(float, __builtin_amdgcn_readfirstlane(__builtin_bit_cast(int, v[0])));
  float s = 0.f, q = 0.f;
#pragma unroll
  for (int i = 0; i < 16; ++i) { const float d = v[i] - x0; s += d; q = fmaf(d, d, q); }
  wsum2(s, q);
  const float md = s * (1.0f / DM), var = rbf(fmaxf(fmaf(-md, md, q * (1.0f / DM)), 0.f));
  const float mb = rbf(x0 + md), inv = __builtin_amdgcn_rcpf(rbf(__builtin_amdgcn_sqrtf(rbf(var + 1e-5f))));
#pragma unroll
  for (int i = 0; i < 16; i += 2) {
    const unsigned d = pk2(v[i] - mb, v[i + 1] - mb), r = pk2(bflo(d) * inv, bfhi(d) * inv);
    v[i] = bflo(r); v[i + 1] = bfhi(r);
  }
}
DI unsigned pkx(float lo, float hi) { return (__float_as_uint(lo) >> 16) | (__float_as_uint(hi) & 0xffff0000u); }
enum { RROW = 0, RCHK = 1, T1 = 2, T2 = 3 };
template <int KIND> struct Opnd {
  static constexpr int NA = KIND == RROW ? 2 : KIND == RCHK ? 1 : 4;
  static constexpr int RPB = KIND >= T1 ? 2 : 1;
  unsigned a[NA];
  DI void init(unsigned base, unsigned off, unsigned lane) {
    const unsigned fr = lane & 15u, g = lane >> 4, q = fr >> 2, p = lane & 3u;
    if constexpr (KIND == RROW) {
#pragma unroll
      for (int kk = 0; kk < 2; ++kk) a[kk] = base + (off + fr) * 128u + 16u * ((4u * kk + g) ^ (fr >> 1));
    } else if constexpr (KIND == RCHK) {
      a[0] = base + g * 2048u + (off + fr) * 16u;
    } else {
      const unsigned x = ((q >> 1) << 1) | (g & 1u);
#pragma unroll
      for (int n = 0; n < 4; ++n)
        a[n] = base + (KIND == T1 ? 4096u : 2048u) * (g >> 1) + 1024u * (off >> 6) + 128u * (4u * (g & 1u) + q) + 32u * ((unsigned)n ^ x) + 8u * p;
    }
  }
  DI bf16x8 ld(int b, int kk) const {
    if constexpr (KIND == RROW) return ldsR(a[kk] + 2048u * b);
    else if constexpr (KIND == RCHK) return ldsR(a[0] + 8192u * kk + 256u * b);
    else { const unsigned ad = a[b & 3] + 1024u * (b >> 2) + 8192u * kk; return cat(ldsT(ad), ldsT(ad + (KIND == T1 ? 2048u : 4096u))); }
  }
  DI void adv(unsigned delta) {
#pragma unroll
    for (int i = 0; i < NA; ++i) { a[i] += delta; asm volatile("" : "+v"(a[i])); }
  }
};

template <int PK, int QK, int PM, int QM, int NS, unsigned STG, int TAIL, int S0, int DRAIN, class F>
DI void gemm_compute(f32x4 (&acc)[PM][QM], Opnd<PK>& P, Opnd<QK>& Q, const int N, F first) {
  bf16x8 pf[2][PM], qf[2][QM];
  constexpr int NR = PM * Opnd<PK>::RPB + QM * Opnd<QK>::RPB, NM = PM * QM, NA = Opnd<PK>::NA + Opnd<QK>::NA;
  auto load = [&](int kk) {
    pf[kk][0] = P.ld(0, kk);
#pragma unroll
    for (int q = 0; q < QM; ++q) qf[kk][q] = Q.ld(q, kk);
#pragma unroll
    for (int p = 1; p < PM; ++p) pf[kk][p] = P.ld(p, kk);
  };
  auto mma = [&](int kk) {
#pragma unroll
    for (int p = 0; p < PM; ++p)
#pragma unroll
      for (int q = 0; q < QM; ++q) acc[p][q] = __builtin_amdgcn_mfma_f32_16x16x32_bf16(pf[kk][p], qf[kk][q], acc[p][q], 0, 0, 0);
  };
  constexpr int ND = (NR + NA + 2 > NM) ? (NR + NA + 2 - NM) : 0;
  BAR();
  load(0);
  int stg = S0;
  auto step = [&](bool sync) {
    SCHED();
    load(1); mma(0);
    stg = (stg == NS - 1) ? 0 : stg + 1;
    const unsigned delta = stg == 0 ? (unsigned)(-(int)((NS - 1) * STG)) : STG;
    P.adv(delta); Q.adv(delta);
#pragma unroll
    for (int i = 0; i < ND; ++i) { SGB(0x8, 1); SGB(0x100, 2); }
#pragma unroll
    for (int i = 0; i < NR - 2 * ND; ++i) { SGB(0x8, 1); SGB(0x100, 1); }
#pragma unroll
    for (int i = 0; i < NA; ++i) { SGB(0x8, 1); SGB(0x2, 1); }
    SGB(0x8, NM);
    SCHED();
    if (sync) { WAITL0(); BAR(); }
    SCHED();
    load(0); mma(1);
#pragma unroll
    for (int i = 0; i < ND; ++i) { SGB(0x8, 1); SGB(0x100, 2); }
#pragma unroll
    for (int i = 0; i < NR - 2 * ND; ++i) { SGB(0x8, 1); SGB(0x100, 1); }
    SGB(0x8, NM);
  };
  if (DRAIN > 0) {
#pragma nounroll
    for (int t = 0; t < DRAIN; ++t) step(true);
    waitv<0>();
    first();
  }
#pragma nounroll
  for (int t = DRAIN; t < N - TAIL; ++t) step(true);
#pragma nounroll
  for (int t = 0; t < TAIL; ++t) step(false);
  SCHED();
}

DI unsigned voffRow(unsigned lane, unsigned ldb, unsigned par) { return (lane >> 3) * ldb + 16u * ((lane & 7u) ^ (4u * par + (lane >> 4))); }
DI unsigned voffT(unsigned lane, unsigned ldb) { return (lane >> 3) * ldb + 16u * ((lane & 7u) ^ (2u * ((((lane >> 4) & 1u) << 1) | (lane >> 5)))); }
struct T4 { unsigned s0, s1, s2, s3;
  DI void set(unsigned so, unsigned lw, unsigned ldb) { s0 = so + 4096u + lw * 16u * ldb; s1 = s0 + 128u - 1024u; s2 = s0 + 8u * ldb - 2048u; s3 = s2 + 128u - 1024u; }
  DI void pin() { PINS(s0); PINS(s1); PINS(s2); PINS(s3); }
  template <bool DEV = false> DI void go(unsigned dst, unsigned vo, rsrc_t r) const { dma64<DEV>(dst, vo, vo, r, s0, s1, s2, s3); } };
template <bool DEV> DI void dmaT4(unsigned dst, unsigned lw, unsigned vo, rsrc_t r, unsigned so, unsigned ldb) { T4 t; t.set(so, lw, ldb); t.go<DEV>(dst + lw * 4096u, vo, r); }

typedef unsigned long long u64;
constexpr unsigned FL_H = 64, FL_O = 320, FL_U = 576, FL_G = 832, FL_X = 3072;
DI void flag_set(u64* p, u64 tag) { __hip_atomic_store(p, tag, __ATOMIC_RELAXED, __HIP_MEMORY_SCOPE_AGENT); }
DI u64 flag_get(const u64* p) { return __hip_atomic_load(p, __ATOMIC_RELAXED, __HIP_MEMORY_SCOPE_AGENT); }
template <int SLOW = 16> DI void flag_wait(const u64* p, u64 tag, u64 mask) {
  const u64 t0 = __builtin_amdgcn_s_memrealtime();
  while ((__ballot(flag_get(p) != tag) & mask) != 0ull) {
    const u64 dt = __builtin_amdgcn_s_memrealtime() - t0;
    if (dt > SPIN_LIMIT) break;
    if (dt > 400ull) __builtin_amdgcn_s_sleep(SLOW); else __builtin_amdgcn_s_sleep(3);
  }
  asm volatile("" ::: "memory");
}

DI float gelu_t(float v) {
  const float z = v * (1.0f + 0.044715f * v * v) * (-2.3022081986f);
  return v * __builtin_amdgcn_rcpf(1.0f + __builtin_amdgcn_exp2f(z));
}

constexpr unsigned QSTG = 49152u;
constexpr unsigned KBASE = 65536u, VBASE = 114688u;
constexpr unsigned KSLOT[3] = {0u, 32768u, 16384u}, VSLOT[3] = {32768u, 16384u, 0u};
DI void touch8k(rsrc_t r, unsigned vo128, unsigned soff) {
  asm volatile("buffer_load_dword v255, %0, %1, %2 offen" :: "v"(vo128), "s"(r), "s"(soff) : "memory", "v255");
}
constexpr float QSCALE = 0.12751743082f;
#define BIGSUM 4096.0f

#define ATTPRIO 0
#define PRIO(x) do { if (ATTPRIO == 1) __builtin_amdgcn_s_setprio(x); else if (ATTPRIO == 2) __builtin_amdgcn_s_setprio(1 - (x)); } while (0)
struct Att {
  bf16x8 qf[8], kv[16], pf[4], pn[2];
  f32x16 o[4], s[2], negm;
  float l, ts;
  unsigned aK[4], aV[2];
};
template <unsigned SLOT, int J> DI void att_k(Att& a) {
  asm volatile("ds_read_b128 %0, %1 offset:%2" : "+v"(a.kv[J]) : "v"(a.aK[(J >> 1) & 3]), "i"(SLOT + 8192u * (J >> 3) + 4096u * (J & 1)) : "memory");
}
template <unsigned SLOT, int J> DI void att_v(Att& a) {
  s16x4 lo = __builtin_shufflevector(a.kv[J], a.kv[J], 0, 1, 2, 3), hi = __builtin_shufflevector(a.kv[J], a.kv[J], 4, 5, 6, 7);
  constexpr unsigned off = SLOT + 4096u * (J >> 2) + 1024u * ((J >> 1) & 1);
  asm volatile("ds_read_b64_tr_b16 %0, %2 offset:%3\n\tds_read_b64_tr_b16 %1, %2 offset:%4" : "+v"(lo), "+v"(hi) : "v"(a.aV[J & 1]), "i"(off), "i"(off + 2048u) : "memory");
  a.kv[J] = cat(lo, hi);
}
DI void att_wait(Att& a) {
  asm volatile("s_waitcnt lgkmcnt(0)" : "+v"(a.kv[0]), "+v"(a.kv[1]), "+v"(a.kv[2]), "+v"(a.kv[3]), "+v"(a.kv[4]), "+v"(a.kv[5]), "+v"(a.kv[6]), "+v"(a.kv[7]),
               "+v"(a.kv[8]), "+v"(a.kv[9]), "+v"(a.kv[10]), "+v"(a.kv[11]), "+v"(a.kv[12]), "+v"(a.kv[13]), "+v"(a.kv[14]), "+v"(a.kv[15]) :: "memory");
}
DI void att_loadK(Att& a, unsigned slot) {
#pragma unroll
  for (int j = 0; j < 16; ++j) a.kv[j] = ldsR(a.aK[(j >> 1) & 3] + slot + 8192u * (j >> 3) + 4096u * (j & 1));
}
DI unsigned lane_id() { unsigned l; asm volatile("v_mbcnt_lo_u32_b32 %0, -1, 0\n\tv_mbcnt_hi_u32_b32 %0, -1, %0" : "=v"(l)); return l; }
DI void add_bcast3(f32x16& t0, f32x16& t1, f32x16& t2, float v) {
  const float one = lane_id() < 32u ? 1.0f : 0.0f;
  asm volatile("s_nop 7\n\ts_nop 7\n\tv_mfma_f32_32x32x2_f32 %0, %3, %4, %0\n\tv_mfma_f32_32x32x2_f32 %1, %3, %4, %1\n\tv_mfma_f32_32x32x2_f32 %2, %3, %4, %2\n\t"
               "s_nop 7\n\ts_nop 7\n\ts_nop 7" : "+v"(t0), "+v"(t1), "+v"(t2) : "v"(one), "v"(v));
}
template <bool FIRST, unsigned SLOT, int J> DI void att_qk1(Att& a) {
  a.s[J & 1] = __builtin_amdgcn_mfma_f32_32x32x16_bf16(a.kv[J], a.qf[J >> 1], J < 2 ? (FIRST ? (f32x16){} : a.negm) : a.s[J & 1], 0, 0, 0);
  SCHED();
  if constexpr (!FIRST) att_v<SLOT, J>(a);
  SCHED();
  if constexpr (J < 15) att_qk1<FIRST, SLOT, J + 1>(a);
}
template <int NEXT, unsigned SLOT, int J> DI void att_pv1(Att& a) {
  a.o[J & 3] = __builtin_amdgcn_mfma_f32_32x32x16_bf16(a.kv[J], a.pf[J >> 2], a.o[J & 3], 0, 0, 0);
  SCHED();
  if constexpr (NEXT == 1) att_k<SLOT, J>(a); else if constexpr (NEXT == 2) att_v<SLOT, J>(a);
  SCHED();
  if constexpr (J < 15) att_pv1<NEXT, SLOT, J + 1>(a);
}
DI float max3(float x, float y, float z) { return __builtin_fmaxf(__builtin_fmaxf(x, y), z); }
DI float att_rowmax(const Att& a) {
  float m0 = max3(a.s[0][0], a.s[0][1], a.s[0][2]), m1 = max3(a.s[1][0], a.s[1][1], a.s[1][2]);
#pragma unroll
  for (int r = 3; r < 15; r += 2) { m0 = max3(m0, a.s[0][r], a.s[0][r + 1]); m1 = max3(m1, a.s[1][r], a.s[1][r + 1]); }
  const float mx = __builtin_fmaxf(max3(m0, m1, a.s[0][15]), a.s[1][15]);
  auto rr = __builtin_amdgcn_permlane32_swap(__float_as_uint(mx), __float_as_uint(mx), false, false);
  return __builtin_fmaxf(__uint_as_float(rr[0]), __uint_as_float(rr[1]));
}
DI float att_exp16(const f32x16& v, bf16x8& p0, bf16x8& p1) {
  float s0 = 0.f, s1 = 0.f;
  u32x4 w[2];
#pragma unroll
  for (int h = 0; h < 2; ++h) {
    float e[8];
#pragma unroll
    for (int r = 0; r < 8; ++r) e[r] = __builtin_amdgcn_exp2f(v[8 * h + r]);
#pragma unroll
    for (int r = 0; r < 8; r += 2) {
      if (h == 0 && r == 0) { s0 = e[0]; s1 = e[1]; } else { s0 += e[r]; asm("" : "+v"(s0)); s1 += e[r + 1]; asm("" : "+v"(s1)); }
      w[h][r >> 1] = pk2(e[r], e[r + 1]);
    }
    asm volatile("" : "+v"(w[h]));
    SCHED();
  }
  p0 = __builtin_bit_cast(bf16x8, w[0]); p1 = __builtin_bit_cast(bf16x8, w[1]);
  return s0 + s1;
}
template <bool FIRST> DI void att_sm1(Att& a) {
  if (FIRST) {
    const float mx = att_rowmax(a);
#pragma unroll
    for (int r = 0; r < 16; ++r) { a.s[0][r] -= mx; a.s[1][r] -= mx; a.negm[r] = -mx; }
  }
  a.ts = att_exp16(a.s[0], a.pn[0], a.pn[1]);
  asm volatile("" : "+v"(a.pn[0]), "+v"(a.pn[1]), "+v"(a.ts), "+v"(a.s[1]), "+v"(a.negm));
}
template <bool FIRST> DI void att_sm2(Att& a) {
  a.ts += att_exp16(a.s[1], a.pf[2], a.pf[3]);
  if (!FIRST) {
    if (__builtin_expect(!__all(a.ts <= BIGSUM), 0)) {
      const float dl = fmaxf(att_rowmax(a), 0.f), fct = __builtin_amdgcn_exp2f(-dl);
#pragma unroll
      for (int db = 0; db < 4; ++db) a.o[db] *= fct;
      a.l *= fct;
      add_bcast3(a.s[0], a.s[1], a.negm, -dl);
      a.ts = att_exp16(a.s[0], a.pn[0], a.pn[1]) + att_exp16(a.s[1], a.pf[2], a.pf[3]);
    }
  }
  a.l += a.ts;
  a.pf[0] = a.pn[0]; a.pf[1] = a.pn[1];
  asm volatile("" : "+v"(a.pf[0]), "+v"(a.pf[1]), "+v"(a.pf[2]), "+v"(a.pf[3]), "+v"(a.l));
}

extern "C" __global__ __launch_bounds__(512, 2) __attribute__((amdgpu_num_vgpr(127)))
void rsk_main(const u16* __restrict__ kc, const u16* __restrict__ vc,
           const u16* __restrict__ wq, const u16* __restrict__ w1, const u16* __restrict__ b1, const u16* __restrict__ wo, const u16* __restrict__ w2,
           const u16* __restrict__ b2, const u16* __restrict__ x, char* __restrict__ ws, u16* __restrict__ out) {
  extern __shared__ __attribute__((aligned(16))) char lds[];
  const unsigned L = (unsigned)(size_t)lds;
  const char* xn = ws + WS_XN; const char* hh = ws + WS_H;
  const unsigned tid = threadIdx.x, lane = tid & 63u, wid = rfl(tid >> 6), grp = wid >> 2, lw = wid & 3u;
  const unsigned bid = blockIdx.x, xcd = bid & 7u, idx = bid >> 3;
  const unsigned qt = (xcd & 1u) * 4u + (idx & 3u), kvh = xcd >> 1, head = kvh * 8u + (idx >> 2);
  const bool hasU = idx < 28u;
  const unsigned udt = (xcd & 1u) * 4u + (idx & 3u), uft = (xcd >> 1) * 7u + (idx >> 2);
  const rsrc_t sK = mkrsrc(kc + ((size_t)kvh * SK + (SK - WIN)) * DH, WIN * DH * 2), sV = mkrsrc(vc + ((size_t)kvh * SK + (SK - WIN)) * DH, WIN * DH * 2);
  const rsrc_t sH = mkrsrc(hh, SEQ * DM * 2), sW1 = mkrsrcB(w1, SEQ * FF * 2);
  const unsigned voK0 = voffRow(lane, 256u, 0), voV = voffT(lane, 256u);
  auto dmaK2 = [&](unsigned slot, unsigned t, unsigned p0) {
    unsigned voK1; asm volatile("v_xor_b32 %0, 64, %1" : "=v"(voK1) : "v"(voK0));
    dma16(L + KBASE + slot + 1024u * p0, voK0, sK, t * 16384u + (p0 & 7u) * 2048u + (p0 >> 3) * 128u);
    dma16(L + KBASE + slot + 1024u * p0 + 1024u, voK1, sK, t * 16384u + (p0 & 7u) * 2048u + 2048u + (p0 >> 3) * 128u); };
  auto dmaV2 = [&](unsigned slot, unsigned t, unsigned p0) {
    dma16(L + VBASE + slot + 1024u * p0, voV, sV, t * 16384u + (p0 >> 1) * 2048u);
    dma16(L + VBASE + slot + 1024u * p0 + 1024u, voV, sV, t * 16384u + (p0 >> 1) * 2048u + 128u); };
  auto tileK = [&](unsigned t) {
#pragma unroll
    for (int p = 0; p < 16; p += 2) dmaK2(KSLOT[t % 3], t, p); };
  auto tileV = [&](unsigned t) {
#pragma unroll
    for (int p = 0; p < 16; p += 2) dmaV2(VSLOT[t % 3], t, p); };
  Att a;
  { const unsigned r32 = lane & 31u, hi = lane >> 5;
#pragma unroll
    for (int j = 0; j < 4; ++j) a.aK[j] = L + KBASE + r32 * 128u + 16u * ((2u * j + hi) ^ ((r32 >> 1) & 7u)); }

  ST_DECL; STAMP(0);
  u64* flags = (u64*)(ws + WS_FLAG);
  const rsrc_t sS = lw == 0 ? mkrsrcB(wq, DM * NH * DH * 2) : mkrsrcB(xn, SEQ * DM * 2);
  unsigned asrc = 4096u + (lw == 0 ? head * 256u : (qt * 256u + (lw - 1u) * 128u) * 2048u), adst = L + lw * 16384u;
  auto astep = [&](unsigned vo0, unsigned vo1) {
    const unsigned sp = lw == 0 ? 8u * 8192u : 16u * 2048u, so = (lw == 0 ? 128u : 8u * 2048u) - 1024u;
#pragma unroll
    for (int q = 0; q < 4; ++q) { const unsigned e = asrc + 2u * q * sp; dma64<true>(adst + 4096u * q, vo0, vo1, sS, e, e + so, e + sp - 2048u, e + sp - 2048u + so); }
    asrc += lw == 0 ? 64u * 8192u : 128u; adst = adst >= L + 2u * QSTG ? adst - 2u * QSTG : adst + QSTG;
  };
  u64 tag;
  {
    const unsigned row = (bid >> 2) * 32u + ((bid >> 1) & 1u) * 16u + (bid & 1u) * 4u + (wid >> 2) * 8u + (wid & 3u);
    u32x4 xa, xb; u32x2 ep;
    asm volatile("buffer_load_dwordx4 %0, %3, %4, %5 offen\n\tbuffer_load_dwordx4 %1, %3, %4, %5 offen offset:1024\n\tglobal_load_dwordx2 %2, %6, %7 sc1"
                 : "=&v"(xa), "=&v"(xb), "=&v"(ep) : "v"(lane * 16u), "s"(mkrsrc(x, SEQ * DM * 2)), "s"(row * 2048u), "v"(0u), "s"(flags) : "memory");
    __builtin_amdgcn_s_setprio(1);
    if (wid == 7) {
      touch8k(code_rsrc(), lane < 16u ? lane * 128u : 0x80000000u, idx * 2048u);
      touch8k(mkrsrc(wq, DM * NH * DH * 2), lane * 128u, bid * 8192u);
      waitv<2>();
    } else waitv<0>();
    asm volatile("" : "+v"(xa), "+v"(xb), "+v"(ep));
    STAMP(16);
    tag = (((u64)rfl(ep[1]) << 32) | rfl(ep[0])) * 0x9E3779B97F4A7C15ull + 0x632BE59BD9B4E019ull;
    float v[16];
#pragma unroll
    for (int i = 0; i < 4; ++i) { v[2 * i] = bflo(xa[i]); v[2 * i + 1] = bfhi(xa[i]); v[8 + 2 * i] = bflo(xb[i]); v[9 + 2 * i] = bfhi(xb[i]); }
    ln16(v);
    const rsrc_t sW = mkrsrc(ws, WS_O);
#pragma unroll
    for (int p = 0; p < 2; ++p) {
      const unsigned lo0 = pkx(v[8 * p], v[8 * p + 1]), lo1 = pkx(v[8 * p + 2], v[8 * p + 3]), hi0 = pkx(v[8 * p + 4], v[8 * p + 5]), hi1 = pkx(v[8 * p + 6], v[8 * p + 7]);
      const bool odd = lane & 1u;
      const unsigned s0 = __builtin_amdgcn_update_dpp(0, (int)(odd ? lo0 : hi0), 0xB1, 0xf, 0xf, false), s1 = __builtin_amdgcn_update_dpp(0, (int)(odd ? lo1 : hi1), 0xB1, 0xf, 0xf, false);
      __builtin_amdgcn_raw_buffer_store_b128(odd ? (u32x4){s0, s1, hi0, hi1} : (u32x4){lo0, lo1, s0, s1}, sW, WS_XN + row * 2048u + lane * 16u + 1024u * p, 0, 16);
    }
    __builtin_amdgcn_s_setprio(0);
    STAMP(17);
    ln16(v);
    constexpr unsigned HT = 147456u;
    *(LAS u32x4*)(size_t)(L + HT + wid * 2048u + lane * 16u) = (u32x4){pkx(v[0], v[1]), pkx(v[2], v[3]), pkx(v[4], v[5]), pkx(v[6], v[7])};
    *(LAS u32x4*)(size_t)(L + HT + wid * 2048u + 1024u + lane * 16u) = (u32x4){pkx(v[8], v[9]), pkx(v[10], v[11]), pkx(v[12], v[13]), pkx(v[14], v[15])};
    STAMP(18);
    waitv<0>();
    if (lane == 0u) flag_set(flags + FL_X + bid * 8u + wid, tag);
    STAMP(19);
    WAITL0(); BAR(); STAMP(20);
    if (grp == 0) {
#pragma unroll
      for (int i = 0; i < 4; ++i) {
        const unsigned d0 = (lw * 4u + i) * 64u, ad = L + HT + ((lane & 15u) >> 2) * 2048u + (d0 + (lane >> 4) * 16u + (lane & 3u) * 4u) * 2u;
        __builtin_amdgcn_raw_buffer_store_b128(__builtin_bit_cast(u32x4, cat(ldsT(ad), ldsT(ad + 8192u))), sW, WS_H + ((d0 >> 7) * 256u + bid) * 2048u + ((d0 & 64u) + lane) * 16u, 0, 16);
      }
    }
  }
  if (wid == 7) {
    const unsigned vo = lane * 128u;
    BAR();
    if (lane == 0u) flag_set(flags + FL_H + bid, tag);
    BAR();
    const rsrc_t sWq = mkrsrc(wq, DM * NH * DH * 2);
#pragma unroll
    for (int j = 1; j < 4; ++j) touch8k(sWq, vo, (j * 256u + bid) * 8192u);
    if (idx < 16u) touch8k(sK, vo, idx * 8192u); else touch8k(sV, vo, (idx - 16u) * 8192u);
    for (unsigned t = 0; t < 8; ++t) BAR();
    const u64 f0 = flag_get(flags + FL_H + lane), f1 = flag_get(flags + FL_H + 64u + lane), f2 = flag_get(flags + FL_H + 128u + lane), f3 = flag_get(flags + FL_H + 192u + lane);
    touch8k(code_rsrc(), lane < 16u ? vo : 0x80000000u, idx * 2048u);
    BAR(); BAR(); BAR();
    if (__any(f0 != tag || f1 != tag || f2 != tag || f3 != tag)) {
#pragma nounroll
      for (unsigned j = 0; j < 256u; j += 64u) flag_wait(flags + FL_H + j + lane, tag, ~0ull);
    }
    if (bid == 0u && lane == 0u) flag_set(flags, tag);
    BAR(); BAR(); BAR(); BAR();
#pragma unroll
    for (int j = 0; j < 3; ++j) touch8k(sW1, vo, (j * 256u + bid) * 8192u + 4096u);
    att_loadK(a, KSLOT[0]);
  } else if (grp == 1) {
    const unsigned vo0 = lw == 0 ? voffT(lane, 8192u) : voffRow(lane, 2048u, 0), vo1 = lw == 0 ? vo0 : vo0 ^ 64u;
    if (lw != 0) {
      const u64* fx = flags + FL_X + qt * 256u + (lw - 1u) * 128u + lane;
      const u64 t0 = __builtin_amdgcn_s_memrealtime();
      while (__any(((flag_get(fx) ^ tag) | (flag_get(fx + 64) ^ tag)) != 0ull)) {
        if (__builtin_amdgcn_s_memrealtime() - t0 > SPIN_LIMIT) break;
        __builtin_amdgcn_s_sleep(XSLEEP);
      }
      asm volatile("" ::: "memory");
      STAMP(21);
    }
    astep(vo0, vo1); astep(vo0, vo1);
    STAMP(22); waitv<16>(); STAMP(23);
    BAR(); STAMP(1);
    astep(vo0, vo1);
#pragma nounroll
    for (unsigned t = 0; t < 13; ++t) { waitv<16>(); BAR(); astep(vo0, vo1); }
    waitv<16>(); BAR();
    if (lw == 0) tileK(0); else if (lw == 1) tileK(2); else tileV(0);
    waitv<16>(); BAR();
    if (lw == 1) tileK(1); else if (lw == 2) tileV(1);
    if (lw == 0) waitv<0>();
    BAR();
    att_loadK(a, KSLOT[0]);
  } else {
    Opnd<T1> P; Opnd<RROW> Q;
    P.init(L, 0u, lane); Q.init(L + 16384u, lw * 64u, lane);
    f32x4 acc[8][4] = {};
    waitv<0>();
    gemm_compute<T1, RROW, 8, 4, 3, QSTG, 0, 0, 0>(acc, P, Q, 16, [] {});
    STAMP(2);
    att_loadK(a, KSLOT[0]);
    const unsigned wa = L + (lane >> 5) * 4096u + (lw * 64u + (lane & 15u)) * 16u + ((lane >> 4) & 1u) * 8u;
#pragma unroll
    for (int n = 0; n < 8; ++n)
#pragma unroll
      for (int m = 0; m < 4; ++m) {
        const f32x4 v = acc[n][m] * QSCALE;
        *(LAS u32x2*)(size_t)(wa + 8192u * n + 256u * m) = (u32x2){pk2(v[0], v[1]), pk2(v[2], v[3])};
      }
  }
  WAITL0(); BAR(); STAMP(3);

  const rsrc_t sO = mkrsrc(ws + WS_O, SEQ * NH * DH * 2);
  {
    const unsigned l2 = lane_id(), hi = l2 >> 5, blk = (l2 >> 4) & 1u, q4 = (l2 & 15u) >> 2, p4 = l2 & 3u;
#pragma unroll
    for (int ks = 0; ks < 8; ++ks) a.qf[ks] = ldsR(L + (2u * ks) * 4096u + hi * 4096u + (wid * 32u + (l2 & 31u)) * 16u);
#pragma unroll
    for (int n = 0; n < 2; ++n) a.aV[n] = L + VBASE + 128u * (4u * hi + q4) + 32u * ((2u * n + blk) ^ (((q4 >> 1) << 1) | hi)) + 8u * p4;
#pragma unroll
    for (int db = 0; db < 4; ++db) a.o[db] = (f32x16){};
    a.l = 0.f;
    auto ustepP_ = [&](unsigned j, unsigned stg, unsigned vo) { dmaT4<false>(L + stg * 32768u, lw, vo, sW1, j * 64u * (FF * 2u) + uft * 256u, FF * 2u); };
    auto ustepQ_ = [&](unsigned j, unsigned stg, unsigned vo) { dma64d(L + stg * 32768u + 16384u + lw * 4096u, vo, sH, (udt * 32u + j) * 16384u + lw * 4096u); };
    const bool pre = hasU && grp == 1;
    if (grp == 1) { if (wid != 7) waitv<0>(); WAITL0(); BAR(); }
    STAMP(4);
    const rsrc_t sWo = mkrsrc(wo, NH * DH * DM * 2), sW2 = mkrsrc(w2, FF * SEQ * 2);
    const unsigned lk = bid * 8192u + (wid >> 1) * 2048u + (wid & 1u) * 128u;
#define LOOK(v) do { if ((v) < 4) touch8k(sW1, voV, lk + ((v) + 3) * 0x200000u + 4096u); else if ((v) < 8) touch8k(sWo, voV, lk + ((v) - 4) * 0x200000u); \
                     else if ((v) < 11) touch8k(sW2, voV, lk + ((v) - 4) * 0x200000u); } while (0)
#define USTEP(i) ((i) < 5 ? (i) - 3 : (i) - 4)
#define ATT_TILE(i, FIRST, WB, WBP, WD, WDP)                                                                                  \
    SCHED(); att_qk1<FIRST, VSLOT[((i) + 2) % 3], 0>(a); BAR(); SCHED();    \
    if ((i) <= 4) dmaK2(KSLOT[(i) % 3], (i) + 3, wid * 2u);                                                                   \
    LOOK(2 * (i));                                                                                                            \
    att_sm1<FIRST>(a);                                                                                                        \
    if ((i) >= 3 && pre) { waitv<WBP>(); if ((i) != 5) ustepP_(USTEP(i), USTEP(i), voffT(lane_id(), FF * 2u)); } else waitv<WB>(); \
    if (!(FIRST)) att_wait(a); SCHED(); BAR();                              \
    if (FIRST) att_loadK(a, KSLOT[1]); else { SCHED(); att_pv1<((i) < 7 ? 1 : 2), ((i) < 7 ? KSLOT[((i) + 1) % 3] : VSLOT[7 % 3]), 0>(a); } \
    BAR(); SCHED();                                                         \
    if ((i) <= 5) dmaV2(VSLOT[((i) + 2) % 3], (i) + 2, wid * 2u);                                                             \
    LOOK(2 * (i) + 1);                                                                                                        \
    att_sm2<FIRST>(a);                                                                                                        \
    if ((i) >= 3 && pre) { waitv<WDP>(); if ((i) != 5) ustepQ_(USTEP(i), USTEP(i), lane_id() * 16u); } else waitv<WD>(); \
    att_wait(a); SCHED(); BAR();
    ATT_TILE(0, true, 63, 63, 63, 63) ATT_TILE(1, false, 63, 63, 10, 10) ATT_TILE(2, false, 10, 10, 10, 10) ATT_TILE(3, false, 10, 10, 10, 14)
    ATT_TILE(4, false, 10, 18, 10, 22) ATT_TILE(5, false, 8, 20, 7, 15) ATT_TILE(6, false, 4, 8, 0, 4) ATT_TILE(7, false, 63, 63, 63, 63)
#undef ATT_TILE
#undef USTEP
#undef LOOK
    SCHED(); att_pv1<0, 0u, 0>(a); STAMP(5);
    if (grp == 0) BAR();
    if (pre) { waitv<24>(); BAR(); STAMP(24); }
  }
  auto ost2 = [&](int db) {
    const unsigned l3 = lane_id(), row = qt * 256u + wid * 32u + (l3 & 31u);
    const unsigned oo = (((row >> 7) * 64u + head * 2u) * 8u + (l3 >> 5)) * 2048u + (row & 127u) * 16u;
#pragma unroll
    for (int i = 0; i < 2; ++i) {
      float w[8];
#pragma unroll
      for (int c = 0; c < 8; ++c) { w[c] = a.o[db][8 * i + c] * a.l; asm("" : "+v"(w[c])); }
      __builtin_amdgcn_raw_buffer_store_b128((u32x4){pk2(w[0], w[1]), pk2(w[2], w[3]), pk2(w[4], w[5]), pk2(w[6], w[7])}, sO, oo + db * 8192u + i * 4096u, 0, 16);
    }
  };
  { auto rr = __builtin_amdgcn_permlane32_swap(__float_as_uint(a.l), __float_as_uint(a.l), false, false);
    a.l = __builtin_amdgcn_rcpf(__uint_as_float(rr[0]) + __uint_as_float(rr[1])); }
  if (!(hasU && grp == 1)) { ost2(0); ost2(1); ost2(2); ost2(3); STAMP(6); }
  const unsigned ln = lane_id();
  const bool isY = xcd >= 4u;
  const unsigned dt = udt, st = ((xcd >> 1) & 1u) * 8u + (idx >> 2), tile = st * 8u + dt;
  u64* fG = flags + FL_G + tile * 16u; u64* fO = flags + FL_O; u64* fU = flags + FL_U + dt * 32u;
  const rsrc_t sG = mkrsrc(ws + WS_G + (size_t)tile * 32768u, 32768u), sU = mkrsrc(ws + WS_U, DM * FF * 2);
  constexpr unsigned XO = 32768u, GO = 65536u;
  if (!hasU) {
    waitv<0>(); BAR();
    if (tid == 256u) flag_set(fO + qt * 32u + head, tag);
    const rsrc_t sW2 = mkrsrc(w2, FF * SEQ * 2);
    const unsigned w = ((idx - 28u) * 8u + xcd) * 8u + wid;
#pragma nounroll
    for (unsigned j = 0; j < 4u * PFDIV; ++j) {
      touch8k(sW2, ln < 64u / PFDIV ? ln * 128u : 0x80000000u, ((((j / PFDIV) + 3u) & 3u) * 256u + w) * 8192u + (j % PFDIV) * (8192u / PFDIV));
      waitv<0>(); __builtin_amdgcn_s_sleep(PFGAP);
    }
    STAMP(7);
  }
  if (grp == 1) {
    const unsigned N = isY ? 56u : 64u;
    const rsrc_t sT = isY ? mkrsrcB(w2, FF * SEQ * 2) : mkrsrcB(wo, NH * DH * DM * 2);
    const rsrc_t sR = isY ? sU : sO;
    const unsigned ldT = isY ? 4096u : 2048u, voT = voffT(ln, ldT), voL = ln * 16u;
    const unsigned tcol = (isY ? st : dt) * 256u, rbase = (isY ? dt : st) * N * 16384u + lw * 4096u;
    const unsigned toff = (isY ? 16384u : 0u) + lw * 4096u, roff = (isY ? 0u : 16384u) + lw * 4096u, g0 = isY ? (xcd >> 1) * 14u : 0u;
    const u64* dep = (isY ? fU : fO + (st >> 1) * 32u) + (ln & 31u);
    auto peek = [&]() { asm volatile("global_load_dwordx2 v[254:255], %0, off sc1" :: "v"(dep) : "memory", "v254", "v255"); };
    auto await = [&](u64 mask, bool peeked) {
      if (peeked) { u64 v; asm volatile("v_mov_b64 %0, v[254:255]" : "=v"(v) :: "memory"); if ((__ballot(v != tag) & mask) == 0ull) return; }
      flag_wait(dep, tag, mask);
    };
    unsigned sb = L + 4u * 32768u;
    auto nexts = [&]() { sb = sb == L + 4u * 32768u ? L : sb + 32768u; };
    unsigned kT = g0;
    auto dT = [&]() { T4 p; p.set(kT * 64u * ldT + tcol, lw, ldT); p.go(sb + toff, voT, sT); ++kT; nexts(); };
    if (hasU) {
      const unsigned voW1 = voffT(ln, FF * 2u), hw = (wid & 1u) * 2u;
      unsigned j = 4;
      auto ufill = [&](auto wait) {
        T4 p, q; p.set(j * 64u * (FF * 2u) + uft * 256u, hw, FF * 2u); q.set(j * 64u * (FF * 2u) + uft * 256u, hw + 1u, FF * 2u);
        unsigned d0 = sb + hw * 4096u, r0 = (udt * 32u + j) * 16384u + hw * 4096u; p.pin(); q.pin(); PINS(d0); PINS(r0);
        wait();
        p.go(d0, voW1, sW1); dma64d(d0 + 16384u, voL, sH, r0); q.go(d0 + 4096u, voW1, sW1); dma64d(d0 + 20480u, voL, sH, r0 + 4096u);
        ++j; nexts();
      };
      if (wid >= 6u) {
        ufill([] {});
        ufill([] { waitv<32>(); BAR(); }); ufill([] { waitv<40>(); BAR(); });
#pragma nounroll
        for (unsigned t = 2; t < 12; ++t) ufill([] { waitv<48>(); BAR(); });
        waitv<48>(); BAR(); ost2(0); waitv<34>(); BAR(); ost2(1); waitv<20>(); BAR(); ost2(2); waitv<6>(); BAR(); ost2(3);
        touch8k(code_rsrc(), ln < 10u ? ln * 128u : 0x80000000u, (idx * 2u + (wid & 1u)) * 1280u);
        for (unsigned t = 16; t < 22; ++t) BAR();
        waitv<0>(); BAR();
        if (tid == 384u) flag_set(fO + qt * 32u + head, tag);
        BAR(); BAR(); BAR(); BAR();
      } else {
        waitv<16>(); BAR(); STAMP(25); waitv<8>(); BAR(); waitv<0>(); BAR();
        ost2(0); BAR(); ost2(1); BAR(); ost2(2); BAR(); ost2(3);
        for (unsigned t = 6; t < 12; ++t) BAR();
        STAMP(26);
        j = 17; sb = L + 2u * 32768u;
#pragma nounroll
        for (unsigned t = 12; t < 27; ++t) ufill([] { waitv<48>(); BAR(); });
        STAMP(28);
      }
      sb = L + 2u * 32768u;
      waitv<48>(); BAR(); dT(); waitv<36>(); BAR(); dT(); peek(); waitv<24>(); BAR(); dT(); waitv<12>(); BAR(); dT(); BAR(); dT();
      if (isY) {
        BAR();
        if (tid == 256u) flag_set(fU + uft, tag);
      } else waitv<12>();
      STAMP(8);
    } else {
      sb = L + 2u * 32768u;
#pragma nounroll
      for (unsigned j = 0; j < 5; ++j) dT();
    }
    if (hasU && !isY) await(0xffffffffull, true);
    else { if (wid == 4u) await(isY ? 0x7full << (g0 >> 1) : 0xffffffffull, false); BAR(); }
#pragma nounroll
    for (unsigned j = 0; j < 5; ++j) { dma64d(sb + roff, voL, sR, rbase + (g0 + j) * 16384u); nexts(); }
    auto dstep = [&](auto wait) {
      T4 p; p.set(kT * 64u * ldT + tcol, lw, ldT);
      unsigned d0 = sb + toff, d1 = sb + roff, r0 = rbase + kT * 16384u; p.pin(); PINS(d0); PINS(d1); PINS(r0);
      wait(); BAR();
      p.go(d0, voT, sT); dma64d(d1, voL, sR, r0);
      ++kT; nexts();
    };
    waitv<16>(); BAR(); STAMP(11);
    dstep([] { waitv<12>(); }); dstep([] { waitv<16>(); }); dstep([] { waitv<20>(); });
    if (!isY && hasU && tid == 256u) flag_set(fU + uft, tag);
#pragma nounroll
    for (unsigned seg = isY ? 0u : 4u; seg < 5u; ++seg) {
      const unsigned n = !isY ? 56u : seg == 0u ? 2u : seg == 2u ? 10u : seg == 4u ? 28u : 4u;
#pragma nounroll
      for (unsigned i = 0; i < n; ++i) dstep([] { waitv<24>(); });
      if (seg == 0u || seg == 2u) peek();
      else if (seg < 4u) { waitv<32>(); await(seg == 1u ? 0x3fffull << 14 : 0x3fffull, true); kT = seg == 1u ? 70u - g0 : 0u; }
    }
    waitv<24>(); BAR();
    if (isY) { waitv<0>(); BAR(); STAMP(12); ST_OUT(1); return; }
    {
      const rsrc_t sX = mkrsrc(x, SEQ * DM * 2);
#pragma unroll
      for (int q = 0; q < 8; ++q)
        dma16(L + XO + (lw * 8u + q) * 1024u, (ln >> 4) * 2048u + 16u * ((ln & 15u) ^ ((4u * q + (ln >> 4)) & 15u)), sX, (st * 128u + lw * 32u + q * 4u) * 2048u + dt * 256u);
    }
    waitv<8>(); BAR();
    STAMP(12);
    {
      flag_wait<6>(fG, tag, 1ull);
      u32x4 g[8];
#pragma unroll
      for (int q = 0; q < 8; ++q) g[q] = __builtin_amdgcn_raw_buffer_load_b128(sG, voL + (lw * 8u + q) * 1024u, 0, 16);
#pragma unroll
      for (int q = 0; q < 8; ++q) *(LAS u32x4*)(size_t)(L + GO + voL + (lw * 8u + q) * 1024u) = g[q];
    }
    waitv<0>(); WAITL0(); STAMP(13); BAR();
  } else {
    const unsigned wp = lw >> 1, wq2 = lw & 1u, fr = ln & 15u, fq = ln >> 4;
    if (hasU) {
      Opnd<T1> P; Opnd<RCHK> Q;
      P.init(L, wp * 64u, ln); Q.init(L + 16384u, wq2 * 64u, ln);
      u32x2 bv[4];
      f32x4 acc[4][4] = {};
      gemm_compute<T1, RCHK, 4, 4, 5, 32768u, 0, 0, 16>(acc, P, Q, 32, [&] {
#pragma unroll
        for (int p = 0; p < 4; ++p) bv[p] = *(const u32x2*)(b1 + uft * 128u + wp * 64u + p * 16u + fq * 4u);
      });
      STAMP(8);
      const unsigned uo = ((udt * 56u + uft * 2u + wp) * 8u + fq) * 2048u + (wq2 * 64u + fr) * 16u;
#pragma unroll
      for (int pp = 0; pp < 2; ++pp) {
        const u32x2 ba = bv[2 * pp], bb = bv[2 * pp + 1];
        const float bias[8] = {bflo(ba[0]), bfhi(ba[0]), bflo(ba[1]), bfhi(ba[1]), bflo(bb[0]), bfhi(bb[0]), bflo(bb[1]), bfhi(bb[1])};
#pragma unroll
        for (int m = 0; m < 4; ++m) {
          float v[8];
#pragma unroll
          for (int j = 0; j < 8; ++j) v[j] = gelu_t(acc[2 * pp + (j >> 2)][m][j & 3] + bias[j]);
          __builtin_amdgcn_raw_buffer_store_b128((u32x4){pk2(v[0], v[1]), pk2(v[2], v[3]), pk2(v[4], v[5]), pk2(v[6], v[7])}, sU, uo + pp * 8192u + m * 256u, 0, 16);
        }
      }
      STAMP(9);
      if (isY) { waitv<0>(); BAR(); }
    }
    if (isY || !hasU) BAR();
    f32x4 acc[4][4] = {};
    const unsigned t4 = (wid * 64u + ln) * 16u;
    if (isY) {
      float bias[4];
#pragma unroll
      for (int q = 0; q < 4; ++q) bias[q] = bflo(b2[st * 128u + wq2 * 64u + q * 16u + fr]);
      Opnd<RCHK> P; Opnd<T2> Q;
      P.init(L + 65536u, wp * 64u, ln); Q.init(L + 65536u + 16384u, wq2 * 64u, ln);
      gemm_compute<RCHK, T2, 4, 4, 5, 32768u, 3, 2, 0>(acc, P, Q, 56, [] {});
      STAMP(12);
#pragma unroll
      for (int j = 0; j < 8; ++j) {
        float g[8];
#pragma unroll
        for (int c = 0; c < 8; ++c) {
          const int q = 2 * (j & 1) + (c >> 2); const float y = acc[j >> 1][q][c & 3] + bias[q];
          g[c] = y * __builtin_amdgcn_rcpf(1.0f + __builtin_amdgcn_exp2f(y * -1.4426950408889634f));
        }
        __builtin_amdgcn_raw_buffer_store_b128((u32x4){pk2(g[0], g[1]), pk2(g[2], g[3]), pk2(g[4], g[5]), pk2(g[6], g[7])}, sG, t4 + j * 4096u, 0, 16);
      }
      waitv<0>(); BAR();
      if (tid == 0u) flag_set(fG, tag);
      STAMP(15); ST_OUT(1); return;
    }
    Opnd<T1> P; Opnd<RCHK> Q;
    P.init(L + 65536u, wp * 64u, ln); Q.init(L + 65536u + 16384u, wq2 * 64u, ln);
    gemm_compute<T1, RCHK, 4, 4, 5, 32768u, 3, 2, 2>(acc, P, Q, 64, [] {});
    STAMP(12);
    BAR();
    STAMP(13);
    u32x4 gp[8];
#pragma unroll
    for (int j = 0; j < 8; ++j) gp[j] = *(const LAS u32x4*)(size_t)(L + GO + t4 + j * 4096u);
    const unsigned xa = L + XO + (wq2 * 64u + fr) * 256u + (fq & 1u) * 8u;
    u32x2 xv[4][4];
#pragma unroll
    for (int p = 0; p < 4; ++p)
#pragma unroll
      for (int q = 0; q < 4; ++q) xv[p][q] = *(const LAS u32x2*)(size_t)(xa + 16u * ((wp * 8u + 2u * p + (fq >> 1)) ^ fr) + q * 4096u);
#pragma unroll
    for (int p = 0; p < 4; ++p)
#pragma unroll
      for (int q = 0; q < 4; ++q) {
        const f32x4 v = acc[p][q]; const unsigned g0 = gp[2 * p + (q >> 1)][2 * (q & 1)], g1 = gp[2 * p + (q >> 1)][2 * (q & 1) + 1];
        *(LAS u32x2*)(size_t)(xa + 16u * ((wp * 8u + 2u * p + (fq >> 1)) ^ fr) + q * 4096u) =
            (u32x2){pk2(fmaf(v[0], bflo(g0), bflo(xv[p][q][0])), fmaf(v[1], bfhi(g0), bfhi(xv[p][q][0]))), pk2(fmaf(v[2], bflo(g1), bflo(xv[p][q][1])), fmaf(v[3], bfhi(g1), bfhi(xv[p][q][1])))};
      }
  }
  WAITL0(); BAR();
#pragma unroll
  for (int q = 0; q < 4; ++q) {
    const unsigned row = wid * 16u + q * 4u + (ln >> 4);
    const u32x4 v = *(const LAS u32x4*)(size_t)(L + XO + row * 256u + 16u * ((ln & 15u) ^ (row & 15u)));
    *(u32x4*)(out + (size_t)(st * 128u + row) * DM + dt * 128u + (ln & 15u) * 8u) = v;
  }
  STAMP(14); waitv<0>(); STAMP(15); ST_OUT(1);
}

extern "C" __global__ void rsk_end() {}
}

extern "C" void kernel_launch(void* const* d_in, const int* in_sizes, int n_in, void* d_out, int out_size, void* d_ws, size_t ws_size, hipStream_t stream) {
  const u16* x = (const u16*)d_in[0]; const u16* kc = (const u16*)d_in[1]; const u16* vc = (const u16*)d_in[2];
  const u16* wq = (const u16*)d_in[3]; const u16* wo = (const u16*)d_in[4];
  const u16* w1 = (const u16*)d_in[6]; const u16* b1 = (const u16*)d_in[7]; const u16* w2 = (const u16*)d_in[8]; const u16* b2 = (const u16*)d_in[9];
#define WSEND 0
  char* ws = (char*)d_ws + (WSEND ? ((ws_size - WS_TOTAL) & ~(size_t)0xfffff) : 0);
  (void)hipFuncSetAttribute((const void*)rsk_main, hipFuncAttributeMaxDynamicSharedMemorySize, LDS_BYTES);
  rsk_main<<<256, 512, LDS_BYTES, stream>>>(kc, vc, wq, w1, b1, wo, w2, b2, x, ws, (u16*)d_out);
}
```

```cpp
#include <hip/hip_runtime.h>
#include <hip/hip_bf16.h>

namespace {
typedef short bf16x8 __attribute__((ext_vector_type(8)));
typedef short s16x4 __attribute__((ext_vector_type(4)));
typedef float f32x2 __attribute__((ext_vector_type(2)));
typedef float f32x4 __attribute__((ext_vector_type(4)));
typedef float f32x16 __attribute__((ext_vector_type(16)));
typedef unsigned u32x4 __attribute__((ext_vector_type(4)));
typedef unsigned u32x2 __attribute__((ext_vector_type(2)));
typedef __bf16 bf2_t __attribute__((ext_vector_type(2)));
typedef unsigned short u16;
typedef __amdgpu_buffer_rsrc_t rsrc_t;
#define LAS __attribute__((address_space(3)))
#define DI __device__ __forceinline__
#define BAR() asm volatile("s_barrier" ::: "memory")
#define WAITL0() do { asm volatile("" ::: "memory"); __builtin_amdgcn_s_waitcnt(0xC07F); asm volatile("" ::: "memory"); } while (0)
#define SCHED() __builtin_amdgcn_sched_barrier(0)
#define SGB(mask, n) __builtin_amdgcn_sched_group_barrier(mask, n, 0)

constexpr int SEQ = 2048, DM = 1024, NH = 32, DH = 128, SK = 2048, WIN = 512, FF = 3584;
constexpr unsigned WS_XN = 0u, WS_H = 4u << 20, WS_O = 8u << 20, WS_U = 24u << 20, WS_G = 32u << 20, WS_FLAG = 36u << 20, WS_TOTAL = 37u << 20;
constexpr unsigned LDS_BYTES = 163840;
constexpr unsigned long long SPIN_LIMIT = 2000000ull;
#define LNPF 1
#define LNAUX 16
#define PFDIV 2
#define PFGAP 0
#define XP 0
#define XSLEEP 2
#define PFWAIT 4
#define WQPF 1
#define OUTAUX 0
#define PFSLEEP 40

#define ST_DECL
#define STAMP(i) do {} while (0)
#define ST_OUT(k)
template <int N> DI void waitv() { asm volatile("s_waitcnt vmcnt(%0)" :: "i"(N) : "memory"); }
DI unsigned rfl(unsigned x) { return __builtin_amdgcn_readfirstlane(x); }
DI rsrc_t mkrsrc(const void* p, unsigned bytes) { return __builtin_amdgcn_make_buffer_rsrc((void*)p, (short)0, (int)bytes, 0x00020000); }
DI void dma16(unsigned m0v, unsigned voff, rsrc_t r, unsigned soff) {
  asm volatile("s_mov_b32 m0, %0\n\ts_nop 0\n\tbuffer_load_dwordx4 %1, %2, %3 offen lds" :: "s"(m0v), "v"(voff), "s"(r), "s"(soff) : "memory");
}
DI rsrc_t code_rsrc() {
  unsigned long long b, e;
  asm volatile("s_getpc_b64 s[40:41]\n\ts_add_u32 s40, s40, rsk_main@rel32@lo+4\n\ts_addc_u32 s41, s41, rsk_main@rel32@hi+12\n\ts_mov_b64 %0, s[40:41]\n\t"
               "s_getpc_b64 s[40:41]\n\ts_add_u32 s40, s40, rsk_end@rel32@lo+4\n\ts_addc_u32 s41, s41, rsk_end@rel32@hi+12\n\ts_mov_b64 %1, s[40:41]"
               : "=&s"(b), "=&s"(e) :: "s40", "s41", "scc");
  return mkrsrc((const void*)b, (e > b && e - b < 0x40000ull) ? (unsigned)(e - b) : 0u);
}
template <bool DEV>
DI void dma64(unsigned m0v, unsigned va, unsigned vb, rsrc_t r, unsigned s0, unsigned s1, unsigned s2, unsigned s3) {
  if constexpr (DEV)
    asm volatile("s_mov_b32 m0, %0\n\ts_nop 0\n\tbuffer_load_dwordx4 %1, %3, %4 offen sc1 lds\n\tbuffer_load_dwordx4 %2, %3, %5 offen offset:1024 sc1 lds\n\t"
                 "buffer_load_dwordx4 %1, %3, %6 offen offset:2048 sc1 lds\n\tbuffer_load_dwordx4 %2, %3, %7 offen offset:3072 sc1 lds"
                 :: "s"(m0v), "v"(va), "v"(vb), "s"(r), "s"(s0), "s"(s1), "s"(s2), "s"(s3) : "memory");
  else
    asm volatile("s_mov_b32 m0, %0\n\ts_nop 0\n\tbuffer_load_dwordx4 %1, %3, %4 offen lds\n\tbuffer_load_dwordx4 %2, %3, %5 offen offset:1024 lds\n\t"
                 "buffer_load_dwordx4 %1, %3, %6 offen offset:2048 lds\n\tbuffer_load_dwordx4 %2, %3, %7 offen offset:3072 lds"
                 :: "s"(m0v), "v"(va), "v"(vb), "s"(r), "s"(s0), "s"(s1), "s"(s2), "s"(s3) : "memory");
}
DI void dma64d(unsigned m0v, unsigned va, rsrc_t r, unsigned s0) {
  asm volatile("s_mov_b32 m0, %0\n\ts_nop 0\n\tbuffer_load_dwordx4 %1, %2, %3 offen sc1 lds\n\tbuffer_load_dwordx4 %1, %2, %3 offen offset:1024 sc1 lds\n\t"
               "buffer_load_dwordx4 %1, %2, %3 offen offset:2048 sc1 lds\n\tbuffer_load_dwordx4 %1, %2, %3 offen offset:3072 sc1 lds"
               :: "s"(m0v), "v"(va), "s"(r), "s"(s0) : "memory");
}
DI rsrc_t mkrsrcB(const void* p, unsigned bytes) { return mkrsrc((const char*)p - 4096, bytes + 4096u); }
#define PINS(x) asm volatile("" : "+s"(x))
DI bf16x8 ldsR(unsigned a) { return *(const LAS bf16x8*)(size_t)a; }
DI s16x4 ldsT(unsigned a) { return __builtin_amdgcn_ds_read_tr16_b64_v4i16((LAS s16x4*)(size_t)a); }
DI bf16x8 cat(s16x4 a, s16x4 b) { return __builtin_shufflevector(a, b, 0, 1, 2, 3, 4, 5, 6, 7); }
DI unsigned pk2(float lo, float hi) { f32x2 v = {lo, hi}; return __builtin_bit_cast(unsigned, __builtin_convertvector(v, bf2_t)); }
DI float bflo(unsigned w) { return __uint_as_float(w << 16); }
DI float bfhi(unsigned w) { return __uint_as_float(w & 0xffff0000u); }
DI float rbf(float x) { return bflo(pk2(x, x) & 0xffffu); }
template <int CTRL, int RM> DI float dpp_add(float v) {
  return v + __builtin_bit_cast(float, __builtin_amdgcn_update_dpp(0, __builtin_bit_cast(int, v), CTRL, RM, 0xf, false));
}
DI void wsum2(float& u, float& v) {
#define WS2(C, M) u = dpp_add<C, M>(u); v = dpp_add<C, M>(v);
  WS2(0x111, 0xf) WS2(0x112, 0xf) WS2(0x114, 0xf) WS2(0x118, 0xf) WS2(0x142, 0xa) WS2(0x143, 0xc)
#undef WS2
  u = __builtin_bit_cast(float, __builtin_amdgcn_readlane(__builtin_bit_cast(int, u), 63));
  v = __builtin_bit_cast(float, __builtin_amdgcn_readlane(__builtin_bit_cast(int, v), 63));
}

DI void ln16(float (&v)[16]) {
  const float x0 = __builtin_bit_cast(float, __builtin_amdgcn_readfirstlane(__builtin_bit_cast(int, v[0])));
  float s = 0.f, q = 0.f;
#pragma unroll
  for (int i = 0; i < 16; ++i) { const float d = v[i] - x0; s += d; q = fmaf(d, d, q); }
  wsum2(s, q);
  const float md = s * (1.0f / DM), var = rbf(fmaxf(fmaf(-md, md, q * (1.0f / DM)), 0.f));
  const float mb = rbf(x0 + md), inv = __builtin_amdgcn_rcpf(rbf(__builtin_amdgcn_sqrtf(rbf(var + 1e-5f))));
#pragma unroll
  for (int i = 0; i < 16; i += 2) {
    const unsigned d = pk2(v[i] - mb, v[i + 1] - mb), r = pk2(bflo(d) * inv, bfhi(d) * inv);
    v[i] = bflo(r); v[i + 1] = bfhi(r);
  }
}
DI unsigned pkx(float lo, float hi) { return (__float_as_uint(lo) >> 16) | (__float_as_uint(hi) & 0xffff0000u); }
enum { RROW = 0, RCHK = 1, T1 = 2, T2 = 3 };
template <int KIND> struct Opnd {
  static constexpr int NA = KIND == RROW ? 2 : KIND == RCHK ? 1 : 4;
  static constexpr int RPB = KIND >= T1 ? 2 : 1;
  unsigned a[NA];
  DI void init(unsigned base, unsigned off, unsigned lane) {
    const unsigned fr = lane & 15u, g = lane >> 4, q = fr >> 2, p = lane & 3u;
    if constexpr (KIND == RROW) {
#pragma unroll
      for (int kk = 0; kk < 2; ++kk) a[kk] = base + (off + fr) * 128u + 16u * ((4u * kk + g) ^ (fr >> 1));
    } else if constexpr (KIND == RCHK) {
      a[0] = base + g * 2048u + (off + fr) * 16u;
    } else {
      const unsigned x = ((q >> 1) << 1) | (g & 1u);
#pragma unroll
      for (int n = 0; n < 4; ++n)
        a[n] = base + (KIND == T1 ? 4096u : 2048u) * (g >> 1) + 1024u * (off >> 6) + 128u * (4u * (g & 1u) + q) + 32u * ((unsigned)n ^ x) + 8u * p;
    }
  }
  DI bf16x8 ld(int b, int kk) const {
    if constexpr (KIND == RROW) return ldsR(a[kk] + 2048u * b);
    else if constexpr (KIND == RCHK) return ldsR(a[0] + 8192u * kk + 256u * b);
    else { const unsigned ad = a[b & 3] + 1024u * (b >> 2) + 8192u * kk; return cat(ldsT(ad), ldsT(ad + (KIND == T1 ? 2048u : 4096u))); }
  }
  DI void adv(unsigned delta) {
#pragma unroll
    for (int i = 0; i < NA; ++i) { a[i] += delta; asm volatile("" : "+v"(a[i])); }
  }
};

template <int PK, int QK, int PM, int QM, int NS, unsigned STG, int TAIL, int S0, int DRAIN, class F>
DI void gemm_compute(f32x4 (&acc)[PM][QM], Opnd<PK>& P, Opnd<QK>& Q, const int N, F first) {
  bf16x8 pf[2][PM], qf[2][QM];
  constexpr int NR = PM * Opnd<PK>::RPB + QM * Opnd<QK>::RPB, NM = PM * QM, NA = Opnd<PK>::NA + Opnd<QK>::NA;
  auto load = [&](int kk) {
    pf[kk][0] = P.ld(0, kk);
#pragma unroll
    for (int q = 0; q < QM; ++q) qf[kk][q] = Q.ld(q, kk);
#pragma unroll
    for (int p = 1; p < PM; ++p) pf[kk][p] = P.ld(p, kk);
  };
  auto mma = [&](int kk) {
#pragma unroll
    for (int p = 0; p < PM; ++p)
#pragma unroll
      for (int q0 = 0; q0 < QM; ++q0) { const int q = (p & 1) ? QM - 1 - q0 : q0; acc[p][q] = __builtin_amdgcn_mfma_f32_16x16x32_bf16(pf[kk][p], qf[kk][q], acc[p][q], 0, 0, 0); }
  };
  constexpr int ND = (NR + NA + 2 > NM) ? (NR + NA + 2 - NM) : 0;
  BAR();
  load(0);
  int stg = S0;
  auto step = [&](bool sync) {
    SCHED();
    load(1); mma(0);
    stg = (stg == NS - 1) ? 0 : stg + 1;
    const unsigned delta = stg == 0 ? (unsigned)(-(int)((NS - 1) * STG)) : STG;
    P.adv(delta); Q.adv(delta);
#pragma unroll
    for (int i = 0; i < ND; ++i) { SGB(0x8, 1); SGB(0x100, 2); }
#pragma unroll
    for (int i = 0; i < NR - 2 * ND; ++i) { SGB(0x8, 1); SGB(0x100, 1); }
#pragma unroll
    for (int i = 0; i < NA; ++i) { SGB(0x8, 1); SGB(0x2, 1); }
    SGB(0x8, NM);
    SCHED();
    if (sync) { WAITL0(); BAR(); }
    SCHED();
    load(0); mma(1);
#pragma unroll
    for (int i = 0; i < ND; ++i) { SGB(0x8, 1); SGB(0x100, 2); }
#pragma unroll
    for (int i = 0; i < NR - 2 * ND; ++i) { SGB(0x8, 1); SGB(0x100, 1); }
    SGB(0x8, NM);
  };
  if (DRAIN > 0) {
#pragma nounroll
    for (int t = 0; t < DRAIN; ++t) step(true);
    waitv<0>();
    first();
  }
#pragma nounroll
  for (int t = DRAIN; t < N - TAIL; ++t) step(true);
#pragma nounroll
  for (int t = 0; t < TAIL; ++t) step(false);
  SCHED();
}

DI unsigned voffRow(unsigned lane, unsigned ldb, unsigned par) { return (lane >> 3) * ldb + 16u * ((lane & 7u) ^ (4u * par + (lane >> 4))); }
DI unsigned voffT(unsigned lane, unsigned ldb) { return (lane >> 3) * ldb + 16u * ((lane & 7u) ^ (2u * ((((lane >> 4) & 1u) << 1) | (lane >> 5)))); }
struct T4 { unsigned s0, s1, s2, s3;
  DI void set(unsigned so, unsigned lw, unsigned ldb) { s0 = so + 4096u + lw * 16u * ldb; s1 = s0 + 128u - 1024u; s2 = s0 + 8u * ldb - 2048u; s3 = s2 + 128u - 1024u; }
  DI void pin() { PINS(s0); PINS(s1); PINS(s2); PINS(s3); }
  template <bool DEV = false> DI void go(unsigned dst, unsigned vo, rsrc_t r) const { dma64<DEV>(dst, vo, vo, r, s0, s1, s2, s3); } };
template <bool DEV> DI void dmaT4(unsigned dst, unsigned lw, unsigned vo, rsrc_t r, unsigned so, unsigned ldb) { T4 t; t.set(so, lw, ldb); t.go<DEV>(dst + lw * 4096u, vo, r); }

typedef unsigned long long u64;
constexpr unsigned FL_H = 64, FL_O = 320, FL_U = 576, FL_G = 832, FL_X = 3072;
DI void flag_set(u64* p, u64 tag) { __hip_atomic_store(p, tag, __ATOMIC_RELAXED, __HIP_MEMORY_SCOPE_AGENT); }
DI u64 flag_get(const u64* p) { return __hip_atomic_load(p, __ATOMIC_RELAXED, __HIP_MEMORY_SCOPE_AGENT); }
template <int SLOW = 16> DI void flag_wait(const u64* p, u64 tag, u64 mask) {
  const u64 t0 = __builtin_amdgcn_s_memrealtime();
  while ((__ballot(flag_get(p) != tag) & mask) != 0ull) {
    const u64 dt = __builtin_amdgcn_s_memrealtime() - t0;
    if (dt > SPIN_LIMIT) break;
    if (dt > 400ull) __builtin_amdgcn_s_sleep(SLOW); else __builtin_amdgcn_s_sleep(3);
  }
  asm volatile("" ::: "memory");
}

DI f32x2 sigm2(f32x2 v, f32x2 z) {
  f32x2 e = {__builtin_amdgcn_exp2f(z[0]), __builtin_amdgcn_exp2f(z[1])};
  e += 1.0f;
  return v * (f32x2){__builtin_amdgcn_rcpf(e[0]), __builtin_amdgcn_rcpf(e[1])};
}
DI unsigned gelu2(f32x2 v) {
  const f32x2 r = sigm2(v, v * __builtin_elementwise_fma(v * v, (f32x2)(-0.1029432397f), (f32x2)(-2.3022081986f)));
  return pk2(r[0], r[1]);
}
DI unsigned silu2(f32x2 v) { const f32x2 r = sigm2(v, v * -1.4426950408889634f); return pk2(r[0], r[1]); }
DI f32x2 lo2(f32x4 v) { return __builtin_shufflevector(v, v, 0, 1); }
DI f32x2 hi2(f32x4 v) { return __builtin_shufflevector(v, v, 2, 3); }
DI f32x2 bf2(unsigned w) { return (f32x2){bflo(w), bfhi(w)}; }

constexpr unsigned QSTG = 49152u;
constexpr unsigned KBASE = 65536u, VBASE = 114688u;
constexpr unsigned KSLOT[3] = {0u, 32768u, 16384u}, VSLOT[3] = {32768u, 16384u, 0u};
DI void touch8k(rsrc_t r, unsigned vo128, unsigned soff) {
  asm volatile("buffer_load_dword v255, %0, %1, %2 offen" :: "v"(vo128), "s"(r), "s"(soff) : "memory", "v255");
}
constexpr float QSCALE = 0.12751743082f;
#define BIGSUM 4096.0f

#define ATTPRIO 0
#define PRIO(x) do { if (ATTPRIO == 1) __builtin_amdgcn_s_setprio(x); else if (ATTPRIO == 2) __builtin_amdgcn_s_setprio(1 - (x)); } while (0)
struct Att {
  bf16x8 qf[8], kv[16], pf[4], pn[2];
  f32x16 o[4], s[2], negm;
  float l, ts;
  unsigned aK[4], aV[2];
};
template <unsigned SLOT, int J> DI void att_k(Att& a) {
  asm volatile("ds_read_b128 %0, %1 offset:%2" : "+v"(a.kv[J]) : "v"(a.aK[(J >> 1) & 3]), "i"(SLOT + 8192u * (J >> 3) + 4096u * (J & 1)) : "memory");
}
template <unsigned SLOT, int J> DI void att_v(Att& a) {
  s16x4 lo = __builtin_shufflevector(a.kv[J], a.kv[J], 0, 1, 2, 3), hi = __builtin_shufflevector(a.kv[J], a.kv[J], 4, 5, 6, 7);
  constexpr unsigned off = SLOT + 4096u * (J >> 2) + 1024u * ((J >> 1) & 1);
  asm volatile("ds_read_b64_tr_b16 %0, %2 offset:%3\n\tds_read_b64_tr_b16 %1, %2 offset:%4" : "+v"(lo), "+v"(hi) : "v"(a.aV[J & 1]), "i"(off), "i"(off + 2048u) : "memory");
  a.kv[J] = cat(lo, hi);
}
DI void att_wait(Att& a) {
  asm volatile("s_waitcnt lgkmcnt(0)" : "+v"(a.kv[0]), "+v"(a.kv[1]), "+v"(a.kv[2]), "+v"(a.kv[3]), "+v"(a.kv[4]), "+v"(a.kv[5]), "+v"(a.kv[6]), "+v"(a.kv[7]),
               "+v"(a.kv[8]), "+v"(a.kv[9]), "+v"(a.kv[10]), "+v"(a.kv[11]), "+v"(a.kv[12]), "+v"(a.kv[13]), "+v"(a.kv[14]), "+v"(a.kv[15]) :: "memory");
}
DI void att_loadK(Att& a, unsigned slot) {
#pragma unroll
  for (int j = 0; j < 16; ++j) a.kv[j] = ldsR(a.aK[(j >> 1) & 3] + slot + 8192u * (j >> 3) + 4096u * (j & 1));
}
DI unsigned lane_id() { unsigned l; asm volatile("v_mbcnt_lo_u32_b32 %0, -1, 0\n\tv_mbcnt_hi_u32_b32 %0, -1, %0" : "=v"(l)); return l; }
DI void add_bcast3(f32x16& t0, f32x16& t1, f32x16& t2, float v) {
  const float one = lane_id() < 32u ? 1.0f : 0.0f;
  asm volatile("s_nop 7\n\ts_nop 7\n\tv_mfma_f32_32x32x2_f32 %0, %3, %4, %0\n\tv_mfma_f32_32x32x2_f32 %1, %3, %4, %1\n\tv_mfma_f32_32x32x2_f32 %2, %3, %4, %2\n\t"
               "s_nop 7\n\ts_nop 7\n\ts_nop 7" : "+v"(t0), "+v"(t1), "+v"(t2) : "v"(one), "v"(v));
}
template <bool FIRST, unsigned SLOT, int J> DI void att_qk1(Att& a) {
  a.s[J & 1] = __builtin_amdgcn_mfma_f32_32x32x16_bf16(a.kv[J], a.qf[J >> 1], J < 2 ? (FIRST ? (f32x16){} : a.negm) : a.s[J & 1], 0, 0, 0);
  SCHED();
  if constexpr (!FIRST) att_v<SLOT, J>(a);
  SCHED();
  if constexpr (J < 15) att_qk1<FIRST, SLOT, J + 1>(a);
}
template <int NEXT, unsigned SLOT, int J> DI void att_pv1(Att& a) {
  a.o[J & 3] = __builtin_amdgcn_mfma_f32_32x32x16_bf16(a.kv[J], a.pf[J >> 2], a.o[J & 3], 0, 0, 0);
  SCHED();
  if constexpr (NEXT == 1) att_k<SLOT, J>(a); else if constexpr (NEXT == 2) att_v<SLOT, J>(a);
  SCHED();
  if constexpr (J < 15) att_pv1<NEXT, SLOT, J + 1>(a);
}
DI float max3(float x, float y, float z) { return __builtin_fmaxf(__builtin_fmaxf(x, y), z); }
DI float att_rowmax(const Att& a) {
  float m0 = max3(a.s[0][0], a.s[0][1], a.s[0][2]), m1 = max3(a.s[1][0], a.s[1][1], a.s[1][2]);
#pragma unroll
  for (int r = 3; r < 15; r += 2) { m0 = max3(m0, a.s[0][r], a.s[0][r + 1]); m1 = max3(m1, a.s[1][r], a.s[1][r + 1]); }
  const float mx = __builtin_fmaxf(max3(m0, m1, a.s[0][15]), a.s[1][15]);
  auto rr = __builtin_amdgcn_permlane32_swap(__float_as_uint(mx), __float_as_uint(mx), false, false);
  return __builtin_fmaxf(__uint_as_float(rr[0]), __uint_as_float(rr[1]));
}
DI float att_exp16(const f32x16& v, bf16x8& p0, bf16x8& p1) {
  float s0 = 0.f, s1 = 0.f;
  u32x4 w[2];
#pragma unroll
  for (int h = 0; h < 2; ++h) {
    float e[8];
#pragma unroll
    for (int r = 0; r < 8; ++r) e[r] = __builtin_amdgcn_exp2f(v[8 * h + r]);
#pragma unroll
    for (int r = 0; r < 8; r += 2) {
      if (h == 0 && r == 0) { s0 = e[0]; s1 = e[1]; } else { s0 += e[r]; asm("" : "+v"(s0)); s1 += e[r + 1]; asm("" : "+v"(s1)); }
      w[h][r >> 1] = pk2(e[r], e[r + 1]);
    }
    asm volatile("" : "+v"(w[h]));
    SCHED();
  }
  p0 = __builtin_bit_cast(bf16x8, w[0]); p1 = __builtin_bit_cast(bf16x8, w[1]);
  return s0 + s1;
}
template <bool FIRST> DI void att_sm1(Att& a) {
  if (FIRST) {
    const float mx = att_rowmax(a);
#pragma unroll
    for (int r = 0; r < 16; ++r) { a.s[0][r] -= mx; a.s[1][r] -= mx; a.negm[r] = -mx; }
  }
  a.ts = att_exp16(a.s[0], a.pn[0], a.pn[1]);
  asm volatile("" : "+v"(a.pn[0]), "+v"(a.pn[1]), "+v"(a.ts), "+v"(a.s[1]), "+v"(a.negm));
}
template <bool FIRST> DI void att_sm2(Att& a) {
  a.ts += att_exp16(a.s[1], a.pf[2], a.pf[3]);
  if (!FIRST) {
    if (__builtin_expect(!__all(a.ts <= BIGSUM), 0)) {
      const float dl = fmaxf(att_rowmax(a), 0.f), fct = __builtin_amdgcn_exp2f(-dl);
#pragma unroll
      for (int db = 0; db < 4; ++db) a.o[db] *= fct;
      a.l *= fct;
      add_bcast3(a.s[0], a.s[1], a.negm, -dl);
      a.ts = att_exp16(a.s[0], a.pn[0], a.pn[1]) + att_exp16(a.s[1], a.pf[2], a.pf[3]);
    }
  }
  a.l += a.ts;
  a.pf[0] = a.pn[0]; a.pf[1] = a.pn[1];
  asm volatile("" : "+v"(a.pf[0]), "+v"(a.pf[1]), "+v"(a.pf[2]), "+v"(a.pf[3]), "+v"(a.l));
}

extern "C" __global__ __launch_bounds__(512, 2) __attribute__((amdgpu_num_vgpr(127)))
void rsk_main(const u16* __restrict__ kc, const u16* __restrict__ vc,
           const u16* __restrict__ wq, const u16* __restrict__ w1, const u16* __restrict__ b1, const u16* __restrict__ wo, const u16* __restrict__ w2,
           const u16* __restrict__ b2, const u16* __restrict__ x, char* __restrict__ ws, u16* __restrict__ out) {
  extern __shared__ __attribute__((aligned(16))) char lds[];
  const unsigned L = (unsigned)(size_t)lds;
  const char* xn = ws + WS_XN; const char* hh = ws + WS_H;
  const unsigned tid = threadIdx.x, lane = tid & 63u, wid = rfl(tid >> 6), grp = wid >> 2, lw = wid & 3u;
  const unsigned bid = blockIdx.x, xcd = bid & 7u, idx = bid >> 3;
  const unsigned qt = (xcd & 1u) * 4u + (idx & 3u), kvh = xcd >> 1, head = kvh * 8u + (idx >> 2);
  const bool hasU = idx < 28u;
  const unsigned udt = (xcd & 1u) * 4u + (idx & 3u), uft = (xcd >> 1) * 7u + (idx >> 2);
  const rsrc_t sK = mkrsrc(kc + ((size_t)kvh * SK + (SK - WIN)) * DH, WIN * DH * 2), sV = mkrsrc(vc + ((size_t)kvh * SK + (SK - WIN)) * DH, WIN * DH * 2);
  const rsrc_t sH = mkrsrc(hh, SEQ * DM * 2), sW1 = mkrsrcB(w1, SEQ * FF * 2);
  const unsigned voK0 = voffRow(lane, 256u, 0), voV = voffT(lane, 256u);
  auto dmaK2 = [&](unsigned slot, unsigned t, unsigned p0) {
    unsigned voK1; asm volatile("v_xor_b32 %0, 64, %1" : "=v"(voK1) : "v"(voK0));
    dma16(L + KBASE + slot + 1024u * p0, voK0, sK, t * 16384u + (p0 & 7u) * 2048u + (p0 >> 3) * 128u);
    dma16(L + KBASE + slot + 1024u * p0 + 1024u, voK1, sK, t * 16384u + (p0 & 7u) * 2048u + 2048u + (p0 >> 3) * 128u); };
  auto dmaV2 = [&](unsigned slot, unsigned t, unsigned p0) {
    dma16(L + VBASE + slot + 1024u * p0, voV, sV, t * 16384u + (p0 >> 1) * 2048u);
    dma16(L + VBASE + slot + 1024u * p0 + 1024u, voV, sV, t * 16384u + (p0 >> 1) * 2048u + 128u); };
  auto tileK = [&](unsigned t) {
#pragma unroll
    for (int p = 0; p < 16; p += 2) dmaK2(KSLOT[t % 3], t, p); };
  auto tileV = [&](unsigned t) {
#pragma unroll
    for (int p = 0; p < 16; p += 2) dmaV2(VSLOT[t % 3], t, p); };
  Att a;
  { const unsigned r32 = lane & 31u, hi = lane >> 5;
#pragma unroll
    for (int j = 0; j < 4; ++j) a.aK[j] = L + KBASE + r32 * 128u + 16u * ((2u * j + hi) ^ ((r32 >> 1) & 7u)); }

  ST_DECL; STAMP(0);
  u64* flags = (u64*)(ws + WS_FLAG);
  const rsrc_t sS = lw == 0 ? mkrsrcB(wq, DM * NH * DH * 2) : mkrsrcB(xn, SEQ * DM * 2);
  unsigned asrc = 4096u + (lw == 0 ? head * 256u : (qt * 256u + (lw - 1u) * 128u) * 2048u), adst = L + lw * 16384u;
  const unsigned avo0 = lw == 0 ? voffT(lane, 8192u) : voffRow(lane, 2048u, 0), avo1 = lw == 0 ? avo0 : avo0 ^ 64u;
  constexpr unsigned HT = 147456u;
  auto astep = [&](unsigned vo0, unsigned vo1) {
    const unsigned sp = lw == 0 ? 8u * 8192u : 16u * 2048u, so = (lw == 0 ? 128u : 8u * 2048u) - 1024u;
#pragma unroll
    for (int q = 0; q < 4; ++q) { const unsigned e = asrc + 2u * q * sp; dma64<true>(adst + 4096u * q, vo0, vo1, sS, e, e + so, e + sp - 2048u, e + sp - 2048u + so); }
    asrc += lw == 0 ? 64u * 8192u : 128u; adst = adst >= L + 2u * QSTG ? adst - 2u * QSTG : adst + QSTG;
  };
  u64 tag;
  {
    const unsigned row = (bid >> 2) * 32u + ((bid >> 1) & 1u) * 16u + (bid & 1u) * 4u + (wid >> 2) * 8u + (wid & 3u);
    u32x4 xa, xb; u32x2 ep;
    asm volatile("buffer_load_dwordx4 %0, %3, %4, %5 offen\n\tbuffer_load_dwordx4 %1, %3, %4, %5 offen offset:1024\n\tglobal_load_dwordx2 %2, %6, %7 sc1"
                 : "=&v"(xa), "=&v"(xb), "=&v"(ep) : "v"(lane * 16u), "s"(mkrsrc(x, SEQ * DM * 2)), "s"(row * 2048u), "v"(0u), "s"(flags) : "memory");
    __builtin_amdgcn_s_setprio(1);
    if (wid == 7) {
      touch8k(code_rsrc(), lane < 16u ? lane * 128u : 0x80000000u, idx * 2048u);
      touch8k(mkrsrc(wq, DM * NH * DH * 2), lane * 128u, bid * 8192u);
      waitv<2>();
    } else waitv<0>();
    asm volatile("" : "+v"(xa), "+v"(xb), "+v"(ep));
    STAMP(16);
    tag = (((u64)rfl(ep[1]) << 32) | rfl(ep[0])) * 0x9E3779B97F4A7C15ull + 0x632BE59BD9B4E019ull;
    float v[16];
#pragma unroll
    for (int i = 0; i < 4; ++i) { v[2 * i] = bflo(xa[i]); v[2 * i + 1] = bfhi(xa[i]); v[8 + 2 * i] = bflo(xb[i]); v[9 + 2 * i] = bfhi(xb[i]); }
    ln16(v);
    const rsrc_t sW = mkrsrc(ws, WS_O);
#pragma unroll
    for (int p = 0; p < 2; ++p) {
      const unsigned lo0 = pkx(v[8 * p], v[8 * p + 1]), lo1 = pkx(v[8 * p + 2], v[8 * p + 3]), hi0 = pkx(v[8 * p + 4], v[8 * p + 5]), hi1 = pkx(v[8 * p + 6], v[8 * p + 7]);
      const bool odd = lane & 1u;
      const unsigned s0 = __builtin_amdgcn_update_dpp(0, (int)(odd ? lo0 : hi0), 0xB1, 0xf, 0xf, false), s1 = __builtin_amdgcn_update_dpp(0, (int)(odd ? lo1 : hi1), 0xB1, 0xf, 0xf, false);
      __builtin_amdgcn_raw_buffer_store_b128(odd ? (u32x4){s0, s1, hi0, hi1} : (u32x4){lo0, lo1, s0, s1}, sW, WS_XN + row * 2048u + lane * 16u + 1024u * p, 0, 16);
    }
    STAMP(17);
    waitv<0>();
    if (lane == 0u) flag_set(flags + FL_X + bid * 8u + wid, tag);
    STAMP(18);
    if (wid == 4u) { astep(avo0, avo1); astep(avo0, avo1); }
    if (grp == 0) __builtin_amdgcn_s_setprio(0);
    ln16(v);
    *(LAS u32x4*)(size_t)(L + HT + wid * 2048u + lane * 16u) = (u32x4){pkx(v[0], v[1]), pkx(v[2], v[3]), pkx(v[4], v[5]), pkx(v[6], v[7])};
    *(LAS u32x4*)(size_t)(L + HT + wid * 2048u + 1024u + lane * 16u) = (u32x4){pkx(v[8], v[9]), pkx(v[10], v[11]), pkx(v[12], v[13]), pkx(v[14], v[15])};
    WAITL0();
    __builtin_amdgcn_s_setprio(0);
    STAMP(19);
  }
  if (wid == 7) {
    const unsigned vo = lane * 128u;
    const rsrc_t sWq = mkrsrc(wq, DM * NH * DH * 2);
    BAR(); BAR();
    touch8k(sWq, vo, (256u + bid) * 8192u);
    {
      const rsrc_t sW = mkrsrc(ws, WS_O);
#pragma nounroll
      for (unsigned i = 0; i < 4; ++i) {
        BAR();
#pragma unroll
        for (int c = 0; c < 4; ++c) {
          const unsigned d0 = (i * 4u + c) * 64u, ad = L + HT + ((lane & 15u) >> 2) * 2048u + (d0 + (lane >> 4) * 16u + (lane & 3u) * 4u) * 2u;
          __builtin_amdgcn_raw_buffer_store_b128(__builtin_bit_cast(u32x4, cat(ldsT(ad), ldsT(ad + 8192u))), sW, WS_H + ((d0 >> 7) * 256u + bid) * 2048u + ((d0 & 64u) + lane) * 16u, 0, 16);
        }
      }
    }
    touch8k(sWq, vo, (512u + bid) * 8192u);
    BAR(); BAR();
    waitv<1>();
    if (lane == 0u) flag_set(flags + FL_H + bid, tag);
    BAR();
    touch8k(sWq, vo, (768u + bid) * 8192u);
    BAR(); BAR();
    if (idx < 16u) touch8k(sK, vo, idx * 8192u); else touch8k(sV, vo, (idx - 16u) * 8192u);
    BAR();
    const u64 f0 = flag_get(flags + FL_H + lane), f1 = flag_get(flags + FL_H + 64u + lane), f2 = flag_get(flags + FL_H + 128u + lane), f3 = flag_get(flags + FL_H + 192u + lane);
    touch8k(code_rsrc(), lane < 16u ? vo : 0x80000000u, idx * 2048u);
    BAR(); BAR();
    if (__any(f0 != tag || f1 != tag || f2 != tag || f3 != tag)) {
#pragma nounroll
      for (unsigned j = 0; j < 256u; j += 64u) flag_wait(flags + FL_H + j + lane, tag, ~0ull);
    }
    if (bid == 0u && lane == 0u) flag_set(flags, tag);
    BAR(); BAR(); BAR();
#pragma unroll
    for (int j = 0; j < 3; ++j) touch8k(sW1, vo, (j * 256u + bid) * 8192u + 4096u);
    att_loadK(a, KSLOT[0]);
  } else if (grp == 1) {
    const unsigned vo0 = avo0, vo1 = avo1;
    if (lw != 0) {
      const u64* fx = flags + FL_X + qt * 256u + (lw - 1u) * 128u + lane;
      const u64 t0 = __builtin_amdgcn_s_memrealtime();
      while (__any(((flag_get(fx) ^ tag) | (flag_get(fx + 64) ^ tag)) != 0ull)) {
        if (__builtin_amdgcn_s_memrealtime() - t0 > SPIN_LIMIT) break;
        __builtin_amdgcn_s_sleep(XSLEEP);
      }
      asm volatile("" ::: "memory");
      STAMP(21);
      astep(vo0, vo1); astep(vo0, vo1);
      STAMP(22);
    }
    waitv<16>(); STAMP(23);
    BAR(); STAMP(1);
    astep(vo0, vo1);
#pragma nounroll
    for (unsigned t = 0; t < 13; ++t) { waitv<16>(); BAR(); astep(vo0, vo1); }
    waitv<16>(); BAR();
    if (lw == 0) tileK(0); else if (lw == 1) tileK(2); else tileV(0);
    waitv<16>(); BAR();
    if (lw == 1) tileK(1); else if (lw == 2) tileV(1);
    if (lw == 0) waitv<0>();
    BAR();
    att_loadK(a, KSLOT[0]);
  } else {
    Opnd<T1> P; Opnd<RROW> Q;
    P.init(L, 0u, lane); Q.init(L + 16384u, lw * 64u, lane);
    f32x4 acc[8][4] = {};
    gemm_compute<T1, RROW, 8, 4, 3, QSTG, 0, 0, 0>(acc, P, Q, 16, [] {});
    STAMP(2);
    att_loadK(a, KSLOT[0]);
    const unsigned wa = L + (lane >> 5) * 4096u + (lw * 64u + (lane & 15u)) * 16u + ((lane >> 4) & 1u) * 8u;
#pragma unroll
    for (int n = 0; n < 8; ++n)
#pragma unroll
      for (int m = 0; m < 4; ++m) {
        const f32x4 v = acc[n][m] * QSCALE;
        *(LAS u32x2*)(size_t)(wa + 8192u * n + 256u * m) = (u32x2){pk2(v[0], v[1]), pk2(v[2], v[3])};
      }
  }
  WAITL0(); BAR(); STAMP(3);

  const rsrc_t sO = mkrsrc(ws + WS_O, SEQ * NH * DH * 2);
  {
    const unsigned l2 = lane_id(), hi = l2 >> 5, blk = (l2 >> 4) & 1u, q4 = (l2 & 15u) >> 2, p4 = l2 & 3u;
#pragma unroll
    for (int ks = 0; ks < 8; ++ks) a.qf[ks] = ldsR(L + (2u * ks) * 4096u + hi * 4096u + (wid * 32u + (l2 & 31u)) * 16u);
#pragma unroll
    for (int n = 0; n < 2; ++n) a.aV[n] = L + VBASE + 128u * (4u * hi + q4) + 32u * ((2u * n + blk) ^ (((q4 >> 1) << 1) | hi)) + 8u * p4;
#pragma unroll
    for (int db = 0; db < 4; ++db) a.o[db] = (f32x16){};
    a.l = 0.f;
    auto ustepP_ = [&](unsigned j, unsigned stg, unsigned vo) { dmaT4<false>(L + stg * 32768u, lw, vo, sW1, j * 64u * (FF * 2u) + uft * 256u, FF * 2u); };
    auto ustepQ_ = [&](unsigned j, unsigned stg, unsigned vo) { dma64d(L + stg * 32768u + 16384u + lw * 4096u, vo, sH, (udt * 32u + j) * 16384u + lw * 4096u); };
    const bool pre = hasU && grp == 1;
    if (grp == 1) { if (wid != 7) waitv<0>(); WAITL0(); BAR(); }
    STAMP(4);
    const rsrc_t sWo = mkrsrc(wo, NH * DH * DM * 2), sW2 = mkrsrc(w2, FF * SEQ * 2);
    const unsigned lk = bid * 8192u + (wid >> 1) * 2048u + (wid & 1u) * 128u;
#define LOOK(v) do { if ((v) < 4) touch8k(sW1, voV, lk + ((v) + 3) * 0x200000u + 4096u); else if ((v) < 8) touch8k(sWo, voV, lk + ((v) - 4) * 0x200000u); \
                     else if ((v) < 12) touch8k(sW2, voV, lk + ((v) - 5) * 0x200000u); } while (0)
#define USTEP(i) ((i) < 5 ? (i) - 3 : (i) - 4)
#define ATT_TILE(i, FIRST, WB, WBP, WD, WDP)                                                                                  \
    SCHED(); att_qk1<FIRST, VSLOT[((i) + 2) % 3], 0>(a); BAR(); SCHED();    \
    if ((i) <= 4) dmaK2(KSLOT[(i) % 3], (i) + 3, wid * 2u);                                                                   \
    LOOK(2 * (i));                                                                                                            \
    att_sm1<FIRST>(a);                                                                                                        \
    if ((i) >= 3 && pre) { waitv<WBP>(); if ((i) != 5) ustepP_(USTEP(i), USTEP(i), voffT(lane_id(), FF * 2u)); } else waitv<WB>(); \
    if (!(FIRST)) att_wait(a); SCHED(); BAR();                              \
    if (FIRST) att_loadK(a, KSLOT[1]); else { SCHED(); att_pv1<((i) < 7 ? 1 : 2), ((i) < 7 ? KSLOT[((i) + 1) % 3] : VSLOT[7 % 3]), 0>(a); } \
    BAR(); SCHED();                                                         \
    if ((i) <= 5) dmaV2(VSLOT[((i) + 2) % 3], (i) + 2, wid * 2u);                                                             \
    LOOK(2 * (i) + 1);                                                                                                        \
    att_sm2<FIRST>(a);                                                                                                        \
    if ((i) >= 3 && pre) { waitv<WDP>(); if ((i) != 5) ustepQ_(USTEP(i), USTEP(i), lane_id() * 16u); } else waitv<WD>(); \
    att_wait(a); SCHED(); BAR();
    ATT_TILE(0, true, 63, 63, 63, 63) ATT_TILE(1, false, 63, 63, 10, 10) ATT_TILE(2, false, 10, 10, 10, 10) ATT_TILE(3, false, 10, 10, 10, 14)
    ATT_TILE(4, false, 10, 18, 10, 22) ATT_TILE(5, false, 8, 20, 8, 16) ATT_TILE(6, false, 5, 9, 1, 5) ATT_TILE(7, false, 63, 63, 63, 63)
#undef ATT_TILE
#undef USTEP
#undef LOOK
    SCHED(); att_pv1<0, 0u, 0>(a); STAMP(5);
    if (grp == 0) BAR();
    if (pre) { waitv<24>(); BAR(); STAMP(24); }
  }
  auto ost2 = [&](int db) {
    const unsigned l3 = lane_id(), row = qt * 256u + wid * 32u + (l3 & 31u);
    const unsigned oo = (((row >> 7) * 64u + head * 2u) * 8u + (l3 >> 5)) * 2048u + (row & 127u) * 16u;
#pragma unroll
    for (int i = 0; i < 2; ++i) {
      float w[8];
#pragma unroll
      for (int c = 0; c < 8; ++c) { w[c] = a.o[db][8 * i + c] * a.l; asm("" : "+v"(w[c])); }
      __builtin_amdgcn_raw_buffer_store_b128((u32x4){pk2(w[0], w[1]), pk2(w[2], w[3]), pk2(w[4], w[5]), pk2(w[6], w[7])}, sO, oo + db * 8192u + i * 4096u, 0, 16);
    }
  };
  { auto rr = __builtin_amdgcn_permlane32_swap(__float_as_uint(a.l), __float_as_uint(a.l), false, false);
    a.l = __builtin_amdgcn_rcpf(__uint_as_float(rr[0]) + __uint_as_float(rr[1])); }
  if (!(hasU && grp == 1)) { ost2(0); ost2(1); ost2(2); ost2(3); STAMP(6); }
  const unsigned ln = lane_id();
  const bool isY = xcd >= 4u;
  const unsigned dt = udt, st = ((xcd >> 1) & 1u) * 8u + (idx >> 2), tile = st * 8u + dt;
  u64* fG = flags + FL_G + tile * 16u; u64* fO = flags + FL_O; u64* fU = flags + FL_U + dt * 32u;
  const rsrc_t sG = mkrsrc(ws + WS_G + (size_t)tile * 32768u, 32768u), sU = mkrsrc(ws + WS_U, DM * FF * 2);
  constexpr unsigned XO = 32768u, GO = 65536u;
  if (!hasU) {
    waitv<0>(); BAR();
    if (tid == 256u) flag_set(fO + qt * 32u + head, tag);
    const rsrc_t sW2 = mkrsrc(w2, FF * SEQ * 2);
    const unsigned w = ((idx - 28u) * 8u + xcd) * 8u + wid;
    for (int j = 0; j < PFWAIT; ++j) __builtin_amdgcn_s_sleep(64);
#pragma nounroll
    for (unsigned j = 0; j < 3u * PFDIV; ++j) {
      touch8k(sW2, ln < 64u / PFDIV ? ln * 128u : 0x80000000u, ((j / PFDIV) * 256u + w) * 8192u + (j % PFDIV) * (8192u / PFDIV));
      waitv<0>(); __builtin_amdgcn_s_sleep(PFGAP);
    }
    STAMP(7);
  }
  if (grp == 1) {
    const unsigned N = isY ? 56u : 64u;
    const rsrc_t sT = isY ? mkrsrcB(w2, FF * SEQ * 2) : mkrsrcB(wo, NH * DH * DM * 2);
    const rsrc_t sR = isY ? sU : sO;
    const unsigned ldT = isY ? 4096u : 2048u, voT = voffT(ln, ldT), voL = ln * 16u;
    const unsigned tcol = (isY ? st : dt) * 256u, rbase = (isY ? dt : st) * N * 16384u + lw * 4096u;
    const unsigned toff = (isY ? 16384u : 0u) + lw * 4096u, roff = (isY ? 0u : 16384u) + lw * 4096u, g0 = isY ? (xcd >> 1) * 14u : 0u;
    const u64* dep = (isY ? fU : fO + (st >> 1) * 32u) + (ln & 31u);
    auto peek = [&]() { asm volatile("global_load_dwordx2 v[254:255], %0, off sc1" :: "v"(dep) : "memory", "v254", "v255"); };
    auto await = [&](u64 mask, bool peeked) {
      if (peeked) { u64 v; asm volatile("v_mov_b64 %0, v[254:255]" : "=v"(v) :: "memory"); if ((__ballot(v != tag) & mask) == 0ull) return; }
      flag_wait(dep, tag, mask);
    };
    unsigned sb = L + 4u * 32768u;
    auto nexts = [&]() { sb = sb == L + 4u * 32768u ? L : sb + 32768u; };
    unsigned kT = g0;
    auto dT = [&]() { T4 p; p.set(kT * 64u * ldT + tcol, lw, ldT); p.go(sb + toff, voT, sT); ++kT; nexts(); };
    if (hasU) {
      const unsigned voW1 = voffT(ln, FF * 2u), hw = (wid & 1u) * 2u;
      unsigned j = 4;
      auto ufill = [&](auto wait) {
        T4 p, q; p.set(j * 64u * (FF * 2u) + uft * 256u, hw, FF * 2u); q.set(j * 64u * (FF * 2u) + uft * 256u, hw + 1u, FF * 2u);
        unsigned d0 = sb + hw * 4096u, r0 = (udt * 32u + j) * 16384u + hw * 4096u; p.pin(); q.pin(); PINS(d0); PINS(r0);
        wait();
        p.go(d0, voW1, sW1); dma64d(d0 + 16384u, voL, sH, r0); q.go(d0 + 4096u, voW1, sW1); dma64d(d0 + 20480u, voL, sH, r0 + 4096u);
        ++j; nexts();
      };
      if (wid >= 6u) {
        ufill([] {});
        ufill([] { waitv<32>(); BAR(); }); ufill([] { waitv<40>(); BAR(); });
#pragma nounroll
        for (unsigned t = 2; t < 12; ++t) ufill([] { waitv<48>(); BAR(); });
        waitv<48>(); BAR(); ost2(0); waitv<34>(); BAR(); ost2(1); waitv<20>(); BAR(); ost2(2); waitv<6>(); BAR(); ost2(3);
        touch8k(code_rsrc(), ln < 10u ? ln * 128u : 0x80000000u, (idx * 2u + (wid & 1u)) * 1280u);
        for (unsigned t = 16; t < 22; ++t) BAR();
        waitv<0>(); BAR();
        if (tid == 384u) flag_set(fO + qt * 32u + head, tag);
        BAR(); BAR(); BAR(); BAR();
      } else {
        waitv<16>(); BAR(); STAMP(25); waitv<8>(); BAR(); waitv<0>(); BAR();
        ost2(0); BAR(); ost2(1); BAR(); ost2(2); BAR(); ost2(3);
        for (unsigned t = 6; t < 12; ++t) BAR();
        STAMP(26);
        j = 17; sb = L + 2u * 32768u;
#pragma nounroll
        for (unsigned t = 12; t < 27; ++t) ufill([] { waitv<48>(); BAR(); });
        STAMP(28);
      }
      sb = L + 2u * 32768u;
      waitv<48>(); BAR(); dT(); waitv<36>(); BAR(); dT(); peek(); waitv<24>(); BAR(); dT(); waitv<12>(); BAR(); dT(); BAR(); dT();
      if (isY) {
        BAR();
        if (tid == 256u) flag_set(fU + uft, tag);
      } else waitv<12>();
      STAMP(8);
    } else {
      sb = L + 2u * 32768u;
#pragma nounroll
      for (unsigned j = 0; j < 5; ++j) dT();
    }
    if (hasU && !isY) await(0xffffffffull, true);
    else { if (wid == 4u) await(isY ? 0x7full << (g0 >> 1) : 0xffffffffull, false); BAR(); }
#pragma nounroll
    for (unsigned j = 0; j < 5; ++j) { dma64d(sb + roff, voL, sR, rbase + (g0 + j) * 16384u); nexts(); }
    auto dstep = [&](auto wait) {
      T4 p; p.set(kT * 64u * ldT + tcol, lw, ldT);
      unsigned d0 = sb + toff, d1 = sb + roff, r0 = rbase + kT * 16384u; p.pin(); PINS(d0); PINS(d1); PINS(r0);
      wait(); BAR();
      p.go(d0, voT, sT); dma64d(d1, voL, sR, r0);
      ++kT; nexts();
    };
    waitv<16>(); BAR(); STAMP(11);
    dstep([] { waitv<12>(); }); dstep([] { waitv<16>(); }); dstep([] { waitv<20>(); });
    if (!isY && hasU && tid == 256u) flag_set(fU + uft, tag);
#pragma nounroll
    for (unsigned seg = isY ? 0u : 4u; seg < 5u; ++seg) {
      const unsigned n = !isY ? 56u : seg == 0u ? 2u : seg == 2u ? 10u : seg == 4u ? 28u : 4u;
#pragma nounroll
      for (unsigned i = 0; i < n; ++i) dstep([] { waitv<24>(); });
      if (seg == 0u || seg == 2u) peek();
      else if (seg < 4u) { waitv<32>(); await(seg == 1u ? 0x3fffull << 14 : 0x3fffull, true); kT = seg == 1u ? 70u - g0 : 0u; }
    }
    waitv<24>(); BAR();
    if (isY) { waitv<0>(); BAR(); STAMP(12); ST_OUT(1); return; }
    {
      const rsrc_t sX = mkrsrc(x, SEQ * DM * 2);
#pragma unroll
      for (int q = 0; q < 8; ++q)
        dma16(L + XO + (lw * 8u + q) * 1024u, (ln >> 4) * 2048u + 16u * ((ln & 15u) ^ ((4u * q + (ln >> 4)) & 15u)), sX, (st * 128u + lw * 32u + q * 4u) * 2048u + dt * 256u);
    }
    waitv<8>(); BAR();
    STAMP(12);
    {
      flag_wait<6>(fG, tag, 1ull);
      u32x4 g[8];
#pragma unroll
      for (int q = 0; q < 8; ++q) g[q] = __builtin_amdgcn_raw_buffer_load_b128(sG, voL + (lw * 8u + q) * 1024u, 0, 16);
#pragma unroll
      for (int q = 0; q < 8; ++q) *(LAS u32x4*)(size_t)(L + GO + voL + (lw * 8u + q) * 1024u) = g[q];
    }
    waitv<0>(); WAITL0(); STAMP(13); BAR();
  } else {
    const unsigned wp = lw >> 1, wq2 = lw & 1u, fr = ln & 15u, fq = ln >> 4;
    if (hasU) {
      Opnd<T1> P; Opnd<RCHK> Q;
      P.init(L, wp * 64u, ln); Q.init(L + 16384u, wq2 * 64u, ln);
      u32x2 bv[4];
      f32x4 acc[4][4] = {};
      gemm_compute<T1, RCHK, 4, 4, 5, 32768u, 0, 0, 16>(acc, P, Q, 32, [&] {
#pragma unroll
        for (int p = 0; p < 4; ++p) bv[p] = *(const u32x2*)(b1 + uft * 128u + wp * 64u + p * 16u + fq * 4u);
      });
      STAMP(8);
      const unsigned uo = ((udt * 56u + uft * 2u + wp) * 8u + fq) * 2048u + (wq2 * 64u + fr) * 16u;
#pragma unroll
      for (int pp = 0; pp < 2; ++pp) {
        const f32x2 b0 = bf2(bv[2 * pp][0]), b1v = bf2(bv[2 * pp][1]), b2v = bf2(bv[2 * pp + 1][0]), b3 = bf2(bv[2 * pp + 1][1]);
#pragma unroll
        for (int m = 0; m < 4; ++m)
          __builtin_amdgcn_raw_buffer_store_b128((u32x4){gelu2(lo2(acc[2 * pp][m]) + b0), gelu2(hi2(acc[2 * pp][m]) + b1v), gelu2(lo2(acc[2 * pp + 1][m]) + b2v), gelu2(hi2(acc[2 * pp + 1][m]) + b3)},
                                                 sU, uo + pp * 8192u + m * 256u, 0, 16);
      }
      STAMP(9);
      if (isY) { waitv<0>(); BAR(); }
    }
    if (isY || !hasU) BAR();
    f32x4 acc[4][4] = {};
    const unsigned t4 = (wid * 64u + ln) * 16u;
    if (isY) {
      float bias[4];
#pragma unroll
      for (int q = 0; q < 4; ++q) bias[q] = bflo(b2[st * 128u + wq2 * 64u + q * 16u + fr]);
      Opnd<RCHK> P; Opnd<T2> Q;
      P.init(L + 65536u, wp * 64u, ln); Q.init(L + 65536u + 16384u, wq2 * 64u, ln);
      gemm_compute<RCHK, T2, 4, 4, 5, 32768u, 3, 2, 0>(acc, P, Q, 56, [] {});
      STAMP(12);
#pragma unroll
      for (int j = 0; j < 8; ++j) {
        const int q = 2 * (j & 1); const f32x4 y0 = acc[j >> 1][q] + bias[q], y1 = acc[j >> 1][q + 1] + bias[q + 1];
        __builtin_amdgcn_raw_buffer_store_b128((u32x4){silu2(lo2(y0)), silu2(hi2(y0)), silu2(lo2(y1)), silu2(hi2(y1))}, sG, t4 + j * 4096u, 0, 16);
      }
      waitv<0>(); BAR();
      if (tid == 0u) flag_set(fG, tag);
      STAMP(15); ST_OUT(1); return;
    }
    Opnd<T1> P; Opnd<RCHK> Q;
    P.init(L + 65536u, wp * 64u, ln); Q.init(L + 65536u + 16384u, wq2 * 64u, ln);
    gemm_compute<T1, RCHK, 4, 4, 5, 32768u, 3, 2, 2>(acc, P, Q, 64, [] {});
    STAMP(12);
    BAR();
    STAMP(13);
    u32x4 gp[8];
#pragma unroll
    for (int j = 0; j < 8; ++j) gp[j] = *(const LAS u32x4*)(size_t)(L + GO + t4 + j * 4096u);
    const unsigned xa = L + XO + (wq2 * 64u + fr) * 256u + (fq & 1u) * 8u;
    u32x2 xv[4][4];
#pragma unroll
    for (int p = 0; p < 4; ++p)
#pragma unroll
      for (int q = 0; q < 4; ++q) xv[p][q] = *(const LAS u32x2*)(size_t)(xa + 16u * ((wp * 8u + 2u * p + (fq >> 1)) ^ fr) + q * 4096u);
#pragma unroll
    for (int p = 0; p < 4; ++p)
#pragma unroll
      for (int q = 0; q < 4; ++q) {
        const f32x4 v = acc[p][q]; const unsigned g0 = gp[2 * p + (q >> 1)][2 * (q & 1)], g1 = gp[2 * p + (q >> 1)][2 * (q & 1) + 1];
        *(LAS u32x2*)(size_t)(xa + 16u * ((wp * 8u + 2u * p + (fq >> 1)) ^ fr) + q * 4096u) =
            (u32x2){pk2(fmaf(v[0], bflo(g0), bflo(xv[p][q][0])), fmaf(v[1], bfhi(g0), bfhi(xv[p][q][0]))), pk2(fmaf(v[2], bflo(g1), bflo(xv[p][q][1])), fmaf(v[3], bfhi(g1), bfhi(xv[p][q][1])))};
      }
  }
  WAITL0(); BAR();
#pragma unroll
  for (int q = 0; q < 4; ++q) {
    const unsigned row = wid * 16u + q * 4u + (ln >> 4);
    const u32x4 v = *(const LAS u32x4*)(size_t)(L + XO + row * 256u + 16u * ((ln & 15u) ^ (row & 15u)));
    __builtin_amdgcn_raw_buffer_store_b128(v, mkrsrc(out, SEQ * DM * 2), (st * 128u + row) * 2048u + dt * 256u + (ln & 15u) * 16u, 0, OUTAUX);
  }
  STAMP(14); waitv<0>(); STAMP(15); ST_OUT(1);
}

extern "C" __global__ void rsk_end() {}
}

extern "C" void kernel_launch(void* const* d_in, const int* in_sizes, int n_in, void* d_out, int out_size, void* d_ws, size_t ws_size, hipStream_t stream) {
  const u16* x = (const u16*)d_in[0]; const u16* kc = (const u16*)d_in[1]; const u16* vc = (const u16*)d_in[2];
  const u16* wq = (const u16*)d_in[3]; const u16* wo = (const u16*)d_in[4];
  const u16* w1 = (const u16*)d_in[6]; const u16* b1 = (const u16*)d_in[7]; const u16* w2 = (const u16*)d_in[8]; const u16* b2 = (const u16*)d_in[9];
#define WSEND 0
  char* ws = (char*)d_ws + (WSEND ? ((ws_size - WS_TOTAL) & ~(size_t)0xfffff) : 0);
  (void)hipFuncSetAttribute((const void*)rsk_main, hipFuncAttributeMaxDynamicSharedMemorySize, LDS_BYTES);
  rsk_main<<<256, 512, LDS_BYTES, stream>>>(kc, vc, wq, w1, b1, wo, w2, b2, x, ws, (u16*)d_out);
}
```

```cpp
#include <hip/hip_runtime.h>
#include <hip/hip_bf16.h>

namespace {
typedef short bf16x8 __attribute__((ext_vector_type(8)));
typedef short s16x4 __attribute__((ext_vector_type(4)));
typedef float f32x2 __attribute__((ext_vector_type(2)));
typedef float f32x4 __attribute__((ext_vector_type(4)));
typedef float f32x16 __attribute__((ext_vector_type(16)));
typedef unsigned u32x4 __attribute__((ext_vector_type(4)));
typedef unsigned u32x2 __attribute__((ext_vector_type(2)));
typedef __bf16 bf2_t __attribute__((ext_vector_type(2)));
typedef unsigned short u16;
typedef __amdgpu_buffer_rsrc_t rsrc_t;
#define LAS __attribute__((address_space(3)))
#define DI __device__ __forceinline__
#define BAR() asm volatile("s_barrier" ::: "memory")
#define WAITL0() do { asm volatile("" ::: "memory"); __builtin_amdgcn_s_waitcnt(0xC07F); asm volatile("" ::: "memory"); } while (0)
#define ALIGN8() asm volatile(".p2align 3")
#define SCHED() __builtin_amdgcn_sched_barrier(0)
#define SGB(mask, n) __builtin_amdgcn_sched_group_barrier(mask, n, 0)

constexpr int SEQ = 2048, DM = 1024, NH = 32, DH = 128, SK = 2048, WIN = 512, FF = 3584;
constexpr unsigned WS_XN = 0u, WS_H = 4u << 20, WS_O = 8u << 20, WS_U = 24u << 20, WS_G = 32u << 20, WS_FLAG = 36u << 20, WS_TOTAL = 37u << 20;
constexpr unsigned LDS_BYTES = 163840;
constexpr unsigned long long SPIN_LIMIT = 2000000ull;
#define LNPF 1
#define LNAUX 16
#define PFDIV 2
#define PFGAP 0
#define XP 0
#define XSLEEP 2
#define PFWAIT 4
#define WQPF 0
#define OUTAUX 0
#define GAL 0
#define PFSLEEP 40

#define ST_DECL
#define STAMP(i) do {} while (0)
#define ST_OUT(k)
template <int N> DI void waitv() { asm volatile("s_waitcnt vmcnt(%0)" :: "i"(N) : "memory"); }
DI unsigned rfl(unsigned x) { return __builtin_amdgcn_readfirstlane(x); }
DI rsrc_t mkrsrc(const void* p, unsigned bytes) { return __builtin_amdgcn_make_buffer_rsrc((void*)p, (short)0, (int)bytes, 0x00020000); }
DI void dma16(unsigned m0v, unsigned voff, rsrc_t r, unsigned soff) {
  asm volatile("s_mov_b32 m0, %0\n\ts_nop 0\n\tbuffer_load_dwordx4 %1, %2, %3 offen lds" :: "s"(m0v), "v"(voff), "s"(r), "s"(soff) : "memory");
}
DI rsrc_t code_rsrc() {
  unsigned long long b, e;
  asm volatile("s_getpc_b64 s[40:41]\n\ts_add_u32 s40, s40, rsk_main@rel32@lo+4\n\ts_addc_u32 s41, s41, rsk_main@rel32@hi+12\n\ts_mov_b64 %0, s[40:41]\n\t"
               "s_getpc_b64 s[40:41]\n\ts_add_u32 s40, s40, rsk_end@rel32@lo+4\n\ts_addc_u32 s41, s41, rsk_end@rel32@hi+12\n\ts_mov_b64 %1, s[40:41]"
               : "=&s"(b), "=&s"(e) :: "s40", "s41", "scc");
  return mkrsrc((const void*)b, (e > b && e - b < 0x40000ull) ? (unsigned)(e - b) : 0u);
}
template <bool DEV>
DI void dma64(unsigned m0v, unsigned va, unsigned vb, rsrc_t r, unsigned s0, unsigned s1, unsigned s2, unsigned s3) {
  if constexpr (DEV)
    asm volatile("s_mov_b32 m0, %0\n\ts_nop 0\n\tbuffer_load_dwordx4 %1, %3, %4 offen sc1 lds\n\tbuffer_load_dwordx4 %2, %3, %5 offen offset:1024 sc1 lds\n\t"
                 "buffer_load_dwordx4 %1, %3, %6 offen offset:2048 sc1 lds\n\tbuffer_load_dwordx4 %2, %3, %7 offen offset:3072 sc1 lds"
                 :: "s"(m0v), "v"(va), "v"(vb), "s"(r), "s"(s0), "s"(s1), "s"(s2), "s"(s3) : "memory");
  else
    asm volatile("s_mov_b32 m0, %0\n\ts_nop 0\n\tbuffer_load_dwordx4 %1, %3, %4 offen lds\n\tbuffer_load_dwordx4 %2, %3, %5 offen offset:1024 lds\n\t"
                 "buffer_load_dwordx4 %1, %3, %6 offen offset:2048 lds\n\tbuffer_load_dwordx4 %2, %3, %7 offen offset:3072 lds"
                 :: "s"(m0v), "v"(va), "v"(vb), "s"(r), "s"(s0), "s"(s1), "s"(s2), "s"(s3) : "memory");
}
DI void dma64d(unsigned m0v, unsigned va, rsrc_t r, unsigned s0) {
  asm volatile("s_mov_b32 m0, %0\n\ts_nop 0\n\tbuffer_load_dwordx4 %1, %2, %3 offen sc1 lds\n\tbuffer_load_dwordx4 %1, %2, %3 offen offset:1024 sc1 lds\n\t"
               "buffer_load_dwordx4 %1, %2, %3 offen offset:2048 sc1 lds\n\tbuffer_load_dwordx4 %1, %2, %3 offen offset:3072 sc1 lds"
               :: "s"(m0v), "v"(va), "s"(r), "s"(s0) : "memory");
}
DI rsrc_t mkrsrcB(const void* p, unsigned bytes) { return mkrsrc((const char*)p - 4096, bytes + 4096u); }
#define PINS(x) asm volatile("" : "+s"(x))
DI bf16x8 ldsR(unsigned a) { return *(const LAS bf16x8*)(size_t)a; }
DI s16x4 ldsT(unsigned a) { return __builtin_amdgcn_ds_read_tr16_b64_v4i16((LAS s16x4*)(size_t)a); }
DI bf16x8 cat(s16x4 a, s16x4 b) { return __builtin_shufflevector(a, b, 0, 1, 2, 3, 4, 5, 6, 7); }
DI unsigned pk2(float lo, float hi) { f32x2 v = {lo, hi}; return __builtin_bit_cast(unsigned, __builtin_convertvector(v, bf2_t)); }
DI float bflo(unsigned w) { return __uint_as_float(w << 16); }
DI float bfhi(unsigned w) { return __uint_as_float(w & 0xffff0000u); }
DI float rbf(float x) { return bflo(pk2(x, x) & 0xffffu); }
template <int CTRL, int RM> DI float dpp_add(float v) {
  return v + __builtin_bit_cast(float, __builtin_amdgcn_update_dpp(0, __builtin_bit_cast(int, v), CTRL, RM, 0xf, false));
}
DI void wsum2(float& u, float& v) {
#define WS2(C, M) u = dpp_add<C, M>(u); v = dpp_add<C, M>(v);
  WS2(0x111, 0xf) WS2(0x112, 0xf) WS2(0x114, 0xf) WS2(0x118, 0xf) WS2(0x142, 0xa) WS2(0x143, 0xc)
#undef WS2
  u = __builtin_bit_cast(float, __builtin_amdgcn_readlane(__builtin_bit_cast(int, u), 63));
  v = __builtin_bit_cast(float, __builtin_amdgcn_readlane(__builtin_bit_cast(int, v), 63));
}

DI void ln16(float (&v)[16]) {
  const float x0 = __builtin_bit_cast(float, __builtin_amdgcn_readfirstlane(__builtin_bit_cast(int, v[0])));
  float s = 0.f, q = 0.f;
#pragma unroll
  for (int i = 0; i < 16; ++i) { const float d = v[i] - x0; s += d; q = fmaf(d, d, q); }
  wsum2(s, q);
  const float md = s * (1.0f / DM), var = rbf(fmaxf(fmaf(-md, md, q * (1.0f / DM)), 0.f));
  const float mb = rbf(x0 + md), inv = __builtin_amdgcn_rcpf(rbf(__builtin_amdgcn_sqrtf(rbf(var + 1e-5f))));
#pragma unroll
  for (int i = 0; i < 16; i += 2) {
    const unsigned d = pk2(v[i] - mb, v[i + 1] - mb), r = pk2(bflo(d) * inv, bfhi(d) * inv);
    v[i] = bflo(r); v[i + 1] = bfhi(r);
  }
}
DI unsigned pkx(float lo, float hi) { return (__float_as_uint(lo) >> 16) | (__float_as_uint(hi) & 0xffff0000u); }
enum { RROW = 0, RCHK = 1, T1 = 2, T2 = 3 };
template <int KIND> struct Opnd {
  static constexpr int NA = KIND == RROW ? 2 : KIND == RCHK ? 1 : 4;
  static constexpr int RPB = KIND >= T1 ? 2 : 1;
  unsigned a[NA];
  DI void init(unsigned base, unsigned off, unsigned lane) {
    const unsigned fr = lane & 15u, g = lane >> 4, q = fr >> 2, p = lane & 3u;
    if constexpr (KIND == RROW) {
#pragma unroll
      for (int kk = 0; kk < 2; ++kk) a[kk] = base + (off + fr) * 128u + 16u * ((4u * kk + g) ^ (fr >> 1));
    } else if constexpr (KIND == RCHK) {
      a[0] = base + g * 2048u + (off + fr) * 16u;
    } else {
      const unsigned x = ((q >> 1) << 1) | (g & 1u);
#pragma unroll
      for (int n = 0; n < 4; ++n)
        a[n] = base + (KIND == T1 ? 4096u : 2048u) * (g >> 1) + 1024u * (off >> 6) + 128u * (4u * (g & 1u) + q) + 32u * ((unsigned)n ^ x) + 8u * p;
    }
  }
  DI bf16x8 ld(int b, int kk) const {
    if constexpr (KIND == RROW) return ldsR(a[kk] + 2048u * b);
    else if constexpr (KIND == RCHK) return ldsR(a[0] + 8192u * kk + 256u * b);
    else { const unsigned ad = a[b & 3] + 1024u * (b >> 2) + 8192u * kk; return cat(ldsT(ad), ldsT(ad + (KIND == T1 ? 2048u : 4096u))); }
  }
  DI void adv(unsigned delta) {
#pragma unroll
    for (int i = 0; i < NA; ++i) { a[i] += delta; asm volatile("" : "+v"(a[i])); }
  }
};

template <int PK, int QK, int PM, int QM, int NS, unsigned STG, int TAIL, int S0, int DRAIN, class F>
DI void gemm_compute(f32x4 (&acc)[PM][QM], Opnd<PK>& P, Opnd<QK>& Q, const int N, F first) {
  bf16x8 pf[2][PM], qf[2][QM];
  constexpr int NR = PM * Opnd<PK>::RPB + QM * Opnd<QK>::RPB, NM = PM * QM, NA = Opnd<PK>::NA + Opnd<QK>::NA;
  auto load = [&](int kk) {
    pf[kk][0] = P.ld(0, kk);
#pragma unroll
    for (int q = 0; q < QM; ++q) qf[kk][q] = Q.ld(q, kk);
#pragma unroll
    for (int p = 1; p < PM; ++p) pf[kk][p] = P.ld(p, kk);
  };
  auto mma = [&](int kk) {
#pragma unroll
    for (int p = 0; p < PM; ++p)
#pragma unroll
      for (int q0 = 0; q0 < QM; ++q0) { const int q = (p & 1) ? QM - 1 - q0 : q0; acc[p][q] = __builtin_amdgcn_mfma_f32_16x16x32_bf16(pf[kk][p], qf[kk][q], acc[p][q], 0, 0, 0); }
  };
  constexpr int ND = (NR + NA + 2 > NM) ? (NR + NA + 2 - NM) : 0;
  BAR();
  load(0);
  int stg = S0;
  auto step = [&](bool sync) {
    SCHED();
    load(1); mma(0);
    stg = (stg == NS - 1) ? 0 : stg + 1;
    const unsigned delta = stg == 0 ? (unsigned)(-(int)((NS - 1) * STG)) : STG;
    P.adv(delta); Q.adv(delta);
#pragma unroll
    for (int i = 0; i < ND; ++i) { SGB(0x8, 1); SGB(0x100, 2); }
#pragma unroll
    for (int i = 0; i < NR - 2 * ND; ++i) { SGB(0x8, 1); SGB(0x100, 1); }
#pragma unroll
    for (int i = 0; i < NA; ++i) { SGB(0x8, 1); SGB(0x2, 1); }
    SGB(0x8, NM);
    SCHED();
    if (sync) { WAITL0(); BAR(); }
    SCHED();
    load(0); mma(1);
#pragma unroll
    for (int i = 0; i < ND; ++i) { SGB(0x8, 1); SGB(0x100, 2); }
#pragma unroll
    for (int i = 0; i < NR - 2 * ND; ++i) { SGB(0x8, 1); SGB(0x100, 1); }
    SGB(0x8, NM);
  };
  if (DRAIN > 0) {
#pragma nounroll
    for (int t = 0; t < DRAIN; ++t) step(true);
    waitv<0>();
    first();
  }
#pragma nounroll
  for (int t = DRAIN; t < N - TAIL; ++t) step(true);
#pragma nounroll
  for (int t = 0; t < TAIL; ++t) step(false);
  SCHED();
}

template <int PK, int QK, int NS, unsigned STG, int TAIL, int S0, int DRAIN, class F>
DI void gemm_full(f32x4 (&acc)[4][4], Opnd<PK>& P, Opnd<QK>& Q, const int N, F hook) {
  bf16x8 pf[2][2][4], qf[2][2][4];
  constexpr int NR = 8 * (Opnd<PK>::RPB + Opnd<QK>::RPB), NA = Opnd<PK>::NA + Opnd<QK>::NA;
  int stg = S0;
  auto load = [&](int b) {
#pragma unroll
    for (int i = 0; i < 4; ++i)
#pragma unroll
      for (int kk = 0; kk < 2; ++kk) { pf[b][kk][i] = P.ld(i, kk); qf[b][kk][i] = Q.ld(i, kk); }
    stg = (stg == NS - 1) ? 0 : stg + 1;
    const unsigned delta = stg == 0 ? (unsigned)(-(int)((NS - 1) * STG)) : STG;
    P.adv(delta); Q.adv(delta);
  };
  auto step = [&](int b, bool sync) {
    if (sync) { WAITL0(); BAR(); }
    SCHED();
    load(b ^ 1);
#pragma unroll
    for (int p = 0; p < 4; ++p)
#pragma unroll
      for (int q0 = 0; q0 < 4; ++q0) {
        const int q = (p & 1) ? 3 - q0 : q0;
        acc[p][q] = __builtin_amdgcn_mfma_f32_16x16x32_bf16(pf[b][0][p], qf[b][0][q], acc[p][q], 0, 0, 0);
        acc[p][q] = __builtin_amdgcn_mfma_f32_16x16x32_bf16(pf[b][1][p], qf[b][1][q], acc[p][q], 0, 0, 0);
      }
#pragma unroll
    for (int i = 0; i < NR; ++i) { SGB(0x8, 1); SGB(0x100, 1); }
#pragma unroll
    for (int i = 0; i < NA; ++i) { SGB(0x8, 1); SGB(0x2, 1); }
    SGB(0x8, 32);
    SCHED();
  };
  BAR();
  load(0);
  if (DRAIN > 0) {
#pragma nounroll
    for (int t = 0; t < DRAIN; t += 2) { step(0, true); step(1, true); }
    waitv<0>();
    hook();
  }
#pragma nounroll
  for (int t = DRAIN; t < N - (TAIL ? 4 : 0); t += 2) { step(0, true); step(1, true); }
  if (TAIL) {
#pragma nounroll
    for (int t = 0; t < 4; t += 2) { step(0, true); step(1, false); }
  }
}

DI unsigned voffRow(unsigned lane, unsigned ldb, unsigned par) { return (lane >> 3) * ldb + 16u * ((lane & 7u) ^ (4u * par + (lane >> 4))); }
DI unsigned voffT(unsigned lane, unsigned ldb) { return (lane >> 3) * ldb + 16u * ((lane & 7u) ^ (2u * ((((lane >> 4) & 1u) << 1) | (lane >> 5)))); }
struct T4 { unsigned s0, s1, s2, s3;
  DI void set(unsigned so, unsigned lw, unsigned ldb) { s0 = so + 4096u + lw * 16u * ldb; s1 = s0 + 128u - 1024u; s2 = s0 + 8u * ldb - 2048u; s3 = s2 + 128u - 1024u; }
  DI void pin() { PINS(s0); PINS(s1); PINS(s2); PINS(s3); }
  template <bool DEV = false> DI void go(unsigned dst, unsigned vo, rsrc_t r) const { dma64<DEV>(dst, vo, vo, r, s0, s1, s2, s3); } };
template <bool DEV> DI void dmaT4(unsigned dst, unsigned lw, unsigned vo, rsrc_t r, unsigned so, unsigned ldb) { T4 t; t.set(so, lw, ldb); t.go<DEV>(dst + lw * 4096u, vo, r); }

typedef unsigned long long u64;
constexpr unsigned FL_H = 64, FL_O = 320, FL_U = 576, FL_G = 832, FL_X = 3072;
DI void flag_set(u64* p, u64 tag) { __hip_atomic_store(p, tag, __ATOMIC_RELAXED, __HIP_MEMORY_SCOPE_AGENT); }
DI u64 flag_get(const u64* p) { return __hip_atomic_load(p, __ATOMIC_RELAXED, __HIP_MEMORY_SCOPE_AGENT); }
template <int SLOW = 16> DI void flag_wait(const u64* p, u64 tag, u64 mask) {
  const u64 t0 = __builtin_amdgcn_s_memrealtime();
  while ((__ballot(flag_get(p) != tag) & mask) != 0ull) {
    const u64 dt = __builtin_amdgcn_s_memrealtime() - t0;
    if (dt > SPIN_LIMIT) break;
    if (dt > 400ull) __builtin_amdgcn_s_sleep(SLOW); else __builtin_amdgcn_s_sleep(3);
  }
  asm volatile("" ::: "memory");
}

DI f32x2 sigm2(f32x2 v, f32x2 z) {
  f32x2 e = {__builtin_amdgcn_exp2f(z[0]), __builtin_amdgcn_exp2f(z[1])};
  e += 1.0f;
  return v * (f32x2){__builtin_amdgcn_rcpf(e[0]), __builtin_amdgcn_rcpf(e[1])};
}
DI unsigned gelu2(f32x2 v) {
  const f32x2 r = sigm2(v, v * __builtin_elementwise_fma(v * v, (f32x2)(-0.1029432397f), (f32x2)(-2.3022081986f)));
  return pk2(r[0], r[1]);
}
DI unsigned silu2(f32x2 v) { const f32x2 r = sigm2(v, v * -1.4426950408889634f); return pk2(r[0], r[1]); }
DI f32x2 lo2(f32x4 v) { return __builtin_shufflevector(v, v, 0, 1); }
DI f32x2 hi2(f32x4 v) { return __builtin_shufflevector(v, v, 2, 3); }
DI f32x2 bf2(unsigned w) { return (f32x2){bflo(w), bfhi(w)}; }

constexpr unsigned QSTG = 49152u;
constexpr unsigned KBASE = 65536u, VBASE = 114688u;
constexpr unsigned KSLOT[3] = {0u, 32768u, 16384u}, VSLOT[3] = {32768u, 16384u, 0u};
DI void touch8k(rsrc_t r, unsigned vo128, unsigned soff) {
  asm volatile("buffer_load_dword v255, %0, %1, %2 offen" :: "v"(vo128), "s"(r), "s"(soff) : "memory", "v255");
}
constexpr float QSCALE = 0.12751743082f;
#define BIGSUM 4096.0f

#define ATTPRIO 0
#define PRIO(x) do { if (ATTPRIO == 1) __builtin_amdgcn_s_setprio(x); else if (ATTPRIO == 2) __builtin_amdgcn_s_setprio(1 - (x)); } while (0)
struct Att {
  bf16x8 qf[8], kv[16], pf[4], pn[2];
  f32x16 o[4], s[2], negm;
  float l, ts;
  unsigned aK[4], aV[2];
};
template <unsigned SLOT, int J> DI void att_k(Att& a) {
  asm volatile("ds_read_b128 %0, %1 offset:%2" : "+v"(a.kv[J]) : "v"(a.aK[(J >> 1) & 3]), "i"(SLOT + 8192u * (J >> 3) + 4096u * (J & 1)) : "memory");
}
template <unsigned SLOT, int J> DI void att_v(Att& a) {
  s16x4 lo = __builtin_shufflevector(a.kv[J], a.kv[J], 0, 1, 2, 3), hi = __builtin_shufflevector(a.kv[J], a.kv[J], 4, 5, 6, 7);
  constexpr unsigned off = SLOT + 4096u * (J >> 2) + 1024u * ((J >> 1) & 1);
  asm volatile("ds_read_b64_tr_b16 %0, %2 offset:%3\n\tds_read_b64_tr_b16 %1, %2 offset:%4" : "+v"(lo), "+v"(hi) : "v"(a.aV[J & 1]), "i"(off), "i"(off + 2048u) : "memory");
  a.kv[J] = cat(lo, hi);
}
DI void att_wait(Att& a) {
  asm volatile("s_waitcnt lgkmcnt(0)" : "+v"(a.kv[0]), "+v"(a.kv[1]), "+v"(a.kv[2]), "+v"(a.kv[3]), "+v"(a.kv[4]), "+v"(a.kv[5]), "+v"(a.kv[6]), "+v"(a.kv[7]),
               "+v"(a.kv[8]), "+v"(a.kv[9]), "+v"(a.kv[10]), "+v"(a.kv[11]), "+v"(a.kv[12]), "+v"(a.kv[13]), "+v"(a.kv[14]), "+v"(a.kv[15]) :: "memory");
}
DI void att_loadK(Att& a, unsigned slot) {
#pragma unroll
  for (int j = 0; j < 16; ++j) a.kv[j] = ldsR(a.aK[(j >> 1) & 3] + slot + 8192u * (j >> 3) + 4096u * (j & 1));
}
DI unsigned lane_id() { unsigned l; asm volatile("v_mbcnt_lo_u32_b32 %0, -1, 0\n\tv_mbcnt_hi_u32_b32 %0, -1, %0" : "=v"(l)); return l; }
DI void add_bcast3(f32x16& t0, f32x16& t1, f32x16& t2, float v) {
  const float one = lane_id() < 32u ? 1.0f : 0.0f;
  asm volatile("s_nop 7\n\ts_nop 7\n\tv_mfma_f32_32x32x2_f32 %0, %3, %4, %0\n\tv_mfma_f32_32x32x2_f32 %1, %3, %4, %1\n\tv_mfma_f32_32x32x2_f32 %2, %3, %4, %2\n\t"
               "s_nop 7\n\ts_nop 7\n\ts_nop 7" : "+v"(t0), "+v"(t1), "+v"(t2) : "v"(one), "v"(v));
}
template <bool FIRST, unsigned SLOT, int J> DI void att_qk1(Att& a) {
  a.s[J & 1] = __builtin_amdgcn_mfma_f32_32x32x16_bf16(a.kv[J], a.qf[J >> 1], J < 2 ? (FIRST ? (f32x16){} : a.negm) : a.s[J & 1], 0, 0, 0);
  SCHED();
  if constexpr (J == 0) ALIGN8();
  if constexpr (!FIRST) att_v<SLOT, J>(a);
  SCHED();
  if constexpr (J < 15) att_qk1<FIRST, SLOT, J + 1>(a);
}
template <int NEXT, unsigned SLOT, int J> DI void att_pv1(Att& a) {
  a.o[J & 3] = __builtin_amdgcn_mfma_f32_32x32x16_bf16(a.kv[J], a.pf[J >> 2], a.o[J & 3], 0, 0, 0);
  SCHED();
  if constexpr (J == 0) ALIGN8();
  if constexpr (NEXT == 1) att_k<SLOT, J>(a); else if constexpr (NEXT == 2) att_v<SLOT, J>(a);
  SCHED();
  if constexpr (J < 15) att_pv1<NEXT, SLOT, J + 1>(a);
}
DI float max3(float x, float y, float z) { return __builtin_fmaxf(__builtin_fmaxf(x, y), z); }
DI float att_rowmax(const Att& a) {
  float m0 = max3(a.s[0][0], a.s[0][1], a.s[0][2]), m1 = max3(a.s[1][0], a.s[1][1], a.s[1][2]);
#pragma unroll
  for (int r = 3; r < 15; r += 2) { m0 = max3(m0, a.s[0][r], a.s[0][r + 1]); m1 = max3(m1, a.s[1][r], a.s[1][r + 1]); }
  const float mx = __builtin_fmaxf(max3(m0, m1, a.s[0][15]), a.s[1][15]);
  auto rr = __builtin_amdgcn_permlane32_swap(__float_as_uint(mx), __float_as_uint(mx), false, false);
  return __builtin_fmaxf(__uint_as_float(rr[0]), __uint_as_float(rr[1]));
}
DI float att_exp16(const f32x16& v, bf16x8& p0, bf16x8& p1) {
  float s0 = 0.f, s1 = 0.f;
  u32x4 w[2];
#pragma unroll
  for (int h = 0; h < 2; ++h) {
    float e[8];
#pragma unroll
    for (int r = 0; r < 8; ++r) e[r] = __builtin_amdgcn_exp2f(v[8 * h + r]);
#pragma unroll
    for (int r = 0; r < 8; r += 2) {
      if (h == 0 && r == 0) { s0 = e[0]; s1 = e[1]; } else { s0 += e[r]; asm("" : "+v"(s0)); s1 += e[r + 1]; asm("" : "+v"(s1)); }
      w[h][r >> 1] = pk2(e[r], e[r + 1]);
    }
    asm volatile("" : "+v"(w[h]));
    SCHED();
  }
  p0 = __builtin_bit_cast(bf16x8, w[0]); p1 = __builtin_bit_cast(bf16x8, w[1]);
  return s0 + s1;
}
template <bool FIRST> DI void att_sm1(Att& a) {
  if (FIRST) {
    const float mx = att_rowmax(a);
#pragma unroll
    for (int r = 0; r < 16; ++r) { a.s[0][r] -= mx; a.s[1][r] -= mx; a.negm[r] = -mx; }
  }
  a.ts = att_exp16(a.s[0], a.pn[0], a.pn[1]);
  asm volatile("" : "+v"(a.pn[0]), "+v"(a.pn[1]), "+v"(a.ts), "+v"(a.s[1]), "+v"(a.negm));
}
template <bool FIRST> DI void att_sm2(Att& a) {
  a.ts += att_exp16(a.s[1], a.pf[2], a.pf[3]);
  if (!FIRST) {
    if (__builtin_expect(!__all(a.ts <= BIGSUM), 0)) {
      const float dl = fmaxf(att_rowmax(a), 0.f), fct = __builtin_amdgcn_exp2f(-dl);
#pragma unroll
      for (int db = 0; db < 4; ++db) a.o[db] *= fct;
      a.l *= fct;
      add_bcast3(a.s[0], a.s[1], a.negm, -dl);
      a.ts = att_exp16(a.s[0], a.pn[0], a.pn[1]) + att_exp16(a.s[1], a.pf[2], a.pf[3]);
    }
  }
  a.l += a.ts;
  a.pf[0] = a.pn[0]; a.pf[1] = a.pn[1];
  asm volatile("" : "+v"(a.pf[0]), "+v"(a.pf[1]), "+v"(a.pf[2]), "+v"(a.pf[3]), "+v"(a.l));
}

extern "C" __global__ __launch_bounds__(512, 2) __attribute__((amdgpu_num_vgpr(127)))
void rsk_main(const u16* __restrict__ x, char* __restrict__ ws, const u16* __restrict__ wq, const u16* __restrict__ kc, const u16* __restrict__ vc,
              const u16* __restrict__ w1, const u16* __restrict__ wo, const u16* __restrict__ w2, const u16* __restrict__ b1, const u16* __restrict__ b2,
              u16* __restrict__ out) {
  if (rfl(threadIdx.x) == 0u) touch8k(code_rsrc(), threadIdx.x * 128u + (threadIdx.x < 48u ? 0u : (blockIdx.x >> 3) * 2048u), 0u);
  extern __shared__ __attribute__((aligned(16))) char lds[];
  const unsigned L = (unsigned)(size_t)lds;
  const char* xn = ws + WS_XN; const char* hh = ws + WS_H;
  const unsigned tid = threadIdx.x, lane = tid & 63u, wid = rfl(tid >> 6), grp = wid >> 2, lw = wid & 3u;
  const unsigned bid = blockIdx.x, xcd = bid & 7u, idx = bid >> 3;
  const unsigned qt = (xcd & 1u) * 4u + (idx & 3u), kvh = xcd >> 1, head = kvh * 8u + (idx >> 2);
  const bool hasU = idx < 28u;
  const unsigned udt = (xcd & 1u) * 4u + (idx & 3u), uft = (xcd >> 1) * 7u + (idx >> 2);
  const unsigned ufc = (idx >> 2) * 4u + (xcd >> 1);
  const rsrc_t sK = mkrsrc(kc + ((size_t)kvh * SK + (SK - WIN)) * DH, WIN * DH * 2), sV = mkrsrc(vc + ((size_t)kvh * SK + (SK - WIN)) * DH, WIN * DH * 2);
  const rsrc_t sH = mkrsrc(hh, SEQ * DM * 2), sW1 = mkrsrcB(w1, SEQ * FF * 2);
  const unsigned voK0 = voffRow(lane, 256u, 0), voV = voffT(lane, 256u);
  auto dmaK2 = [&](unsigned slot, unsigned t, unsigned p0) {
    unsigned voK1; asm volatile("v_xor_b32 %0, 64, %1" : "=v"(voK1) : "v"(voK0));
    dma16(L + KBASE + slot + 1024u * p0, voK0, sK, t * 16384u + (p0 & 7u) * 2048u + (p0 >> 3) * 128u);
    dma16(L + KBASE + slot + 1024u * p0 + 1024u, voK1, sK, t * 16384u + (p0 & 7u) * 2048u + 2048u + (p0 >> 3) * 128u); };
  auto dmaV2 = [&](unsigned slot, unsigned t, unsigned p0) {
    dma16(L + VBASE + slot + 1024u * p0, voV, sV, t * 16384u + (p0 >> 1) * 2048u);
    dma16(L + VBASE + slot + 1024u * p0 + 1024u, voV, sV, t * 16384u + (p0 >> 1) * 2048u + 128u); };
  auto tileK = [&](unsigned t) {
#pragma unroll
    for (int p = 0; p < 16; p += 2) dmaK2(KSLOT[t % 3], t, p); };
  auto tileV = [&](unsigned t) {
#pragma unroll
    for (int p = 0; p < 16; p += 2) dmaV2(VSLOT[t % 3], t, p); };
  Att a;
  { const unsigned r32 = lane & 31u, hi = lane >> 5;
#pragma unroll
    for (int j = 0; j < 4; ++j) a.aK[j] = L + KBASE + r32 * 128u + 16u * ((2u * j + hi) ^ ((r32 >> 1) & 7u)); }

  ST_DECL; STAMP(0);
  u64* flags = (u64*)(ws + WS_FLAG);
  const rsrc_t sS = lw == 0 ? mkrsrcB(wq, DM * NH * DH * 2) : mkrsrcB(xn, SEQ * DM * 2);
  unsigned asrc = 4096u + (lw == 0 ? head * 256u : (qt * 256u + (lw - 1u) * 128u) * 2048u), adst = L + lw * 16384u;
  const unsigned avo0 = lw == 0 ? voffT(lane, 8192u) : voffRow(lane, 2048u, 0), avo1 = lw == 0 ? avo0 : avo0 ^ 64u;
  constexpr unsigned HT = 147456u;
  auto astep = [&](unsigned vo0, unsigned vo1) {
    const unsigned sp = lw == 0 ? 8u * 8192u : 16u * 2048u, so = (lw == 0 ? 128u : 8u * 2048u) - 1024u;
#pragma unroll
    for (int q = 0; q < 4; ++q) { const unsigned e = asrc + 2u * q * sp; dma64<true>(adst + 4096u * q, vo0, vo1, sS, e, e + so, e + sp - 2048u, e + sp - 2048u + so); }
    asrc += lw == 0 ? 64u * 8192u : 128u; adst = adst >= L + 2u * QSTG ? adst - 2u * QSTG : adst + QSTG;
  };
  u64 tag;
  {
    const unsigned row = (bid >> 2) * 32u + ((bid >> 1) & 1u) * 16u + (bid & 1u) * 4u + (wid >> 2) * 8u + (wid & 3u);
    u32x4 xa, xb; u32x2 ep;
    asm volatile("buffer_load_dwordx4 %0, %3, %4, %5 offen\n\tbuffer_load_dwordx4 %1, %3, %4, %5 offen offset:1024\n\tglobal_load_dwordx2 %2, %6, %7 sc1"
                 : "=&v"(xa), "=&v"(xb), "=&v"(ep) : "v"(lane * 16u), "s"(mkrsrc(x, SEQ * DM * 2)), "s"(row * 2048u), "v"(0u), "s"(flags) : "memory");
    __builtin_amdgcn_s_setprio(1);
    if (wid == 7) {
      touch8k(mkrsrc(wq, DM * NH * DH * 2), lane * 128u, bid * 8192u);
      waitv<1>();
    } else waitv<0>();
    asm volatile("" : "+v"(xa), "+v"(xb), "+v"(ep));
    STAMP(16);
    tag = (((u64)rfl(ep[1]) << 32) | rfl(ep[0])) * 0x9E3779B97F4A7C15ull + 0x632BE59BD9B4E019ull;
    float v[16];
#pragma unroll
    for (int i = 0; i < 4; ++i) { v[2 * i] = bflo(xa[i]); v[2 * i + 1] = bfhi(xa[i]); v[8 + 2 * i] = bflo(xb[i]); v[9 + 2 * i] = bfhi(xb[i]); }
    ln16(v);
    const rsrc_t sW = mkrsrc(ws, WS_O);
#pragma unroll
    for (int p = 0; p < 2; ++p) {
      const unsigned lo0 = pkx(v[8 * p], v[8 * p + 1]), lo1 = pkx(v[8 * p + 2], v[8 * p + 3]), hi0 = pkx(v[8 * p + 4], v[8 * p + 5]), hi1 = pkx(v[8 * p + 6], v[8 * p + 7]);
      const bool odd = lane & 1u;
      const unsigned s0 = __builtin_amdgcn_update_dpp(0, (int)(odd ? lo0 : hi0), 0xB1, 0xf, 0xf, false), s1 = __builtin_amdgcn_update_dpp(0, (int)(odd ? lo1 : hi1), 0xB1, 0xf, 0xf, false);
      __builtin_amdgcn_raw_buffer_store_b128(odd ? (u32x4){s0, s1, hi0, hi1} : (u32x4){lo0, lo1, s0, s1}, sW, WS_XN + row * 2048u + lane * 16u + 1024u * p, 0, 16);
    }
    STAMP(17);
    waitv<0>();
    if (lane == 0u) flag_set(flags + FL_X + bid * 8u + wid, tag);
    STAMP(18);
    if (wid == 4u) { astep(avo0, avo1); astep(avo0, avo1); }
    if (grp == 0) __builtin_amdgcn_s_setprio(0);
    ln16(v);
    *(LAS u32x4*)(size_t)(L + HT + wid * 2048u + lane * 16u) = (u32x4){pkx(v[0], v[1]), pkx(v[2], v[3]), pkx(v[4], v[5]), pkx(v[6], v[7])};
    *(LAS u32x4*)(size_t)(L + HT + wid * 2048u + 1024u + lane * 16u) = (u32x4){pkx(v[8], v[9]), pkx(v[10], v[11]), pkx(v[12], v[13]), pkx(v[14], v[15])};
    WAITL0();
    __builtin_amdgcn_s_setprio(0);
    STAMP(19);
  }
  if (wid == 7) {
    const unsigned vo = lane * 128u;
    const rsrc_t sWq = mkrsrc(wq, DM * NH * DH * 2);
    BAR(); BAR();
    touch8k(sWq, vo, (256u + bid) * 8192u);
    {
      const rsrc_t sW = mkrsrc(ws, WS_O);
#pragma nounroll
      for (unsigned i = 0; i < 4; ++i) {
        BAR();
        if (i == 1u) touch8k(sW1, vo, bid * 8192u + 4096u);
#pragma unroll
        for (int c = 0; c < 4; ++c) {
          const unsigned d0 = (i * 4u + c) * 64u, ad = L + HT + ((lane & 15u) >> 2) * 2048u + (d0 + (lane >> 4) * 16u + (lane & 3u) * 4u) * 2u;
          __builtin_amdgcn_raw_buffer_store_b128(__builtin_bit_cast(u32x4, cat(ldsT(ad), ldsT(ad + 8192u))), sW, WS_H + ((d0 >> 7) * 256u + bid) * 2048u + ((d0 & 64u) + lane) * 16u, 0, 16);
        }
      }
    }
    touch8k(sWq, vo, (512u + bid) * 8192u);
    BAR(); BAR();
    waitv<1>();
    if (lane == 0u) flag_set(flags + FL_H + bid, tag);
    touch8k(sWq, vo, (768u + bid) * 8192u);
    BAR(); BAR();
    touch8k(sW1, vo, (256u + bid) * 8192u + 4096u);
    if (idx < 16u) touch8k(sK, vo, idx * 8192u); else touch8k(sV, vo, (idx - 16u) * 8192u);
    BAR(); BAR();
    const u64 f0 = flag_get(flags + FL_H + lane), f1 = flag_get(flags + FL_H + 64u + lane), f2 = flag_get(flags + FL_H + 128u + lane), f3 = flag_get(flags + FL_H + 192u + lane);
    touch8k(code_rsrc(), lane < 16u ? vo : 0x80000000u, idx * 2048u);
    BAR(); BAR();
    if (__any(f0 != tag || f1 != tag || f2 != tag || f3 != tag)) {
#pragma nounroll
      for (unsigned j = 0; j < 256u; j += 64u) flag_wait(flags + FL_H + j + lane, tag, ~0ull);
    }
    if (bid == 0u && lane == 0u) flag_set(flags, tag);
    touch8k(sW1, vo, (512u + bid) * 8192u + 4096u);
    BAR(); BAR(); BAR();
#pragma unroll
    for (int j = 3; j < 6; ++j) touch8k(sW1, vo, (j * 256u + bid) * 8192u + 4096u);
    att_loadK(a, KSLOT[0]);
  } else if (grp == 1) {
    const unsigned vo0 = avo0, vo1 = avo1;
    if (lw != 0) {
      const u64* fx = flags + FL_X + qt * 256u + (lw - 1u) * 128u + lane;
      const u64 t0 = __builtin_amdgcn_s_memrealtime();
      while (__any(((flag_get(fx) ^ tag) | (flag_get(fx + 64) ^ tag)) != 0ull)) {
        if (__builtin_amdgcn_s_memrealtime() - t0 > SPIN_LIMIT) break;
        __builtin_amdgcn_s_sleep(XSLEEP);
      }
      asm volatile("" ::: "memory");
      STAMP(21);
      astep(vo0, vo1); astep(vo0, vo1);
      STAMP(22);
    }
    waitv<16>(); STAMP(23);
    BAR(); STAMP(1);
    astep(vo0, vo1);
#pragma nounroll
    for (unsigned t = 0; t < 13; ++t) { waitv<16>(); BAR(); astep(vo0, vo1); }
    waitv<16>(); BAR();
    if (lw == 0) tileK(0); else if (lw == 1) tileK(2); else tileV(0);
    waitv<16>(); BAR();
    if (lw == 1) tileK(1); else if (lw == 2) tileV(1);
    if (lw == 0) waitv<0>();
    BAR();
    att_loadK(a, KSLOT[0]);
  } else {
    Opnd<T1> P; Opnd<RROW> Q;
    P.init(L, 0u, lane); Q.init(L + 16384u, lw * 64u, lane);
    f32x4 acc[8][4] = {};
    gemm_compute<T1, RROW, 8, 4, 3, QSTG, 0, 0, 0>(acc, P, Q, 16, [] {});
    STAMP(2);
    att_loadK(a, KSLOT[0]);
    const unsigned wa = L + (lane >> 5) * 4096u + (lw * 64u + (lane & 15u)) * 16u + ((lane >> 4) & 1u) * 8u;
#pragma unroll
    for (int n = 0; n < 8; ++n)
#pragma unroll
      for (int m = 0; m < 4; ++m) {
        const f32x4 v = acc[n][m] * QSCALE;
        *(LAS u32x2*)(size_t)(wa + 8192u * n + 256u * m) = (u32x2){pk2(v[0], v[1]), pk2(v[2], v[3])};
      }
  }
  WAITL0(); BAR(); STAMP(3);

  const rsrc_t sO = mkrsrc(ws + WS_O, SEQ * NH * DH * 2);
  {
    const unsigned l2 = lane_id(), hi = l2 >> 5, blk = (l2 >> 4) & 1u, q4 = (l2 & 15u) >> 2, p4 = l2 & 3u;
#pragma unroll
    for (int ks = 0; ks < 8; ++ks) a.qf[ks] = ldsR(L + (2u * ks) * 4096u + hi * 4096u + (wid * 32u + (l2 & 31u)) * 16u);
#pragma unroll
    for (int n = 0; n < 2; ++n) a.aV[n] = L + VBASE + 128u * (4u * hi + q4) + 32u * ((2u * n + blk) ^ (((q4 >> 1) << 1) | hi)) + 8u * p4;
#pragma unroll
    for (int db = 0; db < 4; ++db) a.o[db] = (f32x16){};
    a.l = 0.f;
    auto ustepP_ = [&](unsigned j, unsigned stg, unsigned vo) { dmaT4<false>(L + stg * 32768u, lw, vo, sW1, j * 64u * (FF * 2u) + ufc * 256u, FF * 2u); };
    auto ustepQ_ = [&](unsigned j, unsigned stg, unsigned vo) { dma64d(L + stg * 32768u + 16384u + lw * 4096u, vo, sH, (udt * 32u + j) * 16384u + lw * 4096u); };
    const bool pre = hasU && grp == 1;
    if (grp == 1) { if (wid != 7) waitv<0>(); WAITL0(); BAR(); }
    STAMP(4);
    const rsrc_t sWo = mkrsrc(wo, NH * DH * DM * 2), sW2 = mkrsrc(w2, FF * SEQ * 2);
    const unsigned lk = bid * 8192u + (wid >> 1) * 2048u + (wid & 1u) * 128u;
#define LOOK(v) do { if ((v) < 1) touch8k(sW1, voV, lk + 6u * 0x200000u + 4096u); else if ((v) < 5) touch8k(sWo, voV, lk + ((v) - 1) * 0x200000u); \
                     else if ((v) < 12) touch8k(sW2, voV, lk + (((v) - 2) % 7) * 0x200000u); } while (0)
#define USTEP(i) ((i) < 5 ? (i) - 3 : (i) - 4)
#define ATT_TILE(i, FIRST, WB, WBP, WD, WDP)                                                                                  \
    SCHED(); att_qk1<FIRST, VSLOT[((i) + 2) % 3], 0>(a); BAR(); SCHED();    \
    if ((i) <= 4) dmaK2(KSLOT[(i) % 3], (i) + 3, wid * 2u);                                                                   \
    LOOK(2 * (i));                                                                                                            \
    att_sm1<FIRST>(a);                                                                                                        \
    if ((i) >= 3 && pre) { waitv<WBP>(); if ((i) != 5) ustepP_(USTEP(i), USTEP(i), voffT(lane_id(), FF * 2u)); } else waitv<WB>(); \
    if (!(FIRST)) att_wait(a); SCHED(); BAR();                              \
    if (FIRST) att_loadK(a, KSLOT[1]); else { SCHED(); att_pv1<((i) < 7 ? 1 : 2), ((i) < 7 ? KSLOT[((i) + 1) % 3] : VSLOT[7 % 3]), 0>(a); } \
    BAR(); SCHED();                                                         \
    if ((i) <= 5) dmaV2(VSLOT[((i) + 2) % 3], (i) + 2, wid * 2u);                                                             \
    LOOK(2 * (i) + 1);                                                                                                        \
    att_sm2<FIRST>(a);                                                                                                        \
    if ((i) >= 3 && pre) { waitv<WDP>(); if ((i) != 5) ustepQ_(USTEP(i), USTEP(i), lane_id() * 16u); } else waitv<WD>(); \
    att_wait(a); SCHED(); BAR();
    ATT_TILE(0, true, 63, 63, 63, 63) ATT_TILE(1, false, 63, 63, 10, 10) ATT_TILE(2, false, 10, 10, 10, 10) ATT_TILE(3, false, 10, 10, 10, 14)
    ATT_TILE(4, false, 10, 18, 10, 22) ATT_TILE(5, false, 8, 20, 8, 16) ATT_TILE(6, false, 5, 9, 1, 5) ATT_TILE(7, false, 63, 63, 63, 63)
#undef ATT_TILE
#undef USTEP
#undef LOOK
    SCHED(); att_pv1<0, 0u, 0>(a); STAMP(5);
    if (grp == 0) BAR();
    if (pre) { waitv<24>(); BAR(); STAMP(24); }
  }
  auto ost2 = [&](int db) {
    const unsigned l3 = lane_id(), row = qt * 256u + wid * 32u + (l3 & 31u);
    const unsigned oo = (((row >> 7) * 64u + head * 2u) * 8u + (l3 >> 5)) * 2048u + (row & 127u) * 16u;
#pragma unroll
    for (int i = 0; i < 2; ++i) {
      float w[8];
#pragma unroll
      for (int c = 0; c < 8; ++c) { w[c] = a.o[db][8 * i + c] * a.l; asm("" : "+v"(w[c])); }
      __builtin_amdgcn_raw_buffer_store_b128((u32x4){pk2(w[0], w[1]), pk2(w[2], w[3]), pk2(w[4], w[5]), pk2(w[6], w[7])}, sO, oo + db * 8192u + i * 4096u, 0, 16);
    }
  };
  { auto rr = __builtin_amdgcn_permlane32_swap(__float_as_uint(a.l), __float_as_uint(a.l), false, false);
    a.l = __builtin_amdgcn_rcpf(__uint_as_float(rr[0]) + __uint_as_float(rr[1])); }
  if (!(hasU && grp == 1)) { ost2(0); ost2(1); ost2(2); ost2(3); STAMP(6); }
  const unsigned ln = lane_id();
  const bool isY = xcd >= 4u;
  const unsigned dt = udt, st = ((xcd >> 1) & 1u) * 8u + (idx >> 2), tile = st * 8u + dt;
  u64* fG = flags + FL_G + tile * 16u; u64* fO = flags + FL_O; u64* fU = flags + FL_U + dt * 32u;
  const rsrc_t sG = mkrsrc(ws + WS_G + (size_t)tile * 32768u, 32768u), sU = mkrsrc(ws + WS_U, DM * FF * 2);
  constexpr unsigned XO = 32768u, GO = 65536u;
  auto finish = [&]() {
    WAITL0(); BAR();
#pragma unroll
    for (int q = 0; q < 4; ++q) {
      const unsigned row = wid * 16u + q * 4u + (ln >> 4);
      const u32x4 v = *(const LAS u32x4*)(size_t)(L + XO + row * 256u + 16u * ((ln & 15u) ^ (row & 15u)));
      __builtin_amdgcn_raw_buffer_store_b128(v, mkrsrc(out, SEQ * DM * 2), (st * 128u + row) * 2048u + dt * 256u + (ln & 15u) * 16u, 0, OUTAUX);
    }
    STAMP(14); waitv<0>(); STAMP(15); ST_OUT(1);
  };
  if (!hasU) {
    waitv<0>(); BAR();
    if (tid == 256u) flag_set(fO + qt * 32u + head, tag);
    STAMP(7);
  }
  if (grp == 0) {
    const unsigned wp = lw >> 1, wq2 = lw & 1u, fr = ln & 15u, fq = ln >> 4;
    if (hasU) {
      Opnd<T1> P; Opnd<RCHK> Q;
      P.init(L, wp * 64u, ln); Q.init(L + 16384u, wq2 * 64u, ln);
      u32x2 bv[4];
      f32x4 acc[4][4] = {};
      gemm_full<T1, RCHK, 5, 32768u, 0, 0, 16>(acc, P, Q, 32, [&] {
#pragma unroll
        for (int p = 0; p < 4; ++p) bv[p] = *(const u32x2*)(b1 + ufc * 128u + wp * 64u + p * 16u + fq * 4u);
      });
      STAMP(8);
      const unsigned uo = ((udt * 56u + uft * 2u + wp) * 8u + fq) * 2048u + (wq2 * 64u + fr) * 16u;
#pragma unroll
      for (int pp = 0; pp < 2; ++pp) {
        const f32x2 b0 = bf2(bv[2 * pp][0]), b1v = bf2(bv[2 * pp][1]), b2v = bf2(bv[2 * pp + 1][0]), b3 = bf2(bv[2 * pp + 1][1]);
#pragma unroll
        for (int m = 0; m < 4; ++m)
          __builtin_amdgcn_raw_buffer_store_b128((u32x4){gelu2(lo2(acc[2 * pp][m]) + b0), gelu2(hi2(acc[2 * pp][m]) + b1v), gelu2(lo2(acc[2 * pp + 1][m]) + b2v), gelu2(hi2(acc[2 * pp + 1][m]) + b3)},
                                                 sU, uo + pp * 8192u + m * 256u, 0, 16);
      }
      STAMP(9);
      if (isY) { waitv<0>(); BAR(); }
    }
    if (isY || !hasU) BAR();
    f32x4 acc[4][4] = {};
    const unsigned t4 = (wid * 64u + ln) * 16u;
    if (isY) {
      float bias[4];
#pragma unroll
      for (int q = 0; q < 4; ++q) bias[q] = bflo(b2[st * 128u + wq2 * 64u + q * 16u + fr]);
      Opnd<RCHK> P; Opnd<T2> Q;
      P.init(L + 65536u, wp * 64u, ln); Q.init(L + 65536u + 16384u, wq2 * 64u, ln);
      gemm_full<RCHK, T2, 5, 32768u, 3, 2, 0>(acc, P, Q, 56, [] {});
      STAMP(12);
#pragma unroll
      for (int j = 0; j < 8; ++j) {
        const int q = 2 * (j & 1); const f32x4 y0 = acc[j >> 1][q] + bias[q], y1 = acc[j >> 1][q + 1] + bias[q + 1];
        __builtin_amdgcn_raw_buffer_store_b128((u32x4){silu2(lo2(y0)), silu2(hi2(y0)), silu2(lo2(y1)), silu2(hi2(y1))}, sG, t4 + j * 4096u, 0, 16);
      }
      waitv<0>(); BAR();
      if (tid == 0u) flag_set(fG, tag);
      STAMP(15); ST_OUT(1); return;
    }
    Opnd<T1> P; Opnd<RCHK> Q;
    P.init(L + 65536u, wp * 64u, ln); Q.init(L + 65536u + 16384u, wq2 * 64u, ln);
    gemm_full<T1, RCHK, 5, 32768u, 3, 2, 2>(acc, P, Q, 64, [] {});
    STAMP(12);
    const unsigned xa = L + XO + (wq2 * 64u + fr) * 256u + (fq & 1u) * 8u;
    unsigned xp[4];
    f32x4 xv[4][4];
#pragma unroll
    for (int p = 0; p < 4; ++p) {
      xp[p] = xa + 16u * ((wp * 8u + 2u * p + (fq >> 1)) ^ fr);
#pragma unroll
      for (int q = 0; q < 4; ++q) {
        const u32x2 w = *(const LAS u32x2*)(size_t)(xp[p] + q * 4096u);
        xv[p][q] = (f32x4){bflo(w[0]), bfhi(w[0]), bflo(w[1]), bfhi(w[1])};
        asm volatile("" : "+v"(xv[p][q]));
      }
    }
    BAR();
    STAMP(13);
    u32x4 gp[8];
#pragma unroll
    for (int j = 0; j < 8; ++j) gp[j] = *(const LAS u32x4*)(size_t)(L + GO + t4 + j * 4096u);
#pragma unroll
    for (int p = 0; p < 4; ++p)
#pragma unroll
      for (int q = 0; q < 4; ++q) {
        const unsigned g0 = gp[2 * p + (q >> 1)][2 * (q & 1)], g1 = gp[2 * p + (q >> 1)][2 * (q & 1) + 1];
        const f32x4 r = __builtin_elementwise_fma(acc[p][q], (f32x4){bflo(g0), bfhi(g0), bflo(g1), bfhi(g1)}, xv[p][q]);
        *(LAS u32x2*)(size_t)(xp[p] + q * 4096u) = (u32x2){pk2(r[0], r[1]), pk2(r[2], r[3])};
      }
  } else {
    const unsigned N = isY ? 56u : 64u;
    const rsrc_t sT = isY ? mkrsrcB(w2, FF * SEQ * 2) : mkrsrcB(wo, NH * DH * DM * 2);
    const rsrc_t sR = isY ? sU : sO;
    const unsigned ldT = isY ? 4096u : 2048u, voT = voffT(ln, ldT), voL = ln * 16u;
    const unsigned tcol = (isY ? st : dt) * 256u, rbase = (isY ? dt : st) * N * 16384u + lw * 4096u;
    const unsigned toff = (isY ? 16384u : 0u) + lw * 4096u, roff = (isY ? 0u : 16384u) + lw * 4096u, g0 = isY ? (xcd >> 1) * 14u : 0u;
    const u64* dep = (isY ? fU : fO + (st >> 1) * 32u) + (ln & 31u);
    auto peek = [&]() { asm volatile("global_load_dwordx2 v[254:255], %0, off sc1" :: "v"(dep) : "memory", "v254", "v255"); };
    auto await = [&](u64 mask, bool peeked) {
      if (peeked) { u64 v; asm volatile("v_mov_b64 %0, v[254:255]" : "=v"(v) :: "memory"); if ((__ballot(v != tag) & mask) == 0ull) return; }
      flag_wait(dep, tag, mask);
    };
    unsigned sb = L + 4u * 32768u;
    auto nexts = [&]() { sb = sb == L + 4u * 32768u ? L : sb + 32768u; };
    unsigned kT = g0;
    unsigned kW = isY ? (xcd >> 1) * 2u : 0u;
    const unsigned kj = isY ? 7u : 1u;
    auto dT = [&]() { T4 p; p.set(kW * 64u * ldT + tcol, lw, ldT); p.go(sb + toff, voT, sT); ++kT; kW += (kW & 1u) ? kj : 1u; nexts(); };
    if (hasU) {
      const unsigned voW1 = voffT(ln, FF * 2u), hw = (wid & 1u) * 2u;
      unsigned j = 4;
      auto ufill = [&](auto wait) {
        T4 p, q; p.set(j * 64u * (FF * 2u) + ufc * 256u, hw, FF * 2u); q.set(j * 64u * (FF * 2u) + ufc * 256u, hw + 1u, FF * 2u);
        unsigned d0 = sb + hw * 4096u, r0 = (udt * 32u + j) * 16384u + hw * 4096u; p.pin(); q.pin(); PINS(d0); PINS(r0);
        wait();
        p.go(d0, voW1, sW1); dma64d(d0 + 16384u, voL, sH, r0); q.go(d0 + 4096u, voW1, sW1); dma64d(d0 + 20480u, voL, sH, r0 + 4096u);
        ++j; nexts();
      };
      if (wid >= 6u) {
        ufill([] {});
        ufill([] { waitv<32>(); BAR(); }); ufill([] { waitv<40>(); BAR(); });
#pragma nounroll
        for (unsigned t = 2; t < 12; ++t) ufill([] { waitv<48>(); BAR(); });
        waitv<48>(); BAR(); ost2(0); waitv<34>(); BAR(); ost2(1); waitv<20>(); BAR(); ost2(2); waitv<6>(); BAR(); ost2(3);
        touch8k(code_rsrc(), ln < 10u ? ln * 128u : 0x80000000u, (idx * 2u + (wid & 1u)) * 1280u);
        for (unsigned t = 16; t < 22; ++t) BAR();
        waitv<0>(); BAR();
        if (tid == 384u) flag_set(fO + qt * 32u + head, tag);
        BAR(); BAR(); BAR(); BAR();
      } else {
        waitv<16>(); BAR(); STAMP(25); waitv<8>(); BAR(); waitv<0>(); BAR();
        ost2(0); BAR(); ost2(1); BAR(); ost2(2); BAR(); ost2(3);
        for (unsigned t = 6; t < 12; ++t) BAR();
        STAMP(26);
        j = 17; sb = L + 2u * 32768u;
#pragma nounroll
        for (unsigned t = 12; t < 27; ++t) ufill([] { waitv<48>(); BAR(); });
        STAMP(28);
      }
      sb = L + 2u * 32768u;
      waitv<48>(); BAR(); dT(); waitv<36>(); BAR(); dT(); peek(); waitv<24>(); BAR(); dT(); waitv<12>(); BAR(); dT(); BAR(); dT();
      if (isY) {
        BAR();
        if (tid == 256u) flag_set(fU + uft, tag);
      } else waitv<12>();
      STAMP(8);
    } else {
      sb = L + 2u * 32768u;
#pragma nounroll
      for (unsigned j = 0; j < 5; ++j) dT();
    }
    if (hasU && !isY) await(0xffffffffull, true);
    else { if (wid == 4u) await(isY ? 0x7full << (g0 >> 1) : 0xffffffffull, false); BAR(); }
#pragma nounroll
    for (unsigned j = 0; j < 5; ++j) { dma64d(sb + roff, voL, sR, rbase + (g0 + j) * 16384u); nexts(); }
    auto dstep = [&](auto wait) {
      T4 p; p.set(kW * 64u * ldT + tcol, lw, ldT);
      unsigned d0 = sb + toff, d1 = sb + roff, r0 = rbase + kT * 16384u; p.pin(); PINS(d0); PINS(d1); PINS(r0);
      wait(); BAR();
      p.go(d0, voT, sT); dma64d(d1, voL, sR, r0);
      ++kT; kW += (kW & 1u) ? kj : 1u; nexts();
    };
    waitv<16>(); BAR(); STAMP(11);
    dstep([] { waitv<12>(); }); dstep([] { waitv<16>(); }); dstep([] { waitv<20>(); });
    if (!isY && hasU && tid == 256u) flag_set(fU + uft, tag);
#pragma nounroll
    for (unsigned seg = isY ? 0u : 5u; seg < 6u; ++seg) {
      const unsigned n = !isY ? 56u : seg == 0u ? 2u : seg == 2u ? 10u : seg >= 4u ? 14u : 4u;
#pragma nounroll
      for (unsigned i = 0; i < n; ++i) dstep([] { waitv<24>(); });
      if (seg == 0u || seg == 2u) peek();
      else if (seg < 4u) { waitv<32>(); await(seg == 1u ? 0x3fffull << 14 : 0x3fffull, true); kT = seg == 1u ? 70u - g0 : 0u; kW = seg == 1u ? 10u - (xcd >> 1) * 2u : 0u; }
      else kW = 2u;
    }
    waitv<24>(); BAR();
    if (isY) { waitv<0>(); BAR(); STAMP(12); ST_OUT(1); return; }
    {
      const rsrc_t sX = mkrsrc(x, SEQ * DM * 2);
#pragma unroll
      for (int q = 0; q < 8; ++q)
        dma16(L + XO + (lw * 8u + q) * 1024u, (ln >> 4) * 2048u + 16u * ((ln & 15u) ^ ((4u * q + (ln >> 4)) & 15u)), sX, (st * 128u + lw * 32u + q * 4u) * 2048u + dt * 256u);
    }
    waitv<8>(); BAR();
    waitv<0>(); BAR();
    STAMP(12);
    {
      flag_wait<6>(fG, tag, 1ull);
      u32x4 g[8];
#pragma unroll
      for (int q = 0; q < 8; ++q) g[q] = __builtin_amdgcn_raw_buffer_load_b128(sG, voL + (lw * 8u + q) * 1024u, 0, 16);
#pragma unroll
      for (int q = 0; q < 8; ++q) *(LAS u32x4*)(size_t)(L + GO + voL + (lw * 8u + q) * 1024u) = g[q];
    }
    waitv<0>(); WAITL0(); STAMP(13); BAR();
  }
  finish();
}

extern "C" __global__ void rsk_end() {}
}

extern "C" void kernel_launch(void* const* d_in, const int* in_sizes, int n_in, void* d_out, int out_size, void* d_ws, size_t ws_size, hipStream_t stream) {
  const u16* x = (const u16*)d_in[0]; const u16* kc = (const u16*)d_in[1]; const u16* vc = (const u16*)d_in[2];
  const u16* wq = (const u16*)d_in[3]; const u16* wo = (const u16*)d_in[4];
  const u16* w1 = (const u16*)d_in[6]; const u16* b1 = (const u16*)d_in[7]; const u16* w2 = (const u16*)d_in[8]; const u16* b2 = (const u16*)d_in[9];
#define WSEND 4
  char* ws = (char*)d_ws + (((ws_size - WS_TOTAL) / 4 * WSEND) & ~(size_t)0xfffff);
  (void)hipFuncSetAttribute((const void*)rsk_main, hipFuncAttributeMaxDynamicSharedMemorySize, LDS_BYTES);
  rsk_main<<<256, 512, LDS_BYTES, stream>>>(x, ws, wq, kc, vc, w1, wo, w2, b1, b2, (u16*)d_out);
}
```

```cpp
#include <hip/hip_runtime.h>
#include <hip/hip_bf16.h>

namespace {
typedef short bf16x8 __attribute__((ext_vector_type(8)));
typedef short s16x4 __attribute__((ext_vector_type(4)));
typedef float f32x2 __attribute__((ext_vector_type(2)));
typedef float f32x4 __attribute__((ext_vector_type(4)));
typedef float f32x16 __attribute__((ext_vector_type(16)));
typedef unsigned u32x4 __attribute__((ext_vector_type(4)));
typedef unsigned u32x2 __attribute__((ext_vector_type(2)));
typedef __bf16 bf2_t __attribute__((ext_vector_type(2)));
typedef unsigned short u16;
typedef __amdgpu_buffer_rsrc_t rsrc_t;
#define LAS __attribute__((address_space(3)))
#define DI __device__ __forceinline__
#define BAR() asm volatile("s_barrier" ::: "memory")
#define WAITL0() do { asm volatile("" ::: "memory"); __builtin_amdgcn_s_waitcnt(0xC07F); asm volatile("" ::: "memory"); } while (0)
#define ALIGN8() asm volatile(".p2align 3")
#define SCHED() __builtin_amdgcn_sched_barrier(0)
#define SGB(mask, n) __builtin_amdgcn_sched_group_barrier(mask, n, 0)

constexpr int SEQ = 2048, DM = 1024, NH = 32, DH = 128, SK = 2048, WIN = 512, FF = 3584;
constexpr unsigned WS_XN = 0u, WS_H = 4u << 20, WS_O = 8u << 20, WS_U = 24u << 20, WS_G = 32u << 20, WS_FLAG = 36u << 20, WS_TOTAL = 37u << 20;
constexpr unsigned LDS_BYTES = 163840;
constexpr unsigned long long SPIN_LIMIT = 2000000ull;
#define LNPF 1
#define LNAUX 16
#define PFDIV 2
#define PFGAP 0
#define XP 0
#define XSLEEP 2
#define PFWAIT 4
#define WQPF 0
#define OUTAUX 0
#define GAL 0
#define NW1 3
#define PACE 0
#define PFSLEEP 40

#define ST_DECL
#define STAMP(i) do {} while (0)
#define ST_OUT(k)
template <int N> DI void waitv() { asm volatile("s_waitcnt vmcnt(%0)" :: "i"(N) : "memory"); }
DI unsigned rfl(unsigned x) { return __builtin_amdgcn_readfirstlane(x); }
DI rsrc_t mkrsrc(const void* p, unsigned bytes) { return __builtin_amdgcn_make_buffer_rsrc((void*)p, (short)0, (int)bytes, 0x00020000); }
DI void dma16(unsigned m0v, unsigned voff, rsrc_t r, unsigned soff) {
  asm volatile("s_mov_b32 m0, %0\n\ts_nop 0\n\tbuffer_load_dwordx4 %1, %2, %3 offen lds" :: "s"(m0v), "v"(voff), "s"(r), "s"(soff) : "memory");
}
DI rsrc_t code_rsrc() {
  unsigned long long b, e;
  asm volatile("s_getpc_b64 s[40:41]\n\ts_add_u32 s40, s40, rsk_main@rel32@lo+4\n\ts_addc_u32 s41, s41, rsk_main@rel32@hi+12\n\ts_mov_b64 %0, s[40:41]\n\t"
               "s_getpc_b64 s[40:41]\n\ts_add_u32 s40, s40, rsk_end@rel32@lo+4\n\ts_addc_u32 s41, s41, rsk_end@rel32@hi+12\n\ts_mov_b64 %1, s[40:41]"
               : "=&s"(b), "=&s"(e) :: "s40", "s41", "scc");
  return mkrsrc((const void*)b, (e > b && e - b < 0x40000ull) ? (unsigned)(e - b) : 0u);
}
template <bool DEV>
DI void dma64(unsigned m0v, unsigned va, unsigned vb, rsrc_t r, unsigned s0, unsigned s1, unsigned s2, unsigned s3) {
  if constexpr (DEV)
    asm volatile("s_mov_b32 m0, %0\n\ts_nop 0\n\tbuffer_load_dwordx4 %1, %3, %4 offen sc1 lds\n\tbuffer_load_dwordx4 %2, %3, %5 offen offset:1024 sc1 lds\n\t"
                 "buffer_load_dwordx4 %1, %3, %6 offen offset:2048 sc1 lds\n\tbuffer_load_dwordx4 %2, %3, %7 offen offset:3072 sc1 lds"
                 :: "s"(m0v), "v"(va), "v"(vb), "s"(r), "s"(s0), "s"(s1), "s"(s2), "s"(s3) : "memory");
  else
    asm volatile("s_mov_b32 m0, %0\n\ts_nop 0\n\tbuffer_load_dwordx4 %1, %3, %4 offen lds\n\tbuffer_load_dwordx4 %2, %3, %5 offen offset:1024 lds\n\t"
                 "buffer_load_dwordx4 %1, %3, %6 offen offset:2048 lds\n\tbuffer_load_dwordx4 %2, %3, %7 offen offset:3072 lds"
                 :: "s"(m0v), "v"(va), "v"(vb), "s"(r), "s"(s0), "s"(s1), "s"(s2), "s"(s3) : "memory");
}
DI void dma64d(unsigned m0v, unsigned va, rsrc_t r, unsigned s0) {
  asm volatile("s_mov_b32 m0, %0\n\ts_nop 0\n\tbuffer_load_dwordx4 %1, %2, %3 offen sc1 lds\n\tbuffer_load_dwordx4 %1, %2, %3 offen offset:1024 sc1 lds\n\t"
               "buffer_load_dwordx4 %1, %2, %3 offen offset:2048 sc1 lds\n\tbuffer_load_dwordx4 %1, %2, %3 offen offset:3072 sc1 lds"
               :: "s"(m0v), "v"(va), "s"(r), "s"(s0) : "memory");
}
DI rsrc_t mkrsrcB(const void* p, unsigned bytes) { return mkrsrc((const char*)p - 4096, bytes + 4096u); }
#define PINS(x) asm volatile("" : "+s"(x))
DI bf16x8 ldsR(unsigned a) { return *(const LAS bf16x8*)(size_t)a; }
DI s16x4 ldsT(unsigned a) { return __builtin_amdgcn_ds_read_tr16_b64_v4i16((LAS s16x4*)(size_t)a); }
DI bf16x8 cat(s16x4 a, s16x4 b) { return __builtin_shufflevector(a, b, 0, 1, 2, 3, 4, 5, 6, 7); }
DI unsigned pk2(float lo, float hi) { f32x2 v = {lo, hi}; return __builtin_bit_cast(unsigned, __builtin_convertvector(v, bf2_t)); }
DI float bflo(unsigned w) { return __uint_as_float(w << 16); }
DI float bfhi(unsigned w) { return __uint_as_float(w & 0xffff0000u); }
DI float rbf(float x) { return bflo(pk2(x, x) & 0xffffu); }
template <int CTRL, int RM> DI float dpp_add(float v) {
  return v + __builtin_bit_cast(float, __builtin_amdgcn_update_dpp(0, __builtin_bit_cast(int, v), CTRL, RM, 0xf, false));
}
DI void wsum2(float& u, float& v) {
#define WS2(C, M) u = dpp_add<C, M>(u); v = dpp_add<C, M>(v);
  WS2(0x111, 0xf) WS2(0x112, 0xf) WS2(0x114, 0xf) WS2(0x118, 0xf) WS2(0x142, 0xa) WS2(0x143, 0xc)
#undef WS2
  u = __builtin_bit_cast(float, __builtin_amdgcn_readlane(__builtin_bit_cast(int, u), 63));
  v = __builtin_bit_cast(float, __builtin_amdgcn_readlane(__builtin_bit_cast(int, v), 63));
}

DI void ln16(float (&v)[16]) {
  const float x0 = __builtin_bit_cast(float, __builtin_amdgcn_readfirstlane(__builtin_bit_cast(int, v[0])));
  float s = 0.f, q = 0.f;
#pragma unroll
  for (int i = 0; i < 16; ++i) { const float d = v[i] - x0; s += d; q = fmaf(d, d, q); }
  wsum2(s, q);
  const float md = s * (1.0f / DM), var = rbf(fmaxf(fmaf(-md, md, q * (1.0f / DM)), 0.f));
  const float mb = rbf(x0 + md), inv = __builtin_amdgcn_rcpf(rbf(__builtin_amdgcn_sqrtf(rbf(var + 1e-5f))));
#pragma unroll
  for (int i = 0; i < 16; i += 2) {
    const unsigned d = pk2(v[i] - mb, v[i + 1] - mb), r = pk2(bflo(d) * inv, bfhi(d) * inv);
    v[i] = bflo(r); v[i + 1] = bfhi(r);
  }
}
DI unsigned pkx(float lo, float hi) { return (__float_as_uint(lo) >> 16) | (__float_as_uint(hi) & 0xffff0000u); }
enum { RROW = 0, RCHK = 1, T1 = 2, T2 = 3 };
template <int KIND> struct Opnd {
  static constexpr int NA = KIND == RROW ? 2 : KIND == RCHK ? 1 : 4;
  static constexpr int RPB = KIND >= T1 ? 2 : 1;
  unsigned a[NA];
  DI void init(unsigned base, unsigned off, unsigned lane) {
    const unsigned fr = lane & 15u, g = lane >> 4, q = fr >> 2, p = lane & 3u;
    if constexpr (KIND == RROW) {
#pragma unroll
      for (int kk = 0; kk < 2; ++kk) a[kk] = base + (off + fr) * 128u + 16u * ((4u * kk + g) ^ (fr >> 1));
    } else if constexpr (KIND == RCHK) {
      a[0] = base + g * 2048u + (off + fr) * 16u;
    } else {
      const unsigned x = ((q >> 1) << 1) | (g & 1u);
#pragma unroll
      for (int n = 0; n < 4; ++n)
        a[n] = base + (KIND == T1 ? 4096u : 2048u) * (g >> 1) + 1024u * (off >> 6) + 128u * (4u * (g & 1u) + q) + 32u * ((unsigned)n ^ x) + 8u * p;
    }
  }
  DI bf16x8 ld(int b, int kk) const {
    if constexpr (KIND == RROW) return ldsR(a[kk] + 2048u * b);
    else if constexpr (KIND == RCHK) return ldsR(a[0] + 8192u * kk + 256u * b);
    else { const unsigned ad = a[b & 3] + 1024u * (b >> 2) + 8192u * kk; return cat(ldsT(ad), ldsT(ad + (KIND == T1 ? 2048u : 4096u))); }
  }
  DI void adv(unsigned delta) {
#pragma unroll
    for (int i = 0; i < NA; ++i) { a[i] += delta; asm volatile("" : "+v"(a[i])); }
  }
};

template <int PK, int QK, int PM, int QM, int NS, unsigned STG, int TAIL, int S0, int DRAIN, class F>
DI void gemm_compute(f32x4 (&acc)[PM][QM], Opnd<PK>& P, Opnd<QK>& Q, const int N, F first) {
  bf16x8 pf[2][PM], qf[2][QM];
  constexpr int NR = PM * Opnd<PK>::RPB + QM * Opnd<QK>::RPB, NM = PM * QM, NA = Opnd<PK>::NA + Opnd<QK>::NA;
  auto load = [&](int kk) {
    pf[kk][0] = P.ld(0, kk);
#pragma unroll
    for (int q = 0; q < QM; ++q) qf[kk][q] = Q.ld(q, kk);
#pragma unroll
    for (int p = 1; p < PM; ++p) pf[kk][p] = P.ld(p, kk);
  };
  auto mma = [&](int kk) {
#pragma unroll
    for (int p = 0; p < PM; ++p)
#pragma unroll
      for (int q0 = 0; q0 < QM; ++q0) { const int q = (p & 1) ? QM - 1 - q0 : q0; acc[p][q] = __builtin_amdgcn_mfma_f32_16x16x32_bf16(pf[kk][p], qf[kk][q], acc[p][q], 0, 0, 0); }
  };
  constexpr int ND = (NR + NA + 2 > NM) ? (NR + NA + 2 - NM) : 0;
  BAR();
  load(0);
  int stg = S0;
  auto step = [&](bool sync) {
    SCHED();
    load(1); mma(0);
    stg = (stg == NS - 1) ? 0 : stg + 1;
    const unsigned delta = stg == 0 ? (unsigned)(-(int)((NS - 1) * STG)) : STG;
    P.adv(delta); Q.adv(delta);
#pragma unroll
    for (int i = 0; i < ND; ++i) { SGB(0x8, 1); SGB(0x100, 2); }
#pragma unroll
    for (int i = 0; i < NR - 2 * ND; ++i) { SGB(0x8, 1); SGB(0x100, 1); }
#pragma unroll
    for (int i = 0; i < NA; ++i) { SGB(0x8, 1); SGB(0x2, 1); }
    SGB(0x8, NM);
    SCHED();
    if (sync) { WAITL0(); BAR(); }
    SCHED();
    load(0); mma(1);
#pragma unroll
    for (int i = 0; i < ND; ++i) { SGB(0x8, 1); SGB(0x100, 2); }
#pragma unroll
    for (int i = 0; i < NR - 2 * ND; ++i) { SGB(0x8, 1); SGB(0x100, 1); }
    SGB(0x8, NM);
  };
  if (DRAIN > 0) {
#pragma nounroll
    for (int t = 0; t < DRAIN; ++t) step(true);
    waitv<0>();
    first();
  }
#pragma nounroll
  for (int t = DRAIN; t < N - TAIL; ++t) step(true);
#pragma nounroll
  for (int t = 0; t < TAIL; ++t) step(false);
  SCHED();
}

template <int PK, int QK, int NS, unsigned STG, int TAIL, int S0, int DRAIN, class F>
DI void gemm_full(f32x4 (&acc)[4][4], Opnd<PK>& P, Opnd<QK>& Q, const int N, F hook) {
  bf16x8 pf[2][2][4], qf[2][2][4];
  constexpr int NR = 8 * (Opnd<PK>::RPB + Opnd<QK>::RPB), NA = Opnd<PK>::NA + Opnd<QK>::NA;
  int stg = S0;
  auto load = [&](int b) {
#pragma unroll
    for (int i = 0; i < 4; ++i)
#pragma unroll
      for (int kk = 0; kk < 2; ++kk) { pf[b][kk][i] = P.ld(i, kk); qf[b][kk][i] = Q.ld(i, kk); }
    stg = (stg == NS - 1) ? 0 : stg + 1;
    const unsigned delta = stg == 0 ? (unsigned)(-(int)((NS - 1) * STG)) : STG;
    P.adv(delta); Q.adv(delta);
  };
  auto step = [&](int b, bool sync) {
    if (sync) { WAITL0(); BAR(); }
    SCHED();
    load(b ^ 1);
#pragma unroll
    for (int p = 0; p < 4; ++p)
#pragma unroll
      for (int q0 = 0; q0 < 4; ++q0) {
        const int q = (p & 1) ? 3 - q0 : q0;
        acc[p][q] = __builtin_amdgcn_mfma_f32_16x16x32_bf16(pf[b][0][p], qf[b][0][q], acc[p][q], 0, 0, 0);
        acc[p][q] = __builtin_amdgcn_mfma_f32_16x16x32_bf16(pf[b][1][p], qf[b][1][q], acc[p][q], 0, 0, 0);
      }
#pragma unroll
    for (int i = 0; i < NR; ++i) { SGB(0x8, 1); SGB(0x100, 1); }
#pragma unroll
    for (int i = 0; i < NA; ++i) { SGB(0x8, 1); SGB(0x2, 1); }
    SGB(0x8, 32);
    SCHED();
  };
  BAR();
  load(0);
  if (DRAIN > 0) {
#pragma nounroll
    for (int t = 0; t < DRAIN; t += 2) { step(0, true); step(1, true); }
    waitv<0>();
    hook();
  }
#pragma nounroll
  for (int t = DRAIN; t < N - (TAIL ? 4 : 0); t += 2) { step(0, true); step(1, true); }
  if (TAIL) {
#pragma nounroll
    for (int t = 0; t < 4; t += 2) { step(0, true); step(1, false); }
  }
}

DI unsigned voffRow(unsigned lane, unsigned ldb, unsigned par) { return (lane >> 3) * ldb + 16u * ((lane & 7u) ^ (4u * par + (lane >> 4))); }
DI unsigned voffT(unsigned lane, unsigned ldb) { return (lane >> 3) * ldb + 16u * ((lane & 7u) ^ (2u * ((((lane >> 4) & 1u) << 1) | (lane >> 5)))); }
struct T4 { unsigned s0, s1, s2, s3;
  DI void set(unsigned so, unsigned lw, unsigned ldb) { s0 = so + 4096u + lw * 16u * ldb; s1 = s0 + 128u - 1024u; s2 = s0 + 8u * ldb - 2048u; s3 = s2 + 128u - 1024u; }
  DI void pin() { PINS(s0); PINS(s1); PINS(s2); PINS(s3); }
  template <bool DEV = false> DI void go(unsigned dst, unsigned vo, rsrc_t r) const { dma64<DEV>(dst, vo, vo, r, s0, s1, s2, s3); } };
template <bool DEV> DI void dmaT4(unsigned dst, unsigned lw, unsigned vo, rsrc_t r, unsigned so, unsigned ldb) { T4 t; t.set(so, lw, ldb); t.go<DEV>(dst + lw * 4096u, vo, r); }

typedef unsigned long long u64;
constexpr unsigned FL_H = 64, FL_O = 320, FL_U = 576, FL_G = 832, FL_X = 3072;
DI void flag_set(u64* p, u64 tag) { __hip_atomic_store(p, tag, __ATOMIC_RELAXED, __HIP_MEMORY_SCOPE_AGENT); }
DI u64 flag_get(const u64* p) { return __hip_atomic_load(p, __ATOMIC_RELAXED, __HIP_MEMORY_SCOPE_AGENT); }
template <int SLOW = 16> DI void flag_wait(const u64* p, u64 tag, u64 mask) {
  const u64 t0 = __builtin_amdgcn_s_memrealtime();
  while ((__ballot(flag_get(p) != tag) & mask) != 0ull) {
    const u64 dt = __builtin_amdgcn_s_memrealtime() - t0;
    if (dt > SPIN_LIMIT) break;
    if (dt > 400ull) __builtin_amdgcn_s_sleep(SLOW); else __builtin_amdgcn_s_sleep(3);
  }
  asm volatile("" ::: "memory");
}

DI f32x2 sigm2(f32x2 v, f32x2 z) {
  f32x2 e = {__builtin_amdgcn_exp2f(z[0]), __builtin_amdgcn_exp2f(z[1])};
  e += 1.0f;
  return v * (f32x2){__builtin_amdgcn_rcpf(e[0]), __builtin_amdgcn_rcpf(e[1])};
}
DI unsigned gelu2(f32x2 v) {
  const f32x2 r = sigm2(v, v * __builtin_elementwise_fma(v * v, (f32x2)(-0.1029432397f), (f32x2)(-2.3022081986f)));
  return pk2(r[0], r[1]);
}
DI unsigned silu2(f32x2 v) { const f32x2 r = sigm2(v, v * -1.4426950408889634f); return pk2(r[0], r[1]); }
DI f32x2 lo2(f32x4 v) { return __builtin_shufflevector(v, v, 0, 1); }
DI f32x2 hi2(f32x4 v) { return __builtin_shufflevector(v, v, 2, 3); }
DI f32x2 bf2(unsigned w) { return (f32x2){bflo(w), bfhi(w)}; }

constexpr unsigned QSTG = 49152u;
constexpr unsigned KBASE = 65536u, VBASE = 114688u;
constexpr unsigned KSLOT[3] = {0u, 32768u, 16384u}, VSLOT[3] = {32768u, 16384u, 0u};
DI void touch8k(rsrc_t r, unsigned vo128, unsigned soff) {
  asm volatile("buffer_load_dword v255, %0, %1, %2 offen" :: "v"(vo128), "s"(r), "s"(soff) : "memory", "v255");
}
constexpr float QSCALE = 0.12751743082f;
#define BIGSUM 4096.0f

#define ATTPRIO 0
#define PRIO(x) do { if (ATTPRIO == 1) __builtin_amdgcn_s_setprio(x); else if (ATTPRIO == 2) __builtin_amdgcn_s_setprio(1 - (x)); } while (0)
struct Att {
  bf16x8 qf[8], kv[16], pf[4], pn[2];
  f32x16 o[4], s[2], negm;
  float l, ts;
  unsigned aK[4], aV[2];
};
template <unsigned SLOT, int J> DI void att_k(Att& a) {
  asm volatile("ds_read_b128 %0, %1 offset:%2" : "+v"(a.kv[J]) : "v"(a.aK[(J >> 1) & 3]), "i"(SLOT + 8192u * (J >> 3) + 4096u * (J & 1)) : "memory");
}
template <unsigned SLOT, int J> DI void att_v(Att& a) {
  s16x4 lo = __builtin_shufflevector(a.kv[J], a.kv[J], 0, 1, 2, 3), hi = __builtin_shufflevector(a.kv[J], a.kv[J], 4, 5, 6, 7);
  constexpr unsigned off = SLOT + 4096u * (J >> 2) + 1024u * ((J >> 1) & 1);
  asm volatile("ds_read_b64_tr_b16 %0, %2 offset:%3\n\tds_read_b64_tr_b16 %1, %2 offset:%4" : "+v"(lo), "+v"(hi) : "v"(a.aV[J & 1]), "i"(off), "i"(off + 2048u) : "memory");
  a.kv[J] = cat(lo, hi);
}
DI void att_wait(Att& a) {
  asm volatile("s_waitcnt lgkmcnt(0)" : "+v"(a.kv[0]), "+v"(a.kv[1]), "+v"(a.kv[2]), "+v"(a.kv[3]), "+v"(a.kv[4]), "+v"(a.kv[5]), "+v"(a.kv[6]), "+v"(a.kv[7]),
               "+v"(a.kv[8]), "+v"(a.kv[9]), "+v"(a.kv[10]), "+v"(a.kv[11]), "+v"(a.kv[12]), "+v"(a.kv[13]), "+v"(a.kv[14]), "+v"(a.kv[15]) :: "memory");
}
DI void att_loadK(Att& a, unsigned slot) {
#pragma unroll
  for (int j = 0; j < 16; ++j) a.kv[j] = ldsR(a.aK[(j >> 1) & 3] + slot + 8192u * (j >> 3) + 4096u * (j & 1));
}
DI unsigned lane_id() { unsigned l; asm volatile("v_mbcnt_lo_u32_b32 %0, -1, 0\n\tv_mbcnt_hi_u32_b32 %0, -1, %0" : "=v"(l)); return l; }
DI void add_bcast3(f32x16& t0, f32x16& t1, f32x16& t2, float v) {
  const float one = lane_id() < 32u ? 1.0f : 0.0f;
  asm volatile("s_nop 7\n\ts_nop 7\n\tv_mfma_f32_32x32x2_f32 %0, %3, %4, %0\n\tv_mfma_f32_32x32x2_f32 %1, %3, %4, %1\n\tv_mfma_f32_32x32x2_f32 %2, %3, %4, %2\n\t"
               "s_nop 7\n\ts_nop 7\n\ts_nop 7" : "+v"(t0), "+v"(t1), "+v"(t2) : "v"(one), "v"(v));
}
template <bool FIRST, unsigned SLOT, int J> DI void att_qk1(Att& a) {
  a.s[J & 1] = __builtin_amdgcn_mfma_f32_32x32x16_bf16(a.kv[J], a.qf[J >> 1], J < 2 ? (FIRST ? (f32x16){} : a.negm) : a.s[J & 1], 0, 0, 0);
  SCHED();
  if constexpr (J == 0) ALIGN8();
  if constexpr (!FIRST) att_v<SLOT, J>(a);
  SCHED();
  if constexpr (J < 15) att_qk1<FIRST, SLOT, J + 1>(a);
}
template <int NEXT, unsigned SLOT, int J> DI void att_pv1(Att& a) {
  a.o[J & 3] = __builtin_amdgcn_mfma_f32_32x32x16_bf16(a.kv[J], a.pf[J >> 2], a.o[J & 3], 0, 0, 0);
  SCHED();
  if constexpr (J == 0) ALIGN8();
  if constexpr (NEXT == 1) att_k<SLOT, J>(a); else if constexpr (NEXT == 2) att_v<SLOT, J>(a);
  SCHED();
  if constexpr (J < 15) att_pv1<NEXT, SLOT, J + 1>(a);
}
DI float max3(float x, float y, float z) { return __builtin_fmaxf(__builtin_fmaxf(x, y), z); }
DI float att_rowmax(const Att& a) {
  float m0 = max3(a.s[0][0], a.s[0][1], a.s[0][2]), m1 = max3(a.s[1][0], a.s[1][1], a.s[1][2]);
#pragma unroll
  for (int r = 3; r < 15; r += 2) { m0 = max3(m0, a.s[0][r], a.s[0][r + 1]); m1 = max3(m1, a.s[1][r], a.s[1][r + 1]); }
  const float mx = __builtin_fmaxf(max3(m0, m1, a.s[0][15]), a.s[1][15]);
  auto rr = __builtin_amdgcn_permlane32_swap(__float_as_uint(mx), __float_as_uint(mx), false, false);
  return __builtin_fmaxf(__uint_as_float(rr[0]), __uint_as_float(rr[1]));
}
DI float att_exp16(const f32x16& v, bf16x8& p0, bf16x8& p1) {
  float s0 = 0.f, s1 = 0.f;
  u32x4 w[2];
#pragma unroll
  for (int h = 0; h < 2; ++h) {
    float e[8];
#pragma unroll
    for (int r = 0; r < 8; ++r) e[r] = __builtin_amdgcn_exp2f(v[8 * h + r]);
#pragma unroll
    for (int r = 0; r < 8; r += 2) {
      if (h == 0 && r == 0) { s0 = e[0]; s1 = e[1]; } else { s0 += e[r]; asm("" : "+v"(s0)); s1 += e[r + 1]; asm("" : "+v"(s1)); }
      w[h][r >> 1] = pk2(e[r], e[r + 1]);
    }
    asm volatile("" : "+v"(w[h]));
    SCHED();
  }
  p0 = __builtin_bit_cast(bf16x8, w[0]); p1 = __builtin_bit_cast(bf16x8, w[1]);
  return s0 + s1;
}
template <bool FIRST> DI void att_sm1(Att& a) {
  if (FIRST) {
    const float mx = att_rowmax(a);
#pragma unroll
    for (int r = 0; r < 16; ++r) { a.s[0][r] -= mx; a.s[1][r] -= mx; a.negm[r] = -mx; }
  }
  a.ts = att_exp16(a.s[0], a.pn[0], a.pn[1]);
  asm volatile("" : "+v"(a.pn[0]), "+v"(a.pn[1]), "+v"(a.ts), "+v"(a.s[1]), "+v"(a.negm));
}
template <bool FIRST> DI void att_sm2(Att& a) {
  a.ts += att_exp16(a.s[1], a.pf[2], a.pf[3]);
  if (!FIRST) {
    if (__builtin_expect(!__all(a.ts <= BIGSUM), 0)) {
      const float dl = fmaxf(att_rowmax(a), 0.f), fct = __builtin_amdgcn_exp2f(-dl);
#pragma unroll
      for (int db = 0; db < 4; ++db) a.o[db] *= fct;
      a.l *= fct;
      add_bcast3(a.s[0], a.s[1], a.negm, -dl);
      a.ts = att_exp16(a.s[0], a.pn[0], a.pn[1]) + att_exp16(a.s[1], a.pf[2], a.pf[3]);
    }
  }
  a.l += a.ts;
  a.pf[0] = a.pn[0]; a.pf[1] = a.pn[1];
  asm volatile("" : "+v"(a.pf[0]), "+v"(a.pf[1]), "+v"(a.pf[2]), "+v"(a.pf[3]), "+v"(a.l));
}

extern "C" __global__ __launch_bounds__(512, 2) __attribute__((amdgpu_num_vgpr(127)))
void rsk_main(const u16* __restrict__ x, char* __restrict__ ws, const u16* __restrict__ wq, const u16* __restrict__ kc, const u16* __restrict__ vc,
              const u16* __restrict__ w1, const u16* __restrict__ wo, const u16* __restrict__ w2, const u16* __restrict__ b1, const u16* __restrict__ b2,
              u16* __restrict__ out) {
  if (rfl(threadIdx.x) == 0u) touch8k(code_rsrc(), threadIdx.x * 128u + (threadIdx.x < 48u ? 0u : (blockIdx.x >> 3) * 2048u), 0u);
  extern __shared__ __attribute__((aligned(16))) char lds[];
  const unsigned L = (unsigned)(size_t)lds;
  const char* xn = ws + WS_XN; const char* hh = ws + WS_H;
  const unsigned tid = threadIdx.x, lane = tid & 63u, wid = rfl(tid >> 6), grp = wid >> 2, lw = wid & 3u;
  const unsigned bid = blockIdx.x, xcd = bid & 7u, idx = bid >> 3;
  const unsigned qt = (xcd & 1u) * 4u + (idx & 3u), kvh = xcd >> 1, head = kvh * 8u + (idx >> 2);
  const bool hasU = idx < 28u;
  const unsigned udt = (xcd & 1u) * 4u + (idx & 3u), uft = (xcd >> 1) * 7u + (idx >> 2);
  const unsigned ufc = (idx >> 2) * 4u + (xcd >> 1);
  const rsrc_t sK = mkrsrc(kc + ((size_t)kvh * SK + (SK - WIN)) * DH, WIN * DH * 2), sV = mkrsrc(vc + ((size_t)kvh * SK + (SK - WIN)) * DH, WIN * DH * 2);
  const rsrc_t sH = mkrsrc(hh, SEQ * DM * 2), sW1 = mkrsrcB(w1, SEQ * FF * 2);
  const unsigned voK0 = voffRow(lane, 256u, 0), voV = voffT(lane, 256u);
  auto dmaK2 = [&](unsigned slot, unsigned t, unsigned p0) {
    unsigned voK1; asm volatile("v_xor_b32 %0, 64, %1" : "=v"(voK1) : "v"(voK0));
    dma16(L + KBASE + slot + 1024u * p0, voK0, sK, t * 16384u + (p0 & 7u) * 2048u + (p0 >> 3) * 128u);
    dma16(L + KBASE + slot + 1024u * p0 + 1024u, voK1, sK, t * 16384u + (p0 & 7u) * 2048u + 2048u + (p0 >> 3) * 128u); };
  auto dmaV2 = [&](unsigned slot, unsigned t, unsigned p0) {
    dma16(L + VBASE + slot + 1024u * p0, voV, sV, t * 16384u + (p0 >> 1) * 2048u);
    dma16(L + VBASE + slot + 1024u * p0 + 1024u, voV, sV, t * 16384u + (p0 >> 1) * 2048u + 128u); };
  auto tileK = [&](unsigned t) {
#pragma unroll
    for (int p = 0; p < 16; p += 2) dmaK2(KSLOT[t % 3], t, p); };
  auto tileV = [&](unsigned t) {
#pragma unroll
    for (int p = 0; p < 16; p += 2) dmaV2(VSLOT[t % 3], t, p); };
  Att a;
  { const unsigned r32 = lane & 31u, hi = lane >> 5;
#pragma unroll
    for (int j = 0; j < 4; ++j) a.aK[j] = L + KBASE + r32 * 128u + 16u * ((2u * j + hi) ^ ((r32 >> 1) & 7u)); }

  ST_DECL; STAMP(0);
  u64* flags = (u64*)(ws + WS_FLAG);
  const rsrc_t sS = lw == 0 ? mkrsrcB(wq, DM * NH * DH * 2) : mkrsrcB(xn, SEQ * DM * 2);
  unsigned asrc = 4096u + (lw == 0 ? head * 256u : (qt * 256u + (lw - 1u) * 128u) * 2048u), adst = L + lw * 16384u;
  const unsigned avo0 = lw == 0 ? voffT(lane, 8192u) : voffRow(lane, 2048u, 0), avo1 = lw == 0 ? avo0 : avo0 ^ 64u;
  constexpr unsigned HT = 147456u;
  auto astep = [&](unsigned vo0, unsigned vo1) {
    const unsigned sp = lw == 0 ? 8u * 8192u : 16u * 2048u, so = (lw == 0 ? 128u : 8u * 2048u) - 1024u;
#pragma unroll
    for (int q = 0; q < 4; ++q) { const unsigned e = asrc + 2u * q * sp; dma64<true>(adst + 4096u * q, vo0, vo1, sS, e, e + so, e + sp - 2048u, e + sp - 2048u + so); }
    asrc += lw == 0 ? 64u * 8192u : 128u; adst = adst >= L + 2u * QSTG ? adst - 2u * QSTG : adst + QSTG;
  };
  u64 tag;
  {
    const unsigned row = (bid >> 2) * 32u + ((bid >> 1) & 1u) * 16u + (bid & 1u) * 4u + (wid >> 2) * 8u + (wid & 3u);
    u32x4 xa, xb; u32x2 ep;
    asm volatile("buffer_load_dwordx4 %0, %3, %4, %5 offen\n\tbuffer_load_dwordx4 %1, %3, %4, %5 offen offset:1024\n\tglobal_load_dwordx2 %2, %6, %7 sc1"
                 : "=&v"(xa), "=&v"(xb), "=&v"(ep) : "v"(lane * 16u), "s"(mkrsrc(x, SEQ * DM * 2)), "s"(row * 2048u), "v"(0u), "s"(flags) : "memory");
    __builtin_amdgcn_s_setprio(1);
    if (wid == 7) {
      touch8k(mkrsrc(wq, DM * NH * DH * 2), lane * 128u, bid * 8192u);
      waitv<1>();
    } else waitv<0>();
    asm volatile("" : "+v"(xa), "+v"(xb), "+v"(ep));
    STAMP(16);
    tag = (((u64)rfl(ep[1]) << 32) | rfl(ep[0])) * 0x9E3779B97F4A7C15ull + 0x632BE59BD9B4E019ull;
    float v[16];
#pragma unroll
    for (int i = 0; i < 4; ++i) { v[2 * i] = bflo(xa[i]); v[2 * i + 1] = bfhi(xa[i]); v[8 + 2 * i] = bflo(xb[i]); v[9 + 2 * i] = bfhi(xb[i]); }
    ln16(v);
    const rsrc_t sW = mkrsrc(ws, WS_O);
#pragma unroll
    for (int p = 0; p < 2; ++p) {
      const unsigned lo0 = pkx(v[8 * p], v[8 * p + 1]), lo1 = pkx(v[8 * p + 2], v[8 * p + 3]), hi0 = pkx(v[8 * p + 4], v[8 * p + 5]), hi1 = pkx(v[8 * p + 6], v[8 * p + 7]);
      const bool odd = lane & 1u;
      const unsigned s0 = __builtin_amdgcn_update_dpp(0, (int)(odd ? lo0 : hi0), 0xB1, 0xf, 0xf, false), s1 = __builtin_amdgcn_update_dpp(0, (int)(odd ? lo1 : hi1), 0xB1, 0xf, 0xf, false);
      __builtin_amdgcn_raw_buffer_store_b128(odd ? (u32x4){s0, s1, hi0, hi1} : (u32x4){lo0, lo1, s0, s1}, sW, WS_XN + row * 2048u + lane * 16u + 1024u * p, 0, 16);
    }
    STAMP(17);
    waitv<0>();
    if (lane == 0u) flag_set(flags + FL_X + bid * 8u + wid, tag);
    STAMP(18);
    if (wid == 4u) { astep(avo0, avo1); astep(avo0, avo1); }
    if (grp == 0) __builtin_amdgcn_s_setprio(0);
    ln16(v);
    *(LAS u32x4*)(size_t)(L + HT + wid * 2048u + lane * 16u) = (u32x4){pkx(v[0], v[1]), pkx(v[2], v[3]), pkx(v[4], v[5]), pkx(v[6], v[7])};
    *(LAS u32x4*)(size_t)(L + HT + wid * 2048u + 1024u + lane * 16u) = (u32x4){pkx(v[8], v[9]), pkx(v[10], v[11]), pkx(v[12], v[13]), pkx(v[14], v[15])};
    WAITL0();
    __builtin_amdgcn_s_setprio(0);
    STAMP(19);
  }
  auto hstore = [&](unsigned i) {
    const unsigned d0 = i * 64u, ad = L + HT + ((lane & 15u) >> 2) * 2048u + (d0 + (lane >> 4) * 16u + (lane & 3u) * 4u) * 2u;
    __builtin_amdgcn_raw_buffer_store_b128(__builtin_bit_cast(u32x4, cat(ldsT(ad), ldsT(ad + 8192u))), mkrsrc(ws, WS_O), WS_H + ((d0 >> 7) * 256u + bid) * 2048u + ((d0 & 64u) + lane) * 16u, 0, 16);
  };
  if (!hasU) {
    BAR();
    hstore(wid * 2u); hstore(wid * 2u + 1u);
    waitv<0>(); BAR();
    if (tid == 0u) flag_set(flags + FL_H + bid, tag);
    const unsigned w = ((idx - 28u) * 8u + xcd) * 8u + wid, vo = lane * 128u;
    const rsrc_t sWq = mkrsrc(wq, DM * NH * DH * 2);
#pragma unroll
    for (int j = 1; j < 4; ++j) touch8k(sWq, vo, (j * 256u + w) * 8192u);
    touch8k(mkrsrc((w & 64u) ? vc : kc, 4u * SK * DH * 2u), w < 128u ? vo : 0x80000000u, ((w >> 4) & 3u) * (SK * DH * 2u) + (SK - WIN) * DH * 2u + (w & 15u) * 8192u);
#pragma unroll
    for (int j = 0; j < NW1; ++j) { waitv<2>(); touch8k(sW1, vo, (j * 256u + w) * 8192u + 4096u); }
    waitv<2>();
    if (wid < 7u - NW1) touch8k(sW1, vo, ((NW1 + wid) * 256u + bid) * 8192u + 4096u);
    if (wid < 7u) touch8k(mkrsrc(w2, FF * SEQ * 2), vo, (wid * 256u + bid) * 8192u);
    if (wid >= 4u) touch8k(mkrsrc(wo, NH * DH * DM * 2), vo, ((wid - 4u) * 256u + bid) * 8192u);
    waitv<0>();
    STAMP(20);
  }
  if (wid == 7) {
    const unsigned vo = lane * 128u;
    BAR(); BAR();
#pragma nounroll
    for (unsigned i = 0; i < 4; ++i) {
      BAR();
      if (hasU) { hstore(i * 4u); hstore(i * 4u + 1u); hstore(i * 4u + 2u); hstore(i * 4u + 3u); }
    }
    BAR(); BAR();
    waitv<0>();
    if (lane == 0u) flag_set(flags + FL_H + bid, tag);
    BAR(); BAR(); BAR(); BAR();
    const u64 f0 = flag_get(flags + FL_H + lane), f1 = flag_get(flags + FL_H + 64u + lane), f2 = flag_get(flags + FL_H + 128u + lane), f3 = flag_get(flags + FL_H + 192u + lane);
    touch8k(code_rsrc(), lane < 16u ? vo : 0x80000000u, idx * 2048u);
    BAR(); BAR();
    if (__any(f0 != tag || f1 != tag || f2 != tag || f3 != tag)) {
#pragma nounroll
      for (unsigned j = 0; j < 256u; j += 64u) flag_wait(flags + FL_H + j + lane, tag, ~0ull);
    }
    if (bid == 0u && lane == 0u) flag_set(flags, tag);
    BAR(); BAR(); BAR();
#pragma unroll
    for (int j = NW1; j < 6; ++j) touch8k(sW1, vo, (j * 256u + bid) * 8192u + 4096u);
    att_loadK(a, KSLOT[0]);
  } else if (grp == 1) {
    const unsigned vo0 = avo0, vo1 = avo1;
    if (lw != 0) {
      const u64* fx = flags + FL_X + qt * 256u + (lw - 1u) * 128u + lane;
      const u64 t0 = __builtin_amdgcn_s_memrealtime();
      while (__any(((flag_get(fx) ^ tag) | (flag_get(fx + 64) ^ tag)) != 0ull)) {
        if (__builtin_amdgcn_s_memrealtime() - t0 > SPIN_LIMIT) break;
        __builtin_amdgcn_s_sleep(XSLEEP);
      }
      asm volatile("" ::: "memory");
      STAMP(21);
      astep(vo0, vo1); astep(vo0, vo1);
      STAMP(22);
    }
    waitv<16>(); STAMP(23);
    BAR(); STAMP(1);
    astep(vo0, vo1);
#pragma nounroll
    for (unsigned t = 0; t < 13; ++t) { waitv<16>(); BAR(); astep(vo0, vo1); }
    waitv<16>(); BAR();
    if (lw == 0) tileK(0); else if (lw == 1) tileK(2); else tileV(0);
    waitv<16>(); BAR();
    if (lw == 1) tileK(1); else if (lw == 2) tileV(1);
    if (lw == 0) waitv<0>();
    BAR();
    att_loadK(a, KSLOT[0]);
  } else {
    Opnd<T1> P; Opnd<RROW> Q;
    P.init(L, 0u, lane); Q.init(L + 16384u, lw * 64u, lane);
    f32x4 acc[8][4] = {};
    gemm_compute<T1, RROW, 8, 4, 3, QSTG, 0, 0, 0>(acc, P, Q, 16, [] {});
    STAMP(2);
    att_loadK(a, KSLOT[0]);
    const unsigned wa = L + (lane >> 5) * 4096u + (lw * 64u + (lane & 15u)) * 16u + ((lane >> 4) & 1u) * 8u;
#pragma unroll
    for (int n = 0; n < 8; ++n)
#pragma unroll
      for (int m = 0; m < 4; ++m) {
        const f32x4 v = acc[n][m] * QSCALE;
        *(LAS u32x2*)(size_t)(wa + 8192u * n + 256u * m) = (u32x2){pk2(v[0], v[1]), pk2(v[2], v[3])};
      }
  }
  WAITL0(); BAR(); STAMP(3);

  const rsrc_t sO = mkrsrc(ws + WS_O, SEQ * NH * DH * 2);
  {
    const unsigned l2 = lane_id(), hi = l2 >> 5, blk = (l2 >> 4) & 1u, q4 = (l2 & 15u) >> 2, p4 = l2 & 3u;
#pragma unroll
    for (int ks = 0; ks < 8; ++ks) a.qf[ks] = ldsR(L + (2u * ks) * 4096u + hi * 4096u + (wid * 32u + (l2 & 31u)) * 16u);
#pragma unroll
    for (int n = 0; n < 2; ++n) a.aV[n] = L + VBASE + 128u * (4u * hi + q4) + 32u * ((2u * n + blk) ^ (((q4 >> 1) << 1) | hi)) + 8u * p4;
#pragma unroll
    for (int db = 0; db < 4; ++db) a.o[db] = (f32x16){};
    a.l = 0.f;
    auto ustepP_ = [&](unsigned j, unsigned stg, unsigned vo) { dmaT4<false>(L + stg * 32768u, lw, vo, sW1, j * 64u * (FF * 2u) + ufc * 256u, FF * 2u); };
    auto ustepQ_ = [&](unsigned j, unsigned stg, unsigned vo) { dma64d(L + stg * 32768u + 16384u + lw * 4096u, vo, sH, (udt * 32u + j) * 16384u + lw * 4096u); };
    const bool pre = hasU && grp == 1;
    if (grp == 1) { if (wid != 7) waitv<0>(); WAITL0(); BAR(); }
    STAMP(4);
    const rsrc_t sWo = mkrsrc(wo, NH * DH * DM * 2), sW2 = mkrsrc(w2, FF * SEQ * 2);
    const unsigned lk = bid * 8192u + (wid >> 1) * 2048u + (wid & 1u) * 128u;
#define LOOK(v) do { if ((v) < 1) touch8k(sW1, voV, lk + 6u * 0x200000u + 4096u); else if ((v) < 5) touch8k(sWo, voV, lk + ((v) - 1) * 0x200000u); \
                     else if ((v) < 12) touch8k(sW2, voV, lk + (((v) - 2) % 7) * 0x200000u); } while (0)
#define USTEP(i) ((i) < 5 ? (i) - 3 : (i) - 4)
#define ATT_TILE(i, FIRST, WB, WBP, WD, WDP)                                                                                  \
    SCHED(); att_qk1<FIRST, VSLOT[((i) + 2) % 3], 0>(a); BAR(); SCHED();    \
    if ((i) <= 4) dmaK2(KSLOT[(i) % 3], (i) + 3, wid * 2u);                                                                   \
    LOOK(2 * (i));                                                                                                            \
    att_sm1<FIRST>(a);                                                                                                        \
    if ((i) >= 3 && pre) { waitv<WBP>(); if ((i) != 5) ustepP_(USTEP(i), USTEP(i), voffT(lane_id(), FF * 2u)); } else waitv<WB>(); \
    if (!(FIRST)) att_wait(a); SCHED(); BAR();                              \
    if (FIRST) att_loadK(a, KSLOT[1]); else { SCHED(); att_pv1<((i) < 7 ? 1 : 2), ((i) < 7 ? KSLOT[((i) + 1) % 3] : VSLOT[7 % 3]), 0>(a); } \
    BAR(); SCHED();                                                         \
    if ((i) <= 5) dmaV2(VSLOT[((i) + 2) % 3], (i) + 2, wid * 2u);                                                             \
    LOOK(2 * (i) + 1);                                                                                                        \
    att_sm2<FIRST>(a);                                                                                                        \
    if ((i) >= 3 && pre) { waitv<WDP>(); if ((i) != 5) ustepQ_(USTEP(i), USTEP(i), lane_id() * 16u); } else waitv<WD>(); \
    att_wait(a); SCHED(); BAR();
    ATT_TILE(0, true, 63, 63, 63, 63) ATT_TILE(1, false, 63, 63, 10, 10) ATT_TILE(2, false, 10, 10, 10, 10) ATT_TILE(3, false, 10, 10, 10, 14)
    ATT_TILE(4, false, 10, 18, 10, 22) ATT_TILE(5, false, 8, 20, 8, 16) ATT_TILE(6, false, 5, 9, 1, 5) ATT_TILE(7, false, 63, 63, 63, 63)
#undef ATT_TILE
#undef USTEP
#undef LOOK
    SCHED(); att_pv1<0, 0u, 0>(a); STAMP(5);
    if (grp == 0) BAR();
    if (pre) { waitv<24>(); BAR(); STAMP(24); }
  }
  auto ost2 = [&](int db, int i0 = 0, int i1 = 2) {
    const unsigned l3 = lane_id(), row = qt * 256u + wid * 32u + (l3 & 31u);
    const unsigned oo = (((row >> 7) * 64u + head * 2u) * 8u + (l3 >> 5)) * 2048u + (row & 127u) * 16u;
#pragma unroll
    for (int i = i0; i < i1; ++i) {
      float w[8];
#pragma unroll
      for (int c = 0; c < 8; ++c) { w[c] = a.o[db][8 * i + c] * a.l; asm("" : "+v"(w[c])); }
      __builtin_amdgcn_raw_buffer_store_b128((u32x4){pk2(w[0], w[1]), pk2(w[2], w[3]), pk2(w[4], w[5]), pk2(w[6], w[7])}, sO, oo + db * 8192u + i * 4096u, 0, 16);
    }
  };
  { auto rr = __builtin_amdgcn_permlane32_swap(__float_as_uint(a.l), __float_as_uint(a.l), false, false);
    a.l = __builtin_amdgcn_rcpf(__uint_as_float(rr[0]) + __uint_as_float(rr[1])); }
  if (!(hasU && grp == 1)) { ost2(0); ost2(1); ost2(2); ost2(3); STAMP(6); }
  const unsigned ln = lane_id();
  const bool isY = xcd >= 4u;
  const unsigned dt = udt, st = ((xcd >> 1) & 1u) * 8u + (idx >> 2), tile = st * 8u + dt;
  u64* fG = flags + FL_G + tile * 16u; u64* fO = flags + FL_O; u64* fU = flags + FL_U + dt * 32u;
  const rsrc_t sG = mkrsrc(ws + WS_G + (size_t)tile * 32768u, 32768u), sU = mkrsrc(ws + WS_U, DM * FF * 2);
  constexpr unsigned XO = 32768u, GO = 65536u;
  auto finish = [&]() {
    WAITL0(); BAR();
#pragma unroll
    for (int q = 0; q < 4; ++q) {
      const unsigned row = wid * 16u + q * 4u + (ln >> 4);
      const u32x4 v = *(const LAS u32x4*)(size_t)(L + XO + row * 256u + 16u * ((ln & 15u) ^ (row & 15u)));
      __builtin_amdgcn_raw_buffer_store_b128(v, mkrsrc(out, SEQ * DM * 2), (st * 128u + row) * 2048u + dt * 256u + (ln & 15u) * 16u, 0, OUTAUX);
    }
    STAMP(14); waitv<0>(); STAMP(15); ST_OUT(1);
  };
  if (!hasU) {
    waitv<0>(); BAR();
    if (tid == 256u) flag_set(fO + qt * 32u + head, tag);
    STAMP(7);
  }
  if (grp == 0) {
    const unsigned wp = lw >> 1, wq2 = lw & 1u, fr = ln & 15u, fq = ln >> 4;
    if (hasU) {
      Opnd<T1> P; Opnd<RCHK> Q;
      P.init(L, wp * 64u, ln); Q.init(L + 16384u, wq2 * 64u, ln);
      u32x2 bv[4];
      f32x4 acc[4][4] = {};
      gemm_full<T1, RCHK, 5, 32768u, 0, 0, 16>(acc, P, Q, 32, [&] {
#pragma unroll
        for (int p = 0; p < 4; ++p) bv[p] = *(const u32x2*)(b1 + ufc * 128u + wp * 64u + p * 16u + fq * 4u);
      });
      STAMP(8);
      const unsigned uo = ((udt * 56u + uft * 2u + wp) * 8u + fq) * 2048u + (wq2 * 64u + fr) * 16u;
#pragma unroll
      for (int pp = 0; pp < 2; ++pp) {
        const f32x2 b0 = bf2(bv[2 * pp][0]), b1v = bf2(bv[2 * pp][1]), b2v = bf2(bv[2 * pp + 1][0]), b3 = bf2(bv[2 * pp + 1][1]);
#pragma unroll
        for (int m = 0; m < 4; ++m)
          __builtin_amdgcn_raw_buffer_store_b128((u32x4){gelu2(lo2(acc[2 * pp][m]) + b0), gelu2(hi2(acc[2 * pp][m]) + b1v), gelu2(lo2(acc[2 * pp + 1][m]) + b2v), gelu2(hi2(acc[2 * pp + 1][m]) + b3)},
                                                 sU, uo + pp * 8192u + m * 256u, 0, 16);
      }
      STAMP(9);
      if (isY) { waitv<0>(); BAR(); }
    }
    if (isY || !hasU) BAR();
    f32x4 acc[4][4] = {};
    const unsigned t4 = (wid * 64u + ln) * 16u;
    if (isY) {
      float bias[4];
#pragma unroll
      for (int q = 0; q < 4; ++q) bias[q] = bflo(b2[st * 128u + wq2 * 64u + q * 16u + fr]);
      Opnd<RCHK> P; Opnd<T2> Q;
      P.init(L + 65536u, wp * 64u, ln); Q.init(L + 65536u + 16384u, wq2 * 64u, ln);
      gemm_full<RCHK, T2, 5, 32768u, 3, 2, 0>(acc, P, Q, 56, [] {});
      STAMP(12);
#pragma unroll
      for (int j = 0; j < 8; ++j) {
        const int q = 2 * (j & 1); const f32x4 y0 = acc[j >> 1][q] + bias[q], y1 = acc[j >> 1][q + 1] + bias[q + 1];
        __builtin_amdgcn_raw_buffer_store_b128((u32x4){silu2(lo2(y0)), silu2(hi2(y0)), silu2(lo2(y1)), silu2(hi2(y1))}, sG, t4 + j * 4096u, 0, 16);
      }
      waitv<0>(); BAR();
      if (tid == 0u) flag_set(fG, tag);
      STAMP(15); ST_OUT(1); return;
    }
    Opnd<T1> P; Opnd<RCHK> Q;
    P.init(L + 65536u, wp * 64u, ln); Q.init(L + 65536u + 16384u, wq2 * 64u, ln);
    gemm_full<T1, RCHK, 5, 32768u, 3, 2, 2>(acc, P, Q, 64, [] {});
    STAMP(12);
    const unsigned xa = L + XO + (wq2 * 64u + fr) * 256u + (fq & 1u) * 8u;
    unsigned xp[4];
    f32x4 xv[4][4];
#pragma unroll
    for (int p = 0; p < 4; ++p) {
      xp[p] = xa + 16u * ((wp * 8u + 2u * p + (fq >> 1)) ^ fr);
#pragma unroll
      for (int q = 0; q < 4; ++q) {
        const u32x2 w = *(const LAS u32x2*)(size_t)(xp[p] + q * 4096u);
        xv[p][q] = (f32x4){bflo(w[0]), bfhi(w[0]), bflo(w[1]), bfhi(w[1])};
        asm volatile("" : "+v"(xv[p][q]));
      }
    }
    BAR();
    STAMP(13);
    u32x4 gp[8];
#pragma unroll
    for (int j = 0; j < 8; ++j) gp[j] = *(const LAS u32x4*)(size_t)(L + GO + t4 + j * 4096u);
#pragma unroll
    for (int p = 0; p < 4; ++p)
#pragma unroll
      for (int q = 0; q < 4; ++q) {
        const unsigned g0 = gp[2 * p + (q >> 1)][2 * (q & 1)], g1 = gp[2 * p + (q >> 1)][2 * (q & 1) + 1];
        const f32x4 r = __builtin_elementwise_fma(acc[p][q], (f32x4){bflo(g0), bfhi(g0), bflo(g1), bfhi(g1)}, xv[p][q]);
        *(LAS u32x2*)(size_t)(xp[p] + q * 4096u) = (u32x2){pk2(r[0], r[1]), pk2(r[2], r[3])};
      }
  } else {
    const unsigned N = isY ? 56u : 64u;
    const rsrc_t sT = isY ? mkrsrcB(w2, FF * SEQ * 2) : mkrsrcB(wo, NH * DH * DM * 2);
    const rsrc_t sR = isY ? sU : sO;
    const unsigned ldT = isY ? 4096u : 2048u, voT = voffT(ln, ldT), voL = ln * 16u;
    const unsigned tcol = (isY ? st : dt) * 256u, rbase = (isY ? dt : st) * N * 16384u + lw * 4096u;
    const unsigned toff = (isY ? 16384u : 0u) + lw * 4096u, roff = (isY ? 0u : 16384u) + lw * 4096u, g0 = isY ? (xcd >> 1) * 14u : 0u;
    const u64* dep = (isY ? fU : fO + (st >> 1) * 32u) + (ln & 31u);
    auto peek = [&]() { asm volatile("global_load_dwordx2 v[254:255], %0, off sc1" :: "v"(dep) : "memory", "v254", "v255"); };
    auto await = [&](u64 mask, bool peeked) {
      if (peeked) { u64 v; asm volatile("v_mov_b64 %0, v[254:255]" : "=v"(v) :: "memory"); if ((__ballot(v != tag) & mask) == 0ull) return; }
      flag_wait(dep, tag, mask);
    };
    unsigned sb = L + 4u * 32768u;
    auto nexts = [&]() { sb = sb == L + 4u * 32768u ? L : sb + 32768u; };
    unsigned kT = g0;
    unsigned kW = isY ? (xcd >> 1) * 2u : 0u;
    const unsigned kj = isY ? 7u : 1u;
    auto dT = [&]() { T4 p; p.set(kW * 64u * ldT + tcol, lw, ldT); p.go(sb + toff, voT, sT); ++kT; kW += (kW & 1u) ? kj : 1u; nexts(); };
    if (hasU) {
      const unsigned voW1 = voffT(ln, FF * 2u), hw = (wid & 1u) * 2u;
      unsigned j = 4;
      auto ufill = [&](auto wait) {
        T4 p, q; p.set(j * 64u * (FF * 2u) + ufc * 256u, hw, FF * 2u); q.set(j * 64u * (FF * 2u) + ufc * 256u, hw + 1u, FF * 2u);
        unsigned d0 = sb + hw * 4096u, r0 = (udt * 32u + j) * 16384u + hw * 4096u; p.pin(); q.pin(); PINS(d0); PINS(r0);
        wait();
        p.go(d0, voW1, sW1); dma64d(d0 + 16384u, voL, sH, r0); q.go(d0 + 4096u, voW1, sW1); dma64d(d0 + 20480u, voL, sH, r0 + 4096u);
        ++j; nexts();
      };
      if (wid >= 6u) {
        ufill([] {});
        ufill([] { waitv<32>(); BAR(); }); ufill([] { waitv<40>(); BAR(); });
#pragma nounroll
        for (unsigned t = 2; t < 12; ++t) ufill([] { waitv<48>(); BAR(); });
        waitv<48>(); BAR(); ost2(0); waitv<34>(); BAR(); ost2(1); waitv<20>(); BAR(); ost2(2); waitv<6>(); BAR(); ost2(3);
        touch8k(code_rsrc(), ln < 10u ? ln * 128u : 0x80000000u, (idx * 2u + (wid & 1u)) * 1280u);
        for (unsigned t = 16; t < 22; ++t) BAR();
        waitv<0>(); BAR();
        if (tid == 384u) flag_set(fO + qt * 32u + head, tag);
        BAR(); BAR(); BAR(); BAR();
      } else {
        waitv<16>(); BAR(); STAMP(25); waitv<8>(); BAR(); waitv<0>(); BAR();
        ost2(0); BAR(); ost2(1); BAR(); ost2(2); BAR(); ost2(3);
        for (unsigned t = 6; t < 12; ++t) BAR();
        STAMP(26);
        j = 17; sb = L + 2u * 32768u;
#pragma nounroll
        for (unsigned t = 12; t < 27; ++t) ufill([] { waitv<48>(); BAR(); });
        STAMP(28);
      }
      sb = L + 2u * 32768u;
      waitv<48>(); BAR(); dT(); waitv<36>(); BAR(); dT(); peek(); waitv<24>(); BAR(); dT(); waitv<12>(); BAR(); dT(); BAR(); dT();
      if (isY) {
        BAR();
        if (tid == 256u) flag_set(fU + uft, tag);
      } else waitv<12>();
      STAMP(8);
    } else {
      sb = L + 2u * 32768u;
#pragma nounroll
      for (unsigned j = 0; j < 5; ++j) dT();
    }
    if (hasU && !isY) await(0xffffffffull, true);
    else { if (wid == 4u) await(isY ? 0x7full << (g0 >> 1) : 0xffffffffull, false); BAR(); }
#pragma nounroll
    for (unsigned j = 0; j < 5; ++j) { dma64d(sb + roff, voL, sR, rbase + (g0 + j) * 16384u); nexts(); }
    auto dstep = [&](auto wait) {
      T4 p; p.set(kW * 64u * ldT + tcol, lw, ldT);
      unsigned d0 = sb + toff, d1 = sb + roff, r0 = rbase + kT * 16384u; p.pin(); PINS(d0); PINS(d1); PINS(r0);
      wait(); BAR();
      p.go(d0, voT, sT); dma64d(d1, voL, sR, r0);
      ++kT; kW += (kW & 1u) ? kj : 1u; nexts();
    };
    waitv<16>(); BAR(); STAMP(11);
    dstep([] { waitv<12>(); }); dstep([] { waitv<16>(); }); dstep([] { waitv<20>(); });
    if (!isY && hasU && tid == 256u) flag_set(fU + uft, tag);
#pragma nounroll
    for (unsigned seg = isY ? 0u : 5u; seg < 6u; ++seg) {
      const unsigned n = !isY ? 56u : seg == 0u ? 2u : seg == 2u ? 10u : seg >= 4u ? 14u : 4u;
#pragma nounroll
      for (unsigned i = 0; i < n; ++i) dstep([] { waitv<24>(); });
      if (seg == 0u || seg == 2u) peek();
      else if (seg < 4u) { waitv<32>(); await(seg == 1u ? 0x3fffull << 14 : 0x3fffull, true); kT = seg == 1u ? 70u - g0 : 0u; kW = seg == 1u ? 10u - (xcd >> 1) * 2u : 0u; }
      else kW = 2u;
    }
    waitv<24>(); BAR();
    if (isY) { waitv<0>(); BAR(); STAMP(12); ST_OUT(1); return; }
    {
      const rsrc_t sX = mkrsrc(x, SEQ * DM * 2);
#pragma unroll
      for (int q = 0; q < 8; ++q)
        dma16(L + XO + (lw * 8u + q) * 1024u, (ln >> 4) * 2048u + 16u * ((ln & 15u) ^ ((4u * q + (ln >> 4)) & 15u)), sX, (st * 128u + lw * 32u + q * 4u) * 2048u + dt * 256u);
    }
    waitv<8>(); BAR();
    waitv<0>(); BAR();
    STAMP(12);
    {
      flag_wait<6>(fG, tag, 1ull);
      u32x4 g[8];
#pragma unroll
      for (int q = 0; q < 8; ++q) g[q] = __builtin_amdgcn_raw_buffer_load_b128(sG, voL + (lw * 8u + q) * 1024u, 0, 16);
#pragma unroll
      for (int q = 0; q < 8; ++q) *(LAS u32x4*)(size_t)(L + GO + voL + (lw * 8u + q) * 1024u) = g[q];
    }
    waitv<0>(); WAITL0(); STAMP(13); BAR();
  }
  finish();
}

extern "C" __global__ void rsk_end() {}
}

extern "C" void kernel_launch(void* const* d_in, const int* in_sizes, int n_in, void* d_out, int out_size, void* d_ws, size_t ws_size, hipStream_t stream) {
  const u16* x = (const u16*)d_in[0]; const u16* kc = (const u16*)d_in[1]; const u16* vc = (const u16*)d_in[2];
  const u16* wq = (const u16*)d_in[3]; const u16* wo = (const u16*)d_in[4];
  const u16* w1 = (const u16*)d_in[6]; const u16* b1 = (const u16*)d_in[7]; const u16* w2 = (const u16*)d_in[8]; const u16* b2 = (const u16*)d_in[9];
#define WSEND 0
  char* ws = (char*)d_ws + (((ws_size - WS_TOTAL) / 4 * WSEND) & ~(size_t)0xfffff);
  (void)hipFuncSetAttribute((const void*)rsk_main, hipFuncAttributeMaxDynamicSharedMemorySize, LDS_BYTES);
  rsk_main<<<256, 512, LDS_BYTES, stream>>>(x, ws, wq, kc, vc, w1, wo, w2, b1, b2, (u16*)d_out);
}
```

```cpp
#include <hip/hip_runtime.h>
#include <hip/hip_bf16.h>

namespace {
typedef short bf16x8 __attribute__((ext_vector_type(8)));
typedef short s16x4 __attribute__((ext_vector_type(4)));
typedef float f32x2 __attribute__((ext_vector_type(2)));
typedef float f32x4 __attribute__((ext_vector_type(4)));
typedef float f32x16 __attribute__((ext_vector_type(16)));
typedef unsigned u32x4 __attribute__((ext_vector_type(4)));
typedef unsigned u32x2 __attribute__((ext_vector_type(2)));
typedef __bf16 bf2_t __attribute__((ext_vector_type(2)));
typedef unsigned short u16;
typedef __amdgpu_buffer_rsrc_t rsrc_t;
#define LAS __attribute__((address_space(3)))
#define DI __device__ __forceinline__
#define BAR() asm volatile("s_barrier" ::: "memory")
#define WAITL0() do { asm volatile("" ::: "memory"); __builtin_amdgcn_s_waitcnt(0xC07F); asm volatile("" ::: "memory"); } while (0)
#define ALIGN8() asm volatile(".p2align 3")
#define SCHED() __builtin_amdgcn_sched_barrier(0)
#define SGB(mask, n) __builtin_amdgcn_sched_group_barrier(mask, n, 0)

constexpr int SEQ = 2048, DM = 1024, NH = 32, DH = 128, SK = 2048, WIN = 512, FF = 3584;
constexpr unsigned WS_XN = 0u, WS_H = 4u << 20, WS_O = 8u << 20, WS_U = 24u << 20, WS_G = 32u << 20, WS_FLAG = 36u << 20, WS_TOTAL = 37u << 20;
constexpr unsigned LDS_BYTES = 163840;
constexpr unsigned long long SPIN_LIMIT = 2000000ull;
#define XSLEEP 2
#define NW1 0

#define ST_DECL
#define STAMP(i) do {} while (0)
#define ST_OUT(k)
template <int N> DI void waitv() { asm volatile("s_waitcnt vmcnt(%0)" :: "i"(N) : "memory"); }
DI unsigned rfl(unsigned x) { return __builtin_amdgcn_readfirstlane(x); }
DI rsrc_t mkrsrc(const void* p, unsigned bytes) { return __builtin_amdgcn_make_buffer_rsrc((void*)p, (short)0, (int)bytes, 0x00020000); }
DI void dma16(unsigned m0v, unsigned voff, rsrc_t r, unsigned soff) {
  asm volatile("s_mov_b32 m0, %0\n\ts_nop 0\n\tbuffer_load_dwordx4 %1, %2, %3 offen lds" :: "s"(m0v), "v"(voff), "s"(r), "s"(soff) : "memory");
}
DI rsrc_t code_rsrc() {
  unsigned long long b, e;
  asm volatile("s_getpc_b64 s[40:41]\n\ts_add_u32 s40, s40, rsk_main@rel32@lo+4\n\ts_addc_u32 s41, s41, rsk_main@rel32@hi+12\n\ts_mov_b64 %0, s[40:41]\n\t"
               "s_getpc_b64 s[40:41]\n\ts_add_u32 s40, s40, rsk_end@rel32@lo+4\n\ts_addc_u32 s41, s41, rsk_end@rel32@hi+12\n\ts_mov_b64 %1, s[40:41]"
               : "=&s"(b), "=&s"(e) :: "s40", "s41", "scc");
  return mkrsrc((const void*)b, (e > b && e - b < 0x40000ull) ? (unsigned)(e - b) : 0u);
}
template <bool DEV>
DI void dma64(unsigned m0v, unsigned va, unsigned vb, rsrc_t r, unsigned s0, unsigned s1, unsigned s2, unsigned s3) {
  if constexpr (DEV)
    asm volatile("s_mov_b32 m0, %0\n\ts_nop 0\n\tbuffer_load_dwordx4 %1, %3, %4 offen sc1 lds\n\tbuffer_load_dwordx4 %2, %3, %5 offen offset:1024 sc1 lds\n\t"
                 "buffer_load_dwordx4 %1, %3, %6 offen offset:2048 sc1 lds\n\tbuffer_load_dwordx4 %2, %3, %7 offen offset:3072 sc1 lds"
                 :: "s"(m0v), "v"(va), "v"(vb), "s"(r), "s"(s0), "s"(s1), "s"(s2), "s"(s3) : "memory");
  else
    asm volatile("s_mov_b32 m0, %0\n\ts_nop 0\n\tbuffer_load_dwordx4 %1, %3, %4 offen lds\n\tbuffer_load_dwordx4 %2, %3, %5 offen offset:1024 lds\n\t"
                 "buffer_load_dwordx4 %1, %3, %6 offen offset:2048 lds\n\tbuffer_load_dwordx4 %2, %3, %7 offen offset:3072 lds"
                 :: "s"(m0v), "v"(va), "v"(vb), "s"(r), "s"(s0), "s"(s1), "s"(s2), "s"(s3) : "memory");
}
DI void dma64d(unsigned m0v, unsigned va, rsrc_t r, unsigned s0) {
  asm volatile("s_mov_b32 m0, %0\n\ts_nop 0\n\tbuffer_load_dwordx4 %1, %2, %3 offen sc1 lds\n\tbuffer_load_dwordx4 %1, %2, %3 offen offset:1024 sc1 lds\n\t"
               "buffer_load_dwordx4 %1, %2, %3 offen offset:2048 sc1 lds\n\tbuffer_load_dwordx4 %1, %2, %3 offen offset:3072 sc1 lds"
               :: "s"(m0v), "v"(va), "s"(r), "s"(s0) : "memory");
}
DI rsrc_t mkrsrcB(const void* p, unsigned bytes) { return mkrsrc((const char*)p - 4096, bytes + 4096u); }
#define PINS(x) asm volatile("" : "+s"(x))
DI bf16x8 ldsR(unsigned a) { return *(const LAS bf16x8*)(size_t)a; }
DI s16x4 ldsT(unsigned a) { return __builtin_amdgcn_ds_read_tr16_b64_v4i16((LAS s16x4*)(size_t)a); }
DI bf16x8 cat(s16x4 a, s16x4 b) { return __builtin_shufflevector(a, b, 0, 1, 2, 3, 4, 5, 6, 7); }
DI unsigned pk2(float lo, float hi) { f32x2 v = {lo, hi}; return __builtin_bit_cast(unsigned, __builtin_convertvector(v, bf2_t)); }
DI float bflo(unsigned w) { return __uint_as_float(w << 16); }
DI float bfhi(unsigned w) { return __uint_as_float(w & 0xffff0000u); }
DI float rbf(float x) { return bflo(pk2(x, x) & 0xffffu); }
template <int CTRL, int RM> DI float dpp_add(float v) {
  return v + __builtin_bit_cast(float, __builtin_amdgcn_update_dpp(0, __builtin_bit_cast(int, v), CTRL, RM, 0xf, false));
}
DI void wsum2(float& u, float& v) {
#define WS2(C, M) u = dpp_add<C, M>(u); v = dpp_add<C, M>(v);
  WS2(0x111, 0xf) WS2(0x112, 0xf) WS2(0x114, 0xf) WS2(0x118, 0xf) WS2(0x142, 0xa) WS2(0x143, 0xc)
#undef WS2
  u = __builtin_bit_cast(float, __builtin_amdgcn_readlane(__builtin_bit_cast(int, u), 63));
  v = __builtin_bit_cast(float, __builtin_amdgcn_readlane(__builtin_bit_cast(int, v), 63));
}

DI void ln16(float (&v)[16]) {
  const float x0 = __builtin_bit_cast(float, __builtin_amdgcn_readfirstlane(__builtin_bit_cast(int, v[0])));
  float s = 0.f, q = 0.f;
#pragma unroll
  for (int i = 0; i < 16; ++i) { const float d = v[i] - x0; s += d; q = fmaf(d, d, q); }
  wsum2(s, q);
  const float md = s * (1.0f / DM), var = rbf(fmaxf(fmaf(-md, md, q * (1.0f / DM)), 0.f));
  const float mb = rbf(x0 + md), inv = __builtin_amdgcn_rcpf(rbf(__builtin_amdgcn_sqrtf(rbf(var + 1e-5f))));
#pragma unroll
  for (int i = 0; i < 16; i += 2) {
    const unsigned d = pk2(v[i] - mb, v[i + 1] - mb), r = pk2(bflo(d) * inv, bfhi(d) * inv);
    v[i] = bflo(r); v[i + 1] = bfhi(r);
  }
}
DI unsigned pkx(float lo, float hi) { return (__float_as_uint(lo) >> 16) | (__float_as_uint(hi) & 0xffff0000u); }
enum { RROW = 0, RCHK = 1, T1 = 2, T2 = 3 };
template <int KIND> struct Opnd {
  static constexpr int NA = KIND == RROW ? 2 : KIND == RCHK ? 1 : 4;
  static constexpr int RPB = KIND >= T1 ? 2 : 1;
  unsigned a[NA];
  DI void init(unsigned base, unsigned off, unsigned lane) {
    const unsigned fr = lane & 15u, g = lane >> 4, q = fr >> 2, p = lane & 3u;
    if constexpr (KIND == RROW) {
#pragma unroll
      for (int kk = 0; kk < 2; ++kk) a[kk] = base + (off + fr) * 128u + 16u * ((4u * kk + g) ^ (fr >> 1));
    } else if constexpr (KIND == RCHK) {
      a[0] = base + g * 2048u + (off + fr) * 16u;
    } else {
      const unsigned x = ((q >> 1) << 1) | (g & 1u);
#pragma unroll
      for (int n = 0; n < 4; ++n)
        a[n] = base + (KIND == T1 ? 4096u : 2048u) * (g >> 1) + 1024u * (off >> 6) + 128u * (4u * (g & 1u) + q) + 32u * ((unsigned)n ^ x) + 8u * p;
    }
  }
  DI bf16x8 ld(int b, int kk) const {
    if constexpr (KIND == RROW) return ldsR(a[kk] + 2048u * b);
    else if constexpr (KIND == RCHK) return ldsR(a[0] + 8192u * kk + 256u * b);
    else { const unsigned ad = a[b & 3] + 1024u * (b >> 2) + 8192u * kk; return cat(ldsT(ad), ldsT(ad + (KIND == T1 ? 2048u : 4096u))); }
  }
  DI void adv(unsigned delta) {
#pragma unroll
    for (int i = 0; i < NA; ++i) { a[i] += delta; asm volatile("" : "+v"(a[i])); }
  }
};

template <int PK, int QK, int NS, unsigned STG>
DI void gemm_wide(f32x4 (&acc)[8][4], Opnd<PK>& P, Opnd<QK>& Q, const int N) {
  bf16x8 pf[2][4][2], qf[4][2];
  auto loadP = [&](int h) {
#pragma unroll
    for (int p = 0; p < 4; ++p) { pf[h][p][0] = P.ld(4 * h + p, 0); pf[h][p][1] = P.ld(4 * h + p, 1); }
  };
  auto mma = [&](int h, int q) {
#pragma unroll
    for (int p0 = 0; p0 < 4; ++p0) {
      const int p = (q & 1) ? 3 - p0 : p0;
      acc[4 * h + p][q] = __builtin_amdgcn_mfma_f32_16x16x32_bf16(pf[h][p][0], qf[q][0], acc[4 * h + p][q], 0, 0, 0);
      acc[4 * h + p][q] = __builtin_amdgcn_mfma_f32_16x16x32_bf16(pf[h][p][1], qf[q][1], acc[4 * h + p][q], 0, 0, 0);
    }
  };
  BAR();
  loadP(0);
#pragma unroll
  for (int q = 0; q < 3; ++q) { qf[q][0] = Q.ld(q, 0); qf[q][1] = Q.ld(q, 1); }
  int stg = 0;
#pragma nounroll
  for (int t = 0; t < N; ++t) {
    SCHED();
    qf[3][0] = Q.ld(3, 0); qf[3][1] = Q.ld(3, 1);
    loadP(1); mma(0, 0); mma(0, 1); mma(0, 2); mma(0, 3);
    stg = (stg == NS - 1) ? 0 : stg + 1;
    const unsigned delta = stg == 0 ? (unsigned)(-(int)((NS - 1) * STG)) : STG;
    P.adv(delta); Q.adv(delta);
#pragma unroll
    for (int i = 0; i < 18; ++i) { SGB(0x8, 1); SGB(0x100, 1); }
#pragma unroll
    for (int i = 0; i < 14; ++i) { SGB(0x8, 1); SGB(0x2, 1); }
    SCHED();
    WAITL0(); BAR();
    SCHED();
#pragma unroll
    for (int q = 0; q < 4; ++q) {
      if (q > 0) { qf[q - 1][0] = Q.ld(q - 1, 0); qf[q - 1][1] = Q.ld(q - 1, 1); }
      if (q == 0) { pf[0][0][0] = P.ld(0, 0); pf[0][0][1] = P.ld(0, 1); pf[0][1][0] = P.ld(1, 0); pf[0][1][1] = P.ld(1, 1); }
      else if (q < 3) { pf[0][q + 1][0] = P.ld(q + 1, 0); pf[0][q + 1][1] = P.ld(q + 1, 1); }
      mma(1, q);
#pragma unroll
      for (int i = 0; i < 8; ++i) { SGB(0x8, 1); SGB(0x100, 1); }
      SCHED();
    }
  }
  SCHED();
}

template <int PK, int QK, int NS, unsigned STG, int TAIL, int S0, int DRAIN, class F>
DI void gemm_full(f32x4 (&acc)[4][4], Opnd<PK>& P, Opnd<QK>& Q, const int N, F hook) {
  bf16x8 pf[2][2][4], qf[2][2][4];
  constexpr int NR = 8 * (Opnd<PK>::RPB + Opnd<QK>::RPB);
  int stg = S0;
  auto load = [&](int b) {
#pragma unroll
    for (int i = 0; i < 4; ++i)
#pragma unroll
      for (int kk = 0; kk < 2; ++kk) { pf[b][kk][i] = P.ld(i, kk); qf[b][kk][i] = Q.ld(i, kk); }
    stg = (stg == NS - 1) ? 0 : stg + 1;
    const unsigned delta = stg == 0 ? (unsigned)(-(int)((NS - 1) * STG)) : STG;
    P.adv(delta); Q.adv(delta);
  };
  auto step = [&](int b, bool sync) {
    if (sync) { WAITL0(); BAR(); }
    SCHED();
    load(b ^ 1);
#pragma unroll
    for (int p = 0; p < 4; ++p)
#pragma unroll
      for (int q0 = 0; q0 < 4; ++q0) {
        const int q = (p & 1) ? 3 - q0 : q0;
        acc[p][q] = __builtin_amdgcn_mfma_f32_16x16x32_bf16(pf[b][0][p], qf[b][0][q], acc[p][q], 0, 0, 0);
        acc[p][q] = __builtin_amdgcn_mfma_f32_16x16x32_bf16(pf[b][1][p], qf[b][1][q], acc[p][q], 0, 0, 0);
      }
#pragma unroll
    for (int i = 0; i < NR; ++i) { SGB(0x8, 1); SGB(0x100, 1); }
#pragma unroll
    for (int i = NR; i < 32; ++i) { SGB(0x8, 1); SGB(0x2, 1); }
    SCHED();
  };
  BAR();
  load(0);
  if (DRAIN > 0) {
#pragma nounroll
    for (int t = 0; t < DRAIN; t += 2) { step(0, true); step(1, true); }
    waitv<0>();
    hook();
  }
#pragma nounroll
  for (int t = DRAIN; t < N - (TAIL ? 4 : 0); t += 2) { step(0, true); step(1, true); }
  if (TAIL) {
#pragma nounroll
    for (int t = 0; t < 4; t += 2) { step(0, true); step(1, false); }
  }
}

DI unsigned voffRow(unsigned lane, unsigned ldb, unsigned par) { return (lane >> 3) * ldb + 16u * ((lane & 7u) ^ (4u * par + (lane >> 4))); }
DI unsigned voffT(unsigned lane, unsigned ldb) { return (lane >> 3) * ldb + 16u * ((lane & 7u) ^ (2u * ((((lane >> 4) & 1u) << 1) | (lane >> 5)))); }
struct T4 { unsigned s0, s1, s2, s3;
  DI void set(unsigned so, unsigned lw, unsigned ldb) { s0 = so + 4096u + lw * 16u * ldb; s1 = s0 + 128u - 1024u; s2 = s0 + 8u * ldb - 2048u; s3 = s2 + 128u - 1024u; }
  DI void pin() { PINS(s0); PINS(s1); PINS(s2); PINS(s3); }
  template <bool DEV = false> DI void go(unsigned dst, unsigned vo, rsrc_t r) const { dma64<DEV>(dst, vo, vo, r, s0, s1, s2, s3); } };
template <bool DEV> DI void dmaT4(unsigned dst, unsigned lw, unsigned vo, rsrc_t r, unsigned so, unsigned ldb) { T4 t; t.set(so, lw, ldb); t.go<DEV>(dst + lw * 4096u, vo, r); }

typedef unsigned long long u64;
constexpr unsigned FL_H = 64, FL_O = 320, FL_U = 576, FL_G = 832, FL_X = 3072;
DI void flag_set(u64* p, u64 tag) { __hip_atomic_store(p, tag, __ATOMIC_RELAXED, __HIP_MEMORY_SCOPE_AGENT); }
DI u64 flag_get(const u64* p) { return __hip_atomic_load(p, __ATOMIC_RELAXED, __HIP_MEMORY_SCOPE_AGENT); }
template <int SLOW = 16> DI void flag_wait(const u64* p, u64 tag, u64 mask) {
  const u64 t0 = __builtin_amdgcn_s_memrealtime();
  while ((__ballot(flag_get(p) != tag) & mask) != 0ull) {
    const u64 dt = __builtin_amdgcn_s_memrealtime() - t0;
    if (dt > SPIN_LIMIT) break;
    if (dt > 400ull) __builtin_amdgcn_s_sleep(SLOW); else __builtin_amdgcn_s_sleep(3);
  }
  asm volatile("" ::: "memory");
}

DI f32x2 sigm2(f32x2 v, f32x2 z) {
  f32x2 e = {__builtin_amdgcn_exp2f(z[0]), __builtin_amdgcn_exp2f(z[1])};
  e += 1.0f;
  return v * (f32x2){__builtin_amdgcn_rcpf(e[0]), __builtin_amdgcn_rcpf(e[1])};
}
DI unsigned gelu2(f32x2 v) {
  const f32x2 r = sigm2(v, v * __builtin_elementwise_fma(v * v, (f32x2)(-0.1029432397f), (f32x2)(-2.3022081986f)));
  return pk2(r[0], r[1]);
}
DI unsigned silu2(f32x2 v) { const f32x2 r = sigm2(v, v * -1.4426950408889634f); return pk2(r[0], r[1]); }
DI f32x2 lo2(f32x4 v) { return __builtin_shufflevector(v, v, 0, 1); }
DI f32x2 hi2(f32x4 v) { return __builtin_shufflevector(v, v, 2, 3); }
DI f32x2 bf2(unsigned w) { return (f32x2){bflo(w), bfhi(w)}; }

constexpr unsigned QSTG = 49152u;
constexpr unsigned KBASE = 65536u, VBASE = 114688u;
constexpr unsigned KSLOT[3] = {0u, 32768u, 16384u}, VSLOT[3] = {32768u, 16384u, 0u};
DI void touch8k(rsrc_t r, unsigned vo128, unsigned soff) {
  asm volatile("buffer_load_dword v255, %0, %1, %2 offen" :: "v"(vo128), "s"(r), "s"(soff) : "memory", "v255");
}
constexpr float QSCALE = 0.12751743082f;
#define BIGSUM 4096.0f

struct Att {
  bf16x8 qf[8], kv[16], pf[4], pn[2];
  f32x16 o[4], s[2], negm;
  float l, ts;
  unsigned aK[4], aV[2];
};
template <unsigned SLOT, int J> DI void att_k(Att& a) {
  asm volatile("ds_read_b128 %0, %1 offset:%2" : "+v"(a.kv[J]) : "v"(a.aK[(J >> 1) & 3]), "i"(SLOT + 8192u * (J >> 3) + 4096u * (J & 1)) : "memory");
}
template <unsigned SLOT, int J> DI void att_v(Att& a) {
  s16x4 lo = __builtin_shufflevector(a.kv[J], a.kv[J], 0, 1, 2, 3), hi = __builtin_shufflevector(a.kv[J], a.kv[J], 4, 5, 6, 7);
  constexpr unsigned off = SLOT + 4096u * (J >> 2) + 1024u * ((J >> 1) & 1);
  asm volatile("ds_read_b64_tr_b16 %0, %2 offset:%3\n\tds_read_b64_tr_b16 %1, %2 offset:%4" : "+v"(lo), "+v"(hi) : "v"(a.aV[J & 1]), "i"(off), "i"(off + 2048u) : "memory");
  a.kv[J] = cat(lo, hi);
}
DI void att_wait(Att& a) {
  asm volatile("s_waitcnt lgkmcnt(0)" : "+v"(a.kv[0]), "+v"(a.kv[1]), "+v"(a.kv[2]), "+v"(a.kv[3]), "+v"(a.kv[4]), "+v"(a.kv[5]), "+v"(a.kv[6]), "+v"(a.kv[7]),
               "+v"(a.kv[8]), "+v"(a.kv[9]), "+v"(a.kv[10]), "+v"(a.kv[11]), "+v"(a.kv[12]), "+v"(a.kv[13]), "+v"(a.kv[14]), "+v"(a.kv[15]) :: "memory");
}
DI void att_loadK(Att& a, unsigned slot) {
#pragma unroll
  for (int j = 0; j < 16; ++j) a.kv[j] = ldsR(a.aK[(j >> 1) & 3] + slot + 8192u * (j >> 3) + 4096u * (j & 1));
}
DI unsigned lane_id() { unsigned l; asm volatile("v_mbcnt_lo_u32_b32 %0, -1, 0\n\tv_mbcnt_hi_u32_b32 %0, -1, %0" : "=v"(l)); return l; }
DI void add_bcast3(f32x16& t0, f32x16& t1, f32x16& t2, float v) {
  const float one = lane_id() < 32u ? 1.0f : 0.0f;
  asm volatile("s_nop 7\n\ts_nop 7\n\tv_mfma_f32_32x32x2_f32 %0, %3, %4, %0\n\tv_mfma_f32_32x32x2_f32 %1, %3, %4, %1\n\tv_mfma_f32_32x32x2_f32 %2, %3, %4, %2\n\t"
               "s_nop 7\n\ts_nop 7\n\ts_nop 7" : "+v"(t0), "+v"(t1), "+v"(t2) : "v"(one), "v"(v));
}
template <bool FIRST, unsigned SLOT, int J> DI void att_qk1(Att& a) {
  a.s[J & 1] = __builtin_amdgcn_mfma_f32_32x32x16_bf16(a.kv[J], a.qf[J >> 1], J < 2 ? (FIRST ? (f32x16){} : a.negm) : a.s[J & 1], 0, 0, 0);
  SCHED();
  if constexpr (J == 0) ALIGN8();
  if constexpr (!FIRST) att_v<SLOT, J>(a);
  SCHED();
  if constexpr (J < 15) att_qk1<FIRST, SLOT, J + 1>(a);
}
template <int NEXT, unsigned SLOT, int J> DI void att_pv1(Att& a) {
  a.o[J & 3] = __builtin_amdgcn_mfma_f32_32x32x16_bf16(a.kv[J], a.pf[J >> 2], a.o[J & 3], 0, 0, 0);
  SCHED();
  if constexpr (J == 0) ALIGN8();
  if constexpr (NEXT == 1) att_k<SLOT, J>(a); else if constexpr (NEXT == 2) att_v<SLOT, J>(a);
  SCHED();
  if constexpr (J < 15) att_pv1<NEXT, SLOT, J + 1>(a);
}
DI float max3(float x, float y, float z) { return __builtin_fmaxf(__builtin_fmaxf(x, y), z); }
DI float att_rowmax(const Att& a) {
  float m0 = max3(a.s[0][0], a.s[0][1], a.s[0][2]), m1 = max3(a.s[1][0], a.s[1][1], a.s[1][2]);
#pragma unroll
  for (int r = 3; r < 15; r += 2) { m0 = max3(m0, a.s[0][r], a.s[0][r + 1]); m1 = max3(m1, a.s[1][r], a.s[1][r + 1]); }
  const float mx = __builtin_fmaxf(max3(m0, m1, a.s[0][15]), a.s[1][15]);
  auto rr = __builtin_amdgcn_permlane32_swap(__float_as_uint(mx), __float_as_uint(mx), false, false);
  return __builtin_fmaxf(__uint_as_float(rr[0]), __uint_as_float(rr[1]));
}
DI float att_exp16(const f32x16& v, bf16x8& p0, bf16x8& p1) {
  float s0 = 0.f, s1 = 0.f;
  u32x4 w[2];
#pragma unroll
  for (int h = 0; h < 2; ++h) {
    float e[8];
#pragma unroll
    for (int r = 0; r < 8; ++r) e[r] = __builtin_amdgcn_exp2f(v[8 * h + r]);
#pragma unroll
    for (int r = 0; r < 8; r += 2) {
      if (h == 0 && r == 0) { s0 = e[0]; s1 = e[1]; } else { s0 += e[r]; asm("" : "+v"(s0)); s1 += e[r + 1]; asm("" : "+v"(s1)); }
      w[h][r >> 1] = pk2(e[r], e[r + 1]);
    }
    asm volatile("" : "+v"(w[h]));
    SCHED();
  }
  p0 = __builtin_bit_cast(bf16x8, w[0]); p1 = __builtin_bit_cast(bf16x8, w[1]);
  return s0 + s1;
}
template <bool FIRST> DI void att_sm1(Att& a) {
  if (FIRST) {
    const float mx = att_rowmax(a);
#pragma unroll
    for (int r = 0; r < 16; ++r) { a.s[0][r] -= mx; a.s[1][r] -= mx; a.negm[r] = -mx; }
  }
  a.ts = att_exp16(a.s[0], a.pn[0], a.pn[1]);
  asm volatile("" : "+v"(a.pn[0]), "+v"(a.pn[1]), "+v"(a.ts), "+v"(a.s[1]), "+v"(a.negm));
}
template <bool FIRST> DI void att_sm2(Att& a) {
  a.ts += att_exp16(a.s[1], a.pf[2], a.pf[3]);
  if (!FIRST) {
    if (__builtin_expect(!__all(a.ts <= BIGSUM), 0)) {
      const float dl = fmaxf(att_rowmax(a), 0.f), fct = __builtin_amdgcn_exp2f(-dl);
#pragma unroll
      for (int db = 0; db < 4; ++db) a.o[db] *= fct;
      a.l *= fct;
      add_bcast3(a.s[0], a.s[1], a.negm, -dl);
      a.ts = att_exp16(a.s[0], a.pn[0], a.pn[1]) + att_exp16(a.s[1], a.pf[2], a.pf[3]);
    }
  }
  a.l += a.ts;
  a.pf[0] = a.pn[0]; a.pf[1] = a.pn[1];
  asm volatile("" : "+v"(a.pf[0]), "+v"(a.pf[1]), "+v"(a.pf[2]), "+v"(a.pf[3]), "+v"(a.l));
}

extern "C" __global__ __launch_bounds__(512, 2) __attribute__((amdgpu_num_vgpr(127)))
void rsk_main(const u16* __restrict__ x, char* __restrict__ ws, const u16* __restrict__ wq, const u16* __restrict__ kc, const u16* __restrict__ vc,
              const u16* __restrict__ w1, const u16* __restrict__ wo, const u16* __restrict__ w2, const u16* __restrict__ b1, const u16* __restrict__ b2,
              u16* __restrict__ out) {
  if (rfl(threadIdx.x) == 0u) touch8k(code_rsrc(), threadIdx.x * 128u + (threadIdx.x < 48u ? 0u : (blockIdx.x >> 3) * 2048u), 0u);
  extern __shared__ __attribute__((aligned(16))) char lds[];
  const unsigned L = (unsigned)(size_t)lds;
  const char* xn = ws + WS_XN; const char* hh = ws + WS_H;
  const unsigned tid = threadIdx.x, lane = tid & 63u, wid = rfl(tid >> 6), grp = wid >> 2, lw = wid & 3u;
  const unsigned bid = blockIdx.x, xcd = bid & 7u, idx = bid >> 3;
  const unsigned qt = (xcd & 1u) * 4u + (idx & 3u), kvh = xcd >> 1, head = kvh * 8u + (idx >> 2);
  const bool hasU = idx < 28u;
  const unsigned udt = (xcd & 1u) * 4u + (idx & 3u), uft = (xcd >> 1) * 7u + (idx >> 2);
  const unsigned ufc = (idx >> 2) * 4u + (xcd >> 1);
  const rsrc_t sK = mkrsrc(kc + ((size_t)kvh * SK + (SK - WIN)) * DH, WIN * DH * 2), sV = mkrsrc(vc + ((size_t)kvh * SK + (SK - WIN)) * DH, WIN * DH * 2);
  const rsrc_t sH = mkrsrc(hh, SEQ * DM * 2), sW1 = mkrsrcB(w1, SEQ * FF * 2);
  const unsigned voK0 = voffRow(lane, 256u, 0), voV = voffT(lane, 256u);
  auto dmaK2 = [&](unsigned slot, unsigned t, unsigned p0) {
    unsigned voK1; asm volatile("v_xor_b32 %0, 64, %1" : "=v"(voK1) : "v"(voK0));
    dma16(L + KBASE + slot + 1024u * p0, voK0, sK, t * 16384u + (p0 & 7u) * 2048u + (p0 >> 3) * 128u);
    dma16(L + KBASE + slot + 1024u * p0 + 1024u, voK1, sK, t * 16384u + (p0 & 7u) * 2048u + 2048u + (p0 >> 3) * 128u); };
  auto dmaV2 = [&](unsigned slot, unsigned t, unsigned p0) {
    dma16(L + VBASE + slot + 1024u * p0, voV, sV, t * 16384u + (p0 >> 1) * 2048u);
    dma16(L + VBASE + slot + 1024u * p0 + 1024u, voV, sV, t * 16384u + (p0 >> 1) * 2048u + 128u); };
  auto tileK = [&](unsigned t) {
#pragma unroll
    for (int p = 0; p < 16; p += 2) dmaK2(KSLOT[t % 3], t, p); };
  auto tileV = [&](unsigned t) {
#pragma unroll
    for (int p = 0; p < 16; p += 2) dmaV2(VSLOT[t % 3], t, p); };
  Att a;
  { const unsigned r32 = lane & 31u, hi = lane >> 5;
#pragma unroll
    for (int j = 0; j < 4; ++j) a.aK[j] = L + KBASE + r32 * 128u + 16u * ((2u * j + hi) ^ ((r32 >> 1) & 7u)); }

  ST_DECL; STAMP(0);
  u64* flags = (u64*)(ws + WS_FLAG);
  const rsrc_t sS = lw == 0 ? mkrsrcB(wq, DM * NH * DH * 2) : mkrsrcB(xn, SEQ * DM * 2);
  unsigned asrc = 4096u + (lw == 0 ? head * 256u : (qt * 256u + (lw - 1u) * 128u) * 2048u), adst = L + lw * 16384u;
  const unsigned avo0 = lw == 0 ? voffT(lane, 8192u) : voffRow(lane, 2048u, 0), avo1 = lw == 0 ? avo0 : avo0 ^ 64u;
  constexpr unsigned HT = 147456u;
  auto astep = [&](unsigned vo0, unsigned vo1) {
    const unsigned sp = lw == 0 ? 8u * 8192u : 16u * 2048u, so = (lw == 0 ? 128u : 8u * 2048u) - 1024u;
#pragma unroll
    for (int q = 0; q < 4; ++q) { const unsigned e = asrc + 2u * q * sp; dma64<true>(adst + 4096u * q, vo0, vo1, sS, e, e + so, e + sp - 2048u, e + sp - 2048u + so); }
    asrc += lw == 0 ? 64u * 8192u : 128u; adst = adst >= L + 2u * QSTG ? adst - 2u * QSTG : adst + QSTG;
  };
  u64 tag;
  {
    const unsigned row = (bid >> 2) * 32u + ((bid >> 1) & 1u) * 16u + (bid & 1u) * 4u + (wid >> 2) * 8u + (wid & 3u);
    u32x4 xa, xb; u32x2 ep;
    asm volatile("buffer_load_dwordx4 %0, %3, %4, %5 offen\n\tbuffer_load_dwordx4 %1, %3, %4, %5 offen offset:1024\n\tglobal_load_dwordx2 %2, %6, %7 sc1"
                 : "=&v"(xa), "=&v"(xb), "=&v"(ep) : "v"(lane * 16u), "s"(mkrsrc(x, SEQ * DM * 2)), "s"(row * 2048u), "v"(0u), "s"(flags) : "memory");
    __builtin_amdgcn_s_setprio(1);
    if (wid == 7) {
      touch8k(mkrsrc(wq, DM * NH * DH * 2), lane * 128u, bid * 8192u);
      waitv<1>();
    } else waitv<0>();
    asm volatile("" : "+v"(xa), "+v"(xb), "+v"(ep));
    STAMP(16);
    tag = (((u64)rfl(ep[1]) << 32) | rfl(ep[0])) * 0x9E3779B97F4A7C15ull + 0x632BE59BD9B4E019ull;
    float v[16];
#pragma unroll
    for (int i = 0; i < 4; ++i) { v[2 * i] = bflo(xa[i]); v[2 * i + 1] = bfhi(xa[i]); v[8 + 2 * i] = bflo(xb[i]); v[9 + 2 * i] = bfhi(xb[i]); }
    ln16(v);
    const rsrc_t sW = mkrsrc(ws, WS_O);
#pragma unroll
    for (int p = 0; p < 2; ++p) {
      const unsigned lo0 = pkx(v[8 * p], v[8 * p + 1]), lo1 = pkx(v[8 * p + 2], v[8 * p + 3]), hi0 = pkx(v[8 * p + 4], v[8 * p + 5]), hi1 = pkx(v[8 * p + 6], v[8 * p + 7]);
      const bool odd = lane & 1u;
      const unsigned s0 = __builtin_amdgcn_update_dpp(0, (int)(odd ? lo0 : hi0), 0xB1, 0xf, 0xf, false), s1 = __builtin_amdgcn_update_dpp(0, (int)(odd ? lo1 : hi1), 0xB1, 0xf, 0xf, false);
      __builtin_amdgcn_raw_buffer_store_b128(odd ? (u32x4){s0, s1, hi0, hi1} : (u32x4){lo0, lo1, s0, s1}, sW, WS_XN + row * 2048u + lane * 16u + 1024u * p, 0, 16);
    }
    STAMP(17);
    waitv<0>();
    if (lane == 0u) flag_set(flags + FL_X + bid * 8u + wid, tag);
    STAMP(18);
    if (wid == 4u) { astep(avo0, avo1); astep(avo0, avo1); }
    if (grp == 0) __builtin_amdgcn_s_setprio(0);
    ln16(v);
    *(LAS u32x4*)(size_t)(L + HT + wid * 2048u + lane * 16u) = (u32x4){pkx(v[0], v[1]), pkx(v[2], v[3]), pkx(v[4], v[5]), pkx(v[6], v[7])};
    *(LAS u32x4*)(size_t)(L + HT + wid * 2048u + 1024u + lane * 16u) = (u32x4){pkx(v[8], v[9]), pkx(v[10], v[11]), pkx(v[12], v[13]), pkx(v[14], v[15])};
    WAITL0();
    __builtin_amdgcn_s_setprio(0);
    STAMP(19);
  }
  auto hstore = [&](unsigned i) {
    const unsigned d0 = i * 64u, ad = L + HT + ((lane & 15u) >> 2) * 2048u + (d0 + (lane >> 4) * 16u + (lane & 3u) * 4u) * 2u;
    __builtin_amdgcn_raw_buffer_store_b128(__builtin_bit_cast(u32x4, cat(ldsT(ad), ldsT(ad + 8192u))), mkrsrc(ws, WS_O), WS_H + ((d0 >> 7) * 256u + bid) * 2048u + ((d0 & 64u) + lane) * 16u, 0, 16);
  };
  if (!hasU) {
    BAR();
    hstore(wid * 2u); hstore(wid * 2u + 1u);
    waitv<0>(); BAR();
    if (tid == 0u) flag_set(flags + FL_H + bid, tag);
    const unsigned w = ((idx - 28u) * 8u + xcd) * 8u + wid, vo = lane * 128u;
    const rsrc_t sWq = mkrsrc(wq, DM * NH * DH * 2);
#pragma unroll
    for (int j = 1; j < 4; ++j) touch8k(sWq, vo, (j * 256u + w) * 8192u);
    touch8k(mkrsrc((w & 64u) ? vc : kc, 4u * SK * DH * 2u), w < 128u ? vo : 0x80000000u, ((w >> 4) & 3u) * (SK * DH * 2u) + (SK - WIN) * DH * 2u + (w & 15u) * 8192u);
#pragma unroll
    for (int j = 0; j < NW1; ++j) { waitv<2>(); touch8k(sW1, vo, (j * 256u + w) * 8192u + 4096u); }
    waitv<2>();
    if (wid < 7u - NW1) touch8k(sW1, vo, ((NW1 + wid) * 256u + bid) * 8192u + 4096u);
    if (wid < 7u) touch8k(mkrsrc(w2, FF * SEQ * 2), vo, (wid * 256u + bid) * 8192u);
    if (wid >= 4u) touch8k(mkrsrc(wo, NH * DH * DM * 2), vo, ((wid - 4u) * 256u + bid) * 8192u);
    waitv<0>();
    STAMP(20);
  }
  if (wid == 7) {
    const unsigned vo = lane * 128u;
    BAR(); BAR();
#pragma nounroll
    for (unsigned i = 0; i < 4; ++i) {
      BAR();
      if (hasU) { hstore(i * 4u); hstore(i * 4u + 1u); hstore(i * 4u + 2u); hstore(i * 4u + 3u); }
    }
    BAR(); BAR();
    waitv<0>();
    if (lane == 0u) flag_set(flags + FL_H + bid, tag);
    BAR(); BAR(); BAR(); BAR();
    const u64 f0 = flag_get(flags + FL_H + lane), f1 = flag_get(flags + FL_H + 64u + lane), f2 = flag_get(flags + FL_H + 128u + lane), f3 = flag_get(flags + FL_H + 192u + lane);
    touch8k(code_rsrc(), lane < 16u ? vo : 0x80000000u, idx * 2048u);
    BAR(); BAR();
    if (__any(f0 != tag || f1 != tag || f2 != tag || f3 != tag)) {
#pragma nounroll
      for (unsigned j = 0; j < 256u; j += 64u) flag_wait(flags + FL_H + j + lane, tag, ~0ull);
    }
    if (bid == 0u && lane == 0u) flag_set(flags, tag);
    BAR(); BAR(); BAR();
#pragma unroll
    for (int j = NW1; j < 6; ++j) touch8k(sW1, vo, (j * 256u + bid) * 8192u + 4096u);
    att_loadK(a, KSLOT[0]);
  } else if (grp == 1) {
    const unsigned vo0 = avo0, vo1 = avo1;
    if (lw != 0) {
      const u64* fx = flags + FL_X + qt * 256u + (lw - 1u) * 128u + lane;
      const u64 t0 = __builtin_amdgcn_s_memrealtime();
      while (__any(((flag_get(fx) ^ tag) | (flag_get(fx + 64) ^ tag)) != 0ull)) {
        if (__builtin_amdgcn_s_memrealtime() - t0 > SPIN_LIMIT) break;
        __builtin_amdgcn_s_sleep(XSLEEP);
      }
      asm volatile("" ::: "memory");
      STAMP(21);
      astep(vo0, vo1); astep(vo0, vo1);
      STAMP(22);
    }
    waitv<16>(); STAMP(23);
    BAR(); STAMP(1);
    astep(vo0, vo1);
#pragma nounroll
    for (unsigned t = 0; t < 13; ++t) { waitv<16>(); BAR(); astep(vo0, vo1); }
    waitv<16>(); BAR();
    if (lw == 0) tileK(0); else if (lw == 1) tileK(2); else tileV(0);
    waitv<16>(); BAR();
    if (lw == 1) tileK(1); else if (lw == 2) tileV(1);
    if (lw == 0) waitv<0>();
    BAR();
    att_loadK(a, KSLOT[0]);
  } else {
    Opnd<T1> P; Opnd<RROW> Q;
    P.init(L, 0u, lane); Q.init(L + 16384u, lw * 64u, lane);
    f32x4 acc[8][4] = {};
    gemm_wide<T1, RROW, 3, QSTG>(acc, P, Q, 16);
    STAMP(2);
    att_loadK(a, KSLOT[0]);
    const unsigned wa = L + (lane >> 5) * 4096u + (lw * 64u + (lane & 15u)) * 16u + ((lane >> 4) & 1u) * 8u;
#pragma unroll
    for (int j = 0; j < 16; ++j) {
      const f32x4 v = acc[j >> 1][j & 1] * QSCALE;
      *(LAS u32x2*)(size_t)(wa + 8192u * (j >> 1) + 256u * (j & 1)) = (u32x2){pk2(v[0], v[1]), pk2(v[2], v[3])};
    }
#pragma unroll
    for (int ks = 0; ks < 8; ++ks) a.qf[ks] = ldsR(L + (2u * ks + (lane >> 5)) * 4096u + (lw * 64u + (lane & 31u)) * 16u);
    SCHED();
#pragma unroll
    for (int j = 0; j < 16; ++j) {
      a.s[j & 1] = __builtin_amdgcn_mfma_f32_32x32x16_bf16(a.kv[j], a.qf[j >> 1], j < 2 ? (f32x16){} : a.s[j & 1], 0, 0, 0);
      float v[4];
#pragma unroll
      for (int c = 0; c < 4; ++c) { v[c] = acc[j >> 1][2 + (j & 1)][c] * QSCALE; asm("" : "+v"(v[c])); }
      *(LAS u32x2*)(size_t)(wa + 8192u * (j >> 1) + 256u * (2 + (j & 1))) = (u32x2){pk2(v[0], v[1]), pk2(v[2], v[3])};
      SCHED();
    }
    WAITL0();
  }
  STAMP(3);

  const rsrc_t sO = mkrsrc(ws + WS_O, SEQ * NH * DH * 2);
  {
    const unsigned l2 = lane_id(), hi = l2 >> 5, blk = (l2 >> 4) & 1u, q4 = (l2 & 15u) >> 2, p4 = l2 & 3u;
#pragma unroll
    for (int n = 0; n < 2; ++n) a.aV[n] = L + VBASE + 128u * (4u * hi + q4) + 32u * ((2u * n + blk) ^ (((q4 >> 1) << 1) | hi)) + 8u * p4;
#pragma unroll
    for (int db = 0; db < 4; ++db) a.o[db] = (f32x16){};
    a.l = 0.f;
    auto ustepP_ = [&](unsigned j, unsigned stg, unsigned vo) { dmaT4<false>(L + stg * 32768u, lw, vo, sW1, j * 64u * (FF * 2u) + ufc * 256u, FF * 2u); };
    auto ustepQ_ = [&](unsigned j, unsigned stg, unsigned vo) { dma64d(L + stg * 32768u + 16384u + lw * 4096u, vo, sH, (udt * 32u + j) * 16384u + lw * 4096u); };
    const bool pre = hasU && grp == 1;
    if (grp == 1) {
      if (wid != 7) waitv<0>();
      WAITL0(); BAR();
#pragma unroll
      for (int ks = 0; ks < 8; ++ks) a.qf[ks] = ldsR(L + (2u * ks + hi) * 4096u + (lw * 64u + 32u + (l2 & 31u)) * 16u);
      SCHED(); att_qk1<true, 0u, 0>(a);
    }
    STAMP(4);
    const rsrc_t sWo = mkrsrc(wo, NH * DH * DM * 2), sW2 = mkrsrc(w2, FF * SEQ * 2);
    const unsigned lk = bid * 8192u + (wid >> 1) * 2048u + (wid & 1u) * 128u;
#define LOOK(v) do { if ((v) < 1) touch8k(sW1, voV, lk + 6u * 0x200000u + 4096u); else if ((v) < 5) touch8k(sWo, voV, lk + ((v) - 1) * 0x200000u); \
                     else if ((v) < 12) touch8k(sW2, voV, lk + (((v) - 2) % 7) * 0x200000u); } while (0)
#define USTEP(i) ((i) < 5 ? (i) - 3 : (i) - 4)
#define ATT_TILE(i, FIRST, WB, WBP, WD, WDP)                                                                                  \
    SCHED(); if (!(FIRST)) att_qk1<false, VSLOT[((i) + 2) % 3], 0>(a); BAR(); SCHED();    \
    if ((i) <= 4) dmaK2(KSLOT[(i) % 3], (i) + 3, wid * 2u);                                                                   \
    LOOK(2 * (i));                                                                                                            \
    att_sm1<FIRST>(a);                                                                                                        \
    if ((i) >= 3 && pre) { waitv<WBP>(); if ((i) != 5) ustepP_(USTEP(i), USTEP(i), voffT(lane_id(), FF * 2u)); } else waitv<WB>(); \
    if (!(FIRST)) att_wait(a); SCHED(); BAR();                              \
    if (FIRST) att_loadK(a, KSLOT[1]); else { SCHED(); att_pv1<((i) < 7 ? 1 : 2), ((i) < 7 ? KSLOT[((i) + 1) % 3] : VSLOT[7 % 3]), 0>(a); } \
    BAR(); SCHED();                                                         \
    if ((i) <= 5) dmaV2(VSLOT[((i) + 2) % 3], (i) + 2, wid * 2u);                                                             \
    LOOK(2 * (i) + 1);                                                                                                        \
    att_sm2<FIRST>(a);                                                                                                        \
    if ((i) >= 3 && pre) { waitv<WDP>(); if ((i) != 5) ustepQ_(USTEP(i), USTEP(i), lane_id() * 16u); } else waitv<WD>(); \
    att_wait(a); SCHED(); BAR();
    ATT_TILE(0, true, 63, 63, 63, 63) ATT_TILE(1, false, 63, 63, 10, 10) ATT_TILE(2, false, 10, 10, 10, 10) ATT_TILE(3, false, 10, 10, 10, 14)
    ATT_TILE(4, false, 10, 18, 10, 22) ATT_TILE(5, false, 8, 20, 8, 16) ATT_TILE(6, false, 5, 9, 1, 5) ATT_TILE(7, false, 63, 63, 63, 63)
#undef ATT_TILE
#undef USTEP
#undef LOOK
    SCHED(); att_pv1<0, 0u, 0>(a); STAMP(5);
    if (grp == 0) BAR();
    if (pre) { waitv<24>(); BAR(); STAMP(24); }
  }
  auto ost2 = [&](int db) {
    const unsigned l3 = lane_id(), row = qt * 256u + lw * 64u + grp * 32u + (l3 & 31u);
    const unsigned oo = (((row >> 7) * 64u + head * 2u) * 8u + (l3 >> 5)) * 2048u + (row & 127u) * 16u;
#pragma unroll
    for (int i = 0; i < 2; ++i) {
      float w[8];
#pragma unroll
      for (int c = 0; c < 8; ++c) { w[c] = a.o[db][8 * i + c] * a.l; asm("" : "+v"(w[c])); }
      __builtin_amdgcn_raw_buffer_store_b128((u32x4){pk2(w[0], w[1]), pk2(w[2], w[3]), pk2(w[4], w[5]), pk2(w[6], w[7])}, sO, oo + db * 8192u + i * 4096u, 0, 16);
    }
  };
  { auto rr = __builtin_amdgcn_permlane32_swap(__float_as_uint(a.l), __float_as_uint(a.l), false, false);
    a.l = __builtin_amdgcn_rcpf(__uint_as_float(rr[0]) + __uint_as_float(rr[1])); }
  if (!(hasU && grp == 1)) { ost2(0); ost2(1); ost2(2); ost2(3); STAMP(6); }
  const unsigned ln = lane_id();
  const bool isY = xcd >= 4u;
  const unsigned dt = udt, st = ((xcd >> 1) & 1u) * 8u + (idx >> 2), tile = st * 8u + dt;
  u64* fG = flags + FL_G + tile * 16u; u64* fO = flags + FL_O; u64* fU = flags + FL_U + dt * 32u;
  const rsrc_t sG = mkrsrc(ws + WS_G + (size_t)tile * 32768u, 32768u), sU = mkrsrc(ws + WS_U, DM * FF * 2);
  constexpr unsigned XO = 32768u, GO = 65536u;
  auto finish = [&]() {
    WAITL0(); BAR();
#pragma unroll
    for (int q = 0; q < 4; ++q) {
      const unsigned row = wid * 16u + q * 4u + (ln >> 4);
      const u32x4 v = *(const LAS u32x4*)(size_t)(L + XO + row * 256u + 16u * ((ln & 15u) ^ (row & 15u)));
      __builtin_amdgcn_raw_buffer_store_b128(v, mkrsrc(out, SEQ * DM * 2), (st * 128u + row) * 2048u + dt * 256u + (ln & 15u) * 16u, 0, 0);
    }
    STAMP(14); waitv<0>(); STAMP(15); ST_OUT(1);
  };
  if (!hasU) {
    waitv<0>(); BAR();
    if (tid == 256u) flag_set(fO + qt * 32u + head, tag);
    STAMP(7);
  }
  if (grp == 0) {
    const unsigned wp = lw >> 1, wq2 = lw & 1u, fr = ln & 15u, fq = ln >> 4;
    if (hasU) {
      Opnd<T1> P; Opnd<RCHK> Q;
      P.init(L, wp * 64u, ln); Q.init(L + 16384u, wq2 * 64u, ln);
      u32x2 bv[4];
      f32x4 acc[4][4] = {};
      gemm_full<T1, RCHK, 5, 32768u, 0, 0, 16>(acc, P, Q, 32, [&] {
#pragma unroll
        for (int p = 0; p < 4; ++p) bv[p] = *(const u32x2*)(b1 + ufc * 128u + wp * 64u + p * 16u + fq * 4u);
      });
      STAMP(8);
      const unsigned uo = ((udt * 56u + uft * 2u + wp) * 8u + fq) * 2048u + (wq2 * 64u + fr) * 16u;
#pragma unroll
      for (int pp = 0; pp < 2; ++pp) {
        const f32x2 b0 = bf2(bv[2 * pp][0]), b1v = bf2(bv[2 * pp][1]), b2v = bf2(bv[2 * pp + 1][0]), b3 = bf2(bv[2 * pp + 1][1]);
#pragma unroll
        for (int m = 0; m < 4; ++m)
          __builtin_amdgcn_raw_buffer_store_b128((u32x4){gelu2(lo2(acc[2 * pp][m]) + b0), gelu2(hi2(acc[2 * pp][m]) + b1v), gelu2(lo2(acc[2 * pp + 1][m]) + b2v), gelu2(hi2(acc[2 * pp + 1][m]) + b3)},
                                                 sU, uo + pp * 8192u + m * 256u, 0, 16);
      }
      STAMP(9);
      if (isY) { waitv<0>(); BAR(); }
    }
    if (isY || !hasU) BAR();
    f32x4 acc[4][4] = {};
    const unsigned t4 = (wid * 64u + ln) * 16u;
    if (isY) {
      float bias[4];
#pragma unroll
      for (int q = 0; q < 4; ++q) bias[q] = bflo(b2[st * 128u + wq2 * 64u + q * 16u + fr]);
      Opnd<RCHK> P; Opnd<T2> Q;
      P.init(L + 65536u, wp * 64u, ln); Q.init(L + 65536u + 16384u, wq2 * 64u, ln);
      gemm_full<RCHK, T2, 5, 32768u, 3, 2, 0>(acc, P, Q, 56, [] {});
      STAMP(12);
#pragma unroll
      for (int j = 0; j < 8; ++j) {
        const int q = 2 * (j & 1); const f32x4 y0 = acc[j >> 1][q] + bias[q], y1 = acc[j >> 1][q + 1] + bias[q + 1];
        __builtin_amdgcn_raw_buffer_store_b128((u32x4){silu2(lo2(y0)), silu2(hi2(y0)), silu2(lo2(y1)), silu2(hi2(y1))}, sG, t4 + j * 4096u, 0, 16);
      }
      waitv<0>(); BAR();
      if (tid == 0u) flag_set(fG, tag);
      STAMP(15); ST_OUT(1); return;
    }
    Opnd<T1> P; Opnd<RCHK> Q;
    P.init(L + 65536u, wp * 64u, ln); Q.init(L + 65536u + 16384u, wq2 * 64u, ln);
    gemm_full<T1, RCHK, 5, 32768u, 3, 2, 2>(acc, P, Q, 64, [] {});
    STAMP(12);
    const unsigned xa = L + XO + (wq2 * 64u + fr) * 256u + (fq & 1u) * 8u;
    unsigned xp[4];
    f32x4 xv[4][4];
#pragma unroll
    for (int p = 0; p < 4; ++p) {
      xp[p] = xa + 16u * ((wp * 8u + 2u * p + (fq >> 1)) ^ fr);
#pragma unroll
      for (int q = 0; q < 4; ++q) {
        const u32x2 w = *(const LAS u32x2*)(size_t)(xp[p] + q * 4096u);
        xv[p][q] = (f32x4){bflo(w[0]), bfhi(w[0]), bflo(w[1]), bfhi(w[1])};
        asm volatile("" : "+v"(xv[p][q]));
      }
    }
    BAR();
    STAMP(13);
    u32x4 gp[8];
#pragma unroll
    for (int j = 0; j < 8; ++j) gp[j] = *(const LAS u32x4*)(size_t)(L + GO + t4 + j * 4096u);
#pragma unroll
    for (int p = 0; p < 4; ++p)
#pragma unroll
      for (int q = 0; q < 4; ++q) {
        const unsigned g0 = gp[2 * p + (q >> 1)][2 * (q & 1)], g1 = gp[2 * p + (q >> 1)][2 * (q & 1) + 1];
        const f32x4 r = __builtin_elementwise_fma(acc[p][q], (f32x4){bflo(g0), bfhi(g0), bflo(g1), bfhi(g1)}, xv[p][q]);
        *(LAS u32x2*)(size_t)(xp[p] + q * 4096u) = (u32x2){pk2(r[0], r[1]), pk2(r[2], r[3])};
      }
  } else {
    const unsigned N = isY ? 56u : 64u;
    const rsrc_t sT = isY ? mkrsrcB(w2, FF * SEQ * 2) : mkrsrcB(wo, NH * DH * DM * 2);
    const rsrc_t sR = isY ? sU : sO;
    const unsigned ldT = isY ? 4096u : 2048u, voT = voffT(ln, ldT), voL = ln * 16u;
    const unsigned tcol = (isY ? st : dt) * 256u, rbase = (isY ? dt : st) * N * 16384u + lw * 4096u;
    const unsigned toff = (isY ? 16384u : 0u) + lw * 4096u, roff = (isY ? 0u : 16384u) + lw * 4096u, g0 = isY ? (xcd >> 1) * 14u : 0u;
    const u64* dep = (isY ? fU : fO + (st >> 1) * 32u) + (ln & 31u);
    auto peek = [&]() { asm volatile("global_load_dwordx2 v[254:255], %0, off sc1" :: "v"(dep) : "memory", "v254", "v255"); };
    auto await = [&](u64 mask, bool peeked) {
      if (peeked) { u64 v; asm volatile("v_mov_b64 %0, v[254:255]" : "=v"(v) :: "memory"); if ((__ballot(v != tag) & mask) == 0ull) return; }
      flag_wait(dep, tag, mask);
    };
    unsigned sb = L + 4u * 32768u;
    auto nexts = [&]() { sb = sb == L + 4u * 32768u ? L : sb + 32768u; };
    unsigned kT = g0;
    unsigned kW = isY ? (xcd >> 1) * 2u : 0u;
    const unsigned kj = isY ? 7u : 1u;
    auto dT = [&]() { T4 p; p.set(kW * 64u * ldT + tcol, lw, ldT); p.go(sb + toff, voT, sT); ++kT; kW += (kW & 1u) ? kj : 1u; nexts(); };
    if (hasU) {
      const unsigned voW1 = voffT(ln, FF * 2u), hw = (wid & 1u) * 2u;
      unsigned j = 4;
      auto ufill = [&](auto wait) {
        T4 p, q; p.set(j * 64u * (FF * 2u) + ufc * 256u, hw, FF * 2u); q.set(j * 64u * (FF * 2u) + ufc * 256u, hw + 1u, FF * 2u);
        unsigned d0 = sb + hw * 4096u, r0 = (udt * 32u + j) * 16384u + hw * 4096u; p.pin(); q.pin(); PINS(d0); PINS(r0);
        wait();
        p.go(d0, voW1, sW1); dma64d(d0 + 16384u, voL, sH, r0); q.go(d0 + 4096u, voW1, sW1); dma64d(d0 + 20480u, voL, sH, r0 + 4096u);
        ++j; nexts();
      };
      if (wid >= 6u) {
        ufill([] {});
        ufill([] { waitv<32>(); BAR(); }); ufill([] { waitv<40>(); BAR(); });
#pragma nounroll
        for (unsigned t = 2; t < 12; ++t) ufill([] { waitv<48>(); BAR(); });
        waitv<48>(); BAR(); ost2(0); waitv<34>(); BAR(); ost2(1); waitv<20>(); BAR(); ost2(2); waitv<6>(); BAR(); ost2(3);
        touch8k(code_rsrc(), ln < 10u ? ln * 128u : 0x80000000u, (idx * 2u + (wid & 1u)) * 1280u);
        for (unsigned t = 16; t < 22; ++t) BAR();
        waitv<0>(); BAR();
        if (tid == 384u) flag_set(fO + qt * 32u + head, tag);
        BAR(); BAR(); BAR(); BAR();
      } else {
        waitv<16>(); BAR(); STAMP(25); waitv<8>(); BAR(); waitv<0>(); BAR();
        ost2(0); BAR(); ost2(1); BAR(); ost2(2); BAR(); ost2(3);
        for (unsigned t = 6; t < 12; ++t) BAR();
        STAMP(26);
        j = 17; sb = L + 2u * 32768u;
#pragma nounroll
        for (unsigned t = 12; t < 27; ++t) ufill([] { waitv<48>(); BAR(); });
        STAMP(28);
      }
      sb = L + 2u * 32768u;
      waitv<48>(); BAR(); dT(); waitv<36>(); BAR(); dT(); peek(); waitv<24>(); BAR(); dT(); waitv<12>(); BAR(); dT(); BAR(); dT();
      if (isY) {
        BAR();
        if (tid == 256u) flag_set(fU + uft, tag);
      } else waitv<12>();
      STAMP(8);
    } else {
      sb = L + 2u * 32768u;
#pragma nounroll
      for (unsigned j = 0; j < 5; ++j) dT();
    }
    if (hasU && !isY) await(0xffffffffull, true);
    else { if (wid == 4u) await(isY ? 0x7full << (g0 >> 1) : 0xffffffffull, false); BAR(); }
#pragma nounroll
    for (unsigned j = 0; j < 5; ++j) { dma64d(sb + roff, voL, sR, rbase + (g0 + j) * 16384u); nexts(); }
    auto dstep = [&](auto wait) {
      T4 p; p.set(kW * 64u * ldT + tcol, lw, ldT);
      unsigned d0 = sb + toff, d1 = sb + roff, r0 = rbase + kT * 16384u; p.pin(); PINS(d0); PINS(d1); PINS(r0);
      wait(); BAR();
      p.go(d0, voT, sT); dma64d(d1, voL, sR, r0);
      ++kT; kW += (kW & 1u) ? kj : 1u; nexts();
    };
    waitv<16>(); BAR(); STAMP(11);
    dstep([] { waitv<12>(); }); dstep([] { waitv<16>(); }); dstep([] { waitv<20>(); });
    if (!isY && hasU && tid == 256u) flag_set(fU + uft, tag);
#pragma nounroll
    for (unsigned seg = isY ? 0u : 5u; seg < 6u; ++seg) {
      const unsigned n = !isY ? 56u : seg == 0u ? 2u : seg == 2u ? 10u : seg >= 4u ? 14u : 4u;
#pragma nounroll
      for (unsigned i = 0; i < n; ++i) dstep([] { waitv<24>(); });
      if (seg == 0u || seg == 2u) peek();
      else if (seg < 4u) { waitv<32>(); await(seg == 1u ? 0x3fffull << 14 : 0x3fffull, true); kT = seg == 1u ? 70u - g0 : 0u; kW = seg == 1u ? 10u - (xcd >> 1) * 2u : 0u; }
      else kW = 2u;
    }
    waitv<24>(); BAR();
    if (isY) { waitv<0>(); BAR(); STAMP(12); ST_OUT(1); return; }
    {
      const rsrc_t sX = mkrsrc(x, SEQ * DM * 2);
#pragma unroll
      for (int q = 0; q < 8; ++q)
        dma16(L + XO + (lw * 8u + q) * 1024u, (ln >> 4) * 2048u + 16u * ((ln & 15u) ^ ((4u * q + (ln >> 4)) & 15u)), sX, (st * 128u + lw * 32u + q * 4u) * 2048u + dt * 256u);
    }
    waitv<8>(); BAR();
    waitv<0>(); BAR();
    STAMP(12);
    {
      flag_wait<6>(fG, tag, 1ull);
      u32x4 g[8];
#pragma unroll
      for (int q = 0; q < 8; ++q) g[q] = __builtin_amdgcn_raw_buffer_load_b128(sG, voL + (lw * 8u + q) * 1024u, 0, 16);
#pragma unroll
      for (int q = 0; q < 8; ++q) *(LAS u32x4*)(size_t)(L + GO + voL + (lw * 8u + q) * 1024u) = g[q];
    }
    waitv<0>(); WAITL0(); STAMP(13); BAR();
  }
  finish();
}

extern "C" __global__ void rsk_end() {}
}

extern "C" void kernel_launch(void* const* d_in, const int* in_sizes, int n_in, void* d_out, int out_size, void* d_ws, size_t ws_size, hipStream_t stream) {
  const u16* x = (const u16*)d_in[0]; const u16* kc = (const u16*)d_in[1]; const u16* vc = (const u16*)d_in[2];
  const u16* wq = (const u16*)d_in[3]; const u16* wo = (const u16*)d_in[4];
  const u16* w1 = (const u16*)d_in[6]; const u16* b1 = (const u16*)d_in[7]; const u16* w2 = (const u16*)d_in[8]; const u16* b2 = (const u16*)d_in[9];
#define WSEND 0
  char* ws = (char*)d_ws + (((ws_size - WS_TOTAL) / 4 * WSEND) & ~(size_t)0xfffff);
  (void)hipFuncSetAttribute((const void*)rsk_main, hipFuncAttributeMaxDynamicSharedMemorySize, LDS_BYTES);
  rsk_main<<<256, 512, LDS_BYTES, stream>>>(x, ws, wq, kc, vc, w1, wo, w2, b1, b2, (u16*)d_out);
}
```

```cpp
#include <hip/hip_runtime.h>
#include <hip/hip_bf16.h>

namespace {
typedef short bf16x8 __attribute__((ext_vector_type(8)));
typedef short s16x4 __attribute__((ext_vector_type(4)));
typedef float f32x2 __attribute__((ext_vector_type(2)));
typedef float f32x4 __attribute__((ext_vector_type(4)));
typedef float f32x16 __attribute__((ext_vector_type(16)));
typedef unsigned u32x4 __attribute__((ext_vector_type(4)));
typedef unsigned u32x2 __attribute__((ext_vector_type(2)));
typedef __bf16 bf2_t __attribute__((ext_vector_type(2)));
typedef unsigned short u16;
typedef __amdgpu_buffer_rsrc_t rsrc_t;
#define LAS __attribute__((address_space(3)))
#define DI __device__ __forceinline__
#define BAR() asm volatile("s_barrier" ::: "memory")
#define WAITL0() do { asm volatile("" ::: "memory"); __builtin_amdgcn_s_waitcnt(0xC07F); asm volatile("" ::: "memory"); } while (0)
#define ALIGN8() asm volatile(".p2align 3")
#define SCHED() __builtin_amdgcn_sched_barrier(0)
#define SGB(mask, n) __builtin_amdgcn_sched_group_barrier(mask, n, 0)

constexpr int SEQ = 2048, DM = 1024, NH = 32, DH = 128, SK = 2048, WIN = 512, FF = 3584;
constexpr unsigned WS_XN = 0u, WS_H = 4u << 20, WS_O = 8u << 20, WS_U = 24u << 20, WS_G = 32u << 20, WS_FLAG = 36u << 20, WS_TOTAL = 37u << 20;
constexpr unsigned LDS_BYTES = 163840;
constexpr unsigned long long SPIN_LIMIT = 2000000ull;
#define XSLEEP 2
#define NW1 1

#define ST_DECL
#define STAMP(i) do {} while (0)
#define ST_OUT(k)
template <int N> DI void waitv() { asm volatile("s_waitcnt vmcnt(%0)" :: "i"(N) : "memory"); }
DI unsigned rfl(unsigned x) { return __builtin_amdgcn_readfirstlane(x); }
DI rsrc_t mkrsrc(const void* p, unsigned bytes) { return __builtin_amdgcn_make_buffer_rsrc((void*)p, (short)0, (int)bytes, 0x00020000); }
DI void dma16(unsigned m0v, unsigned voff, rsrc_t r, unsigned soff) {
  asm volatile("s_mov_b32 m0, %0\n\ts_nop 0\n\tbuffer_load_dwordx4 %1, %2, %3 offen lds" :: "s"(m0v), "v"(voff), "s"(r), "s"(soff) : "memory");
}
DI rsrc_t code_rsrc() {
  unsigned long long b, e;
  asm volatile("s_getpc_b64 s[40:41]\n\ts_add_u32 s40, s40, rsk_main@rel32@lo+4\n\ts_addc_u32 s41, s41, rsk_main@rel32@hi+12\n\ts_mov_b64 %0, s[40:41]\n\t"
               "s_getpc_b64 s[40:41]\n\ts_add_u32 s40, s40, rsk_end@rel32@lo+4\n\ts_addc_u32 s41, s41, rsk_end@rel32@hi+12\n\ts_mov_b64 %1, s[40:41]"
               : "=&s"(b), "=&s"(e) :: "s40", "s41", "scc");
  return mkrsrc((const void*)b, (e > b && e - b < 0x40000ull) ? (unsigned)(e - b) : 0u);
}
template <bool DEV>
DI void dma64(unsigned m0v, unsigned va, unsigned vb, rsrc_t r, unsigned s0, unsigned s1, unsigned s2, unsigned s3) {
  if constexpr (DEV)
    asm volatile("s_mov_b32 m0, %0\n\ts_nop 0\n\tbuffer_load_dwordx4 %1, %3, %4 offen sc1 lds\n\tbuffer_load_dwordx4 %2, %3, %5 offen offset:1024 sc1 lds\n\t"
                 "buffer_load_dwordx4 %1, %3, %6 offen offset:2048 sc1 lds\n\tbuffer_load_dwordx4 %2, %3, %7 offen offset:3072 sc1 lds"
                 :: "s"(m0v), "v"(va), "v"(vb), "s"(r), "s"(s0), "s"(s1), "s"(s2), "s"(s3) : "memory");
  else
    asm volatile("s_mov_b32 m0, %0\n\ts_nop 0\n\tbuffer_load_dwordx4 %1, %3, %4 offen lds\n\tbuffer_load_dwordx4 %2, %3, %5 offen offset:1024 lds\n\t"
                 "buffer_load_dwordx4 %1, %3, %6 offen offset:2048 lds\n\tbuffer_load_dwordx4 %2, %3, %7 offen offset:3072 lds"
                 :: "s"(m0v), "v"(va), "v"(vb), "s"(r), "s"(s0), "s"(s1), "s"(s2), "s"(s3) : "memory");
}
DI void dma64d(unsigned m0v, unsigned va, rsrc_t r, unsigned s0) {
  asm volatile("s_mov_b32 m0, %0\n\ts_nop 0\n\tbuffer_load_dwordx4 %1, %2, %3 offen sc1 lds\n\tbuffer_load_dwordx4 %1, %2, %3 offen offset:1024 sc1 lds\n\t"
               "buffer_load_dwordx4 %1, %2, %3 offen offset:2048 sc1 lds\n\tbuffer_load_dwordx4 %1, %2, %3 offen offset:3072 sc1 lds"
               :: "s"(m0v), "v"(va), "s"(r), "s"(s0) : "memory");
}
DI rsrc_t mkrsrcB(const void* p, unsigned bytes) { return mkrsrc((const char*)p - 4096, bytes + 4096u); }
#define PINS(x) asm volatile("" : "+s"(x))
DI bf16x8 ldsR(unsigned a) { return *(const LAS bf16x8*)(size_t)a; }
DI s16x4 ldsT(unsigned a) { return __builtin_amdgcn_ds_read_tr16_b64_v4i16((LAS s16x4*)(size_t)a); }
DI bf16x8 cat(s16x4 a, s16x4 b) { return __builtin_shufflevector(a, b, 0, 1, 2, 3, 4, 5, 6, 7); }
DI unsigned pk2(float lo, float hi) { f32x2 v = {lo, hi}; return __builtin_bit_cast(unsigned, __builtin_convertvector(v, bf2_t)); }
DI float bflo(unsigned w) { return __uint_as_float(w << 16); }
DI float bfhi(unsigned w) { return __uint_as_float(w & 0xffff0000u); }
DI float rbf(float x) { return bflo(pk2(x, x) & 0xffffu); }
template <int CTRL, int RM> DI float dpp_add(float v) {
  return v + __builtin_bit_cast(float, __builtin_amdgcn_update_dpp(0, __builtin_bit_cast(int, v), CTRL, RM, 0xf, false));
}
DI void wsum2(float& u, float& v) {
#define WS2(C, M) u = dpp_add<C, M>(u); v = dpp_add<C, M>(v);
  WS2(0x111, 0xf) WS2(0x112, 0xf) WS2(0x114, 0xf) WS2(0x118, 0xf) WS2(0x142, 0xa) WS2(0x143, 0xc)
#undef WS2
  u = __builtin_bit_cast(float, __builtin_amdgcn_readlane(__builtin_bit_cast(int, u), 63));
  v = __builtin_bit_cast(float, __builtin_amdgcn_readlane(__builtin_bit_cast(int, v), 63));
}

DI void ln16(float (&v)[16]) {
  const float x0 = __builtin_bit_cast(float, __builtin_amdgcn_readfirstlane(__builtin_bit_cast(int, v[0])));
  float s = 0.f, q = 0.f;
#pragma unroll
  for (int i = 0; i < 16; ++i) { const float d = v[i] - x0; s += d; q = fmaf(d, d, q); }
  wsum2(s, q);
  const float md = s * (1.0f / DM), var = rbf(fmaxf(fmaf(-md, md, q * (1.0f / DM)), 0.f));
  const float mb = rbf(x0 + md), inv = __builtin_amdgcn_rcpf(rbf(__builtin_amdgcn_sqrtf(rbf(var + 1e-5f))));
#pragma unroll
  for (int i = 0; i < 16; i += 2) {
    const unsigned d = pk2(v[i] - mb, v[i + 1] - mb), r = pk2(bflo(d) * inv, bfhi(d) * inv);
    v[i] = bflo(r); v[i + 1] = bfhi(r);
  }
}
DI unsigned pkx(float lo, float hi) { return (__float_as_uint(lo) >> 16) | (__float_as_uint(hi) & 0xffff0000u); }
enum { RROW = 0, RCHK = 1, T1 = 2, T2 = 3 };
template <int KIND> struct Opnd {
  static constexpr int NA = KIND == RROW ? 2 : KIND == RCHK ? 1 : 4;
  static constexpr int RPB = KIND >= T1 ? 2 : 1;
  unsigned a[NA];
  DI void init(unsigned base, unsigned off, unsigned lane) {
    const unsigned fr = lane & 15u, g = lane >> 4, q = fr >> 2, p = lane & 3u;
    if constexpr (KIND == RROW) {
#pragma unroll
      for (int kk = 0; kk < 2; ++kk) a[kk] = base + (off + fr) * 128u + 16u * ((4u * kk + g) ^ (fr >> 1));
    } else if constexpr (KIND == RCHK) {
      a[0] = base + g * 2048u + (off + fr) * 16u;
    } else {
      const unsigned x = ((q >> 1) << 1) | (g & 1u);
#pragma unroll
      for (int n = 0; n < 4; ++n)
        a[n] = base + (KIND == T1 ? 4096u : 2048u) * (g >> 1) + 1024u * (off >> 6) + 128u * (4u * (g & 1u) + q) + 32u * ((unsigned)n ^ x) + 8u * p;
    }
  }
  DI bf16x8 ld(int b, int kk) const {
    if constexpr (KIND == RROW) return ldsR(a[kk] + 2048u * b);
    else if constexpr (KIND == RCHK) return ldsR(a[0] + 8192u * kk + 256u * b);
    else { const unsigned ad = a[b & 3] + 1024u * (b >> 2) + 8192u * kk; return cat(ldsT(ad), ldsT(ad + (KIND == T1 ? 2048u : 4096u))); }
  }
  DI void adv(unsigned delta) {
#pragma unroll
    for (int i = 0; i < NA; ++i) { a[i] += delta; asm volatile("" : "+v"(a[i])); }
  }
};

template <int PK, int QK, int NS, unsigned STG>
DI void gemm_wide(f32x4 (&acc)[8][4], Opnd<PK>& P, Opnd<QK>& Q, const int N) {
  bf16x8 pf[2][4][2], qf[4][2];
  auto loadP = [&](int h) {
#pragma unroll
    for (int p = 0; p < 4; ++p) { pf[h][p][0] = P.ld(4 * h + p, 0); pf[h][p][1] = P.ld(4 * h + p, 1); }
  };
  auto mma = [&](int h, int q) {
#pragma unroll
    for (int p0 = 0; p0 < 4; ++p0) {
      const int p = (q & 1) ? 3 - p0 : p0;
      acc[4 * h + p][q] = __builtin_amdgcn_mfma_f32_16x16x32_bf16(pf[h][p][0], qf[q][0], acc[4 * h + p][q], 0, 0, 0);
      acc[4 * h + p][q] = __builtin_amdgcn_mfma_f32_16x16x32_bf16(pf[h][p][1], qf[q][1], acc[4 * h + p][q], 0, 0, 0);
    }
  };
  BAR();
  loadP(0);
#pragma unroll
  for (int q = 0; q < 3; ++q) { qf[q][0] = Q.ld(q, 0); qf[q][1] = Q.ld(q, 1); }
  int stg = 0;
#pragma nounroll
  for (int t = 0; t < N; ++t) {
    SCHED();
    qf[3][0] = Q.ld(3, 0); qf[3][1] = Q.ld(3, 1);
    loadP(1); mma(0, 0); mma(0, 1); mma(0, 2); mma(0, 3);
    stg = (stg == NS - 1) ? 0 : stg + 1;
    const unsigned delta = stg == 0 ? (unsigned)(-(int)((NS - 1) * STG)) : STG;
    P.adv(delta); Q.adv(delta);
#pragma unroll
    for (int i = 0; i < 18; ++i) { SGB(0x8, 1); SGB(0x100, 1); }
#pragma unroll
    for (int i = 0; i < 14; ++i) { SGB(0x8, 1); SGB(0x2, 1); }
    SCHED();
    WAITL0(); BAR();
    SCHED();
#pragma unroll
    for (int q = 0; q < 4; ++q) {
      if (q > 0) { qf[q - 1][0] = Q.ld(q - 1, 0); qf[q - 1][1] = Q.ld(q - 1, 1); }
      if (q == 0) { pf[0][0][0] = P.ld(0, 0); pf[0][0][1] = P.ld(0, 1); pf[0][1][0] = P.ld(1, 0); pf[0][1][1] = P.ld(1, 1); }
      else if (q < 3) { pf[0][q + 1][0] = P.ld(q + 1, 0); pf[0][q + 1][1] = P.ld(q + 1, 1); }
      mma(1, q);
#pragma unroll
      for (int i = 0; i < 8; ++i) { SGB(0x8, 1); SGB(0x100, 1); }
      SCHED();
    }
  }
  SCHED();
}

template <int PK, int QK, int NS, unsigned STG, int TAIL, int S0, int DRAIN, class F>
DI void gemm_full(f32x4 (&acc)[4][4], Opnd<PK>& P, Opnd<QK>& Q, const int N, F hook) {
  bf16x8 pf[2][2][4], qf[2][2][4];
  constexpr int NR = 8 * (Opnd<PK>::RPB + Opnd<QK>::RPB);
  int stg = S0;
  auto load = [&](int b) {
#pragma unroll
    for (int i = 0; i < 4; ++i)
#pragma unroll
      for (int kk = 0; kk < 2; ++kk) { pf[b][kk][i] = P.ld(i, kk); qf[b][kk][i] = Q.ld(i, kk); }
    stg = (stg == NS - 1) ? 0 : stg + 1;
    const unsigned delta = stg == 0 ? (unsigned)(-(int)((NS - 1) * STG)) : STG;
    P.adv(delta); Q.adv(delta);
  };
  auto step = [&](int b, bool sync) {
    if (sync) { WAITL0(); BAR(); }
    SCHED();
    load(b ^ 1);
#pragma unroll
    for (int p = 0; p < 4; ++p)
#pragma unroll
      for (int q0 = 0; q0 < 4; ++q0) {
        const int q = (p & 1) ? 3 - q0 : q0;
        acc[p][q] = __builtin_amdgcn_mfma_f32_16x16x32_bf16(pf[b][0][p], qf[b][0][q], acc[p][q], 0, 0, 0);
        acc[p][q] = __builtin_amdgcn_mfma_f32_16x16x32_bf16(pf[b][1][p], qf[b][1][q], acc[p][q], 0, 0, 0);
      }
#pragma unroll
    for (int i = 0; i < NR; ++i) { SGB(0x8, 1); SGB(0x100, 1); }
#pragma unroll
    for (int i = NR; i < 32; ++i) { SGB(0x8, 1); SGB(0x2, 1); }
    SCHED();
  };
  BAR();
  load(0);
  if (DRAIN > 0) {
#pragma nounroll
    for (int t = 0; t < DRAIN; t += 2) { step(0, true); step(1, true); }
    waitv<0>();
    hook();
  }
#pragma nounroll
  for (int t = DRAIN; t < N - (TAIL ? 4 : 0); t += 2) { step(0, true); step(1, true); }
  if (TAIL) {
#pragma nounroll
    for (int t = 0; t < 4; t += 2) { step(0, true); step(1, false); }
  }
}

DI unsigned voffRow(unsigned lane, unsigned ldb, unsigned par) { return (lane >> 3) * ldb + 16u * ((lane & 7u) ^ (4u * par + (lane >> 4))); }
DI unsigned voffT(unsigned lane, unsigned ldb) { return (lane >> 3) * ldb + 16u * ((lane & 7u) ^ (2u * ((((lane >> 4) & 1u) << 1) | (lane >> 5)))); }
struct T4 { unsigned s0, s1, s2, s3;
  DI void set(unsigned so, unsigned lw, unsigned ldb) { s0 = so + 4096u + lw * 16u * ldb; s1 = s0 + 128u - 1024u; s2 = s0 + 8u * ldb - 2048u; s3 = s2 + 128u - 1024u; }
  DI void pin() { PINS(s0); PINS(s1); PINS(s2); PINS(s3); }
  template <bool DEV = false> DI void go(unsigned dst, unsigned vo, rsrc_t r) const { dma64<DEV>(dst, vo, vo, r, s0, s1, s2, s3); } };
template <bool DEV> DI void dmaT4(unsigned dst, unsigned lw, unsigned vo, rsrc_t r, unsigned so, unsigned ldb) { T4 t; t.set(so, lw, ldb); t.go<DEV>(dst + lw * 4096u, vo, r); }

typedef unsigned long long u64;
constexpr unsigned FL_H = 64, FL_O = 320, FL_U = 576, FL_G = 832, FL_X = 3072;
DI void flag_set(u64* p, u64 tag) { __hip_atomic_store(p, tag, __ATOMIC_RELAXED, __HIP_MEMORY_SCOPE_AGENT); }
DI u64 flag_get(const u64* p) { return __hip_atomic_load(p, __ATOMIC_RELAXED, __HIP_MEMORY_SCOPE_AGENT); }
template <int SLOW = 16> DI void flag_wait(const u64* p, u64 tag, u64 mask) {
  const u64 t0 = __builtin_amdgcn_s_memrealtime();
  while ((__ballot(flag_get(p) != tag) & mask) != 0ull) {
    const u64 dt = __builtin_amdgcn_s_memrealtime() - t0;
    if (dt > SPIN_LIMIT) break;
    if (dt > 400ull) __builtin_amdgcn_s_sleep(SLOW); else __builtin_amdgcn_s_sleep(3);
  }
  asm volatile("" ::: "memory");
}

DI f32x2 sigm2(f32x2 v, f32x2 z) {
  f32x2 e = {__builtin_amdgcn_exp2f(z[0]), __builtin_amdgcn_exp2f(z[1])};
  e += 1.0f;
  return v * (f32x2){__builtin_amdgcn_rcpf(e[0]), __builtin_amdgcn_rcpf(e[1])};
}
DI unsigned gelu2(f32x2 v) {
  const f32x2 r = sigm2(v, v * __builtin_elementwise_fma(v * v, (f32x2)(-0.1029432397f), (f32x2)(-2.3022081986f)));
  return pk2(r[0], r[1]);
}
DI unsigned silu2(f32x2 v) { const f32x2 r = sigm2(v, v * -1.4426950408889634f); return pk2(r[0], r[1]); }
DI f32x2 lo2(f32x4 v) { return __builtin_shufflevector(v, v, 0, 1); }
DI f32x2 hi2(f32x4 v) { return __builtin_shufflevector(v, v, 2, 3); }
DI f32x2 bf2(unsigned w) { return (f32x2){bflo(w), bfhi(w)}; }

constexpr unsigned QSTG = 49152u;
constexpr unsigned KBASE = 65536u, VBASE = 114688u;
constexpr unsigned KSLOT[3] = {0u, 32768u, 16384u}, VSLOT[3] = {32768u, 16384u, 0u};
DI void touch8k(rsrc_t r, unsigned vo128, unsigned soff) {
  asm volatile("buffer_load_dword v255, %0, %1, %2 offen" :: "v"(vo128), "s"(r), "s"(soff) : "memory", "v255");
}
DI void touchnt(rsrc_t r, unsigned vo, unsigned soff) {
  asm volatile("buffer_load_dword v255, %0, %1, %2 offen " :: "v"(vo), "s"(r), "s"(soff) : "memory", "v255");
}
constexpr float QSCALE = 0.12751743082f;
#define BIGSUM 4096.0f

struct Att {
  bf16x8 qf[8], kv[16], pf[4], pn[2];
  f32x16 o[4], s[2], negm;
  float l, ts;
  unsigned aK[4], aV[2];
};
template <unsigned SLOT, int J> DI void att_k(Att& a) {
  asm volatile("ds_read_b128 %0, %1 offset:%2" : "+v"(a.kv[J]) : "v"(a.aK[(J >> 1) & 3]), "i"(SLOT + 8192u * (J >> 3) + 4096u * (J & 1)) : "memory");
}
template <unsigned SLOT, int J> DI void att_v(Att& a) {
  s16x4 lo = __builtin_shufflevector(a.kv[J], a.kv[J], 0, 1, 2, 3), hi = __builtin_shufflevector(a.kv[J], a.kv[J], 4, 5, 6, 7);
  constexpr unsigned off = SLOT + 4096u * (J >> 2) + 1024u * ((J >> 1) & 1);
  asm volatile("ds_read_b64_tr_b16 %0, %2 offset:%3\n\tds_read_b64_tr_b16 %1, %2 offset:%4" : "+v"(lo), "+v"(hi) : "v"(a.aV[J & 1]), "i"(off), "i"(off + 2048u) : "memory");
  a.kv[J] = cat(lo, hi);
}
DI void att_wait(Att& a) {
  asm volatile("s_waitcnt lgkmcnt(0)" : "+v"(a.kv[0]), "+v"(a.kv[1]), "+v"(a.kv[2]), "+v"(a.kv[3]), "+v"(a.kv[4]), "+v"(a.kv[5]), "+v"(a.kv[6]), "+v"(a.kv[7]),
               "+v"(a.kv[8]), "+v"(a.kv[9]), "+v"(a.kv[10]), "+v"(a.kv[11]), "+v"(a.kv[12]), "+v"(a.kv[13]), "+v"(a.kv[14]), "+v"(a.kv[15]) :: "memory");
}
DI void att_loadK(Att& a, unsigned slot) {
#pragma unroll
  for (int j = 0; j < 16; ++j) a.kv[j] = ldsR(a.aK[(j >> 1) & 3] + slot + 8192u * (j >> 3) + 4096u * (j & 1));
}
DI unsigned lane_id() { unsigned l; asm volatile("v_mbcnt_lo_u32_b32 %0, -1, 0\n\tv_mbcnt_hi_u32_b32 %0, -1, %0" : "=v"(l)); return l; }
DI void add_bcast3(f32x16& t0, f32x16& t1, f32x16& t2, float v) {
  const float one = lane_id() < 32u ? 1.0f : 0.0f;
  asm volatile("s_nop 7\n\ts_nop 7\n\tv_mfma_f32_32x32x2_f32 %0, %3, %4, %0\n\tv_mfma_f32_32x32x2_f32 %1, %3, %4, %1\n\tv_mfma_f32_32x32x2_f32 %2, %3, %4, %2\n\t"
               "s_nop 7\n\ts_nop 7\n\ts_nop 7" : "+v"(t0), "+v"(t1), "+v"(t2) : "v"(one), "v"(v));
}
template <bool FIRST, unsigned SLOT, int J> DI void att_qk1(Att& a) {
  a.s[J & 1] = __builtin_amdgcn_mfma_f32_32x32x16_bf16(a.kv[J], a.qf[J >> 1], J < 2 ? (FIRST ? (f32x16){} : a.negm) : a.s[J & 1], 0, 0, 0);
  SCHED();
  if constexpr (J == 0) ALIGN8();
  if constexpr (!FIRST) att_v<SLOT, J>(a);
  SCHED();
  if constexpr (J < 15) att_qk1<FIRST, SLOT, J + 1>(a);
}
template <int NEXT, unsigned SLOT, int J> DI void att_pv1(Att& a) {
  a.o[J & 3] = __builtin_amdgcn_mfma_f32_32x32x16_bf16(a.kv[J], a.pf[J >> 2], a.o[J & 3], 0, 0, 0);
  SCHED();
  if constexpr (J == 0) ALIGN8();
  if constexpr (NEXT == 1) att_k<SLOT, J>(a); else if constexpr (NEXT == 2) att_v<SLOT, J>(a);
  SCHED();
  if constexpr (J < 15) att_pv1<NEXT, SLOT, J + 1>(a);
}
DI float max3(float x, float y, float z) { return __builtin_fmaxf(__builtin_fmaxf(x, y), z); }
DI float att_rowmax(const Att& a) {
  float m0 = max3(a.s[0][0], a.s[0][1], a.s[0][2]), m1 = max3(a.s[1][0], a.s[1][1], a.s[1][2]);
#pragma unroll
  for (int r = 3; r < 15; r += 2) { m0 = max3(m0, a.s[0][r], a.s[0][r + 1]); m1 = max3(m1, a.s[1][r], a.s[1][r + 1]); }
  const float mx = __builtin_fmaxf(max3(m0, m1, a.s[0][15]), a.s[1][15]);
  auto rr = __builtin_amdgcn_permlane32_swap(__float_as_uint(mx), __float_as_uint(mx), false, false);
  return __builtin_fmaxf(__uint_as_float(rr[0]), __uint_as_float(rr[1]));
}
DI float att_exp16(const f32x16& v, bf16x8& p0, bf16x8& p1) {
  float s0 = 0.f, s1 = 0.f;
  u32x4 w[2];
#pragma unroll
  for (int h = 0; h < 2; ++h) {
    float e[8];
#pragma unroll
    for (int r = 0; r < 8; ++r) e[r] = __builtin_amdgcn_exp2f(v[8 * h + r]);
#pragma unroll
    for (int r = 0; r < 8; r += 2) {
      if (h == 0 && r == 0) { s0 = e[0]; s1 = e[1]; } else { s0 += e[r]; asm("" : "+v"(s0)); s1 += e[r + 1]; asm("" : "+v"(s1)); }
      w[h][r >> 1] = pk2(e[r], e[r + 1]);
    }
    asm volatile("" : "+v"(w[h]));
    SCHED();
  }
  p0 = __builtin_bit_cast(bf16x8, w[0]); p1 = __builtin_bit_cast(bf16x8, w[1]);
  return s0 + s1;
}
template <bool FIRST> DI void att_sm1(Att& a) {
  if (FIRST) {
    const float mx = att_rowmax(a);
#pragma unroll
    for (int r = 0; r < 16; ++r) { a.s[0][r] -= mx; a.s[1][r] -= mx; a.negm[r] = -mx; }
  }
  a.ts = att_exp16(a.s[0], a.pn[0], a.pn[1]);
  asm volatile("" : "+v"(a.pn[0]), "+v"(a.pn[1]), "+v"(a.ts), "+v"(a.s[1]), "+v"(a.negm));
}
template <bool FIRST> DI void att_sm2(Att& a) {
  a.ts += att_exp16(a.s[1], a.pf[2], a.pf[3]);
  if (!FIRST) {
    if (__builtin_expect(!__all(a.ts <= BIGSUM), 0)) {
      const float dl = fmaxf(att_rowmax(a), 0.f), fct = __builtin_amdgcn_exp2f(-dl);
#pragma unroll
      for (int db = 0; db < 4; ++db) a.o[db] *= fct;
      a.l *= fct;
      add_bcast3(a.s[0], a.s[1], a.negm, -dl);
      a.ts = att_exp16(a.s[0], a.pn[0], a.pn[1]) + att_exp16(a.s[1], a.pf[2], a.pf[3]);
    }
  }
  a.l += a.ts;
  a.pf[0] = a.pn[0]; a.pf[1] = a.pn[1];
  asm volatile("" : "+v"(a.pf[0]), "+v"(a.pf[1]), "+v"(a.pf[2]), "+v"(a.pf[3]), "+v"(a.l));
}

extern "C" __global__ __launch_bounds__(512, 2) __attribute__((amdgpu_num_vgpr(127)))
void rsk_main(const u16* __restrict__ x, char* __restrict__ ws, const u16* __restrict__ wq, const u16* __restrict__ kc, const u16* __restrict__ vc,
              const u16* __restrict__ w1, const u16* __restrict__ wo, const u16* __restrict__ w2, const u16* __restrict__ b1, const u16* __restrict__ b2,
              u16* __restrict__ out) {
  if (rfl(threadIdx.x) == 0u) touch8k(code_rsrc(), threadIdx.x * 128u + (threadIdx.x < 48u ? 0u : (blockIdx.x >> 3) * 2048u), 0u);
  extern __shared__ __attribute__((aligned(16))) char lds[];
  const unsigned L = (unsigned)(size_t)lds;
  const char* xn = ws + WS_XN; const char* hh = ws + WS_H;
  const unsigned tid = threadIdx.x, lane = tid & 63u, wid = rfl(tid >> 6), grp = wid >> 2, lw = wid & 3u;
  const unsigned bid = blockIdx.x, xcd = bid & 7u, idx = bid >> 3;
  const unsigned qt = (xcd & 1u) * 4u + (idx & 3u), kvh = xcd >> 1, head = kvh * 8u + (idx >> 2);
  const bool hasU = idx < 28u;
  const unsigned udt = (xcd & 1u) * 4u + (idx & 3u), uft = (xcd >> 1) * 7u + (idx >> 2);
  const unsigned ufc = (idx >> 2) * 4u + (xcd >> 1);
  const rsrc_t sK = mkrsrc(kc + ((size_t)kvh * SK + (SK - WIN)) * DH, WIN * DH * 2), sV = mkrsrc(vc + ((size_t)kvh * SK + (SK - WIN)) * DH, WIN * DH * 2);
  const rsrc_t sH = mkrsrc(hh, SEQ * DM * 2), sW1 = mkrsrcB(w1, SEQ * FF * 2);
  const unsigned voK0 = voffRow(lane, 256u, 0), voV = voffT(lane, 256u);
  auto dmaK2 = [&](unsigned slot, unsigned t, unsigned p0) {
    unsigned voK1; asm volatile("v_xor_b32 %0, 64, %1" : "=v"(voK1) : "v"(voK0));
    dma16(L + KBASE + slot + 1024u * p0, voK0, sK, t * 16384u + (p0 & 7u) * 2048u + (p0 >> 3) * 128u);
    dma16(L + KBASE + slot + 1024u * p0 + 1024u, voK1, sK, t * 16384u + (p0 & 7u) * 2048u + 2048u + (p0 >> 3) * 128u); };
  auto dmaV2 = [&](unsigned slot, unsigned t, unsigned p0) {
    dma16(L + VBASE + slot + 1024u * p0, voV, sV, t * 16384u + (p0 >> 1) * 2048u);
    dma16(L + VBASE + slot + 1024u * p0 + 1024u, voV, sV, t * 16384u + (p0 >> 1) * 2048u + 128u); };
  auto tileK = [&](unsigned t) {
#pragma unroll
    for (int p = 0; p < 16; p += 2) dmaK2(KSLOT[t % 3], t, p); };
  auto tileV = [&](unsigned t) {
#pragma unroll
    for (int p = 0; p < 16; p += 2) dmaV2(VSLOT[t % 3], t, p); };
  Att a;
  { const unsigned r32 = lane & 31u, hi = lane >> 5;
#pragma unroll
    for (int j = 0; j < 4; ++j) a.aK[j] = L + KBASE + r32 * 128u + 16u * ((2u * j + hi) ^ ((r32 >> 1) & 7u)); }

  ST_DECL; STAMP(0);
  u64* flags = (u64*)(ws + WS_FLAG);
  const rsrc_t sS = lw == 0 ? mkrsrcB(wq, DM * NH * DH * 2) : mkrsrcB(xn, SEQ * DM * 2);
  unsigned asrc = 4096u + (lw == 0 ? head * 256u : (qt * 256u + (lw - 1u) * 128u) * 2048u), adst = L + lw * 16384u;
  const unsigned avo0 = lw == 0 ? voffT(lane, 8192u) : voffRow(lane, 2048u, 0), avo1 = lw == 0 ? avo0 : avo0 ^ 64u;
  constexpr unsigned HT = 147456u;
  auto astep = [&](unsigned vo0, unsigned vo1) {
    const unsigned sp = lw == 0 ? 8u * 8192u : 16u * 2048u, so = (lw == 0 ? 128u : 8u * 2048u) - 1024u;
#pragma unroll
    for (int q = 0; q < 4; ++q) { const unsigned e = asrc + 2u * q * sp; dma64<true>(adst + 4096u * q, vo0, vo1, sS, e, e + so, e + sp - 2048u, e + sp - 2048u + so); }
    asrc += lw == 0 ? 64u * 8192u : 128u; adst = adst >= L + 2u * QSTG ? adst - 2u * QSTG : adst + QSTG;
  };
  u64 tag;
  {
    const unsigned row = (bid >> 2) * 32u + ((bid >> 1) & 1u) * 16u + (bid & 1u) * 4u + (wid >> 2) * 8u + (wid & 3u);
    u32x4 xa, xb; u32x2 ep;
    asm volatile("buffer_load_dwordx4 %0, %3, %4, %5 offen\n\tbuffer_load_dwordx4 %1, %3, %4, %5 offen offset:1024\n\tglobal_load_dwordx2 %2, %6, %7 sc1"
                 : "=&v"(xa), "=&v"(xb), "=&v"(ep) : "v"(lane * 16u), "s"(mkrsrc(x, SEQ * DM * 2)), "s"(row * 2048u), "v"(0u), "s"(flags) : "memory");
    __builtin_amdgcn_s_setprio(1);
    if (wid == 7) {
      touch8k(mkrsrc(wq, DM * NH * DH * 2), lane * 128u, bid * 8192u);
      waitv<1>();
    } else waitv<0>();
    asm volatile("" : "+v"(xa), "+v"(xb), "+v"(ep));
    STAMP(16);
    tag = (((u64)rfl(ep[1]) << 32) | rfl(ep[0])) * 0x9E3779B97F4A7C15ull + 0x632BE59BD9B4E019ull;
    float v[16];
#pragma unroll
    for (int i = 0; i < 4; ++i) { v[2 * i] = bflo(xa[i]); v[2 * i + 1] = bfhi(xa[i]); v[8 + 2 * i] = bflo(xb[i]); v[9 + 2 * i] = bfhi(xb[i]); }
    ln16(v);
    const rsrc_t sW = mkrsrc(ws, WS_O);
#pragma unroll
    for (int p = 0; p < 2; ++p) {
      const unsigned lo0 = pkx(v[8 * p], v[8 * p + 1]), lo1 = pkx(v[8 * p + 2], v[8 * p + 3]), hi0 = pkx(v[8 * p + 4], v[8 * p + 5]), hi1 = pkx(v[8 * p + 6], v[8 * p + 7]);
      const bool odd = lane & 1u;
      const unsigned s0 = __builtin_amdgcn_update_dpp(0, (int)(odd ? lo0 : hi0), 0xB1, 0xf, 0xf, false), s1 = __builtin_amdgcn_update_dpp(0, (int)(odd ? lo1 : hi1), 0xB1, 0xf, 0xf, false);
      __builtin_amdgcn_raw_buffer_store_b128(odd ? (u32x4){s0, s1, hi0, hi1} : (u32x4){lo0, lo1, s0, s1}, sW, WS_XN + row * 2048u + lane * 16u + 1024u * p, 0, 16);
    }
    STAMP(17);
    waitv<0>();
    if (lane == 0u) flag_set(flags + FL_X + bid * 8u + wid, tag);
    STAMP(18);
    if (wid == 4u) { astep(avo0, avo1); astep(avo0, avo1); }
    if (grp == 0) __builtin_amdgcn_s_setprio(0);
    ln16(v);
    *(LAS u32x4*)(size_t)(L + HT + wid * 2048u + lane * 16u) = (u32x4){pkx(v[0], v[1]), pkx(v[2], v[3]), pkx(v[4], v[5]), pkx(v[6], v[7])};
    *(LAS u32x4*)(size_t)(L + HT + wid * 2048u + 1024u + lane * 16u) = (u32x4){pkx(v[8], v[9]), pkx(v[10], v[11]), pkx(v[12], v[13]), pkx(v[14], v[15])};
    WAITL0();
    __builtin_amdgcn_s_setprio(0);
    STAMP(19);
  }
  auto hstore = [&](unsigned i) {
    const unsigned d0 = i * 64u, ad = L + HT + ((lane & 15u) >> 2) * 2048u + (d0 + (lane >> 4) * 16u + (lane & 3u) * 4u) * 2u;
    __builtin_amdgcn_raw_buffer_store_b128(__builtin_bit_cast(u32x4, cat(ldsT(ad), ldsT(ad + 8192u))), mkrsrc(ws, WS_O), WS_H + ((d0 >> 7) * 256u + bid) * 2048u + ((d0 & 64u) + lane) * 16u, 0, 16);
  };
  if (!hasU) {
    BAR();
    hstore(wid * 2u); hstore(wid * 2u + 1u);
    waitv<0>(); BAR();
    if (tid == 0u) flag_set(flags + FL_H + bid, tag);
    const unsigned w = ((idx - 28u) * 8u + xcd) * 8u + wid, vo = lane * 128u;
    const rsrc_t sWq = mkrsrc(wq, DM * NH * DH * 2);
#pragma unroll
    for (int j = 1; j < 4; ++j) touch8k(sWq, vo, (j * 256u + w) * 8192u);
    touch8k(mkrsrc((w & 64u) ? vc : kc, 4u * SK * DH * 2u), w < 128u ? vo : 0x80000000u, ((w >> 4) & 3u) * (SK * DH * 2u) + (SK - WIN) * DH * 2u + (w & 15u) * 8192u);
#pragma unroll
    for (int j = 0; j < 1; ++j) { waitv<2>(); touch8k(mkrsrc(w2, FF * SEQ * 2), vo, (j * 256u + w) * 8192u); }
    waitv<2>();
    if (wid == 0u) touch8k(sW1, vo, (6u * 256u + bid) * 8192u + 4096u);
    if (wid < 7u) touch8k(mkrsrc(w2, FF * SEQ * 2), vo, (wid * 256u + bid) * 8192u);
    if (wid >= 4u) touch8k(mkrsrc(wo, NH * DH * DM * 2), vo, ((wid - 4u) * 256u + bid) * 8192u);
    waitv<0>();
    STAMP(20);
  }
  if (wid == 7) {
    const unsigned vo = lane * 128u;
    BAR(); BAR();
#pragma nounroll
    for (unsigned i = 0; i < 4; ++i) {
      BAR();
      if (hasU) { hstore(i * 4u); hstore(i * 4u + 1u); hstore(i * 4u + 2u); hstore(i * 4u + 3u); }
    }
    BAR(); BAR();
    waitv<0>();
    if (lane == 0u) flag_set(flags + FL_H + bid, tag);
    BAR(); BAR(); BAR(); BAR();
    const u64 f0 = flag_get(flags + FL_H + lane), f1 = flag_get(flags + FL_H + 64u + lane), f2 = flag_get(flags + FL_H + 128u + lane), f3 = flag_get(flags + FL_H + 192u + lane);
    touch8k(code_rsrc(), lane < 16u ? vo : 0x80000000u, idx * 2048u);
    BAR(); BAR();
    if (__any(f0 != tag || f1 != tag || f2 != tag || f3 != tag)) {
#pragma nounroll
      for (unsigned j = 0; j < 256u; j += 64u) flag_wait(flags + FL_H + j + lane, tag, ~0ull);
    }
    if (bid == 0u && lane == 0u) flag_set(flags, tag);
    BAR(); BAR(); BAR();
#pragma unroll
    for (int j = 0; j < 4; ++j) touch8k(mkrsrc(wo, NH * DH * DM * 2), vo, (j * 256u + bid) * 8192u);
    touch8k(mkrsrc(w2, FF * SEQ * 2), vo, (256u + bid) * 8192u);
    att_loadK(a, KSLOT[0]);
  } else if (grp == 1) {
    const unsigned vo0 = avo0, vo1 = avo1;
    if (lw != 0) {
      const u64* fx = flags + FL_X + qt * 256u + (lw - 1u) * 128u + lane;
      const u64 t0 = __builtin_amdgcn_s_memrealtime();
      while (__any(((flag_get(fx) ^ tag) | (flag_get(fx + 64) ^ tag)) != 0ull)) {
        if (__builtin_amdgcn_s_memrealtime() - t0 > SPIN_LIMIT) break;
        __builtin_amdgcn_s_sleep(XSLEEP);
      }
      asm volatile("" ::: "memory");
      STAMP(21);
      astep(vo0, vo1); astep(vo0, vo1);
      STAMP(22);
    }
    waitv<16>(); STAMP(23);
    BAR(); STAMP(1);
    astep(vo0, vo1);
#pragma nounroll
    for (unsigned t = 0; t < 13; ++t) { waitv<16>(); BAR(); astep(vo0, vo1); }
    waitv<16>(); BAR();
    if (lw == 0) tileK(0); else if (lw == 1) tileK(2); else tileV(0);
    waitv<16>(); BAR();
    if (lw == 1) tileK(1); else if (lw == 2) tileV(1);
    if (lw == 0) waitv<0>();
    BAR();
    att_loadK(a, KSLOT[0]);
  } else {
    Opnd<T1> P; Opnd<RROW> Q;
    P.init(L, 0u, lane); Q.init(L + 16384u, lw * 64u, lane);
    f32x4 acc[8][4] = {};
    gemm_wide<T1, RROW, 3, QSTG>(acc, P, Q, 16);
    STAMP(2);
    att_loadK(a, KSLOT[0]);
    const unsigned wa = L + (lane >> 5) * 4096u + (lw * 64u + (lane & 15u)) * 16u + ((lane >> 4) & 1u) * 8u;
#pragma unroll
    for (int j = 0; j < 16; ++j) {
      const f32x4 v = acc[j >> 1][j & 1] * QSCALE;
      *(LAS u32x2*)(size_t)(wa + 8192u * (j >> 1) + 256u * (j & 1)) = (u32x2){pk2(v[0], v[1]), pk2(v[2], v[3])};
    }
#pragma unroll
    for (int ks = 0; ks < 8; ++ks) a.qf[ks] = ldsR(L + (2u * ks + (lane >> 5)) * 4096u + (lw * 64u + (lane & 31u)) * 16u);
    SCHED();
#pragma unroll
    for (int j = 0; j < 16; ++j) {
      a.s[j & 1] = __builtin_amdgcn_mfma_f32_32x32x16_bf16(a.kv[j], a.qf[j >> 1], j < 2 ? (f32x16){} : a.s[j & 1], 0, 0, 0);
      float v[4];
#pragma unroll
      for (int c = 0; c < 4; ++c) { v[c] = acc[j >> 1][2 + (j & 1)][c] * QSCALE; asm("" : "+v"(v[c])); }
      *(LAS u32x2*)(size_t)(wa + 8192u * (j >> 1) + 256u * (2 + (j & 1))) = (u32x2){pk2(v[0], v[1]), pk2(v[2], v[3])};
      SCHED();
    }
    WAITL0();
  }
  STAMP(3);

  const rsrc_t sO = mkrsrc(ws + WS_O, SEQ * NH * DH * 2);
  {
    const unsigned l2 = lane_id(), hi = l2 >> 5, blk = (l2 >> 4) & 1u, q4 = (l2 & 15u) >> 2, p4 = l2 & 3u;
#pragma unroll
    for (int n = 0; n < 2; ++n) a.aV[n] = L + VBASE + 128u * (4u * hi + q4) + 32u * ((2u * n + blk) ^ (((q4 >> 1) << 1) | hi)) + 8u * p4;
#pragma unroll
    for (int db = 0; db < 4; ++db) a.o[db] = (f32x16){};
    a.l = 0.f;
    auto ustepP_ = [&](unsigned j, unsigned stg, unsigned vo) { dmaT4<false>(L + stg * 32768u, lw, vo, sW1, j * 64u * (FF * 2u) + ufc * 256u, FF * 2u); };
    auto ustepQ_ = [&](unsigned j, unsigned stg, unsigned vo) { dma64d(L + stg * 32768u + 16384u + lw * 4096u, vo, sH, (udt * 32u + j) * 16384u + lw * 4096u); };
    const bool pre = hasU && grp == 1;
    if (grp == 1) {
      if (wid != 7) waitv<0>();
      WAITL0(); BAR();
#pragma unroll
      for (int ks = 0; ks < 8; ++ks) a.qf[ks] = ldsR(L + (2u * ks + hi) * 4096u + (lw * 64u + 32u + (l2 & 31u)) * 16u);
      SCHED(); att_qk1<true, 0u, 0>(a);
    }
    STAMP(4);
    const rsrc_t sWo = mkrsrc(wo, NH * DH * DM * 2), sW2 = mkrsrc(w2, FF * SEQ * 2);
    const unsigned lk = bid * 8192u + (wid >> 1) * 2048u + (wid & 1u) * 128u;
    auto near = [&](unsigned k) {
      const unsigned l4 = lane_id(), wv = idx * 8u + wid;
      touchnt(sW1, l4 < 14u && hasU ? (l4 >> 1) * 1024u + (l4 & 1u) * 128u : 0x80000000u, 4096u + (k * 224u + wv) * (FF * 2u) + (xcd >> 1) * 256u);
    };
#define LOOK(v) do { if ((v) < 1) touch8k(sW1, voV, lk + 6u * 0x200000u + 4096u); else if ((v) < 6) touch8k(sW2, voV, lk + ((v) + 1) * 0x200000u); \
                     else if ((v) < 14) near((v) - 6); } while (0)
#define USTEP(i) ((i) < 5 ? (i) - 3 : (i) - 4)
#define ATT_TILE(i, FIRST, WB, WBP, WD, WDP)                                                                                  \
    SCHED(); if (!(FIRST)) att_qk1<false, VSLOT[((i) + 2) % 3], 0>(a); BAR(); SCHED();    \
    if ((i) <= 4) dmaK2(KSLOT[(i) % 3], (i) + 3, wid * 2u);                                                                   \
    LOOK(2 * (i));                                                                                                            \
    att_sm1<FIRST>(a);                                                                                                        \
    if ((i) >= 3 && pre) { waitv<WBP>(); if ((i) != 5) ustepP_(USTEP(i), USTEP(i), voffT(lane_id(), FF * 2u)); } else waitv<WB>(); \
    if (!(FIRST)) att_wait(a); SCHED(); BAR();                              \
    if (FIRST) att_loadK(a, KSLOT[1]); else { SCHED(); att_pv1<((i) < 7 ? 1 : 2), ((i) < 7 ? KSLOT[((i) + 1) % 3] : VSLOT[7 % 3]), 0>(a); } \
    BAR(); SCHED();                                                         \
    if ((i) <= 5) dmaV2(VSLOT[((i) + 2) % 3], (i) + 2, wid * 2u);                                                             \
    LOOK(2 * (i) + 1);                                                                                                        \
    att_sm2<FIRST>(a);                                                                                                        \
    if ((i) >= 3 && pre) { waitv<WDP>(); if ((i) != 5) ustepQ_(USTEP(i), USTEP(i), lane_id() * 16u); } else waitv<WD>(); \
    att_wait(a); SCHED(); BAR();
    ATT_TILE(0, true, 63, 63, 63, 63) ATT_TILE(1, false, 63, 63, 10, 10) ATT_TILE(2, false, 10, 10, 10, 10) ATT_TILE(3, false, 10, 10, 10, 14)
    ATT_TILE(4, false, 10, 18, 10, 22) ATT_TILE(5, false, 8, 20, 8, 16) ATT_TILE(6, false, 6, 10, 3, 7) ATT_TILE(7, false, 63, 63, 63, 63)
#undef ATT_TILE
#undef USTEP
#undef LOOK
    SCHED(); att_pv1<0, 0u, 0>(a); STAMP(5);
    if (grp == 0) BAR();
    if (pre) { waitv<24>(); BAR(); STAMP(24); }
  }
  auto ost2 = [&](int db) {
    const unsigned l3 = lane_id(), row = qt * 256u + lw * 64u + grp * 32u + (l3 & 31u);
    const unsigned oo = (((row >> 7) * 64u + head * 2u) * 8u + (l3 >> 5)) * 2048u + (row & 127u) * 16u;
#pragma unroll
    for (int i = 0; i < 2; ++i) {
      float w[8];
#pragma unroll
      for (int c = 0; c < 8; ++c) { w[c] = a.o[db][8 * i + c] * a.l; asm("" : "+v"(w[c])); }
      __builtin_amdgcn_raw_buffer_store_b128((u32x4){pk2(w[0], w[1]), pk2(w[2], w[3]), pk2(w[4], w[5]), pk2(w[6], w[7])}, sO, oo + db * 8192u + i * 4096u, 0, 16);
    }
  };
  { auto rr = __builtin_amdgcn_permlane32_swap(__float_as_uint(a.l), __float_as_uint(a.l), false, false);
    a.l = __builtin_amdgcn_rcpf(__uint_as_float(rr[0]) + __uint_as_float(rr[1])); }
  if (!(hasU && grp == 1)) { ost2(0); ost2(1); ost2(2); ost2(3); STAMP(6); }
  const unsigned ln = lane_id();
  const bool isY = xcd >= 4u;
  const unsigned dt = udt, st = ((xcd >> 1) & 1u) * 8u + (idx >> 2), tile = st * 8u + dt;
  u64* fG = flags + FL_G + tile * 16u; u64* fO = flags + FL_O; u64* fU = flags + FL_U + dt * 32u;
  const rsrc_t sG = mkrsrc(ws + WS_G + (size_t)tile * 32768u, 32768u), sU = mkrsrc(ws + WS_U, DM * FF * 2);
  constexpr unsigned XO = 32768u, GO = 65536u;
  auto finish = [&]() {
    WAITL0(); BAR();
#pragma unroll
    for (int q = 0; q < 4; ++q) {
      const unsigned row = wid * 16u + q * 4u + (ln >> 4);
      const u32x4 v = *(const LAS u32x4*)(size_t)(L + XO + row * 256u + 16u * ((ln & 15u) ^ (row & 15u)));
      __builtin_amdgcn_raw_buffer_store_b128(v, mkrsrc(out, SEQ * DM * 2), (st * 128u + row) * 2048u + dt * 256u + (ln & 15u) * 16u, 0, 0);
    }
    STAMP(14); waitv<0>(); STAMP(15); ST_OUT(1);
  };
  if (!hasU) {
    waitv<0>(); BAR();
    if (tid == 256u) flag_set(fO + qt * 32u + head, tag);
    STAMP(7);
  }
  if (grp == 0) {
    const unsigned wp = lw >> 1, wq2 = lw & 1u, fr = ln & 15u, fq = ln >> 4;
    if (hasU) {
      Opnd<T1> P; Opnd<RCHK> Q;
      P.init(L, wp * 64u, ln); Q.init(L + 16384u, wq2 * 64u, ln);
      u32x2 bv[4];
      f32x4 acc[4][4] = {};
      gemm_full<T1, RCHK, 5, 32768u, 0, 0, 16>(acc, P, Q, 32, [&] {
#pragma unroll
        for (int p = 0; p < 4; ++p) bv[p] = *(const u32x2*)(b1 + ufc * 128u + wp * 64u + p * 16u + fq * 4u);
      });
      STAMP(8);
      const unsigned uo = ((udt * 56u + uft * 2u + wp) * 8u + fq) * 2048u + (wq2 * 64u + fr) * 16u;
#pragma unroll
      for (int pp = 0; pp < 2; ++pp) {
        const f32x2 b0 = bf2(bv[2 * pp][0]), b1v = bf2(bv[2 * pp][1]), b2v = bf2(bv[2 * pp + 1][0]), b3 = bf2(bv[2 * pp + 1][1]);
#pragma unroll
        for (int m = 0; m < 4; ++m)
          __builtin_amdgcn_raw_buffer_store_b128((u32x4){gelu2(lo2(acc[2 * pp][m]) + b0), gelu2(hi2(acc[2 * pp][m]) + b1v), gelu2(lo2(acc[2 * pp + 1][m]) + b2v), gelu2(hi2(acc[2 * pp + 1][m]) + b3)},
                                                 sU, uo + pp * 8192u + m * 256u, 0, 16);
      }
      STAMP(9);
      if (isY) { waitv<0>(); BAR(); }
    }
    if (isY || !hasU) BAR();
    f32x4 acc[4][4] = {};
    const unsigned t4 = (wid * 64u + ln) * 16u;
    if (isY) {
      float bias[4];
#pragma unroll
      for (int q = 0; q < 4; ++q) bias[q] = bflo(b2[st * 128u + wq2 * 64u + q * 16u + fr]);
      Opnd<RCHK> P; Opnd<T2> Q;
      P.init(L + 65536u, wp * 64u, ln); Q.init(L + 65536u + 16384u, wq2 * 64u, ln);
      gemm_full<RCHK, T2, 5, 32768u, 3, 2, 0>(acc, P, Q, 56, [] {});
      STAMP(12);
#pragma unroll
      for (int j = 0; j < 8; ++j) {
        const int q = 2 * (j & 1); const f32x4 y0 = acc[j >> 1][q] + bias[q], y1 = acc[j >> 1][q + 1] + bias[q + 1];
        __builtin_amdgcn_raw_buffer_store_b128((u32x4){silu2(lo2(y0)), silu2(hi2(y0)), silu2(lo2(y1)), silu2(hi2(y1))}, sG, t4 + j * 4096u, 0, 16);
      }
      waitv<0>(); BAR();
      if (tid == 0u) flag_set(fG, tag);
      STAMP(15); ST_OUT(1); return;
    }
    Opnd<T1> P; Opnd<RCHK> Q;
    P.init(L + 65536u, wp * 64u, ln); Q.init(L + 65536u + 16384u, wq2 * 64u, ln);
    gemm_full<T1, RCHK, 5, 32768u, 3, 2, 2>(acc, P, Q, 64, [] {});
    STAMP(12);
    const unsigned xa = L + XO + (wq2 * 64u + fr) * 256u + (fq & 1u) * 8u;
    unsigned xp[4];
    f32x4 xv[4][4];
#pragma unroll
    for (int p = 0; p < 4; ++p) {
      xp[p] = xa + 16u * ((wp * 8u + 2u * p + (fq >> 1)) ^ fr);
#pragma unroll
      for (int q = 0; q < 4; ++q) {
        const u32x2 w = *(const LAS u32x2*)(size_t)(xp[p] + q * 4096u);
        xv[p][q] = (f32x4){bflo(w[0]), bfhi(w[0]), bflo(w[1]), bfhi(w[1])};
        asm volatile("" : "+v"(xv[p][q]));
      }
    }
    BAR();
    STAMP(13);
    u32x4 gp[8];
#pragma unroll
    for (int j = 0; j < 8; ++j) gp[j] = *(const LAS u32x4*)(size_t)(L + GO + t4 + j * 4096u);
#pragma unroll
    for (int p = 0; p < 4; ++p)
#pragma unroll
      for (int q = 0; q < 4; ++q) {
        const unsigned g0 = gp[2 * p + (q >> 1)][2 * (q & 1)], g1 = gp[2 * p + (q >> 1)][2 * (q & 1) + 1];
        const f32x4 r = __builtin_elementwise_fma(acc[p][q], (f32x4){bflo(g0), bfhi(g0), bflo(g1), bfhi(g1)}, xv[p][q]);
        *(LAS u32x2*)(size_t)(xp[p] + q * 4096u) = (u32x2){pk2(r[0], r[1]), pk2(r[2], r[3])};
      }
  } else {
    const unsigned N = isY ? 56u : 64u;
    const rsrc_t sT = isY ? mkrsrcB(w2, FF * SEQ * 2) : mkrsrcB(wo, NH * DH * DM * 2);
    const rsrc_t sR = isY ? sU : sO;
    const unsigned ldT = isY ? 4096u : 2048u, voT = voffT(ln, ldT), voL = ln * 16u;
    const unsigned tcol = (isY ? st : dt) * 256u, rbase = (isY ? dt : st) * N * 16384u + lw * 4096u;
    const unsigned toff = (isY ? 16384u : 0u) + lw * 4096u, roff = (isY ? 0u : 16384u) + lw * 4096u, g0 = isY ? (xcd >> 1) * 14u : 0u;
    const u64* dep = (isY ? fU : fO + (st >> 1) * 32u) + (ln & 31u);
    auto peek = [&]() { asm volatile("global_load_dwordx2 v[254:255], %0, off sc1" :: "v"(dep) : "memory", "v254", "v255"); };
    auto await = [&](u64 mask, bool peeked) {
      if (peeked) { u64 v; asm volatile("v_mov_b64 %0, v[254:255]" : "=v"(v) :: "memory"); if ((__ballot(v != tag) & mask) == 0ull) return; }
      flag_wait(dep, tag, mask);
    };
    unsigned sb = L + 4u * 32768u;
    auto nexts = [&]() { sb = sb == L + 4u * 32768u ? L : sb + 32768u; };
    unsigned kT = g0;
    unsigned kW = isY ? (xcd >> 1) * 2u : 0u;
    const unsigned kj = isY ? 7u : 1u;
    auto dT = [&]() { T4 p; p.set(kW * 64u * ldT + tcol, lw, ldT); p.go(sb + toff, voT, sT); ++kT; kW += (kW & 1u) ? kj : 1u; nexts(); };
    if (hasU) {
      const unsigned voW1 = voffT(ln, FF * 2u), hw = (wid & 1u) * 2u;
      unsigned j = 4;
      auto ufill = [&](auto wait) {
        T4 p, q; p.set(j * 64u * (FF * 2u) + ufc * 256u, hw, FF * 2u); q.set(j * 64u * (FF * 2u) + ufc * 256u, hw + 1u, FF * 2u);
        unsigned d0 = sb + hw * 4096u, r0 = (udt * 32u + j) * 16384u + hw * 4096u; p.pin(); q.pin(); PINS(d0); PINS(r0);
        wait();
        p.go(d0, voW1, sW1); dma64d(d0 + 16384u, voL, sH, r0); q.go(d0 + 4096u, voW1, sW1); dma64d(d0 + 20480u, voL, sH, r0 + 4096u);
        ++j; nexts();
      };
      if (wid >= 6u) {
        ufill([] {});
        ufill([] { waitv<32>(); BAR(); }); ufill([] { waitv<40>(); BAR(); });
#pragma nounroll
        for (unsigned t = 2; t < 12; ++t) ufill([] { waitv<48>(); BAR(); });
        waitv<48>(); BAR(); ost2(0); waitv<34>(); BAR(); ost2(1); waitv<20>(); BAR(); ost2(2); waitv<6>(); BAR(); ost2(3);
        touch8k(code_rsrc(), ln < 10u ? ln * 128u : 0x80000000u, (idx * 2u + (wid & 1u)) * 1280u);
        for (unsigned t = 16; t < 22; ++t) BAR();
        waitv<0>(); BAR();
        if (tid == 384u) flag_set(fO + qt * 32u + head, tag);
        BAR(); BAR(); BAR(); BAR();
      } else {
        waitv<16>(); BAR(); STAMP(25); waitv<8>(); BAR(); waitv<0>(); BAR();
        ost2(0); BAR(); ost2(1); BAR(); ost2(2); BAR(); ost2(3);
        for (unsigned t = 6; t < 12; ++t) BAR();
        STAMP(26);
        j = 17; sb = L + 2u * 32768u;
#pragma nounroll
        for (unsigned t = 12; t < 27; ++t) ufill([] { waitv<48>(); BAR(); });
        STAMP(28);
      }
      sb = L + 2u * 32768u;
      waitv<48>(); BAR(); dT(); waitv<36>(); BAR(); dT(); peek(); waitv<24>(); BAR(); dT(); waitv<12>(); BAR(); dT(); BAR(); dT();
      if (isY) {
        BAR();
        if (tid == 256u) flag_set(fU + uft, tag);
      } else waitv<12>();
      STAMP(8);
    } else {
      sb = L + 2u * 32768u;
#pragma nounroll
      for (unsigned j = 0; j < 5; ++j) dT();
    }
    if (hasU && !isY) await(0xffffffffull, true);
    else { if (wid == 4u) await(isY ? 0x7full << (g0 >> 1) : 0xffffffffull, false); BAR(); }
#pragma nounroll
    for (unsigned j = 0; j < 5; ++j) { dma64d(sb + roff, voL, sR, rbase + (g0 + j) * 16384u); nexts(); }
    auto dstep = [&](auto wait) {
      T4 p; p.set(kW * 64u * ldT + tcol, lw, ldT);
      unsigned d0 = sb + toff, d1 = sb + roff, r0 = rbase + kT * 16384u; p.pin(); PINS(d0); PINS(d1); PINS(r0);
      wait(); BAR();
      p.go(d0, voT, sT); dma64d(d1, voL, sR, r0);
      ++kT; kW += (kW & 1u) ? kj : 1u; nexts();
    };
    waitv<16>(); BAR(); STAMP(11);
    dstep([] { waitv<12>(); }); dstep([] { waitv<16>(); }); dstep([] { waitv<20>(); });
    if (!isY && hasU && tid == 256u) flag_set(fU + uft, tag);
#pragma nounroll
    for (unsigned seg = isY ? 0u : 5u; seg < 6u; ++seg) {
      const unsigned n = !isY ? 56u : seg == 0u ? 2u : seg == 2u ? 10u : seg >= 4u ? 14u : 4u;
#pragma nounroll
      for (unsigned i = 0; i < n; ++i) dstep([] { waitv<24>(); });
      if (seg == 0u || seg == 2u) peek();
      else if (seg < 4u) { waitv<32>(); await(seg == 1u ? 0x3fffull << 14 : 0x3fffull, true); kT = seg == 1u ? 70u - g0 : 0u; kW = seg == 1u ? 10u - (xcd >> 1) * 2u : 0u; }
      else kW = 2u;
    }
    waitv<24>(); BAR();
    if (isY) { waitv<0>(); BAR(); STAMP(12); ST_OUT(1); return; }
    {
      const rsrc_t sX = mkrsrc(x, SEQ * DM * 2);
#pragma unroll
      for (int q = 0; q < 8; ++q)
        dma16(L + XO + (lw * 8u + q) * 1024u, (ln >> 4) * 2048u + 16u * ((ln & 15u) ^ ((4u * q + (ln >> 4)) & 15u)), sX, (st * 128u + lw * 32u + q * 4u) * 2048u + dt * 256u);
    }
    waitv<8>(); BAR();
    waitv<0>(); BAR();
    STAMP(12);
    {
      flag_wait<6>(fG, tag, 1ull);
      u32x4 g[8];
#pragma unroll
      for (int q = 0; q < 8; ++q) g[q] = __builtin_amdgcn_raw_buffer_load_b128(sG, voL + (lw * 8u + q) * 1024u, 0, 16);
#pragma unroll
      for (int q = 0; q < 8; ++q) *(LAS u32x4*)(size_t)(L + GO + voL + (lw * 8u + q) * 1024u) = g[q];
    }
    waitv<0>(); WAITL0(); STAMP(13); BAR();
  }
  finish();
}

extern "C" __global__ void rsk_end() {}
}

extern "C" void kernel_launch(void* const* d_in, const int* in_sizes, int n_in, void* d_out, int out_size, void* d_ws, size_t ws_size, hipStream_t stream) {
  const u16* x = (const u16*)d_in[0]; const u16* kc = (const u16*)d_in[1]; const u16* vc = (const u16*)d_in[2];
  const u16* wq = (const u16*)d_in[3]; const u16* wo = (const u16*)d_in[4];
  const u16* w1 = (const u16*)d_in[6]; const u16* b1 = (const u16*)d_in[7]; const u16* w2 = (const u16*)d_in[8]; const u16* b2 = (const u16*)d_in[9];
#define WSEND 0
  char* ws = (char*)d_ws + (((ws_size - WS_TOTAL) / 4 * WSEND) & ~(size_t)0xfffff);
  (void)hipFuncSetAttribute((const void*)rsk_main, hipFuncAttributeMaxDynamicSharedMemorySize, LDS_BYTES);
  rsk_main<<<256, 512, LDS_BYTES, stream>>>(x, ws, wq, kc, vc, w1, wo, w2, b1, b2, (u16*)d_out);
}
```

```cpp
#include <hip/hip_runtime.h>
#include <hip/hip_bf16.h>

namespace {
typedef short bf16x8 __attribute__((ext_vector_type(8)));
typedef short s16x4 __attribute__((ext_vector_type(4)));
typedef float f32x2 __attribute__((ext_vector_type(2)));
typedef float f32x4 __attribute__((ext_vector_type(4)));
typedef float f32x16 __attribute__((ext_vector_type(16)));
typedef unsigned u32x4 __attribute__((ext_vector_type(4)));
typedef unsigned u32x2 __attribute__((ext_vector_type(2)));
typedef __bf16 bf2_t __attribute__((ext_vector_type(2)));
typedef unsigned short u16;
typedef __amdgpu_buffer_rsrc_t rsrc_t;
#define LAS __attribute__((address_space(3)))
#define DI __device__ __forceinline__
#define BAR() asm volatile("s_barrier" ::: "memory")
#define WAITL0() do { asm volatile("" ::: "memory"); __builtin_amdgcn_s_waitcnt(0xC07F); asm volatile("" ::: "memory"); } while (0)
#define ALIGN8() asm volatile(".p2align 3")
#define SCHED() __builtin_amdgcn_sched_barrier(0)
#define SGB(mask, n) __builtin_amdgcn_sched_group_barrier(mask, n, 0)

constexpr int SEQ = 2048, DM = 1024, NH = 32, DH = 128, SK = 2048, WIN = 512, FF = 3584;
constexpr unsigned WS_XN = 0u, WS_H = 4u << 20, WS_O = 8u << 20, WS_U = 24u << 20, WS_G = 32u << 20, WS_FLAG = 36u << 20;
constexpr unsigned LDS_BYTES = 163840;
constexpr unsigned long long SPIN_LIMIT = 2000000ull;
constexpr int NW1 = 1;

template <int N> DI void waitv() { asm volatile("s_waitcnt vmcnt(%0)" :: "i"(N) : "memory"); }
DI unsigned rfl(unsigned x) { return __builtin_amdgcn_readfirstlane(x); }
DI rsrc_t mkrsrc(const void* p, unsigned bytes) { return __builtin_amdgcn_make_buffer_rsrc((void*)p, (short)0, (int)bytes, 0x00020000); }
DI void dma16(unsigned m0v, unsigned voff, rsrc_t r, unsigned soff) {
  asm volatile("s_mov_b32 m0, %0\n\ts_nop 0\n\tbuffer_load_dwordx4 %1, %2, %3 offen lds" :: "s"(m0v), "v"(voff), "s"(r), "s"(soff) : "memory");
}
DI rsrc_t code_rsrc() {
  unsigned long long b, e;
  asm volatile("s_getpc_b64 s[40:41]\n\ts_add_u32 s40, s40, rsk_main@rel32@lo+4\n\ts_addc_u32 s41, s41, rsk_main@rel32@hi+12\n\ts_mov_b64 %0, s[40:41]\n\t"
               "s_getpc_b64 s[40:41]\n\ts_add_u32 s40, s40, rsk_end@rel32@lo+4\n\ts_addc_u32 s41, s41, rsk_end@rel32@hi+12\n\ts_mov_b64 %1, s[40:41]"
               : "=&s"(b), "=&s"(e) :: "s40", "s41", "scc");
  return mkrsrc((const void*)b, (e > b && e - b < 0x40000ull) ? (unsigned)(e - b) : 0u);
}
template <bool DEV>
DI void dma64(unsigned m0v, unsigned va, unsigned vb, rsrc_t r, unsigned s0, unsigned s1, unsigned s2, unsigned s3) {
  if constexpr (DEV)
    asm volatile("s_mov_b32 m0, %0\n\ts_nop 0\n\tbuffer_load_dwordx4 %1, %3, %4 offen sc1 lds\n\tbuffer_load_dwordx4 %2, %3, %5 offen offset:1024 sc1 lds\n\t"
                 "buffer_load_dwordx4 %1, %3, %6 offen offset:2048 sc1 lds\n\tbuffer_load_dwordx4 %2, %3, %7 offen offset:3072 sc1 lds"
                 :: "s"(m0v), "v"(va), "v"(vb), "s"(r), "s"(s0), "s"(s1), "s"(s2), "s"(s3) : "memory");
  else
    asm volatile("s_mov_b32 m0, %0\n\ts_nop 0\n\tbuffer_load_dwordx4 %1, %3, %4 offen lds\n\tbuffer_load_dwordx4 %2, %3, %5 offen offset:1024 lds\n\t"
                 "buffer_load_dwordx4 %1, %3, %6 offen offset:2048 lds\n\tbuffer_load_dwordx4 %2, %3, %7 offen offset:3072 lds"
                 :: "s"(m0v), "v"(va), "v"(vb), "s"(r), "s"(s0), "s"(s1), "s"(s2), "s"(s3) : "memory");
}
DI void dma64d(unsigned m0v, unsigned va, rsrc_t r, unsigned s0) {
  asm volatile("s_mov_b32 m0, %0\n\ts_nop 0\n\tbuffer_load_dwordx4 %1, %2, %3 offen sc1 lds\n\tbuffer_load_dwordx4 %1, %2, %3 offen offset:1024 sc1 lds\n\t"
               "buffer_load_dwordx4 %1, %2, %3 offen offset:2048 sc1 lds\n\tbuffer_load_dwordx4 %1, %2, %3 offen offset:3072 sc1 lds"
               :: "s"(m0v), "v"(va), "s"(r), "s"(s0) : "memory");
}
DI rsrc_t mkrsrcB(const void* p, unsigned bytes) { return mkrsrc((const char*)p - 4096, bytes + 4096u); }
#define PINS(x) asm volatile("" : "+s"(x))
DI bf16x8 ldsR(unsigned a) { return *(const LAS bf16x8*)(size_t)a; }
DI s16x4 ldsT(unsigned a) { return __builtin_amdgcn_ds_read_tr16_b64_v4i16((LAS s16x4*)(size_t)a); }
DI bf16x8 cat(s16x4 a, s16x4 b) { return __builtin_shufflevector(a, b, 0, 1, 2, 3, 4, 5, 6, 7); }
DI unsigned pk2(float lo, float hi) { f32x2 v = {lo, hi}; return __builtin_bit_cast(unsigned, __builtin_convertvector(v, bf2_t)); }
DI float bflo(unsigned w) { return __uint_as_float(w << 16); }
DI float bfhi(unsigned w) { return __uint_as_float(w & 0xffff0000u); }
DI float rbf(float x) { return bflo(pk2(x, x) & 0xffffu); }
template <int CTRL, int RM> DI float dpp_add(float v) {
  return v + __builtin_bit_cast(float, __builtin_amdgcn_update_dpp(0, __builtin_bit_cast(int, v), CTRL, RM, 0xf, false));
}
DI void wsum2(float& u, float& v) {
#define WS2(C, M) u = dpp_add<C, M>(u); v = dpp_add<C, M>(v);
  WS2(0x111, 0xf) WS2(0x112, 0xf) WS2(0x114, 0xf) WS2(0x118, 0xf) WS2(0x142, 0xa) WS2(0x143, 0xc)
#undef WS2
  u = __builtin_bit_cast(float, __builtin_amdgcn_readlane(__builtin_bit_cast(int, u), 63));
  v = __builtin_bit_cast(float, __builtin_amdgcn_readlane(__builtin_bit_cast(int, v), 63));
}

DI void ln16(float (&v)[16]) {
  const float x0 = __builtin_bit_cast(float, __builtin_amdgcn_readfirstlane(__builtin_bit_cast(int, v[0])));
  float s = 0.f, q = 0.f;
#pragma unroll
  for (int i = 0; i < 16; ++i) { const float d = v[i] - x0; s += d; q = fmaf(d, d, q); }
  wsum2(s, q);
  const float md = s * (1.0f / DM), var = rbf(fmaxf(fmaf(-md, md, q * (1.0f / DM)), 0.f));
  const float mb = rbf(x0 + md), inv = __builtin_amdgcn_rcpf(rbf(__builtin_amdgcn_sqrtf(rbf(var + 1e-5f))));
#pragma unroll
  for (int i = 0; i < 16; i += 2) {
    const unsigned d = pk2(v[i] - mb, v[i + 1] - mb), r = pk2(bflo(d) * inv, bfhi(d) * inv);
    v[i] = bflo(r); v[i + 1] = bfhi(r);
  }
}
DI unsigned pkx(float lo, float hi) { return (__float_as_uint(lo) >> 16) | (__float_as_uint(hi) & 0xffff0000u); }
enum { RROW = 0, RCHK = 1, T1 = 2, T2 = 3 };
template <int KIND> struct Opnd {
  static constexpr int NA = KIND == RROW ? 2 : KIND == RCHK ? 1 : 4;
  static constexpr int RPB = KIND >= T1 ? 2 : 1;
  unsigned a[NA];
  DI void init(unsigned base, unsigned off, unsigned lane) {
    const unsigned fr = lane & 15u, g = lane >> 4, q = fr >> 2, p = lane & 3u;
    if constexpr (KIND == RROW) {
#pragma unroll
      for (int kk = 0; kk < 2; ++kk) a[kk] = base + (off + fr) * 128u + 16u * ((4u * kk + g) ^ (fr >> 1));
    } else if constexpr (KIND == RCHK) {
      a[0] = base + g * 2048u + (off + fr) * 16u;
    } else {
      const unsigned x = ((q >> 1) << 1) | (g & 1u);
#pragma unroll
      for (int n = 0; n < 4; ++n)
        a[n] = base + (KIND == T1 ? 4096u : 2048u) * (g >> 1) + 1024u * (off >> 6) + 128u * (4u * (g & 1u) + q) + 32u * ((unsigned)n ^ x) + 8u * p;
    }
  }
  DI bf16x8 ld(int b, int kk) const {
    if constexpr (KIND == RROW) return ldsR(a[kk] + 2048u * b);
    else if constexpr (KIND == RCHK) return ldsR(a[0] + 8192u * kk + 256u * b);
    else { const unsigned ad = a[b & 3] + 1024u * (b >> 2) + 8192u * kk; return cat(ldsT(ad), ldsT(ad + (KIND == T1 ? 2048u : 4096u))); }
  }
  DI void adv(unsigned delta) {
#pragma unroll
    for (int i = 0; i < NA; ++i) { a[i] += delta; asm volatile("" : "+v"(a[i])); }
  }
};

template <int PK, int QK, int NS, unsigned STG>
DI void gemm_wide(f32x4 (&acc)[8][4], Opnd<PK>& P, Opnd<QK>& Q, const int N) {
  bf16x8 pf[2][4][2], qf[4][2];
  auto loadP = [&](int h) {
#pragma unroll
    for (int p = 0; p < 4; ++p) { pf[h][p][0] = P.ld(4 * h + p, 0); pf[h][p][1] = P.ld(4 * h + p, 1); }
  };
  auto mma = [&](int h, int q) {
#pragma unroll
    for (int p0 = 0; p0 < 4; ++p0) {
      const int p = (q & 1) ? 3 - p0 : p0;
      acc[4 * h + p][q] = __builtin_amdgcn_mfma_f32_16x16x32_bf16(pf[h][p][0], qf[q][0], acc[4 * h + p][q], 0, 0, 0);
      acc[4 * h + p][q] = __builtin_amdgcn_mfma_f32_16x16x32_bf16(pf[h][p][1], qf[q][1], acc[4 * h + p][q], 0, 0, 0);
    }
  };
  BAR();
  loadP(0);
#pragma unroll
  for (int q = 0; q < 3; ++q) { qf[q][0] = Q.ld(q, 0); qf[q][1] = Q.ld(q, 1); }
  int stg = 0;
#pragma nounroll
  for (int t = 0; t < N; ++t) {
    SCHED();
    qf[3][0] = Q.ld(3, 0); qf[3][1] = Q.ld(3, 1);
    loadP(1); mma(0, 0); mma(0, 1); mma(0, 2); mma(0, 3);
    stg = (stg == NS - 1) ? 0 : stg + 1;
    const unsigned delta = stg == 0 ? (unsigned)(-(int)((NS - 1) * STG)) : STG;
    P.adv(delta); Q.adv(delta);
#pragma unroll
    for (int i = 0; i < 18; ++i) { SGB(0x8, 1); SGB(0x100, 1); }
#pragma unroll
    for (int i = 0; i < 14; ++i) { SGB(0x8, 1); SGB(0x2, 1); }
    SCHED();
    WAITL0(); BAR();
    SCHED();
#pragma unroll
    for (int q = 0; q < 4; ++q) {
      if (q > 0) { qf[q - 1][0] = Q.ld(q - 1, 0); qf[q - 1][1] = Q.ld(q - 1, 1); }
      if (q == 0) { pf[0][0][0] = P.ld(0, 0); pf[0][0][1] = P.ld(0, 1); pf[0][1][0] = P.ld(1, 0); pf[0][1][1] = P.ld(1, 1); }
      else if (q < 3) { pf[0][q + 1][0] = P.ld(q + 1, 0); pf[0][q + 1][1] = P.ld(q + 1, 1); }
      mma(1, q);
#pragma unroll
      for (int i = 0; i < 8; ++i) { SGB(0x8, 1); SGB(0x100, 1); }
      SCHED();
    }
  }
  SCHED();
}

template <int PK, int QK, int NS, unsigned STG, int TAIL, int S0, int DRAIN, class F>
DI void gemm_full(f32x4 (&acc)[4][4], Opnd<PK>& P, Opnd<QK>& Q, const int N, F hook) {
  bf16x8 pf[2][2][4], qf[2][2][4];
  constexpr int NR = 8 * (Opnd<PK>::RPB + Opnd<QK>::RPB);
  int stg = S0;
  auto load = [&](int b) {
#pragma unroll
    for (int i = 0; i < 4; ++i)
#pragma unroll
      for (int kk = 0; kk < 2; ++kk) { pf[b][kk][i] = P.ld(i, kk); qf[b][kk][i] = Q.ld(i, kk); }
    stg = (stg == NS - 1) ? 0 : stg + 1;
    const unsigned delta = stg == 0 ? (unsigned)(-(int)((NS - 1) * STG)) : STG;
    P.adv(delta); Q.adv(delta);
  };
  auto step = [&](int b, bool sync) {
    if (sync) { WAITL0(); BAR(); }
    SCHED();
    load(b ^ 1);
#pragma unroll
    for (int p = 0; p < 4; ++p)
#pragma unroll
      for (int q0 = 0; q0 < 4; ++q0) {
        const int q = (p & 1) ? 3 - q0 : q0;
        acc[p][q] = __builtin_amdgcn_mfma_f32_16x16x32_bf16(pf[b][0][p], qf[b][0][q], acc[p][q], 0, 0, 0);
        acc[p][q] = __builtin_amdgcn_mfma_f32_16x16x32_bf16(pf[b][1][p], qf[b][1][q], acc[p][q], 0, 0, 0);
      }
#pragma unroll
    for (int i = 0; i < NR; ++i) { SGB(0x8, 1); SGB(0x100, 1); }
#pragma unroll
    for (int i = NR; i < 32; ++i) { SGB(0x8, 1); SGB(0x2, 1); }
    SCHED();
  };
  BAR();
  load(0);
  if (DRAIN > 0) {
#pragma nounroll
    for (int t = 0; t < DRAIN; t += 2) { step(0, true); step(1, true); }
    waitv<0>();
    hook();
  }
#pragma nounroll
  for (int t = DRAIN; t < N - (TAIL ? 4 : 0); t += 2) { step(0, true); step(1, true); }
  if (TAIL) {
#pragma nounroll
    for (int t = 0; t < 4; t += 2) { step(0, true); step(1, false); }
  }
}

DI unsigned voffRow(unsigned lane, unsigned ldb, unsigned par) { return (lane >> 3) * ldb + 16u * ((lane & 7u) ^ (4u * par + (lane >> 4))); }
DI unsigned voffT(unsigned lane, unsigned ldb) { return (lane >> 3) * ldb + 16u * ((lane & 7u) ^ (2u * ((((lane >> 4) & 1u) << 1) | (lane >> 5)))); }
struct T4 { unsigned s0, s1, s2, s3;
  DI void set(unsigned so, unsigned lw, unsigned ldb) { s0 = so + 4096u + lw * 16u * ldb; s1 = s0 + 128u - 1024u; s2 = s0 + 8u * ldb - 2048u; s3 = s2 + 128u - 1024u; }
  DI void pin() { PINS(s0); PINS(s1); PINS(s2); PINS(s3); }
  template <bool DEV = false> DI void go(unsigned dst, unsigned vo, rsrc_t r) const { dma64<DEV>(dst, vo, vo, r, s0, s1, s2, s3); } };

typedef unsigned long long u64;
constexpr unsigned FL_H = 64, FL_O = 320, FL_U = 576, FL_G = 832, FL_X = 3072;
DI void flag_set(u64* p, u64 tag) { __hip_atomic_store(p, tag, __ATOMIC_RELAXED, __HIP_MEMORY_SCOPE_AGENT); }
DI u64 flag_get(const u64* p) { return __hip_atomic_load(p, __ATOMIC_RELAXED, __HIP_MEMORY_SCOPE_AGENT); }
template <int SLOW = 16> DI void flag_wait(const u64* p, u64 tag, u64 mask) {
  const u64 t0 = __builtin_amdgcn_s_memrealtime();
  while ((__ballot(flag_get(p) != tag) & mask) != 0ull) {
    const u64 dt = __builtin_amdgcn_s_memrealtime() - t0;
    if (dt > SPIN_LIMIT) break;
    if (dt > 400ull) __builtin_amdgcn_s_sleep(SLOW); else __builtin_amdgcn_s_sleep(3);
  }
  asm volatile("" ::: "memory");
}

DI f32x2 sigm2(f32x2 v, f32x2 z) {
  f32x2 e = {__builtin_amdgcn_exp2f(z[0]), __builtin_amdgcn_exp2f(z[1])};
  e += 1.0f;
  return v * (f32x2){__builtin_amdgcn_rcpf(e[0]), __builtin_amdgcn_rcpf(e[1])};
}
DI unsigned gelu2(f32x2 v) {
  const f32x2 r = sigm2(v, v * __builtin_elementwise_fma(v * v, (f32x2)(-0.1029432397f), (f32x2)(-2.3022081986f)));
  return pk2(r[0], r[1]);
}
DI unsigned silu2(f32x2 v) { const f32x2 r = sigm2(v, v * -1.4426950408889634f); return pk2(r[0], r[1]); }
DI f32x2 lo2(f32x4 v) { return __builtin_shufflevector(v, v, 0, 1); }
DI f32x2 hi2(f32x4 v) { return __builtin_shufflevector(v, v, 2, 3); }
DI f32x2 bf2(unsigned w) { return (f32x2){bflo(w), bfhi(w)}; }

constexpr unsigned QSTG = 49152u;
constexpr unsigned KBASE = 65536u, VBASE = 114688u;
constexpr unsigned KSLOT[3] = {0u, 32768u, 16384u}, VSLOT[3] = {32768u, 16384u, 0u};
DI void touch8k(rsrc_t r, unsigned vo128, unsigned soff) {
  asm volatile("buffer_load_dword v255, %0, %1, %2 offen" :: "v"(vo128), "s"(r), "s"(soff) : "memory", "v255");
}
constexpr float QSCALE = 0.12751743082f;
#define BIGSUM 4096.0f

struct Att {
  bf16x8 qf[8], kv[16], pf[4], pn[2];
  f32x16 o[4], s[2], negm;
  float l, ts;
  unsigned aK[4], aV[2];
};
template <unsigned SLOT, int J> DI void att_k(Att& a) {
  asm volatile("ds_read_b128 %0, %1 offset:%2" : "+v"(a.kv[J]) : "v"(a.aK[(J >> 1) & 3]), "i"(SLOT + 8192u * (J >> 3) + 4096u * (J & 1)) : "memory");
}
template <unsigned SLOT, int J> DI void att_v(Att& a) {
  s16x4 lo = __builtin_shufflevector(a.kv[J], a.kv[J], 0, 1, 2, 3), hi = __builtin_shufflevector(a.kv[J], a.kv[J], 4, 5, 6, 7);
  constexpr unsigned off = SLOT + 4096u * (J >> 2) + 1024u * ((J >> 1) & 1);
  asm volatile("ds_read_b64_tr_b16 %0, %2 offset:%3\n\tds_read_b64_tr_b16 %1, %2 offset:%4" : "+v"(lo), "+v"(hi) : "v"(a.aV[J & 1]), "i"(off), "i"(off + 2048u) : "memory");
  a.kv[J] = cat(lo, hi);
}
DI void att_wait(Att& a) {
  asm volatile("s_waitcnt lgkmcnt(0)" : "+v"(a.kv[0]), "+v"(a.kv[1]), "+v"(a.kv[2]), "+v"(a.kv[3]), "+v"(a.kv[4]), "+v"(a.kv[5]), "+v"(a.kv[6]), "+v"(a.kv[7]),
               "+v"(a.kv[8]), "+v"(a.kv[9]), "+v"(a.kv[10]), "+v"(a.kv[11]), "+v"(a.kv[12]), "+v"(a.kv[13]), "+v"(a.kv[14]), "+v"(a.kv[15]) :: "memory");
}
DI void att_loadK(Att& a, unsigned slot) {
#pragma unroll
  for (int j = 0; j < 16; ++j) a.kv[j] = ldsR(a.aK[(j >> 1) & 3] + slot + 8192u * (j >> 3) + 4096u * (j & 1));
}
DI unsigned lane_id() { unsigned l; asm volatile("v_mbcnt_lo_u32_b32 %0, -1, 0\n\tv_mbcnt_hi_u32_b32 %0, -1, %0" : "=v"(l)); return l; }
DI void add_bcast3(f32x16& t0, f32x16& t1, f32x16& t2, float v) {
  const float one = lane_id() < 32u ? 1.0f : 0.0f;
  asm volatile("s_nop 7\n\ts_nop 7\n\tv_mfma_f32_32x32x2_f32 %0, %3, %4, %0\n\tv_mfma_f32_32x32x2_f32 %1, %3, %4, %1\n\tv_mfma_f32_32x32x2_f32 %2, %3, %4, %2\n\t"
               "s_nop 7\n\ts_nop 7\n\ts_nop 7" : "+v"(t0), "+v"(t1), "+v"(t2) : "v"(one), "v"(v));
}
template <bool FIRST, unsigned SLOT, int J> DI void att_qk1(Att& a) {
  a.s[J & 1] = __builtin_amdgcn_mfma_f32_32x32x16_bf16(a.kv[J], a.qf[J >> 1], J < 2 ? (FIRST ? (f32x16){} : a.negm) : a.s[J & 1], 0, 0, 0);
  SCHED();
  if constexpr (J == 0) ALIGN8();
  if constexpr (!FIRST) att_v<SLOT, J>(a);
  SCHED();
  if constexpr (J < 15) att_qk1<FIRST, SLOT, J + 1>(a);
}
template <int NEXT, unsigned SLOT, int J> DI void att_pv1(Att& a) {
  a.o[J & 3] = __builtin_amdgcn_mfma_f32_32x32x16_bf16(a.kv[J], a.pf[J >> 2], a.o[J & 3], 0, 0, 0);
  SCHED();
  if constexpr (J == 0) ALIGN8();
  if constexpr (NEXT == 1) att_k<SLOT, J>(a); else if constexpr (NEXT == 2) att_v<SLOT, J>(a);
  SCHED();
  if constexpr (J < 15) att_pv1<NEXT, SLOT, J + 1>(a);
}
DI float max3(float x, float y, float z) { return __builtin_fmaxf(__builtin_fmaxf(x, y), z); }
DI float att_rowmax(const Att& a) {
  float m0 = max3(a.s[0][0], a.s[0][1], a.s[0][2]), m1 = max3(a.s[1][0], a.s[1][1], a.s[1][2]);
#pragma unroll
  for (int r = 3; r < 15; r += 2) { m0 = max3(m0, a.s[0][r], a.s[0][r + 1]); m1 = max3(m1, a.s[1][r], a.s[1][r + 1]); }
  const float mx = __builtin_fmaxf(max3(m0, m1, a.s[0][15]), a.s[1][15]);
  auto rr = __builtin_amdgcn_permlane32_swap(__float_as_uint(mx), __float_as_uint(mx), false, false);
  return __builtin_fmaxf(__uint_as_float(rr[0]), __uint_as_float(rr[1]));
}
DI float att_exp16(const f32x16& v, bf16x8& p0, bf16x8& p1) {
  float s0 = 0.f, s1 = 0.f;
  u32x4 w[2];
#pragma unroll
  for (int h = 0; h < 2; ++h) {
    float e[8];
#pragma unroll
    for (int r = 0; r < 8; ++r) e[r] = __builtin_amdgcn_exp2f(v[8 * h + r]);
#pragma unroll
    for (int r = 0; r < 8; r += 2) {
      if (h == 0 && r == 0) { s0 = e[0]; s1 = e[1]; } else { s0 += e[r]; asm("" : "+v"(s0)); s1 += e[r + 1]; asm("" : "+v"(s1)); }
      w[h][r >> 1] = pk2(e[r], e[r + 1]);
    }
    asm volatile("" : "+v"(w[h]));
    SCHED();
  }
  p0 = __builtin_bit_cast(bf16x8, w[0]); p1 = __builtin_bit_cast(bf16x8, w[1]);
  return s0 + s1;
}
template <bool FIRST> DI void att_sm1(Att& a) {
  if (FIRST) {
    const float mx = att_rowmax(a);
#pragma unroll
    for (int r = 0; r < 16; ++r) { a.s[0][r] -= mx; a.s[1][r] -= mx; a.negm[r] = -mx; }
  }
  a.ts = att_exp16(a.s[0], a.pn[0], a.pn[1]);
  asm volatile("" : "+v"(a.pn[0]), "+v"(a.pn[1]), "+v"(a.ts), "+v"(a.s[1]), "+v"(a.negm));
}
template <bool FIRST> DI void att_sm2(Att& a) {
  a.ts += att_exp16(a.s[1], a.pf[2], a.pf[3]);
  if (!FIRST) {
    if (__builtin_expect(!__all(a.ts <= BIGSUM), 0)) {
      const float dl = fmaxf(att_rowmax(a), 0.f), fct = __builtin_amdgcn_exp2f(-dl);
#pragma unroll
      for (int db = 0; db < 4; ++db) a.o[db] *= fct;
      a.l *= fct;
      add_bcast3(a.s[0], a.s[1], a.negm, -dl);
      a.ts = att_exp16(a.s[0], a.pn[0], a.pn[1]) + att_exp16(a.s[1], a.pf[2], a.pf[3]);
    }
  }
  a.l += a.ts;
  a.pf[0] = a.pn[0]; a.pf[1] = a.pn[1];
  asm volatile("" : "+v"(a.pf[0]), "+v"(a.pf[1]), "+v"(a.pf[2]), "+v"(a.pf[3]), "+v"(a.l));
}

extern "C" __global__ __launch_bounds__(512, 2) __attribute__((amdgpu_num_vgpr(127)))
void rsk_main(const u16* __restrict__ x, char* __restrict__ ws, const u16* __restrict__ wq, const u16* __restrict__ kc, const u16* __restrict__ vc,
              const u16* __restrict__ w1, const u16* __restrict__ wo, const u16* __restrict__ w2, const u16* __restrict__ b1, const u16* __restrict__ b2,
              u16* __restrict__ out) {
  if (rfl(threadIdx.x) == 0u) touch8k(code_rsrc(), threadIdx.x * 128u + (threadIdx.x < 48u ? 0u : (blockIdx.x >> 3) * 2048u), 0u);
  extern __shared__ __attribute__((aligned(16))) char lds[];
  const unsigned L = (unsigned)(size_t)lds;
  const char* xn = ws + WS_XN; const char* hh = ws + WS_H;
  const unsigned tid = threadIdx.x, lane = tid & 63u, wid = rfl(tid >> 6), grp = wid >> 2, lw = wid & 3u;
  const unsigned bid = blockIdx.x, xcd = bid & 7u, idx = bid >> 3;
  const unsigned qt = (xcd & 1u) * 4u + (idx & 3u), kvh = xcd >> 1, head = kvh * 8u + (idx >> 2);
  const bool hasU = idx < 28u;
  const unsigned udt = (xcd & 1u) * 4u + (idx & 3u), uft = (xcd >> 1) * 7u + (idx >> 2);
  const unsigned ufc = (idx >> 2) * 4u + (xcd >> 1);
  const rsrc_t sK = mkrsrc(kc + ((size_t)kvh * SK + (SK - WIN)) * DH, WIN * DH * 2), sV = mkrsrc(vc + ((size_t)kvh * SK + (SK - WIN)) * DH, WIN * DH * 2);
  const rsrc_t sH = mkrsrc(hh, SEQ * DM * 2), sW1 = mkrsrcB(w1, SEQ * FF * 2);
  const unsigned voK0 = voffRow(lane, 256u, 0), voV = voffT(lane, 256u);
  auto dmaK2 = [&](unsigned slot, unsigned t, unsigned p0) {
    unsigned voK1; asm volatile("v_xor_b32 %0, 64, %1" : "=v"(voK1) : "v"(voK0));
    dma16(L + KBASE + slot + 1024u * p0, voK0, sK, t * 16384u + (p0 & 7u) * 2048u + (p0 >> 3) * 128u);
    dma16(L + KBASE + slot + 1024u * p0 + 1024u, voK1, sK, t * 16384u + (p0 & 7u) * 2048u + 2048u + (p0 >> 3) * 128u); };
  auto dmaV2 = [&](unsigned slot, unsigned t, unsigned p0) {
    dma16(L + VBASE + slot + 1024u * p0, voV, sV, t * 16384u + (p0 >> 1) * 2048u);
    dma16(L + VBASE + slot + 1024u * p0 + 1024u, voV, sV, t * 16384u + (p0 >> 1) * 2048u + 128u); };
  auto tileK = [&](unsigned t) {
#pragma unroll
    for (int p = 0; p < 16; p += 2) dmaK2(KSLOT[t % 3], t, p); };
  auto tileV = [&](unsigned t) {
#pragma unroll
    for (int p = 0; p < 16; p += 2) dmaV2(VSLOT[t % 3], t, p); };
  Att a;
  { const unsigned r32 = lane & 31u, hi = lane >> 5;
#pragma unroll
    for (int j = 0; j < 4; ++j) a.aK[j] = L + KBASE + r32 * 128u + 16u * ((2u * j + hi) ^ ((r32 >> 1) & 7u)); }

  u64* flags = (u64*)(ws + WS_FLAG);
  const rsrc_t sS = lw == 0 ? mkrsrcB(wq, DM * NH * DH * 2) : mkrsrcB(xn, SEQ * DM * 2);
  unsigned asrc = 4096u + (lw == 0 ? head * 256u : (qt * 256u + (lw - 1u) * 128u) * 2048u), adst = L + lw * 16384u;
  const unsigned avo0 = lw == 0 ? voffT(lane, 8192u) : voffRow(lane, 2048u, 0), avo1 = lw == 0 ? avo0 : avo0 ^ 64u;
  constexpr unsigned HT = 147456u;
  auto astep = [&](unsigned vo0, unsigned vo1) {
    const unsigned sp = lw == 0 ? 8u * 8192u : 16u * 2048u, so = (lw == 0 ? 128u : 8u * 2048u) - 1024u;
#pragma unroll
    for (int q = 0; q < 4; ++q) { const unsigned e = asrc + 2u * q * sp; dma64<true>(adst + 4096u * q, vo0, vo1, sS, e, e + so, e + sp - 2048u, e + sp - 2048u + so); }
    asrc += lw == 0 ? 64u * 8192u : 128u; adst = adst >= L + 2u * QSTG ? adst - 2u * QSTG : adst + QSTG;
  };
  u64 tag;
  {
    const unsigned row = (bid >> 2) * 32u + ((bid >> 1) & 1u) * 16u + (bid & 1u) * 4u + (wid >> 2) * 8u + (wid & 3u);
    u32x4 xa, xb; u32x2 ep;
    asm volatile("buffer_load_dwordx4 %0, %3, %4, %5 offen\n\tbuffer_load_dwordx4 %1, %3, %4, %5 offen offset:1024\n\tglobal_load_dwordx2 %2, %6, %7 sc1"
                 : "=&v"(xa), "=&v"(xb), "=&v"(ep) : "v"(lane * 16u), "s"(mkrsrc(x, SEQ * DM * 2)), "s"(row * 2048u), "v"(0u), "s"(flags) : "memory");
    __builtin_amdgcn_s_setprio(1);
    if (wid == 7) {
      touch8k(mkrsrc(wq, DM * NH * DH * 2), lane * 128u, bid * 8192u);
      waitv<1>();
    } else waitv<0>();
    asm volatile("" : "+v"(xa), "+v"(xb), "+v"(ep));
    tag = (((u64)rfl(ep[1]) << 32) | rfl(ep[0])) * 0x9E3779B97F4A7C15ull + 0x632BE59BD9B4E019ull;
    float v[16];
#pragma unroll
    for (int i = 0; i < 4; ++i) { v[2 * i] = bflo(xa[i]); v[2 * i + 1] = bfhi(xa[i]); v[8 + 2 * i] = bflo(xb[i]); v[9 + 2 * i] = bfhi(xb[i]); }
    ln16(v);
    const rsrc_t sW = mkrsrc(ws, WS_O);
#pragma unroll
    for (int p = 0; p < 2; ++p) {
      const unsigned lo0 = pkx(v[8 * p], v[8 * p + 1]), lo1 = pkx(v[8 * p + 2], v[8 * p + 3]), hi0 = pkx(v[8 * p + 4], v[8 * p + 5]), hi1 = pkx(v[8 * p + 6], v[8 * p + 7]);
      const bool odd = lane & 1u;
      const unsigned s0 = __builtin_amdgcn_update_dpp(0, (int)(odd ? lo0 : hi0), 0xB1, 0xf, 0xf, false), s1 = __builtin_amdgcn_update_dpp(0, (int)(odd ? lo1 : hi1), 0xB1, 0xf, 0xf, false);
      __builtin_amdgcn_raw_buffer_store_b128(odd ? (u32x4){s0, s1, hi0, hi1} : (u32x4){lo0, lo1, s0, s1}, sW, WS_XN + row * 2048u + lane * 16u + 1024u * p, 0, 16);
    }
    waitv<0>();
    if (lane == 0u) flag_set(flags + FL_X + bid * 8u + wid, tag);
    if (wid == 4u) { astep(avo0, avo1); astep(avo0, avo1); }
    if (grp == 0) __builtin_amdgcn_s_setprio(0);
    ln16(v);
    *(LAS u32x4*)(size_t)(L + HT + wid * 2048u + lane * 16u) = (u32x4){pkx(v[0], v[1]), pkx(v[2], v[3]), pkx(v[4], v[5]), pkx(v[6], v[7])};
    *(LAS u32x4*)(size_t)(L + HT + wid * 2048u + 1024u + lane * 16u) = (u32x4){pkx(v[8], v[9]), pkx(v[10], v[11]), pkx(v[12], v[13]), pkx(v[14], v[15])};
    WAITL0();
    __builtin_amdgcn_s_setprio(0);
  }
  auto hstore = [&](unsigned i) {
    const unsigned d0 = i * 64u, ad = L + HT + ((lane & 15u) >> 2) * 2048u + (d0 + (lane >> 4) * 16u + (lane & 3u) * 4u) * 2u;
    __builtin_amdgcn_raw_buffer_store_b128(__builtin_bit_cast(u32x4, cat(ldsT(ad), ldsT(ad + 8192u))), mkrsrc(ws, WS_O), WS_H + ((d0 >> 7) * 256u + bid) * 2048u + ((d0 & 64u) + lane) * 16u, 0, 16);
  };
  if (!hasU) {
    BAR();
    hstore(wid * 2u); hstore(wid * 2u + 1u);
    waitv<0>(); BAR();
    if (tid == 0u) flag_set(flags + FL_H + bid, tag);
    const unsigned w = ((idx - 28u) * 8u + xcd) * 8u + wid, vo = lane * 128u;
    const rsrc_t sWq = mkrsrc(wq, DM * NH * DH * 2);
#pragma unroll
    for (int j = 1; j < 4; ++j) touch8k(sWq, vo, (j * 256u + w) * 8192u);
    touch8k(mkrsrc((w & 64u) ? vc : kc, 4u * SK * DH * 2u), w < 128u ? vo : 0x80000000u, ((w >> 4) & 3u) * (SK * DH * 2u) + (SK - WIN) * DH * 2u + (w & 15u) * 8192u);
#pragma unroll
    for (int j = 0; j < NW1; ++j) { waitv<2>(); touch8k(sW1, vo, (j * 256u + w) * 8192u + 4096u); }
    waitv<2>();
    if (wid < 7u - NW1) touch8k(sW1, vo, ((NW1 + wid) * 256u + bid) * 8192u + 4096u);
    if (wid < 7u) touch8k(mkrsrc(w2, FF * SEQ * 2), vo, (wid * 256u + bid) * 8192u);
    if (wid >= 4u) touch8k(mkrsrc(wo, NH * DH * DM * 2), vo, ((wid - 4u) * 256u + bid) * 8192u);
    waitv<0>();
  }
  if (wid == 7) {
    const unsigned vo = lane * 128u;
    BAR(); BAR();
#pragma nounroll
    for (unsigned i = 0; i < 4; ++i) {
      BAR();
      if (hasU) { hstore(i * 4u); hstore(i * 4u + 1u); hstore(i * 4u + 2u); hstore(i * 4u + 3u); }
    }
    BAR(); BAR();
    waitv<0>();
    if (lane == 0u) flag_set(flags + FL_H + bid, tag);
    BAR(); BAR(); BAR(); BAR();
    const u64 f0 = flag_get(flags + FL_H + lane), f1 = flag_get(flags + FL_H + 64u + lane), f2 = flag_get(flags + FL_H + 128u + lane), f3 = flag_get(flags + FL_H + 192u + lane);
    touch8k(code_rsrc(), lane < 16u ? vo : 0x80000000u, idx * 2048u);
    BAR(); BAR();
    if (__any(f0 != tag || f1 != tag || f2 != tag || f3 != tag)) {
#pragma nounroll
      for (unsigned j = 0; j < 256u; j += 64u) flag_wait(flags + FL_H + j + lane, tag, ~0ull);
    }
    if (bid == 0u && lane == 0u) flag_set(flags, tag);
    BAR(); BAR(); BAR();
#pragma unroll
    for (int j = NW1; j < 6; ++j) touch8k(sW1, vo, (j * 256u + bid) * 8192u + 4096u);
    att_loadK(a, KSLOT[0]);
  } else if (grp == 1) {
    const unsigned vo0 = avo0, vo1 = avo1;
    if (lw != 0) {
      const u64* fx = flags + FL_X + qt * 256u + (lw - 1u) * 128u + lane;
      const u64 t0 = __builtin_amdgcn_s_memrealtime();
      while (__any(((flag_get(fx) ^ tag) | (flag_get(fx + 64) ^ tag)) != 0ull)) {
        if (__builtin_amdgcn_s_memrealtime() - t0 > SPIN_LIMIT) break;
        __builtin_amdgcn_s_sleep(2);
      }
      asm volatile("" ::: "memory");
      astep(vo0, vo1); astep(vo0, vo1);
    }
    waitv<16>();
    BAR();
    astep(vo0, vo1);
#pragma nounroll
    for (unsigned t = 0; t < 13; ++t) { waitv<16>(); BAR(); astep(vo0, vo1); }
    waitv<16>(); BAR();
    if (lw == 0) tileK(0); else if (lw == 1) tileK(2); else tileV(0);
    waitv<16>(); BAR();
    if (lw == 1) tileK(1); else if (lw == 2) tileV(1);
    if (lw == 0) waitv<0>();
    BAR();
    att_loadK(a, KSLOT[0]);
  } else {
    Opnd<T1> P; Opnd<RROW> Q;
    P.init(L, 0u, lane); Q.init(L + 16384u, lw * 64u, lane);
    f32x4 acc[8][4] = {};
    gemm_wide<T1, RROW, 3, QSTG>(acc, P, Q, 16);
    att_loadK(a, KSLOT[0]);
    const unsigned wa = L + (lane >> 5) * 4096u + (lw * 64u + (lane & 15u)) * 16u + ((lane >> 4) & 1u) * 8u;
#pragma unroll
    for (int j = 0; j < 16; ++j) {
      const f32x4 v = acc[j >> 1][j & 1] * QSCALE;
      *(LAS u32x2*)(size_t)(wa + 8192u * (j >> 1) + 256u * (j & 1)) = (u32x2){pk2(v[0], v[1]), pk2(v[2], v[3])};
    }
#pragma unroll
    for (int ks = 0; ks < 8; ++ks) a.qf[ks] = ldsR(L + (2u * ks + (lane >> 5)) * 4096u + (lw * 64u + (lane & 31u)) * 16u);
    SCHED();
#pragma unroll
    for (int j = 0; j < 16; ++j) {
      a.s[j & 1] = __builtin_amdgcn_mfma_f32_32x32x16_bf16(a.kv[j], a.qf[j >> 1], j < 2 ? (f32x16){} : a.s[j & 1], 0, 0, 0);
      float v[4];
#pragma unroll
      for (int c = 0; c < 4; ++c) { v[c] = acc[j >> 1][2 + (j & 1)][c] * QSCALE; asm("" : "+v"(v[c])); }
      *(LAS u32x2*)(size_t)(wa + 8192u * (j >> 1) + 256u * (2 + (j & 1))) = (u32x2){pk2(v[0], v[1]), pk2(v[2], v[3])};
      SCHED();
    }
    WAITL0();
  }

  const rsrc_t sO = mkrsrc(ws + WS_O, SEQ * NH * DH * 2);
  {
    const unsigned l2 = lane_id(), hi = l2 >> 5, blk = (l2 >> 4) & 1u, q4 = (l2 & 15u) >> 2, p4 = l2 & 3u;
#pragma unroll
    for (int n = 0; n < 2; ++n) a.aV[n] = L + VBASE + 128u * (4u * hi + q4) + 32u * ((2u * n + blk) ^ (((q4 >> 1) << 1) | hi)) + 8u * p4;
#pragma unroll
    for (int db = 0; db < 4; ++db) a.o[db] = (f32x16){};
    a.l = 0.f;
    const rsrc_t sW1u = mkrsrc((const char*)w1 - 4096, hasU ? SEQ * FF * 2 + 4096u : 0u), sHu = mkrsrc(hh, hasU ? SEQ * DM * 2 : 0u);
    unsigned voU = 0;
    auto uP = [&](unsigned j) {
      const unsigned so = 4096u + (j * 64u + wid * 8u) * (FF * 2u) + ufc * 256u;
      asm volatile("s_mov_b32 m0, %0\n\ts_nop 0\n\tbuffer_load_dwordx4 %1, %2, %3 offen lds\n\tbuffer_load_dwordx4 %1, %2, %4 offen offset:1024 lds"
                   :: "s"(L + j * 32768u + wid * 2048u), "v"(voU), "s"(sW1u), "s"(so), "s"(so + 128u - 1024u) : "memory"); };
    auto uQ = [&](unsigned j) {
      asm volatile("s_mov_b32 m0, %0\n\ts_nop 0\n\tbuffer_load_dwordx4 %1, %2, %3 offen sc1 lds\n\tbuffer_load_dwordx4 %1, %2, %3 offen offset:1024 sc1 lds"
                   :: "s"(L + j * 32768u + 16384u + wid * 2048u), "v"(lane_id() * 16u), "s"(sHu), "s"((udt * 32u + j) * 16384u + wid * 2048u) : "memory"); };
    const bool pre = hasU && grp == 1;
    if (grp == 1) {
      if (wid != 7) waitv<0>();
      WAITL0(); BAR();
#pragma unroll
      for (int ks = 0; ks < 8; ++ks) a.qf[ks] = ldsR(L + (2u * ks + hi) * 4096u + (lw * 64u + 32u + (l2 & 31u)) * 16u);
      SCHED(); att_qk1<true, 0u, 0>(a);
    }
    const rsrc_t sWo = mkrsrc(wo, NH * DH * DM * 2), sW2 = mkrsrc(w2, FF * SEQ * 2);
    const unsigned lk = bid * 8192u + (wid >> 1) * 2048u + (wid & 1u) * 128u;
#define LOOK(v) do { if ((v) < 1) touch8k(sW1, voV, lk + 6u * 0x200000u + 4096u); else if ((v) < 5) touch8k(sWo, voV, lk + ((v) - 1) * 0x200000u); \
                     else if ((v) < 12) touch8k(sW2, voV, lk + (((v) - 2) % 7) * 0x200000u); } while (0)
#define ATT_TILE(i, FIRST, WB, WD)                                                                                            \
    SCHED(); if (!(FIRST)) att_qk1<false, VSLOT[((i) + 2) % 3], 0>(a); BAR(); SCHED();                                        \
    if ((i) <= 4) dmaK2(KSLOT[(i) % 3], (i) + 3, wid * 2u);                                                                   \
    LOOK(2 * (i));                                                                                                            \
    if ((i) == 5) { voU = voffT(lane_id(), FF * 2u); uP(0); uQ(0); } else if ((i) == 6) { uQ(1); uP(2); } else if ((i) == 7) uP(3); \
    att_sm1<FIRST>(a);                                                                                                        \
    waitv<WB>();                                                                                                              \
    if (!(FIRST)) att_wait(a); SCHED(); BAR();                                                                                \
    if (FIRST) att_loadK(a, KSLOT[1]); else { SCHED(); att_pv1<((i) < 7 ? 1 : 2), ((i) < 7 ? KSLOT[((i) + 1) % 3] : VSLOT[7 % 3]), 0>(a); } \
    BAR(); SCHED();                                                                                                           \
    if ((i) <= 5) dmaV2(VSLOT[((i) + 2) % 3], (i) + 2, wid * 2u);                                                             \
    LOOK(2 * (i) + 1);                                                                                                        \
    if ((i) == 5) uP(1); else if ((i) == 6) uQ(2); else if ((i) == 7) uQ(3);                                                  \
    att_sm2<FIRST>(a);                                                                                                        \
    waitv<WD>();                                                                                                              \
    att_wait(a); SCHED(); BAR();
    ATT_TILE(0, true, 63, 63) ATT_TILE(1, false, 63, 10) ATT_TILE(2, false, 10, 10) ATT_TILE(3, false, 10, 10)
    ATT_TILE(4, false, 10, 10) ATT_TILE(5, false, 12, 14) ATT_TILE(6, false, 15, 9) ATT_TILE(7, false, 63, 63)
#undef ATT_TILE
#undef LOOK
    SCHED(); att_pv1<0, 0u, 0>(a);
    if (grp == 0) { BAR(); waitv<0>(); }
    if (pre) { waitv<2>(); BAR(); }
  }
  auto ost2 = [&](int db) {
    const unsigned l3 = lane_id(), row = qt * 256u + lw * 64u + grp * 32u + (l3 & 31u);
    const unsigned oo = (((row >> 7) * 64u + head * 2u) * 8u + (l3 >> 5)) * 2048u + (row & 127u) * 16u;
#pragma unroll
    for (int i = 0; i < 2; ++i) {
      float w[8];
#pragma unroll
      for (int c = 0; c < 8; ++c) { w[c] = a.o[db][8 * i + c] * a.l; asm("" : "+v"(w[c])); }
      __builtin_amdgcn_raw_buffer_store_b128((u32x4){pk2(w[0], w[1]), pk2(w[2], w[3]), pk2(w[4], w[5]), pk2(w[6], w[7])}, sO, oo + db * 8192u + i * 4096u, 0, 16);
    }
  };
  { auto rr = __builtin_amdgcn_permlane32_swap(__float_as_uint(a.l), __float_as_uint(a.l), false, false);
    a.l = __builtin_amdgcn_rcpf(__uint_as_float(rr[0]) + __uint_as_float(rr[1])); }
  if (!(hasU && grp == 1)) { ost2(0); ost2(1); ost2(2); ost2(3); }
  const unsigned ln = lane_id();
  const bool isY = xcd >= 4u;
  const unsigned dt = udt, st = ((xcd >> 1) & 1u) * 8u + (idx >> 2), tile = st * 8u + dt;
  u64* fG = flags + FL_G + tile * 16u; u64* fO = flags + FL_O; u64* fU = flags + FL_U + dt * 32u;
  const rsrc_t sG = mkrsrc(ws + WS_G + (size_t)tile * 32768u, 32768u), sU = mkrsrc(ws + WS_U, DM * FF * 2);
  constexpr unsigned XO = 32768u, GO = 65536u;
  auto finish = [&]() {
    WAITL0(); BAR();
#pragma unroll
    for (int q = 0; q < 4; ++q) {
      const unsigned row = wid * 16u + q * 4u + (ln >> 4);
      const u32x4 v = *(const LAS u32x4*)(size_t)(L + XO + row * 256u + 16u * ((ln & 15u) ^ (row & 15u)));
      __builtin_amdgcn_raw_buffer_store_b128(v, mkrsrc(out, SEQ * DM * 2), (st * 128u + row) * 2048u + dt * 256u + (ln & 15u) * 16u, 0, 0);
    }
  };
  if (!hasU) {
    waitv<0>(); BAR();
    if (tid == 256u) flag_set(fO + qt * 32u + head, tag);
  }
  if (grp == 0) {
    const unsigned wp = lw >> 1, wq2 = lw & 1u, fr = ln & 15u, fq = ln >> 4;
    if (hasU) {
      Opnd<T1> P; Opnd<RCHK> Q;
      P.init(L, wp * 64u, ln); Q.init(L + 16384u, wq2 * 64u, ln);
      u32x2 bv[4];
      f32x4 acc[4][4] = {};
      gemm_full<T1, RCHK, 5, 32768u, 0, 0, 16>(acc, P, Q, 32, [&] {
#pragma unroll
        for (int p = 0; p < 4; ++p) bv[p] = *(const u32x2*)(b1 + ufc * 128u + wp * 64u + p * 16u + fq * 4u);
      });
      const unsigned uo = ((udt * 56u + uft * 2u + wp) * 8u + fq) * 2048u + (wq2 * 64u + fr) * 16u;
#pragma unroll
      for (int pp = 0; pp < 2; ++pp) {
        const f32x2 b0 = bf2(bv[2 * pp][0]), b1v = bf2(bv[2 * pp][1]), b2v = bf2(bv[2 * pp + 1][0]), b3 = bf2(bv[2 * pp + 1][1]);
#pragma unroll
        for (int m = 0; m < 4; ++m)
          __builtin_amdgcn_raw_buffer_store_b128((u32x4){gelu2(lo2(acc[2 * pp][m]) + b0), gelu2(hi2(acc[2 * pp][m]) + b1v), gelu2(lo2(acc[2 * pp + 1][m]) + b2v), gelu2(hi2(acc[2 * pp + 1][m]) + b3)},
                                                 sU, uo + pp * 8192u + m * 256u, 0, 16);
      }
      if (isY) { waitv<0>(); BAR(); }
    }
    if (isY || !hasU) BAR();
    f32x4 acc[4][4] = {};
    const unsigned t4 = (wid * 64u + ln) * 16u;
    if (isY) {
      float bias[4];
#pragma unroll
      for (int q = 0; q < 4; ++q) bias[q] = bflo(b2[st * 128u + wq2 * 64u + q * 16u + fr]);
      Opnd<RCHK> P; Opnd<T2> Q;
      P.init(L + 65536u, wp * 64u, ln); Q.init(L + 65536u + 16384u, wq2 * 64u, ln);
      gemm_full<RCHK, T2, 5, 32768u, 3, 2, 0>(acc, P, Q, 56, [] {});
#pragma unroll
      for (int j = 0; j < 8; ++j) {
        const int q = 2 * (j & 1); const f32x4 y0 = acc[j >> 1][q] + bias[q], y1 = acc[j >> 1][q + 1] + bias[q + 1];
        __builtin_amdgcn_raw_buffer_store_b128((u32x4){silu2(lo2(y0)), silu2(hi2(y0)), silu2(lo2(y1)), silu2(hi2(y1))}, sG, t4 + j * 4096u, 0, 16);
      }
      waitv<0>(); BAR();
      if (tid == 0u) flag_set(fG, tag);
      return;
    }
    Opnd<T1> P; Opnd<RCHK> Q;
    P.init(L + 65536u, wp * 64u, ln); Q.init(L + 65536u + 16384u, wq2 * 64u, ln);
    gemm_full<T1, RCHK, 5, 32768u, 3, 2, 2>(acc, P, Q, 64, [] {});
    const unsigned xa = L + XO + (wq2 * 64u + fr) * 256u + (fq & 1u) * 8u;
    unsigned xp[4];
    f32x4 xv[4][4];
#pragma unroll
    for (int p = 0; p < 4; ++p) {
      xp[p] = xa + 16u * ((wp * 8u + 2u * p + (fq >> 1)) ^ fr);
#pragma unroll
      for (int q = 0; q < 4; ++q) {
        const u32x2 w = *(const LAS u32x2*)(size_t)(xp[p] + q * 4096u);
        xv[p][q] = (f32x4){bflo(w[0]), bfhi(w[0]), bflo(w[1]), bfhi(w[1])};
        asm volatile("" : "+v"(xv[p][q]));
      }
    }
    BAR();
    u32x4 gp[8];
#pragma unroll
    for (int j = 0; j < 8; ++j) gp[j] = *(const LAS u32x4*)(size_t)(L + GO + t4 + j * 4096u);
#pragma unroll
    for (int p = 0; p < 4; ++p)
#pragma unroll
      for (int q = 0; q < 4; ++q) {
        const unsigned g0 = gp[2 * p + (q >> 1)][2 * (q & 1)], g1 = gp[2 * p + (q >> 1)][2 * (q & 1) + 1];
        const f32x4 r = __builtin_elementwise_fma(acc[p][q], (f32x4){bflo(g0), bfhi(g0), bflo(g1), bfhi(g1)}, xv[p][q]);
        *(LAS u32x2*)(size_t)(xp[p] + q * 4096u) = (u32x2){pk2(r[0], r[1]), pk2(r[2], r[3])};
      }
  } else {
    const unsigned N = isY ? 56u : 64u;
    const rsrc_t sT = isY ? mkrsrcB(w2, FF * SEQ * 2) : mkrsrcB(wo, NH * DH * DM * 2);
    const rsrc_t sR = isY ? sU : sO;
    const unsigned ldT = isY ? 4096u : 2048u, voT = voffT(ln, ldT), voL = ln * 16u;
    const unsigned tcol = (isY ? st : dt) * 256u, rbase = (isY ? dt : st) * N * 16384u + lw * 4096u;
    const unsigned toff = (isY ? 16384u : 0u) + lw * 4096u, roff = (isY ? 0u : 16384u) + lw * 4096u, g0 = isY ? (xcd >> 1) * 14u : 0u;
    const u64* dep = (isY ? fU : fO + (st >> 1) * 32u) + (ln & 31u);
    auto peek = [&]() { asm volatile("global_load_dwordx2 v[254:255], %0, off sc1" :: "v"(dep) : "memory", "v254", "v255"); };
    auto await = [&](u64 mask, bool peeked) {
      if (peeked) { u64 v; asm volatile("v_mov_b64 %0, v[254:255]" : "=v"(v) :: "memory"); if ((__ballot(v != tag) & mask) == 0ull) return; }
      flag_wait(dep, tag, mask);
    };
    unsigned sb = L + 4u * 32768u;
    auto nexts = [&]() { sb = sb == L + 4u * 32768u ? L : sb + 32768u; };
    unsigned kT = g0;
    unsigned kW = isY ? (xcd >> 1) * 2u : 0u;
    const unsigned kj = isY ? 7u : 1u;
    auto dT = [&]() { T4 p; p.set(kW * 64u * ldT + tcol, lw, ldT); p.go(sb + toff, voT, sT); ++kT; kW += (kW & 1u) ? kj : 1u; nexts(); };
    if (hasU) {
      const unsigned voW1 = voffT(ln, FF * 2u), hw = (wid & 1u) * 2u;
      unsigned j = 4;
      auto ufill = [&](auto wait) {
        T4 p, q; p.set(j * 64u * (FF * 2u) + ufc * 256u, hw, FF * 2u); q.set(j * 64u * (FF * 2u) + ufc * 256u, hw + 1u, FF * 2u);
        unsigned d0 = sb + hw * 4096u, r0 = (udt * 32u + j) * 16384u + hw * 4096u; p.pin(); q.pin(); PINS(d0); PINS(r0);
        wait();
        p.go(d0, voW1, sW1); dma64d(d0 + 16384u, voL, sH, r0); q.go(d0 + 4096u, voW1, sW1); dma64d(d0 + 20480u, voL, sH, r0 + 4096u);
        ++j; nexts();
      };
      if (wid >= 6u) {
        ufill([] {});
#pragma nounroll
        for (unsigned t = 0; t < 12; ++t) ufill([] { waitv<48>(); BAR(); });
        waitv<48>(); BAR(); ost2(0); waitv<34>(); BAR(); ost2(1); waitv<20>(); BAR(); ost2(2); waitv<6>(); BAR(); ost2(3);
        touch8k(code_rsrc(), ln < 10u ? ln * 128u : 0x80000000u, (idx * 2u + (wid & 1u)) * 1280u);
        for (unsigned t = 16; t < 22; ++t) BAR();
        waitv<0>(); BAR();
        if (tid == 384u) flag_set(fO + qt * 32u + head, tag);
        BAR(); BAR(); BAR(); BAR();
      } else {
        BAR(); BAR(); waitv<0>(); BAR();
        ost2(0); BAR(); ost2(1); BAR(); ost2(2); BAR(); ost2(3);
        for (unsigned t = 6; t < 12; ++t) BAR();
        j = 17; sb = L + 2u * 32768u;
#pragma nounroll
        for (unsigned t = 12; t < 27; ++t) ufill([] { waitv<48>(); BAR(); });
      }
      sb = L + 2u * 32768u;
      waitv<48>(); BAR(); dT(); waitv<36>(); BAR(); dT(); peek(); waitv<24>(); BAR(); dT(); waitv<12>(); BAR(); dT(); BAR(); dT();
      if (isY) {
        BAR();
        if (tid == 256u) flag_set(fU + uft, tag);
      } else waitv<12>();
    } else {
      sb = L + 2u * 32768u;
#pragma nounroll
      for (unsigned j = 0; j < 5; ++j) dT();
    }
    if (hasU && !isY) await(0xffffffffull, true);
    else { if (wid == 4u) await(isY ? 0x7full << (g0 >> 1) : 0xffffffffull, false); BAR(); }
#pragma nounroll
    for (unsigned j = 0; j < 5; ++j) { dma64d(sb + roff, voL, sR, rbase + (g0 + j) * 16384u); nexts(); }
    auto dstep = [&](auto wait) {
      T4 p; p.set(kW * 64u * ldT + tcol, lw, ldT);
      unsigned d0 = sb + toff, d1 = sb + roff, r0 = rbase + kT * 16384u; p.pin(); PINS(d0); PINS(d1); PINS(r0);
      wait(); BAR();
      p.go(d0, voT, sT); dma64d(d1, voL, sR, r0);
      ++kT; kW += (kW & 1u) ? kj : 1u; nexts();
    };
    waitv<16>(); BAR();
    dstep([] { waitv<12>(); }); dstep([] { waitv<16>(); }); dstep([] { waitv<20>(); });
    if (!isY && hasU && tid == 256u) flag_set(fU + uft, tag);
#pragma nounroll
    for (unsigned seg = isY ? 0u : 5u; seg < 6u; ++seg) {
      const unsigned n = !isY ? 56u : seg == 0u ? 2u : seg == 2u ? 10u : seg >= 4u ? 14u : 4u;
#pragma nounroll
      for (unsigned i = 0; i < n; ++i) dstep([] { waitv<24>(); });
      if (seg == 0u || seg == 2u) peek();
      else if (seg < 4u) { waitv<32>(); await(seg == 1u ? 0x3fffull << 14 : 0x3fffull, true); kT = seg == 1u ? 70u - g0 : 0u; kW = seg == 1u ? 10u - (xcd >> 1) * 2u : 0u; }
      else kW = 2u;
    }
    waitv<24>(); BAR();
    if (isY) { waitv<0>(); BAR(); return; }
    {
      const rsrc_t sX = mkrsrc(x, SEQ * DM * 2);
#pragma unroll
      for (int q = 0; q < 8; ++q)
        dma16(L + XO + (lw * 8u + q) * 1024u, (ln >> 4) * 2048u + 16u * ((ln & 15u) ^ ((4u * q + (ln >> 4)) & 15u)), sX, (st * 128u + lw * 32u + q * 4u) * 2048u + dt * 256u);
    }
    waitv<8>(); BAR();
    waitv<0>(); BAR();
    {
      flag_wait<6>(fG, tag, 1ull);
      u32x4 g[8];
#pragma unroll
      for (int q = 0; q < 8; ++q) g[q] = __builtin_amdgcn_raw_buffer_load_b128(sG, voL + (lw * 8u + q) * 1024u, 0, 16);
#pragma unroll
      for (int q = 0; q < 8; ++q) *(LAS u32x4*)(size_t)(L + GO + voL + (lw * 8u + q) * 1024u) = g[q];
    }
    waitv<0>(); WAITL0(); BAR();
  }
  finish();
}

extern "C" __global__ void rsk_end() {}
}

extern "C" void kernel_launch(void* const* d_in, const int* in_sizes, int n_in, void* d_out, int out_size, void* d_ws, size_t ws_size, hipStream_t stream) {
  const u16* x = (const u16*)d_in[0]; const u16* kc = (const u16*)d_in[1]; const u16* vc = (const u16*)d_in[2];
  const u16* wq = (const u16*)d_in[3]; const u16* wo = (const u16*)d_in[4];
  const u16* w1 = (const u16*)d_in[6]; const u16* b1 = (const u16*)d_in[7]; const u16* w2 = (const u16*)d_in[8]; const u16* b2 = (const u16*)d_in[9];
  char* ws = (char*)d_ws;
  (void)hipFuncSetAttribute((const void*)rsk_main, hipFuncAttributeMaxDynamicSharedMemorySize, LDS_BYTES);
  rsk_main<<<256, 512, LDS_BYTES, stream>>>(x, ws, wq, kc, vc, w1, wo, w2, b1, b2, (u16*)d_out);
}
```

```cpp
#include <hip/hip_runtime.h>
#include <hip/hip_bf16.h>

namespace {
typedef short bf16x8 __attribute__((ext_vector_type(8)));
typedef short s16x4 __attribute__((ext_vector_type(4)));
typedef float f32x2 __attribute__((ext_vector_type(2)));
typedef float f32x4 __attribute__((ext_vector_type(4)));
typedef float f32x16 __attribute__((ext_vector_type(16)));
typedef unsigned u32x4 __attribute__((ext_vector_type(4)));
typedef unsigned u32x2 __attribute__((ext_vector_type(2)));
typedef __bf16 bf2_t __attribute__((ext_vector_type(2)));
typedef unsigned short u16;
typedef __amdgpu_buffer_rsrc_t rsrc_t;
#define LAS __attribute__((address_space(3)))
#define DI __device__ __forceinline__
#define BAR() asm volatile("s_barrier" ::: "memory")
#define WAITL0() do { asm volatile("" ::: "memory"); __builtin_amdgcn_s_waitcnt(0xC07F); asm volatile("" ::: "memory"); } while (0)
#define ALIGN8() asm volatile(".p2align 3")
#define SCHED() __builtin_amdgcn_sched_barrier(0)
#define SGB(mask, n) __builtin_amdgcn_sched_group_barrier(mask, n, 0)

constexpr int SEQ = 2048, DM = 1024, NH = 32, DH = 128, SK = 2048, WIN = 512, FF = 3584;
constexpr unsigned WS_XN = 0u, WS_H = 4u << 20, WS_O = 8u << 20, WS_U = 24u << 20, WS_G = 32u << 20, WS_FLAG = 36u << 20;
constexpr unsigned LDS_BYTES = 163840;
constexpr unsigned long long SPIN_LIMIT = 2000000ull;
constexpr int NW1 = 1;

#define ST_DECL
#define STAMP(i) do {} while (0)
#define ST_OUT(k)
template <int N> DI void waitv() { asm volatile("s_waitcnt vmcnt(%0)" :: "i"(N) : "memory"); }
DI unsigned rfl(unsigned x) { return __builtin_amdgcn_readfirstlane(x); }
DI rsrc_t mkrsrc(const void* p, unsigned bytes) { return __builtin_amdgcn_make_buffer_rsrc((void*)p, (short)0, (int)bytes, 0x00020000); }
DI void dma16(unsigned m0v, unsigned voff, rsrc_t r, unsigned soff) {
  asm volatile("s_mov_b32 m0, %0\n\ts_nop 0\n\tbuffer_load_dwordx4 %1, %2, %3 offen lds" :: "s"(m0v), "v"(voff), "s"(r), "s"(soff) : "memory");
}
DI rsrc_t code_rsrc() {
  unsigned long long b, e;
  asm volatile("s_getpc_b64 s[40:41]\n\ts_add_u32 s40, s40, rsk_main@rel32@lo+4\n\ts_addc_u32 s41, s41, rsk_main@rel32@hi+12\n\ts_mov_b64 %0, s[40:41]\n\t"
               "s_getpc_b64 s[40:41]\n\ts_add_u32 s40, s40, rsk_end@rel32@lo+4\n\ts_addc_u32 s41, s41, rsk_end@rel32@hi+12\n\ts_mov_b64 %1, s[40:41]"
               : "=&s"(b), "=&s"(e) :: "s40", "s41", "scc");
  return mkrsrc((const void*)b, (e > b && e - b < 0x40000ull) ? (unsigned)(e - b) : 0u);
}
template <bool DEV>
DI void dma64(unsigned m0v, unsigned va, unsigned vb, rsrc_t r, unsigned s0, unsigned s1, unsigned s2, unsigned s3) {
  if constexpr (DEV)
    asm volatile("s_mov_b32 m0, %0\n\ts_nop 0\n\tbuffer_load_dwordx4 %1, %3, %4 offen sc1 lds\n\tbuffer_load_dwordx4 %2, %3, %5 offen offset:1024 sc1 lds\n\t"
                 "buffer_load_dwordx4 %1, %3, %6 offen offset:2048 sc1 lds\n\tbuffer_load_dwordx4 %2, %3, %7 offen offset:3072 sc1 lds"
                 :: "s"(m0v), "v"(va), "v"(vb), "s"(r), "s"(s0), "s"(s1), "s"(s2), "s"(s3) : "memory");
  else
    asm volatile("s_mov_b32 m0, %0\n\ts_nop 0\n\tbuffer_load_dwordx4 %1, %3, %4 offen lds\n\tbuffer_load_dwordx4 %2, %3, %5 offen offset:1024 lds\n\t"
                 "buffer_load_dwordx4 %1, %3, %6 offen offset:2048 lds\n\tbuffer_load_dwordx4 %2, %3, %7 offen offset:3072 lds"
                 :: "s"(m0v), "v"(va), "v"(vb), "s"(r), "s"(s0), "s"(s1), "s"(s2), "s"(s3) : "memory");
}
DI void dma64d(unsigned m0v, unsigned va, rsrc_t r, unsigned s0) {
  asm volatile("s_mov_b32 m0, %0\n\ts_nop 0\n\tbuffer_load_dwordx4 %1, %2, %3 offen sc1 lds\n\tbuffer_load_dwordx4 %1, %2, %3 offen offset:1024 sc1 lds\n\t"
               "buffer_load_dwordx4 %1, %2, %3 offen offset:2048 sc1 lds\n\tbuffer_load_dwordx4 %1, %2, %3 offen offset:3072 sc1 lds"
               :: "s"(m0v), "v"(va), "s"(r), "s"(s0) : "memory");
}
DI rsrc_t mkrsrcB(const void* p, unsigned bytes) { return mkrsrc((const char*)p - 4096, bytes + 4096u); }
#define PINS(x) asm volatile("" : "+s"(x))
DI bf16x8 ldsR(unsigned a) { return *(const LAS bf16x8*)(size_t)a; }
DI s16x4 ldsT(unsigned a) { return __builtin_amdgcn_ds_read_tr16_b64_v4i16((LAS s16x4*)(size_t)a); }
DI bf16x8 cat(s16x4 a, s16x4 b) { return __builtin_shufflevector(a, b, 0, 1, 2, 3, 4, 5, 6, 7); }
DI unsigned pk2(float lo, float hi) { f32x2 v = {lo, hi}; return __builtin_bit_cast(unsigned, __builtin_convertvector(v, bf2_t)); }
DI float bflo(unsigned w) { return __uint_as_float(w << 16); }
DI float bfhi(unsigned w) { return __uint_as_float(w & 0xffff0000u); }
DI float rbf(float x) { return bflo(pk2(x, x) & 0xffffu); }
template <int CTRL, int RM> DI float dpp_add(float v) {
  return v + __builtin_bit_cast(float, __builtin_amdgcn_update_dpp(0, __builtin_bit_cast(int, v), CTRL, RM, 0xf, false));
}
DI void wsum2(float& u, float& v) {
#define WS2(C, M) u = dpp_add<C, M>(u); v = dpp_add<C, M>(v);
  WS2(0x111, 0xf) WS2(0x112, 0xf) WS2(0x114, 0xf) WS2(0x118, 0xf) WS2(0x142, 0xa) WS2(0x143, 0xc)
#undef WS2
  u = __builtin_bit_cast(float, __builtin_amdgcn_readlane(__builtin_bit_cast(int, u), 63));
  v = __builtin_bit_cast(float, __builtin_amdgcn_readlane(__builtin_bit_cast(int, v), 63));
}

DI void ln16(float (&v)[16]) {
  const float x0 = __builtin_bit_cast(float, __builtin_amdgcn_readfirstlane(__builtin_bit_cast(int, v[0])));
  float s = 0.f, q = 0.f;
#pragma unroll
  for (int i = 0; i < 16; ++i) { const float d = v[i] - x0; s += d; q = fmaf(d, d, q); }
  wsum2(s, q);
  const float md = s * (1.0f / DM), var = rbf(fmaxf(fmaf(-md, md, q * (1.0f / DM)), 0.f));
  const float mb = rbf(x0 + md), inv = __builtin_amdgcn_rcpf(rbf(__builtin_amdgcn_sqrtf(rbf(var + 1e-5f))));
#pragma unroll
  for (int i = 0; i < 16; i += 2) {
    const unsigned d = pk2(v[i] - mb, v[i + 1] - mb), r = pk2(bflo(d) * inv, bfhi(d) * inv);
    v[i] = bflo(r); v[i + 1] = bfhi(r);
  }
}
DI unsigned pkx(float lo, float hi) { return (__float_as_uint(lo) >> 16) | (__float_as_uint(hi) & 0xffff0000u); }
enum { RROW = 0, RCHK = 1, T1 = 2, T2 = 3 };
template <int KIND> struct Opnd {
  static constexpr int NA = KIND == RROW ? 2 : KIND == RCHK ? 1 : 4;
  static constexpr int RPB = KIND >= T1 ? 2 : 1;
  unsigned a[NA];
  DI void init(unsigned base, unsigned off, unsigned lane) {
    const unsigned fr = lane & 15u, g = lane >> 4, q = fr >> 2, p = lane & 3u;
    if constexpr (KIND == RROW) {
#pragma unroll
      for (int kk = 0; kk < 2; ++kk) a[kk] = base + (off + fr) * 128u + 16u * ((4u * kk + g) ^ (fr >> 1));
    } else if constexpr (KIND == RCHK) {
      a[0] = base + g * 2048u + (off + fr) * 16u;
    } else {
      const unsigned x = ((q >> 1) << 1) | (g & 1u);
#pragma unroll
      for (int n = 0; n < 4; ++n)
        a[n] = base + (KIND == T1 ? 4096u : 2048u) * (g >> 1) + 1024u * (off >> 6) + 128u * (4u * (g & 1u) + q) + 32u * ((unsigned)n ^ x) + 8u * p;
    }
  }
  DI bf16x8 ld(int b, int kk) const {
    if constexpr (KIND == RROW) return ldsR(a[kk] + 2048u * b);
    else if constexpr (KIND == RCHK) return ldsR(a[0] + 8192u * kk + 256u * b);
    else { const unsigned ad = a[b & 3] + 1024u * (b >> 2) + 8192u * kk; return cat(ldsT(ad), ldsT(ad + (KIND == T1 ? 2048u : 4096u))); }
  }
  DI void adv(unsigned delta) {
#pragma unroll
    for (int i = 0; i < NA; ++i) { a[i] += delta; asm volatile("" : "+v"(a[i])); }
  }
};

template <int PK, int QK, int NS, unsigned STG>
DI void gemm_wide(f32x4 (&acc)[8][4], Opnd<PK>& P, Opnd<QK>& Q, const int N) {
  bf16x8 pf[2][4][2], qf[4][2];
  auto loadP = [&](int h) {
#pragma unroll
    for (int p = 0; p < 4; ++p) { pf[h][p][0] = P.ld(4 * h + p, 0); pf[h][p][1] = P.ld(4 * h + p, 1); }
  };
  auto mma = [&](int h, int q) {
#pragma unroll
    for (int p0 = 0; p0 < 4; ++p0) {
      const int p = (q & 1) ? 3 - p0 : p0;
      acc[4 * h + p][q] = __builtin_amdgcn_mfma_f32_16x16x32_bf16(pf[h][p][0], qf[q][0], acc[4 * h + p][q], 0, 0, 0);
      acc[4 * h + p][q] = __builtin_amdgcn_mfma_f32_16x16x32_bf16(pf[h][p][1], qf[q][1], acc[4 * h + p][q], 0, 0, 0);
    }
  };
  BAR();
  loadP(0);
#pragma unroll
  for (int q = 0; q < 3; ++q) { qf[q][0] = Q.ld(q, 0); qf[q][1] = Q.ld(q, 1); }
  int stg = 0;
#pragma nounroll
  for (int t = 0; t < N; ++t) {
    SCHED();
    qf[3][0] = Q.ld(3, 0); qf[3][1] = Q.ld(3, 1);
    loadP(1); mma(0, 0); mma(0, 1); mma(0, 2); mma(0, 3);
    stg = (stg == NS - 1) ? 0 : stg + 1;
    const unsigned delta = stg == 0 ? (unsigned)(-(int)((NS - 1) * STG)) : STG;
    P.adv(delta); Q.adv(delta);
#pragma unroll
    for (int i = 0; i < 18; ++i) { SGB(0x8, 1); SGB(0x100, 1); }
#pragma unroll
    for (int i = 0; i < 14; ++i) { SGB(0x8, 1); SGB(0x2, 1); }
    SCHED();
    WAITL0(); BAR();
    SCHED();
#pragma unroll
    for (int q = 0; q < 4; ++q) {
      if (q > 0) { qf[q - 1][0] = Q.ld(q - 1, 0); qf[q - 1][1] = Q.ld(q - 1, 1); }
      if (q == 0) { pf[0][0][0] = P.ld(0, 0); pf[0][0][1] = P.ld(0, 1); pf[0][1][0] = P.ld(1, 0); pf[0][1][1] = P.ld(1, 1); }
      else if (q < 3) { pf[0][q + 1][0] = P.ld(q + 1, 0); pf[0][q + 1][1] = P.ld(q + 1, 1); }
      mma(1, q);
#pragma unroll
      for (int i = 0; i < 8; ++i) { SGB(0x8, 1); SGB(0x100, 1); }
      SCHED();
    }
  }
  SCHED();
}

template <int PK, int QK, int NS, unsigned STG, int TAIL, int S0, int DRAIN, class F>
DI void gemm_full(f32x4 (&acc)[4][4], Opnd<PK>& P, Opnd<QK>& Q, const int N, F hook) {
  bf16x8 pf[2][2][4], qf[2][2][4];
  constexpr int NR = 8 * (Opnd<PK>::RPB + Opnd<QK>::RPB);
  int stg = S0;
  auto load = [&](int b) {
#pragma unroll
    for (int i = 0; i < 4; ++i)
#pragma unroll
      for (int kk = 0; kk < 2; ++kk) { pf[b][kk][i] = P.ld(i, kk); qf[b][kk][i] = Q.ld(i, kk); }
    stg = (stg == NS - 1) ? 0 : stg + 1;
    const unsigned delta = stg == 0 ? (unsigned)(-(int)((NS - 1) * STG)) : STG;
    P.adv(delta); Q.adv(delta);
  };
  auto step = [&](int b, bool sync) {
    if (sync) { WAITL0(); BAR(); }
    SCHED();
    load(b ^ 1);
#pragma unroll
    for (int p = 0; p < 4; ++p)
#pragma unroll
      for (int q0 = 0; q0 < 4; ++q0) {
        const int q = (p & 1) ? 3 - q0 : q0;
        acc[p][q] = __builtin_amdgcn_mfma_f32_16x16x32_bf16(pf[b][0][p], qf[b][0][q], acc[p][q], 0, 0, 0);
        acc[p][q] = __builtin_amdgcn_mfma_f32_16x16x32_bf16(pf[b][1][p], qf[b][1][q], acc[p][q], 0, 0, 0);
      }
#pragma unroll
    for (int i = 0; i < NR; ++i) { SGB(0x8, 1); SGB(0x100, 1); }
#pragma unroll
    for (int i = NR; i < 32; ++i) { SGB(0x8, 1); SGB(0x2, 1); }
    SCHED();
  };
  BAR();
  load(0);
  if (DRAIN > 0) {
#pragma nounroll
    for (int t = 0; t < DRAIN; t += 2) { step(0, true); step(1, true); }
    waitv<0>();
    hook();
  }
#pragma nounroll
  for (int t = DRAIN; t < N - (TAIL ? 4 : 0); t += 2) { step(0, true); step(1, true); }
  if (TAIL) {
#pragma nounroll
    for (int t = 0; t < 4; t += 2) { step(0, true); step(1, false); }
  }
}

DI unsigned voffRow(unsigned lane, unsigned ldb, unsigned par) { return (lane >> 3) * ldb + 16u * ((lane & 7u) ^ (4u * par + (lane >> 4))); }
DI unsigned voffT(unsigned lane, unsigned ldb) { return (lane >> 3) * ldb + 16u * ((lane & 7u) ^ (2u * ((((lane >> 4) & 1u) << 1) | (lane >> 5)))); }
struct T4 { unsigned s0, s1, s2, s3;
  DI void set(unsigned so, unsigned lw, unsigned ldb) { s0 = so + 4096u + lw * 16u * ldb; s1 = s0 + 128u - 1024u; s2 = s0 + 8u * ldb - 2048u; s3 = s2 + 128u - 1024u; }
  DI void pin() { PINS(s0); PINS(s1); PINS(s2); PINS(s3); }
  template <bool DEV = false> DI void go(unsigned dst, unsigned vo, rsrc_t r) const { dma64<DEV>(dst, vo, vo, r, s0, s1, s2, s3); } };

typedef unsigned long long u64;
constexpr unsigned FL_H = 64, FL_O = 320, FL_U = 576, FL_G = 832, FL_X = 3072;
DI void flag_set(u64* p, u64 tag) { __hip_atomic_store(p, tag, __ATOMIC_RELAXED, __HIP_MEMORY_SCOPE_AGENT); }
DI u64 flag_get(const u64* p) { return __hip_atomic_load(p, __ATOMIC_RELAXED, __HIP_MEMORY_SCOPE_AGENT); }
template <int SLOW = 16> DI void flag_wait(const u64* p, u64 tag, u64 mask) {
  const u64 t0 = __builtin_amdgcn_s_memrealtime();
  while ((__ballot(flag_get(p) != tag) & mask) != 0ull) {
    const u64 dt = __builtin_amdgcn_s_memrealtime() - t0;
    if (dt > SPIN_LIMIT) break;
    if (dt > 400ull) __builtin_amdgcn_s_sleep(SLOW); else __builtin_amdgcn_s_sleep(3);
  }
  asm volatile("" ::: "memory");
}

DI f32x2 sigm2(f32x2 v, f32x2 z) {
  f32x2 e = {__builtin_amdgcn_exp2f(z[0]), __builtin_amdgcn_exp2f(z[1])};
  e += 1.0f;
  return v * (f32x2){__builtin_amdgcn_rcpf(e[0]), __builtin_amdgcn_rcpf(e[1])};
}
DI unsigned gelu2(f32x2 v) {
  const f32x2 r = sigm2(v, v * __builtin_elementwise_fma(v * v, (f32x2)(-0.1029432397f), (f32x2)(-2.3022081986f)));
  return pk2(r[0], r[1]);
}
DI unsigned silu2(f32x2 v) { const f32x2 r = sigm2(v, v * -1.4426950408889634f); return pk2(r[0], r[1]); }
DI f32x2 lo2(f32x4 v) { return __builtin_shufflevector(v, v, 0, 1); }
DI f32x2 hi2(f32x4 v) { return __builtin_shufflevector(v, v, 2, 3); }
DI f32x2 bf2(unsigned w) { return (f32x2){bflo(w), bfhi(w)}; }

constexpr unsigned QSTG = 49152u;
constexpr unsigned KBASE = 65536u, VBASE = 114688u;
constexpr unsigned KSLOT[3] = {0u, 32768u, 16384u}, VSLOT[3] = {32768u, 16384u, 0u};
DI void touch8k(rsrc_t r, unsigned vo128, unsigned soff) {
  asm volatile("buffer_load_dword v255, %0, %1, %2 offen" :: "v"(vo128), "s"(r), "s"(soff) : "memory", "v255");
}
constexpr float QSCALE = 0.12751743082f;
#define BIGSUM 4096.0f

struct Att {
  bf16x8 qf[8], kv[16], pf[4], pn[2];
  f32x16 o[4], s[2], negm;
  float l, ts;
  unsigned aK[4], aV[2];
};
template <unsigned SLOT, int J> DI void att_k(Att& a) {
  asm volatile("ds_read_b128 %0, %1 offset:%2" : "+v"(a.kv[J]) : "v"(a.aK[(J >> 1) & 3]), "i"(SLOT + 8192u * (J >> 3) + 4096u * (J & 1)) : "memory");
}
template <unsigned SLOT, int J> DI void att_v(Att& a) {
  s16x4 lo = __builtin_shufflevector(a.kv[J], a.kv[J], 0, 1, 2, 3), hi = __builtin_shufflevector(a.kv[J], a.kv[J], 4, 5, 6, 7);
  constexpr unsigned off = SLOT + 4096u * (J >> 2) + 1024u * ((J >> 1) & 1);
  asm volatile("ds_read_b64_tr_b16 %0, %2 offset:%3\n\tds_read_b64_tr_b16 %1, %2 offset:%4" : "+v"(lo), "+v"(hi) : "v"(a.aV[J & 1]), "i"(off), "i"(off + 2048u) : "memory");
  a.kv[J] = cat(lo, hi);
}
DI void att_wait(Att& a) {
  asm volatile("s_waitcnt lgkmcnt(0)" : "+v"(a.kv[0]), "+v"(a.kv[1]), "+v"(a.kv[2]), "+v"(a.kv[3]), "+v"(a.kv[4]), "+v"(a.kv[5]), "+v"(a.kv[6]), "+v"(a.kv[7]),
               "+v"(a.kv[8]), "+v"(a.kv[9]), "+v"(a.kv[10]), "+v"(a.kv[11]), "+v"(a.kv[12]), "+v"(a.kv[13]), "+v"(a.kv[14]), "+v"(a.kv[15]) :: "memory");
}
DI void att_loadK(Att& a, unsigned slot) {
#pragma unroll
  for (int j = 0; j < 16; ++j) a.kv[j] = ldsR(a.aK[(j >> 1) & 3] + slot + 8192u * (j >> 3) + 4096u * (j & 1));
}
DI unsigned lane_id() { unsigned l; asm volatile("v_mbcnt_lo_u32_b32 %0, -1, 0\n\tv_mbcnt_hi_u32_b32 %0, -1, %0" : "=v"(l)); return l; }
DI void add_bcast3(f32x16& t0, f32x16& t1, f32x16& t2, float v) {
  const float one = lane_id() < 32u ? 1.0f : 0.0f;
  asm volatile("s_nop 7\n\ts_nop 7\n\tv_mfma_f32_32x32x2_f32 %0, %3, %4, %0\n\tv_mfma_f32_32x32x2_f32 %1, %3, %4, %1\n\tv_mfma_f32_32x32x2_f32 %2, %3, %4, %2\n\t"
               "s_nop 7\n\ts_nop 7\n\ts_nop 7" : "+v"(t0), "+v"(t1), "+v"(t2) : "v"(one), "v"(v));
}
template <bool FIRST, unsigned SLOT, int J> DI void att_qk1(Att& a) {
  a.s[J & 1] = __builtin_amdgcn_mfma_f32_32x32x16_bf16(a.kv[J], a.qf[J >> 1], J < 2 ? (FIRST ? (f32x16){} : a.negm) : a.s[J & 1], 0, 0, 0);
  SCHED();
  if constexpr (J == 0) ALIGN8();
  if constexpr (!FIRST) att_v<SLOT, J>(a);
  SCHED();
  if constexpr (J < 15) att_qk1<FIRST, SLOT, J + 1>(a);
}
template <int NEXT, unsigned SLOT, int J> DI void att_pv1(Att& a) {
  a.o[J & 3] = __builtin_amdgcn_mfma_f32_32x32x16_bf16(a.kv[J], a.pf[J >> 2], a.o[J & 3], 0, 0, 0);
  SCHED();
  if constexpr (J == 0) ALIGN8();
  if constexpr (NEXT == 1) att_k<SLOT, J>(a); else if constexpr (NEXT == 2) att_v<SLOT, J>(a);
  SCHED();
  if constexpr (J < 15) att_pv1<NEXT, SLOT, J + 1>(a);
}
DI float max3(float x, float y, float z) { return __builtin_fmaxf(__builtin_fmaxf(x, y), z); }
DI float att_rowmax(const Att& a) {
  float m0 = max3(a.s[0][0], a.s[0][1], a.s[0][2]), m1 = max3(a.s[1][0], a.s[1][1], a.s[1][2]);
#pragma unroll
  for (int r = 3; r < 15; r += 2) { m0 = max3(m0, a.s[0][r], a.s[0][r + 1]); m1 = max3(m1, a.s[1][r], a.s[1][r + 1]); }
  const float mx = __builtin_fmaxf(max3(m0, m1, a.s[0][15]), a.s[1][15]);
  auto rr = __builtin_amdgcn_permlane32_swap(__float_as_uint(mx), __float_as_uint(mx), false, false);
  return __builtin_fmaxf(__uint_as_float(rr[0]), __uint_as_float(rr[1]));
}
DI float att_exp16(const f32x16& v, bf16x8& p0, bf16x8& p1) {
  float s0 = 0.f, s1 = 0.f;
  u32x4 w[2];
#pragma unroll
  for (int h = 0; h < 2; ++h) {
    float e[8];
#pragma unroll
    for (int r = 0; r < 8; ++r) e[r] = __builtin_amdgcn_exp2f(v[8 * h + r]);
#pragma unroll
    for (int r = 0; r < 8; r += 2) {
      if (h == 0 && r == 0) { s0 = e[0]; s1 = e[1]; } else { s0 += e[r]; asm("" : "+v"(s0)); s1 += e[r + 1]; asm("" : "+v"(s1)); }
      w[h][r >> 1] = pk2(e[r], e[r + 1]);
    }
    asm volatile("" : "+v"(w[h]));
    SCHED();
  }
  p0 = __builtin_bit_cast(bf16x8, w[0]); p1 = __builtin_bit_cast(bf16x8, w[1]);
  return s0 + s1;
}
template <bool FIRST> DI void att_sm1(Att& a) {
  if (FIRST) {
    const float mx = att_rowmax(a);
#pragma unroll
    for (int r = 0; r < 16; ++r) { a.s[0][r] -= mx; a.s[1][r] -= mx; a.negm[r] = -mx; }
  }
  a.ts = att_exp16(a.s[0], a.pn[0], a.pn[1]);
  asm volatile("" : "+v"(a.pn[0]), "+v"(a.pn[1]), "+v"(a.ts), "+v"(a.s[1]), "+v"(a.negm));
}
template <bool FIRST> DI void att_sm2(Att& a) {
  a.ts += att_exp16(a.s[1], a.pf[2], a.pf[3]);
  if (!FIRST) {
    if (__builtin_expect(!__all(a.ts <= BIGSUM), 0)) {
      const float dl = fmaxf(att_rowmax(a), 0.f), fct = __builtin_amdgcn_exp2f(-dl);
#pragma unroll
      for (int db = 0; db < 4; ++db) a.o[db] *= fct;
      a.l *= fct;
      add_bcast3(a.s[0], a.s[1], a.negm, -dl);
      a.ts = att_exp16(a.s[0], a.pn[0], a.pn[1]) + att_exp16(a.s[1], a.pf[2], a.pf[3]);
    }
  }
  a.l += a.ts;
  a.pf[0] = a.pn[0]; a.pf[1] = a.pn[1];
  asm volatile("" : "+v"(a.pf[0]), "+v"(a.pf[1]), "+v"(a.pf[2]), "+v"(a.pf[3]), "+v"(a.l));
}

extern "C" __global__ __launch_bounds__(512, 2) __attribute__((amdgpu_num_vgpr(127)))
void rsk_main(const u16* __restrict__ x, char* __restrict__ ws, const u16* __restrict__ wq, const u16* __restrict__ kc, const u16* __restrict__ vc,
              const u16* __restrict__ w1, const u16* __restrict__ wo, const u16* __restrict__ w2, const u16* __restrict__ b1, const u16* __restrict__ b2,
              u16* __restrict__ out) {
  if (rfl(threadIdx.x) == 0u) touch8k(code_rsrc(), threadIdx.x * 128u + (threadIdx.x < 48u ? 0u : (blockIdx.x >> 3) * 2048u), 0u);
  extern __shared__ __attribute__((aligned(16))) char lds[];
  const unsigned L = (unsigned)(size_t)lds;
  const char* xn = ws + WS_XN; const char* hh = ws + WS_H;
  const unsigned tid = threadIdx.x, lane = tid & 63u, wid = rfl(tid >> 6), grp = wid >> 2, lw = wid & 3u;
  const unsigned bid = blockIdx.x, xcd = bid & 7u, idx = bid >> 3;
  const unsigned qt = (xcd & 1u) * 4u + (idx & 3u), kvh = xcd >> 1, head = kvh * 8u + (idx >> 2);
  const bool hasU = idx < 28u;
  const unsigned udt = (xcd & 1u) * 4u + (idx & 3u), uft = (xcd >> 1) * 7u + (idx >> 2);
  const unsigned ufc = (idx >> 2) * 4u + (xcd >> 1);
  const rsrc_t sK = mkrsrc(kc + ((size_t)kvh * SK + (SK - WIN)) * DH, WIN * DH * 2), sV = mkrsrc(vc + ((size_t)kvh * SK + (SK - WIN)) * DH, WIN * DH * 2);
  const rsrc_t sH = mkrsrc(hh, SEQ * DM * 2), sW1 = mkrsrcB(w1, SEQ * FF * 2);
  const unsigned voK0 = voffRow(lane, 256u, 0), voV = voffT(lane, 256u);
  auto dmaK2 = [&](unsigned slot, unsigned t, unsigned p0) {
    unsigned voK1; asm volatile("v_xor_b32 %0, 64, %1" : "=v"(voK1) : "v"(voK0));
    dma16(L + KBASE + slot + 1024u * p0, voK0, sK, t * 16384u + (p0 & 7u) * 2048u + (p0 >> 3) * 128u);
    dma16(L + KBASE + slot + 1024u * p0 + 1024u, voK1, sK, t * 16384u + (p0 & 7u) * 2048u + 2048u + (p0 >> 3) * 128u); };
  auto dmaV2 = [&](unsigned slot, unsigned t, unsigned p0) {
    dma16(L + VBASE + slot + 1024u * p0, voV, sV, t * 16384u + (p0 >> 1) * 2048u);
    dma16(L + VBASE + slot + 1024u * p0 + 1024u, voV, sV, t * 16384u + (p0 >> 1) * 2048u + 128u); };
  auto tileK = [&](unsigned t) {
#pragma unroll
    for (int p = 0; p < 16; p += 2) dmaK2(KSLOT[t % 3], t, p); };
  auto tileV = [&](unsigned t) {
#pragma unroll
    for (int p = 0; p < 16; p += 2) dmaV2(VSLOT[t % 3], t, p); };
  Att a;
  { const unsigned r32 = lane & 31u, hi = lane >> 5;
#pragma unroll
    for (int j = 0; j < 4; ++j) a.aK[j] = L + KBASE + r32 * 128u + 16u * ((2u * j + hi) ^ ((r32 >> 1) & 7u)); }

  ST_DECL; STAMP(0);
  u64* flags = (u64*)(ws + WS_FLAG);
  const rsrc_t sS = lw == 0 ? mkrsrcB(wq, DM * NH * DH * 2) : mkrsrcB(xn, SEQ * DM * 2);
  unsigned asrc = 4096u + (lw == 0 ? head * 256u : (qt * 256u + (lw - 1u) * 128u) * 2048u), adst = L + lw * 16384u;
  const unsigned avo0 = lw == 0 ? voffT(lane, 8192u) : voffRow(lane, 2048u, 0), avo1 = lw == 0 ? avo0 : avo0 ^ 64u;
  constexpr unsigned HT = 147456u;
  auto astep = [&](unsigned vo0, unsigned vo1) {
    const unsigned sp = lw == 0 ? 8u * 8192u : 16u * 2048u, so = (lw == 0 ? 128u : 8u * 2048u) - 1024u;
#pragma unroll
    for (int q = 0; q < 4; ++q) { const unsigned e = asrc + 2u * q * sp; dma64<true>(adst + 4096u * q, vo0, vo1, sS, e, e + so, e + sp - 2048u, e + sp - 2048u + so); }
    asrc += lw == 0 ? 64u * 8192u : 128u; adst = adst >= L + 2u * QSTG ? adst - 2u * QSTG : adst + QSTG;
  };
  u64 tag;
  {
    const unsigned row = (bid >> 2) * 32u + ((bid >> 1) & 1u) * 16u + (bid & 1u) * 4u + (wid >> 2) * 8u + (wid & 3u);
    u32x4 xa, xb; u32x2 ep;
    asm volatile("buffer_load_dwordx4 %0, %3, %4, %5 offen\n\tbuffer_load_dwordx4 %1, %3, %4, %5 offen offset:1024\n\tglobal_load_dwordx2 %2, %6, %7 sc1"
                 : "=&v"(xa), "=&v"(xb), "=&v"(ep) : "v"(lane * 16u), "s"(mkrsrc(x, SEQ * DM * 2)), "s"(row * 2048u), "v"(0u), "s"(flags) : "memory");
    __builtin_amdgcn_s_setprio(1);
    if (wid == 7) {
      touch8k(mkrsrc(wq, DM * NH * DH * 2), lane * 128u, bid * 8192u);
      waitv<1>();
    } else waitv<0>();
    asm volatile("" : "+v"(xa), "+v"(xb), "+v"(ep));
    STAMP(16);
    tag = (((u64)rfl(ep[1]) << 32) | rfl(ep[0])) * 0x9E3779B97F4A7C15ull + 0x632BE59BD9B4E019ull;
    float v[16];
#pragma unroll
    for (int i = 0; i < 4; ++i) { v[2 * i] = bflo(xa[i]); v[2 * i + 1] = bfhi(xa[i]); v[8 + 2 * i] = bflo(xb[i]); v[9 + 2 * i] = bfhi(xb[i]); }
    ln16(v);
    const rsrc_t sW = mkrsrc(ws, WS_O);
#pragma unroll
    for (int p = 0; p < 2; ++p) {
      const unsigned lo0 = pkx(v[8 * p], v[8 * p + 1]), lo1 = pkx(v[8 * p + 2], v[8 * p + 3]), hi0 = pkx(v[8 * p + 4], v[8 * p + 5]), hi1 = pkx(v[8 * p + 6], v[8 * p + 7]);
      const bool odd = lane & 1u;
      const unsigned s0 = __builtin_amdgcn_update_dpp(0, (int)(odd ? lo0 : hi0), 0xB1, 0xf, 0xf, false), s1 = __builtin_amdgcn_update_dpp(0, (int)(odd ? lo1 : hi1), 0xB1, 0xf, 0xf, false);
      __builtin_amdgcn_raw_buffer_store_b128(odd ? (u32x4){s0, s1, hi0, hi1} : (u32x4){lo0, lo1, s0, s1}, sW, WS_XN + row * 2048u + lane * 16u + 1024u * p, 0, 16);
    }
    STAMP(17);
    waitv<0>();
    if (lane == 0u) flag_set(flags + FL_X + bid * 8u + wid, tag);
    STAMP(18);
    if (wid == 4u) { astep(avo0, avo1); astep(avo0, avo1); }
    if (grp == 0) __builtin_amdgcn_s_setprio(0);
    ln16(v);
    *(LAS u32x4*)(size_t)(L + HT + wid * 2048u + lane * 16u) = (u32x4){pkx(v[0], v[1]), pkx(v[2], v[3]), pkx(v[4], v[5]), pkx(v[6], v[7])};
    *(LAS u32x4*)(size_t)(L + HT + wid * 2048u + 1024u + lane * 16u) = (u32x4){pkx(v[8], v[9]), pkx(v[10], v[11]), pkx(v[12], v[13]), pkx(v[14], v[15])};
    WAITL0();
    __builtin_amdgcn_s_setprio(0);
    STAMP(19);
  }
  auto hstore = [&](unsigned i) {
    const unsigned d0 = i * 64u, ad = L + HT + ((lane & 15u) >> 2) * 2048u + (d0 + (lane >> 4) * 16u + (lane & 3u) * 4u) * 2u;
    __builtin_amdgcn_raw_buffer_store_b128(__builtin_bit_cast(u32x4, cat(ldsT(ad), ldsT(ad + 8192u))), mkrsrc(ws, WS_O), WS_H + ((d0 >> 7) * 256u + bid) * 2048u + ((d0 & 64u) + lane) * 16u, 0, 16);
  };
  if (wid == 7) {
    const unsigned vo = lane * 128u;
    const rsrc_t sWq = mkrsrc(wq, DM * NH * DH * 2);
    BAR(); BAR();
    touch8k(sWq, vo, (256u + bid) * 8192u);
#pragma nounroll
    for (unsigned i = 0; i < 4; ++i) {
      BAR();
      if (i == 1u) touch8k(sW1, vo, bid * 8192u + 4096u);
      if (i == 2u) touch8k(sW1, vo, (768u + bid) * 8192u + 4096u);
      hstore(i * 4u); hstore(i * 4u + 1u); hstore(i * 4u + 2u); hstore(i * 4u + 3u);
    }
    touch8k(sWq, vo, (512u + bid) * 8192u);
    BAR(); BAR();
    waitv<1>();
    if (lane == 0u) flag_set(flags + FL_H + bid, tag);
    touch8k(sWq, vo, (768u + bid) * 8192u);
    BAR();
    touch8k(sW1, vo, (1024u + bid) * 8192u + 4096u);
    BAR();
    touch8k(sW1, vo, (256u + bid) * 8192u + 4096u);
    if (idx < 16u) touch8k(sK, vo, idx * 8192u); else touch8k(sV, vo, (idx - 16u) * 8192u);
    BAR(); BAR();
    const u64 f0 = flag_get(flags + FL_H + lane), f1 = flag_get(flags + FL_H + 64u + lane), f2 = flag_get(flags + FL_H + 128u + lane), f3 = flag_get(flags + FL_H + 192u + lane);
    touch8k(code_rsrc(), lane < 16u ? vo : 0x80000000u, idx * 2048u);
    BAR(); BAR();
    if (__any(f0 != tag || f1 != tag || f2 != tag || f3 != tag)) {
#pragma nounroll
      for (unsigned j = 0; j < 256u; j += 64u) flag_wait(flags + FL_H + j + lane, tag, ~0ull);
    }
    if (bid == 0u && lane == 0u) flag_set(flags, tag);
    touch8k(sW1, vo, (512u + bid) * 8192u + 4096u);
    BAR(); BAR(); BAR();
#pragma unroll
    for (int j = 5; j < 6; ++j) touch8k(sW1, vo, (j * 256u + bid) * 8192u + 4096u);
    att_loadK(a, KSLOT[0]);
  } else if (grp == 1) {
    const unsigned vo0 = avo0, vo1 = avo1;
    if (lw != 0) {
      const u64* fx = flags + FL_X + qt * 256u + (lw - 1u) * 128u + lane;
      const u64 t0 = __builtin_amdgcn_s_memrealtime();
      while (__any(((flag_get(fx) ^ tag) | (flag_get(fx + 64) ^ tag)) != 0ull)) {
        if (__builtin_amdgcn_s_memrealtime() - t0 > SPIN_LIMIT) break;
        __builtin_amdgcn_s_sleep(2);
      }
      asm volatile("" ::: "memory");
      STAMP(21);
      astep(vo0, vo1); astep(vo0, vo1);
      STAMP(22);
    }
    waitv<16>(); STAMP(23);
    BAR(); STAMP(1);
    astep(vo0, vo1);
#pragma nounroll
    for (unsigned t = 0; t < 13; ++t) { waitv<16>(); BAR(); astep(vo0, vo1); }
    waitv<16>(); BAR();
    if (lw == 0) tileK(0); else if (lw == 1) tileK(2); else tileV(0);
    waitv<16>(); BAR();
    if (lw == 1) tileK(1); else if (lw == 2) tileV(1);
    if (lw == 0) waitv<0>();
    BAR();
    att_loadK(a, KSLOT[0]);
  } else {
    Opnd<T1> P; Opnd<RROW> Q;
    P.init(L, 0u, lane); Q.init(L + 16384u, lw * 64u, lane);
    f32x4 acc[8][4] = {};
    gemm_wide<T1, RROW, 3, QSTG>(acc, P, Q, 16);
    STAMP(2);
    att_loadK(a, KSLOT[0]);
    const unsigned wa = L + (lane >> 5) * 4096u + (lw * 64u + (lane & 15u)) * 16u + ((lane >> 4) & 1u) * 8u;
#pragma unroll
    for (int j = 0; j < 16; ++j) {
      const f32x4 v = acc[j >> 1][j & 1] * QSCALE;
      *(LAS u32x2*)(size_t)(wa + 8192u * (j >> 1) + 256u * (j & 1)) = (u32x2){pk2(v[0], v[1]), pk2(v[2], v[3])};
    }
#pragma unroll
    for (int ks = 0; ks < 8; ++ks) a.qf[ks] = ldsR(L + (2u * ks + (lane >> 5)) * 4096u + (lw * 64u + (lane & 31u)) * 16u);
    SCHED();
#pragma unroll
    for (int j = 0; j < 16; ++j) {
      a.s[j & 1] = __builtin_amdgcn_mfma_f32_32x32x16_bf16(a.kv[j], a.qf[j >> 1], j < 2 ? (f32x16){} : a.s[j & 1], 0, 0, 0);
      float v[4];
#pragma unroll
      for (int c = 0; c < 4; ++c) { v[c] = acc[j >> 1][2 + (j & 1)][c] * QSCALE; asm("" : "+v"(v[c])); }
      *(LAS u32x2*)(size_t)(wa + 8192u * (j >> 1) + 256u * (2 + (j & 1))) = (u32x2){pk2(v[0], v[1]), pk2(v[2], v[3])};
      SCHED();
    }
    WAITL0();
  }
  STAMP(3);

  const rsrc_t sO = mkrsrc(ws + WS_O, SEQ * NH * DH * 2);
  {
    const unsigned l2 = lane_id(), hi = l2 >> 5, blk = (l2 >> 4) & 1u, q4 = (l2 & 15u) >> 2, p4 = l2 & 3u;
#pragma unroll
    for (int n = 0; n < 2; ++n) a.aV[n] = L + VBASE + 128u * (4u * hi + q4) + 32u * ((2u * n + blk) ^ (((q4 >> 1) << 1) | hi)) + 8u * p4;
#pragma unroll
    for (int db = 0; db < 4; ++db) a.o[db] = (f32x16){};
    a.l = 0.f;
    const rsrc_t sW1u = mkrsrc((const char*)w1 - 4096, hasU ? SEQ * FF * 2 + 4096u : 0u), sHu = mkrsrc(hh, hasU ? SEQ * DM * 2 : 0u);
    unsigned voU = 0;
    auto uP = [&](unsigned j) {
      const unsigned so = 4096u + (j * 64u + wid * 8u) * (FF * 2u) + ufc * 256u;
      asm volatile("s_mov_b32 m0, %0\n\ts_nop 0\n\tbuffer_load_dwordx4 %1, %2, %3 offen lds\n\tbuffer_load_dwordx4 %1, %2, %4 offen offset:1024 lds"
                   :: "s"(L + j * 32768u + wid * 2048u), "v"(voU), "s"(sW1u), "s"(so), "s"(so + 128u - 1024u) : "memory"); };
    auto uQ = [&](unsigned j) {
      asm volatile("s_mov_b32 m0, %0\n\ts_nop 0\n\tbuffer_load_dwordx4 %1, %2, %3 offen sc1 lds\n\tbuffer_load_dwordx4 %1, %2, %3 offen offset:1024 sc1 lds"
                   :: "s"(L + j * 32768u + 16384u + wid * 2048u), "v"(lane_id() * 16u), "s"(sHu), "s"((udt * 32u + j) * 16384u + wid * 2048u) : "memory"); };
    const bool pre = hasU && grp == 1;
    if (grp == 1) {
      if (wid != 7) waitv<0>();
      WAITL0(); BAR();
#pragma unroll
      for (int ks = 0; ks < 8; ++ks) a.qf[ks] = ldsR(L + (2u * ks + hi) * 4096u + (lw * 64u + 32u + (l2 & 31u)) * 16u);
      SCHED(); att_qk1<true, 0u, 0>(a);
    }
    STAMP(4);
    const rsrc_t sWo = mkrsrc(wo, NH * DH * DM * 2), sW2 = mkrsrc(w2, FF * SEQ * 2);
    const unsigned lk = bid * 8192u + (wid >> 1) * 2048u + (wid & 1u) * 128u;
#define LOOK(v) do { if ((v) < 1) touch8k(sW1, voV, lk + 6u * 0x200000u + 4096u); else if ((v) < 5) touch8k(sWo, voV, lk + ((v) - 1) * 0x200000u); \
                     else if ((v) < 12) touch8k(sW2, voV, lk + (((v) - 2) % 7) * 0x200000u); } while (0)
#define ATT_TILE(i, FIRST, WB, WD)                                                                                            \
    SCHED(); if (!(FIRST)) att_qk1<false, VSLOT[((i) + 2) % 3], 0>(a); BAR(); SCHED();                                        \
    if ((i) <= 4) dmaK2(KSLOT[(i) % 3], (i) + 3, wid * 2u);                                                                   \
    LOOK(2 * (i));                                                                                                            \
    if ((i) == 5) { voU = voffT(lane_id(), FF * 2u); uP(0); uQ(0); } else if ((i) == 6) { uQ(1); uP(2); } else if ((i) == 7) uP(3); \
    att_sm1<FIRST>(a);                                                                                                        \
    waitv<WB>();                                                                                                              \
    if (!(FIRST)) att_wait(a); SCHED(); BAR();                                                                                \
    if (FIRST) att_loadK(a, KSLOT[1]); else { SCHED(); att_pv1<((i) < 7 ? 1 : 2), ((i) < 7 ? KSLOT[((i) + 1) % 3] : VSLOT[7 % 3]), 0>(a); } \
    BAR(); SCHED();                                                                                                           \
    if ((i) <= 5) dmaV2(VSLOT[((i) + 2) % 3], (i) + 2, wid * 2u);                                                             \
    LOOK(2 * (i) + 1);                                                                                                        \
    if ((i) == 5) uP(1); else if ((i) == 6) uQ(2); else if ((i) == 7) uQ(3);                                                  \
    att_sm2<FIRST>(a);                                                                                                        \
    waitv<WD>();                                                                                                              \
    att_wait(a); SCHED(); BAR();
    ATT_TILE(0, true, 63, 63) ATT_TILE(1, false, 63, 10) ATT_TILE(2, false, 10, 10) ATT_TILE(3, false, 10, 10)
    ATT_TILE(4, false, 10, 10) ATT_TILE(5, false, 12, 14) ATT_TILE(6, false, 15, 9) ATT_TILE(7, false, 63, 63)
#undef ATT_TILE
#undef LOOK
    SCHED(); att_pv1<0, 0u, 0>(a); STAMP(5);
    if (grp == 0) { BAR(); waitv<0>(); }
    if (pre) { waitv<2>(); BAR(); STAMP(24); }
  }
  auto ost2 = [&](int db) {
    const unsigned l3 = lane_id(), row = qt * 256u + lw * 64u + grp * 32u + (l3 & 31u);
    const unsigned oo = (((row >> 7) * 64u + head * 2u) * 8u + (l3 >> 5)) * 2048u + (row & 127u) * 16u;
#pragma unroll
    for (int i = 0; i < 2; ++i) {
      float w[8];
#pragma unroll
      for (int c = 0; c < 8; ++c) { w[c] = a.o[db][8 * i + c] * a.l; asm("" : "+v"(w[c])); }
      __builtin_amdgcn_raw_buffer_store_b128((u32x4){pk2(w[0], w[1]), pk2(w[2], w[3]), pk2(w[4], w[5]), pk2(w[6], w[7])}, sO, oo + db * 8192u + i * 4096u, 0, 16);
    }
  };
  { auto rr = __builtin_amdgcn_permlane32_swap(__float_as_uint(a.l), __float_as_uint(a.l), false, false);
    a.l = __builtin_amdgcn_rcpf(__uint_as_float(rr[0]) + __uint_as_float(rr[1])); }
  if (!(hasU && grp == 1)) { ost2(0); ost2(1); ost2(2); ost2(3); STAMP(6); }
  const unsigned ln = lane_id();
  const bool isY = xcd >= 4u;
  const unsigned dt = udt, st = ((xcd >> 1) & 1u) * 8u + (idx >> 2), tile = st * 8u + dt;
  u64* fG = flags + FL_G + tile * 16u; u64* fO = flags + FL_O; u64* fU = flags + FL_U + dt * 32u;
  const rsrc_t sG = mkrsrc(ws + WS_G + (size_t)tile * 32768u, 32768u), sU = mkrsrc(ws + WS_U, DM * FF * 2);
  constexpr unsigned XO = 32768u, GO = 65536u;
  auto finish = [&]() {
    WAITL0(); BAR();
#pragma unroll
    for (int q = 0; q < 4; ++q) {
      const unsigned row = wid * 16u + q * 4u + (ln >> 4);
      const u32x4 v = *(const LAS u32x4*)(size_t)(L + XO + row * 256u + 16u * ((ln & 15u) ^ (row & 15u)));
      __builtin_amdgcn_raw_buffer_store_b128(v, mkrsrc(out, SEQ * DM * 2), (st * 128u + row) * 2048u + dt * 256u + (ln & 15u) * 16u, 0, 0);
    }
    STAMP(14); waitv<0>(); STAMP(15); ST_OUT(1);
  };
  if (!hasU) {
    waitv<0>(); BAR();
    if (tid == 256u) flag_set(fO + qt * 32u + head, tag);
    STAMP(7);
  }
  if (grp == 0) {
    const unsigned wp = lw >> 1, wq2 = lw & 1u, fr = ln & 15u, fq = ln >> 4;
    if (hasU) {
      Opnd<T1> P; Opnd<RCHK> Q;
      P.init(L, wp * 64u, ln); Q.init(L + 16384u, wq2 * 64u, ln);
      u32x2 bv[4];
      f32x4 acc[4][4] = {};
      gemm_full<T1, RCHK, 5, 32768u, 0, 0, 16>(acc, P, Q, 32, [&] {
#pragma unroll
        for (int p = 0; p < 4; ++p) bv[p] = *(const u32x2*)(b1 + ufc * 128u + wp * 64u + p * 16u + fq * 4u);
      });
      STAMP(8);
      const unsigned uo = ((udt * 56u + uft * 2u + wp) * 8u + fq) * 2048u + (wq2 * 64u + fr) * 16u;
#pragma unroll
      for (int pp = 0; pp < 2; ++pp) {
        const f32x2 b0 = bf2(bv[2 * pp][0]), b1v = bf2(bv[2 * pp][1]), b2v = bf2(bv[2 * pp + 1][0]), b3 = bf2(bv[2 * pp + 1][1]);
#pragma unroll
        for (int m = 0; m < 4; ++m)
          __builtin_amdgcn_raw_buffer_store_b128((u32x4){gelu2(lo2(acc[2 * pp][m]) + b0), gelu2(hi2(acc[2 * pp][m]) + b1v), gelu2(lo2(acc[2 * pp + 1][m]) + b2v), gelu2(hi2(acc[2 * pp + 1][m]) + b3)},
                                                 sU, uo + pp * 8192u + m * 256u, 0, 16);
      }
      STAMP(9);
      if (isY) { waitv<0>(); BAR(); }
    }
    if (isY || !hasU) BAR();
    f32x4 acc[4][4] = {};
    const unsigned t4 = (wid * 64u + ln) * 16u;
    if (isY) {
      float bias[4];
#pragma unroll
      for (int q = 0; q < 4; ++q) bias[q] = bflo(b2[st * 128u + wq2 * 64u + q * 16u + fr]);
      Opnd<RCHK> P; Opnd<T2> Q;
      P.init(L + 65536u, wp * 64u, ln); Q.init(L + 65536u + 16384u, wq2 * 64u, ln);
      gemm_full<RCHK, T2, 5, 32768u, 3, 2, 0>(acc, P, Q, 56, [] {});
      STAMP(12);
#pragma unroll
      for (int j = 0; j < 8; ++j) {
        const int q = 2 * (j & 1); const f32x4 y0 = acc[j >> 1][q] + bias[q], y1 = acc[j >> 1][q + 1] + bias[q + 1];
        __builtin_amdgcn_raw_buffer_store_b128((u32x4){silu2(lo2(y0)), silu2(hi2(y0)), silu2(lo2(y1)), silu2(hi2(y1))}, sG, t4 + j * 4096u, 0, 16);
      }
      waitv<0>(); BAR();
      if (tid == 0u) flag_set(fG, tag);
      STAMP(15); ST_OUT(1); return;
    }
    Opnd<T1> P; Opnd<RCHK> Q;
    P.init(L + 65536u, wp * 64u, ln); Q.init(L + 65536u + 16384u, wq2 * 64u, ln);
    gemm_full<T1, RCHK, 5, 32768u, 3, 2, 2>(acc, P, Q, 64, [] {});
    STAMP(12);
    const unsigned xa = L + XO + (wq2 * 64u + fr) * 256u + (fq & 1u) * 8u;
    unsigned xp[4];
    f32x4 xv[4][4];
#pragma unroll
    for (int p = 0; p < 4; ++p) {
      xp[p] = xa + 16u * ((wp * 8u + 2u * p + (fq >> 1)) ^ fr);
#pragma unroll
      for (int q = 0; q < 4; ++q) {
        const u32x2 w = *(const LAS u32x2*)(size_t)(xp[p] + q * 4096u);
        xv[p][q] = (f32x4){bflo(w[0]), bfhi(w[0]), bflo(w[1]), bfhi(w[1])};
        asm volatile("" : "+v"(xv[p][q]));
      }
    }
    BAR();
    STAMP(13);
    u32x4 gp[8];
#pragma unroll
    for (int j = 0; j < 8; ++j) gp[j] = *(const LAS u32x4*)(size_t)(L + GO + t4 + j * 4096u);
#pragma unroll
    for (int p = 0; p < 4; ++p)
#pragma unroll
      for (int q = 0; q < 4; ++q) {
        const unsigned g0 = gp[2 * p + (q >> 1)][2 * (q & 1)], g1 = gp[2 * p + (q >> 1)][2 * (q & 1) + 1];
        const f32x4 r = __builtin_elementwise_fma(acc[p][q], (f32x4){bflo(g0), bfhi(g0), bflo(g1), bfhi(g1)}, xv[p][q]);
        *(LAS u32x2*)(size_t)(xp[p] + q * 4096u) = (u32x2){pk2(r[0], r[1]), pk2(r[2], r[3])};
      }
  } else {
    const unsigned N = isY ? 56u : 64u;
    const rsrc_t sT = isY ? mkrsrcB(w2, FF * SEQ * 2) : mkrsrcB(wo, NH * DH * DM * 2);
    const rsrc_t sR = isY ? sU : sO;
    const unsigned ldT = isY ? 4096u : 2048u, voT = voffT(ln, ldT), voL = ln * 16u;
    const unsigned tcol = (isY ? st : dt) * 256u, rbase = (isY ? dt : st) * N * 16384u + lw * 4096u;
    const unsigned toff = (isY ? 16384u : 0u) + lw * 4096u, roff = (isY ? 0u : 16384u) + lw * 4096u, g0 = isY ? (xcd >> 1) * 14u : 0u;
    const u64* dep = (isY ? fU : fO + (st >> 1) * 32u) + (ln & 31u);
    auto peek = [&]() { asm volatile("global_load_dwordx2 v[254:255], %0, off sc1" :: "v"(dep) : "memory", "v254", "v255"); };
    auto await = [&](u64 mask, bool peeked) {
      if (peeked) { u64 v; asm volatile("v_mov_b64 %0, v[254:255]" : "=v"(v) :: "memory"); if ((__ballot(v != tag) & mask) == 0ull) return; }
      flag_wait(dep, tag, mask);
    };
    unsigned sb = L + 4u * 32768u;
    auto nexts = [&]() { sb = sb == L + 4u * 32768u ? L : sb + 32768u; };
    unsigned kT = g0;
    unsigned kW = isY ? (xcd >> 1) * 2u : 0u;
    const unsigned kj = isY ? 7u : 1u;
    auto dT = [&]() { T4 p; p.set(kW * 64u * ldT + tcol, lw, ldT); p.go(sb + toff, voT, sT); ++kT; kW += (kW & 1u) ? kj : 1u; nexts(); };
    if (hasU) {
      const unsigned voW1 = voffT(ln, FF * 2u), hw = (wid & 1u) * 2u;
      unsigned j = 4;
      auto ufill = [&](auto wait) {
        T4 p, q; p.set(j * 64u * (FF * 2u) + ufc * 256u, hw, FF * 2u); q.set(j * 64u * (FF * 2u) + ufc * 256u, hw + 1u, FF * 2u);
        unsigned d0 = sb + hw * 4096u, r0 = (udt * 32u + j) * 16384u + hw * 4096u; p.pin(); q.pin(); PINS(d0); PINS(r0);
        wait();
        p.go(d0, voW1, sW1); dma64d(d0 + 16384u, voL, sH, r0); q.go(d0 + 4096u, voW1, sW1); dma64d(d0 + 20480u, voL, sH, r0 + 4096u);
        ++j; nexts();
      };
      if (wid >= 6u) {
        ufill([] {});
#pragma nounroll
        for (unsigned t = 0; t < 12; ++t) ufill([] { waitv<48>(); BAR(); });
        waitv<48>(); BAR(); ost2(0); waitv<34>(); BAR(); ost2(1); waitv<20>(); BAR(); ost2(2); waitv<6>(); BAR(); ost2(3);
        touch8k(code_rsrc(), ln < 10u ? ln * 128u : 0x80000000u, (idx * 2u + (wid & 1u)) * 1280u);
        for (unsigned t = 16; t < 22; ++t) BAR();
        waitv<0>(); BAR();
        if (tid == 384u) flag_set(fO + qt * 32u + head, tag);
        BAR(); BAR(); BAR(); BAR();
      } else {
        BAR(); STAMP(25); BAR(); waitv<0>(); BAR();
        ost2(0); BAR(); ost2(1); BAR(); ost2(2); BAR(); ost2(3);
        for (unsigned t = 6; t < 12; ++t) BAR();
        STAMP(26);
        j = 17; sb = L + 2u * 32768u;
#pragma nounroll
        for (unsigned t = 12; t < 27; ++t) ufill([] { waitv<48>(); BAR(); });
        STAMP(28);
      }
      sb = L + 2u * 32768u;
      waitv<48>(); BAR(); dT(); waitv<36>(); BAR(); dT(); peek(); waitv<24>(); BAR(); dT(); waitv<12>(); BAR(); dT(); BAR(); dT();
      if (isY) {
        BAR();
        if (tid == 256u) flag_set(fU + uft, tag);
      } else waitv<12>();
      STAMP(8);
    } else {
      sb = L + 2u * 32768u;
#pragma nounroll
      for (unsigned j = 0; j < 5; ++j) dT();
    }
    if (hasU && !isY) await(0xffffffffull, true);
    else { if (wid == 4u) await(isY ? 0x7full << (g0 >> 1) : 0xffffffffull, false); BAR(); }
#pragma nounroll
    for (unsigned j = 0; j < 5; ++j) { dma64d(sb + roff, voL, sR, rbase + (g0 + j) * 16384u); nexts(); }
    auto dstep = [&](auto wait) {
      T4 p; p.set(kW * 64u * ldT + tcol, lw, ldT);
      unsigned d0 = sb + toff, d1 = sb + roff, r0 = rbase + kT * 16384u; p.pin(); PINS(d0); PINS(d1); PINS(r0);
      wait(); BAR();
      p.go(d0, voT, sT); dma64d(d1, voL, sR, r0);
      ++kT; kW += (kW & 1u) ? kj : 1u; nexts();
    };
    waitv<16>(); BAR(); STAMP(11);
    dstep([] { waitv<12>(); }); dstep([] { waitv<16>(); }); dstep([] { waitv<20>(); });
    if (!isY && hasU && tid == 256u) flag_set(fU + uft, tag);
#pragma nounroll
    for (unsigned seg = isY ? 0u : 5u; seg < 6u; ++seg) {
      const unsigned n = !isY ? 56u : seg == 0u ? 2u : seg == 2u ? 10u : seg >= 4u ? 14u : 4u;
#pragma nounroll
      for (unsigned i = 0; i < n; ++i) dstep([] { waitv<24>(); });
      if (seg == 0u || seg == 2u) peek();
      else if (seg < 4u) { waitv<32>(); await(seg == 1u ? 0x3fffull << 14 : 0x3fffull, true); kT = seg == 1u ? 70u - g0 : 0u; kW = seg == 1u ? 10u - (xcd >> 1) * 2u : 0u; }
      else kW = 2u;
    }
    waitv<24>(); BAR();
    if (isY) { waitv<0>(); BAR(); STAMP(12); ST_OUT(1); return; }
    {
      const rsrc_t sX = mkrsrc(x, SEQ * DM * 2);
#pragma unroll
      for (int q = 0; q < 8; ++q)
        dma16(L + XO + (lw * 8u + q) * 1024u, (ln >> 4) * 2048u + 16u * ((ln & 15u) ^ ((4u * q + (ln >> 4)) & 15u)), sX, (st * 128u + lw * 32u + q * 4u) * 2048u + dt * 256u);
    }
    waitv<8>(); BAR();
    waitv<0>(); BAR();
    STAMP(12);
    {
      flag_wait<6>(fG, tag, 1ull);
      u32x4 g[8];
#pragma unroll
      for (int q = 0; q < 8; ++q) g[q] = __builtin_amdgcn_raw_buffer_load_b128(sG, voL + (lw * 8u + q) * 1024u, 0, 16);
#pragma unroll
      for (int q = 0; q < 8; ++q) *(LAS u32x4*)(size_t)(L + GO + voL + (lw * 8u + q) * 1024u) = g[q];
    }
    waitv<0>(); WAITL0(); STAMP(13); BAR();
  }
  finish();
}

extern "C" __global__ void rsk_end() {}
}

extern "C" void kernel_launch(void* const* d_in, const int* in_sizes, int n_in, void* d_out, int out_size, void* d_ws, size_t ws_size, hipStream_t stream) {
  const u16* x = (const u16*)d_in[0]; const u16* kc = (const u16*)d_in[1]; const u16* vc = (const u16*)d_in[2];
  const u16* wq = (const u16*)d_in[3]; const u16* wo = (const u16*)d_in[4];
  const u16* w1 = (const u16*)d_in[6]; const u16* b1 = (const u16*)d_in[7]; const u16* w2 = (const u16*)d_in[8]; const u16* b2 = (const u16*)d_in[9];
  char* ws = (char*)d_ws;
  (void)hipFuncSetAttribute((const void*)rsk_main, hipFuncAttributeMaxDynamicSharedMemorySize, LDS_BYTES);
  rsk_main<<<256, 512, LDS_BYTES, stream>>>(x, ws, wq, kc, vc, w1, wo, w2, b1, b2, (u16*)d_out);
}
```

```cpp
#include <hip/hip_runtime.h>
#include <hip/hip_bf16.h>

namespace {
typedef short bf16x8 __attribute__((ext_vector_type(8)));
typedef short s16x4 __attribute__((ext_vector_type(4)));
typedef float f32x2 __attribute__((ext_vector_type(2)));
typedef float f32x4 __attribute__((ext_vector_type(4)));
typedef float f32x16 __attribute__((ext_vector_type(16)));
typedef unsigned u32x4 __attribute__((ext_vector_type(4)));
typedef unsigned u32x2 __attribute__((ext_vector_type(2)));
typedef __bf16 bf2_t __attribute__((ext_vector_type(2)));
typedef unsigned short u16;
typedef __amdgpu_buffer_rsrc_t rsrc_t;
#define LAS __attribute__((address_space(3)))
#define DI __device__ __forceinline__
#define BAR() asm volatile("s_barrier" ::: "memory")
#define WAITL0() do { asm volatile("" ::: "memory"); __builtin_amdgcn_s_waitcnt(0xC07F); asm volatile("" ::: "memory"); } while (0)
#define ALIGN8() asm volatile(".p2align 3")
#define SCHED() __builtin_amdgcn_sched_barrier(0)
#define SGB(mask, n) __builtin_amdgcn_sched_group_barrier(mask, n, 0)

constexpr int SEQ = 2048, DM = 1024, NH = 32, DH = 128, SK = 2048, WIN = 512, FF = 3584;
constexpr unsigned WS_XN = 0u, WS_H = 4u << 20, WS_O = 8u << 20, WS_U = 24u << 20, WS_G = 32u << 20, WS_FLAG = 36u << 20;
constexpr unsigned LDS_BYTES = 163840;
constexpr unsigned long long SPIN_LIMIT = 2000000ull;
constexpr int NW1 = 1;

#define ST_DECL
#define STAMP(i) do {} while (0)
#define ST_OUT(k)
template <int N> DI void waitv() { asm volatile("s_waitcnt vmcnt(%0)" :: "i"(N) : "memory"); }
DI unsigned rfl(unsigned x) { return __builtin_amdgcn_readfirstlane(x); }
DI rsrc_t mkrsrc(const void* p, unsigned bytes) { return __builtin_amdgcn_make_buffer_rsrc((void*)p, (short)0, (int)bytes, 0x00020000); }
DI void dma16(unsigned m0v, unsigned voff, rsrc_t r, unsigned soff) {
  asm volatile("s_mov_b32 m0, %0\n\ts_nop 0\n\tbuffer_load_dwordx4 %1, %2, %3 offen lds" :: "s"(m0v), "v"(voff), "s"(r), "s"(soff) : "memory");
}
DI rsrc_t code_rsrc() {
  unsigned long long b, e;
  asm volatile("s_getpc_b64 s[40:41]\n\ts_add_u32 s40, s40, rsk_main@rel32@lo+4\n\ts_addc_u32 s41, s41, rsk_main@rel32@hi+12\n\ts_mov_b64 %0, s[40:41]\n\t"
               "s_getpc_b64 s[40:41]\n\ts_add_u32 s40, s40, rsk_end@rel32@lo+4\n\ts_addc_u32 s41, s41, rsk_end@rel32@hi+12\n\ts_mov_b64 %1, s[40:41]"
               : "=&s"(b), "=&s"(e) :: "s40", "s41", "scc");
  return mkrsrc((const void*)b, (e > b && e - b < 0x40000ull) ? (unsigned)(e - b) : 0u);
}
template <bool DEV>
DI void dma64(unsigned m0v, unsigned va, unsigned vb, rsrc_t r, unsigned s0, unsigned s1, unsigned s2, unsigned s3) {
  if constexpr (DEV)
    asm volatile("s_mov_b32 m0, %0\n\ts_nop 0\n\tbuffer_load_dwordx4 %1, %3, %4 offen sc1 lds\n\tbuffer_load_dwordx4 %2, %3, %5 offen offset:1024 sc1 lds\n\t"
                 "buffer_load_dwordx4 %1, %3, %6 offen offset:2048 sc1 lds\n\tbuffer_load_dwordx4 %2, %3, %7 offen offset:3072 sc1 lds"
                 :: "s"(m0v), "v"(va), "v"(vb), "s"(r), "s"(s0), "s"(s1), "s"(s2), "s"(s3) : "memory");
  else
    asm volatile("s_mov_b32 m0, %0\n\ts_nop 0\n\tbuffer_load_dwordx4 %1, %3, %4 offen lds\n\tbuffer_load_dwordx4 %2, %3, %5 offen offset:1024 lds\n\t"
                 "buffer_load_dwordx4 %1, %3, %6 offen offset:2048 lds\n\tbuffer_load_dwordx4 %2, %3, %7 offen offset:3072 lds"
                 :: "s"(m0v), "v"(va), "v"(vb), "s"(r), "s"(s0), "s"(s1), "s"(s2), "s"(s3) : "memory");
}
DI void dma64d(unsigned m0v, unsigned va, rsrc_t r, unsigned s0) {
  asm volatile("s_mov_b32 m0, %0\n\ts_nop 0\n\tbuffer_load_dwordx4 %1, %2, %3 offen sc1 lds\n\tbuffer_load_dwordx4 %1, %2, %3 offen offset:1024 sc1 lds\n\t"
               "buffer_load_dwordx4 %1, %2, %3 offen offset:2048 sc1 lds\n\tbuffer_load_dwordx4 %1, %2, %3 offen offset:3072 sc1 lds"
               :: "s"(m0v), "v"(va), "s"(r), "s"(s0) : "memory");
}
DI rsrc_t mkrsrcB(const void* p, unsigned bytes) { return mkrsrc((const char*)p - 4096, bytes + 4096u); }
#define PINS(x) asm volatile("" : "+s"(x))
DI bf16x8 ldsR(unsigned a) { return *(const LAS bf16x8*)(size_t)a; }
DI s16x4 ldsT(unsigned a) { return __builtin_amdgcn_ds_read_tr16_b64_v4i16((LAS s16x4*)(size_t)a); }
DI bf16x8 cat(s16x4 a, s16x4 b) { return __builtin_shufflevector(a, b, 0, 1, 2, 3, 4, 5, 6, 7); }
DI unsigned pk2(float lo, float hi) { f32x2 v = {lo, hi}; return __builtin_bit_cast(unsigned, __builtin_convertvector(v, bf2_t)); }
DI float bflo(unsigned w) { return __uint_as_float(w << 16); }
DI float bfhi(unsigned w) { return __uint_as_float(w & 0xffff0000u); }
DI float rbf(float x) { return bflo(pk2(x, x) & 0xffffu); }
template <int CTRL, int RM> DI float dpp_add(float v) {
  return v + __builtin_bit_cast(float, __builtin_amdgcn_update_dpp(0, __builtin_bit_cast(int, v), CTRL, RM, 0xf, false));
}
DI void wsum2(float& u, float& v) {
#define WS2(C, M) u = dpp_add<C, M>(u); v = dpp_add<C, M>(v);
  WS2(0x111, 0xf) WS2(0x112, 0xf) WS2(0x114, 0xf) WS2(0x118, 0xf) WS2(0x142, 0xa) WS2(0x143, 0xc)
#undef WS2
  u = __builtin_bit_cast(float, __builtin_amdgcn_readlane(__builtin_bit_cast(int, u), 63));
  v = __builtin_bit_cast(float, __builtin_amdgcn_readlane(__builtin_bit_cast(int, v), 63));
}

DI void ln16(float (&v)[16]) {
  const float x0 = __builtin_bit_cast(float, __builtin_amdgcn_readfirstlane(__builtin_bit_cast(int, v[0])));
  float s = 0.f, q = 0.f;
#pragma unroll
  for (int i = 0; i < 16; ++i) { const float d = v[i] - x0; s += d; q = fmaf(d, d, q); }
  wsum2(s, q);
  const float md = s * (1.0f / DM), var = rbf(fmaxf(fmaf(-md, md, q * (1.0f / DM)), 0.f));
  const float mb = rbf(x0 + md), inv = __builtin_amdgcn_rcpf(rbf(__builtin_amdgcn_sqrtf(rbf(var + 1e-5f))));
#pragma unroll
  for (int i = 0; i < 16; i += 2) {
    const unsigned d = pk2(v[i] - mb, v[i + 1] - mb), r = pk2(bflo(d) * inv, bfhi(d) * inv);
    v[i] = bflo(r); v[i + 1] = bfhi(r);
  }
}
DI unsigned pkx(float lo, float hi) { return (__float_as_uint(lo) >> 16) | (__float_as_uint(hi) & 0xffff0000u); }
enum { RROW = 0, RCHK = 1, T1 = 2, T2 = 3 };
template <int KIND> struct Opnd {
  static constexpr int NA = KIND == RROW ? 2 : KIND == RCHK ? 1 : 4;
  static constexpr int RPB = KIND >= T1 ? 2 : 1;
  unsigned a[NA];
  DI void init(unsigned base, unsigned off, unsigned lane) {
    const unsigned fr = lane & 15u, g = lane >> 4, q = fr >> 2, p = lane & 3u;
    if constexpr (KIND == RROW) {
#pragma unroll
      for (int kk = 0; kk < 2; ++kk) a[kk] = base + (off + fr) * 128u + 16u * ((4u * kk + g) ^ (fr >> 1));
    } else if constexpr (KIND == RCHK) {
      a[0] = base + g * 2048u + (off + fr) * 16u;
    } else {
      const unsigned x = ((q >> 1) << 1) | (g & 1u);
#pragma unroll
      for (int n = 0; n < 4; ++n)
        a[n] = base + (KIND == T1 ? 4096u : 2048u) * (g >> 1) + 1024u * (off >> 6) + 128u * (4u * (g & 1u) + q) + 32u * ((unsigned)n ^ x) + 8u * p;
    }
  }
  DI bf16x8 ld(int b, int kk) const {
    if constexpr (KIND == RROW) return ldsR(a[kk] + 2048u * b);
    else if constexpr (KIND == RCHK) return ldsR(a[0] + 8192u * kk + 256u * b);
    else { const unsigned ad = a[b & 3] + 1024u * (b >> 2) + 8192u * kk; return cat(ldsT(ad), ldsT(ad + (KIND == T1 ? 2048u : 4096u))); }
  }
  DI void adv(unsigned delta) {
#pragma unroll
    for (int i = 0; i < NA; ++i) { a[i] += delta; asm volatile("" : "+v"(a[i])); }
  }
};

template <int PK, int QK, int NS, unsigned STG>
DI void gemm_wide(f32x4 (&acc)[8][4], Opnd<PK>& P, Opnd<QK>& Q, const int N) {
  bf16x8 pf[2][4][2], qf[4][2];
  auto loadP = [&](int h) {
#pragma unroll
    for (int p = 0; p < 4; ++p) { pf[h][p][0] = P.ld(4 * h + p, 0); pf[h][p][1] = P.ld(4 * h + p, 1); }
  };
  auto mma = [&](int h, int q) {
#pragma unroll
    for (int p0 = 0; p0 < 4; ++p0) {
      const int p = (q & 1) ? 3 - p0 : p0;
      acc[4 * h + p][q] = __builtin_amdgcn_mfma_f32_16x16x32_bf16(pf[h][p][0], qf[q][0], acc[4 * h + p][q], 0, 0, 0);
      acc[4 * h + p][q] = __builtin_amdgcn_mfma_f32_16x16x32_bf16(pf[h][p][1], qf[q][1], acc[4 * h + p][q], 0, 0, 0);
    }
  };
  BAR();
  loadP(0);
#pragma unroll
  for (int q = 0; q < 3; ++q) { qf[q][0] = Q.ld(q, 0); qf[q][1] = Q.ld(q, 1); }
  int stg = 0;
#pragma nounroll
  for (int t = 0; t < N; ++t) {
    SCHED();
    qf[3][0] = Q.ld(3, 0); qf[3][1] = Q.ld(3, 1);
    loadP(1); mma(0, 0); mma(0, 1); mma(0, 2); mma(0, 3);
    stg = (stg == NS - 1) ? 0 : stg + 1;
    const unsigned delta = stg == 0 ? (unsigned)(-(int)((NS - 1) * STG)) : STG;
    P.adv(delta); Q.adv(delta);
#pragma unroll
    for (int i = 0; i < 18; ++i) { SGB(0x8, 1); SGB(0x100, 1); }
#pragma unroll
    for (int i = 0; i < 14; ++i) { SGB(0x8, 1); SGB(0x2, 1); }
    SCHED();
    WAITL0(); BAR();
    SCHED();
#pragma unroll
    for (int q = 0; q < 4; ++q) {
      if (q > 0) { qf[q - 1][0] = Q.ld(q - 1, 0); qf[q - 1][1] = Q.ld(q - 1, 1); }
      if (q == 0) { pf[0][0][0] = P.ld(0, 0); pf[0][0][1] = P.ld(0, 1); pf[0][1][0] = P.ld(1, 0); pf[0][1][1] = P.ld(1, 1); }
      else if (q < 3) { pf[0][q + 1][0] = P.ld(q + 1, 0); pf[0][q + 1][1] = P.ld(q + 1, 1); }
      mma(1, q);
#pragma unroll
      for (int i = 0; i < 8; ++i) { SGB(0x8, 1); SGB(0x100, 1); }
      SCHED();
    }
  }
  SCHED();
}

template <int PK, int QK, int NS, unsigned STG, int TAIL, int S0, int DRAIN, class F>
DI void gemm_full(f32x4 (&acc)[4][4], Opnd<PK>& P, Opnd<QK>& Q, const int N, F hook) {
  bf16x8 pf[2][2][4], qf[2][2][4];
  constexpr int NR = 8 * (Opnd<PK>::RPB + Opnd<QK>::RPB);
  int stg = S0;
  auto load = [&](int b) {
#pragma unroll
    for (int i = 0; i < 4; ++i)
#pragma unroll
      for (int kk = 0; kk < 2; ++kk) { pf[b][kk][i] = P.ld(i, kk); qf[b][kk][i] = Q.ld(i, kk); }
    stg = (stg == NS - 1) ? 0 : stg + 1;
    const unsigned delta = stg == 0 ? (unsigned)(-(int)((NS - 1) * STG)) : STG;
    P.adv(delta); Q.adv(delta);
  };
  auto step = [&](int b, bool sync) {
    if (sync) { WAITL0(); BAR(); }
    SCHED();
    load(b ^ 1);
#pragma unroll
    for (int p = 0; p < 4; ++p)
#pragma unroll
      for (int q0 = 0; q0 < 4; ++q0) {
        const int q = (p & 1) ? 3 - q0 : q0;
        acc[p][q] = __builtin_amdgcn_mfma_f32_16x16x32_bf16(pf[b][0][p], qf[b][0][q], acc[p][q], 0, 0, 0);
        acc[p][q] = __builtin_amdgcn_mfma_f32_16x16x32_bf16(pf[b][1][p], qf[b][1][q], acc[p][q], 0, 0, 0);
      }
#pragma unroll
    for (int i = 0; i < NR; ++i) { SGB(0x8, 1); SGB(0x100, 1); }
#pragma unroll
    for (int i = NR; i < 32; ++i) { SGB(0x8, 1); SGB(0x2, 1); }
    SCHED();
  };
  BAR();
  load(0);
  if (DRAIN > 0) {
#pragma nounroll
    for (int t = 0; t < DRAIN; t += 2) { step(0, true); step(1, true); }
    waitv<0>();
    hook();
  }
#pragma nounroll
  for (int t = DRAIN; t < N - (TAIL ? 4 : 0); t += 2) { step(0, true); step(1, true); }
  if (TAIL) {
#pragma nounroll
    for (int t = 0; t < 4; t += 2) { step(0, true); step(1, false); }
  }
}

DI unsigned voffRow(unsigned lane, unsigned ldb, unsigned par) { return (lane >> 3) * ldb + 16u * ((lane & 7u) ^ (4u * par + (lane >> 4))); }
DI unsigned voffT(unsigned lane, unsigned ldb) { return (lane >> 3) * ldb + 16u * ((lane & 7u) ^ (2u * ((((lane >> 4) & 1u) << 1) | (lane >> 5)))); }
struct T4 { unsigned s0, s1, s2, s3;
  DI void set(unsigned so, unsigned lw, unsigned ldb) { s0 = so + 4096u + lw * 16u * ldb; s1 = s0 + 128u - 1024u; s2 = s0 + 8u * ldb - 2048u; s3 = s2 + 128u - 1024u; }
  DI void pin() { PINS(s0); PINS(s1); PINS(s2); PINS(s3); }
  template <bool DEV = false> DI void go(unsigned dst, unsigned vo, rsrc_t r) const { dma64<DEV>(dst, vo, vo, r, s0, s1, s2, s3); } };

typedef unsigned long long u64;
constexpr unsigned FL_H = 64, FL_O = 320, FL_U = 576, FL_G = 832, FL_X = 3072;
DI void flag_set(u64* p, u64 tag) { __hip_atomic_store(p, tag, __ATOMIC_RELAXED, __HIP_MEMORY_SCOPE_AGENT); }
DI u64 flag_get(const u64* p) { return __hip_atomic_load(p, __ATOMIC_RELAXED, __HIP_MEMORY_SCOPE_AGENT); }
template <int SLOW = 16> DI void flag_wait(const u64* p, u64 tag, u64 mask) {
  const u64 t0 = __builtin_amdgcn_s_memrealtime();
  while ((__ballot(flag_get(p) != tag) & mask) != 0ull) {
    const u64 dt = __builtin_amdgcn_s_memrealtime() - t0;
    if (dt > SPIN_LIMIT) break;
    if (dt > 400ull) __builtin_amdgcn_s_sleep(SLOW); else __builtin_amdgcn_s_sleep(3);
  }
  asm volatile("" ::: "memory");
}

DI f32x2 sigm2(f32x2 v, f32x2 z) {
  f32x2 e = {__builtin_amdgcn_exp2f(z[0]), __builtin_amdgcn_exp2f(z[1])};
  e += 1.0f;
  return v * (f32x2){__builtin_amdgcn_rcpf(e[0]), __builtin_amdgcn_rcpf(e[1])};
}
DI unsigned gelu2(f32x2 v) {
  const f32x2 r = sigm2(v, v * __builtin_elementwise_fma(v * v, (f32x2)(-0.1029432397f), (f32x2)(-2.3022081986f)));
  return pk2(r[0], r[1]);
}
DI unsigned silu2(f32x2 v) { const f32x2 r = sigm2(v, v * -1.4426950408889634f); return pk2(r[0], r[1]); }
DI f32x2 lo2(f32x4 v) { return __builtin_shufflevector(v, v, 0, 1); }
DI f32x2 hi2(f32x4 v) { return __builtin_shufflevector(v, v, 2, 3); }
DI f32x2 bf2(unsigned w) { return (f32x2){bflo(w), bfhi(w)}; }

constexpr unsigned QSTG = 49152u;
constexpr unsigned KBASE = 65536u, VBASE = 114688u;
constexpr unsigned KSLOT[3] = {0u, 32768u, 16384u}, VSLOT[3] = {32768u, 16384u, 0u};
DI void touch8k(rsrc_t r, unsigned vo128, unsigned soff) {
  asm volatile("buffer_load_dword v255, %0, %1, %2 offen" :: "v"(vo128), "s"(r), "s"(soff) : "memory", "v255");
}
constexpr float QSCALE = 0.12751743082f;
#define BIGSUM 4096.0f

struct Att {
  bf16x8 qf[8], kv[16], pf[4], pn[2];
  f32x16 o[4], s[2], negm;
  float l, ts;
  unsigned aK[4], aV[2];
};
template <unsigned SLOT, int J> DI void att_k(Att& a) {
  asm volatile("ds_read_b128 %0, %1 offset:%2" : "+v"(a.kv[J]) : "v"(a.aK[(J >> 1) & 3]), "i"(SLOT + 8192u * (J >> 3) + 4096u * (J & 1)) : "memory");
}
template <unsigned SLOT, int J> DI void att_v(Att& a) {
  s16x4 lo = __builtin_shufflevector(a.kv[J], a.kv[J], 0, 1, 2, 3), hi = __builtin_shufflevector(a.kv[J], a.kv[J], 4, 5, 6, 7);
  constexpr unsigned off = SLOT + 4096u * (J >> 2) + 1024u * ((J >> 1) & 1);
  asm volatile("ds_read_b64_tr_b16 %0, %2 offset:%3\n\tds_read_b64_tr_b16 %1, %2 offset:%4" : "+v"(lo), "+v"(hi) : "v"(a.aV[J & 1]), "i"(off), "i"(off + 2048u) : "memory");
  a.kv[J] = cat(lo, hi);
}
DI void att_wait(Att& a) {
  asm volatile("s_waitcnt lgkmcnt(0)" : "+v"(a.kv[0]), "+v"(a.kv[1]), "+v"(a.kv[2]), "+v"(a.kv[3]), "+v"(a.kv[4]), "+v"(a.kv[5]), "+v"(a.kv[6]), "+v"(a.kv[7]),
               "+v"(a.kv[8]), "+v"(a.kv[9]), "+v"(a.kv[10]), "+v"(a.kv[11]), "+v"(a.kv[12]), "+v"(a.kv[13]), "+v"(a.kv[14]), "+v"(a.kv[15]) :: "memory");
}
DI void att_loadK(Att& a, unsigned slot) {
#pragma unroll
  for (int j = 0; j < 16; ++j) a.kv[j] = ldsR(a.aK[(j >> 1) & 3] + slot + 8192u * (j >> 3) + 4096u * (j & 1));
}
DI unsigned lane_id() { unsigned l; asm volatile("v_mbcnt_lo_u32_b32 %0, -1, 0\n\tv_mbcnt_hi_u32_b32 %0, -1, %0" : "=v"(l)); return l; }
DI void add_bcast3(f32x16& t0, f32x16& t1, f32x16& t2, float v) {
  const float one = lane_id() < 32u ? 1.0f : 0.0f;
  asm volatile("s_nop 7\n\ts_nop 7\n\tv_mfma_f32_32x32x2_f32 %0, %3, %4, %0\n\tv_mfma_f32_32x32x2_f32 %1, %3, %4, %1\n\tv_mfma_f32_32x32x2_f32 %2, %3, %4, %2\n\t"
               "s_nop 7\n\ts_nop 7\n\ts_nop 7" : "+v"(t0), "+v"(t1), "+v"(t2) : "v"(one), "v"(v));
}
template <bool FIRST, unsigned SLOT, int J> DI void att_qk1(Att& a) {
  a.s[J & 1] = __builtin_amdgcn_mfma_f32_32x32x16_bf16(a.kv[J], a.qf[J >> 1], J < 2 ? (FIRST ? (f32x16){} : a.negm) : a.s[J & 1], 0, 0, 0);
  SCHED();
  if constexpr (J == 0) ALIGN8();
  if constexpr (!FIRST) att_v<SLOT, J>(a);
  SCHED();
  if constexpr (J < 15) att_qk1<FIRST, SLOT, J + 1>(a);
}
template <int NEXT, unsigned SLOT, int J> DI void att_pv1(Att& a) {
  a.o[J & 3] = __builtin_amdgcn_mfma_f32_32x32x16_bf16(a.kv[J], a.pf[J >> 2], a.o[J & 3], 0, 0, 0);
  SCHED();
  if constexpr (J == 0) ALIGN8();
  if constexpr (NEXT == 1) att_k<SLOT, J>(a); else if constexpr (NEXT == 2) att_v<SLOT, J>(a);
  SCHED();
  if constexpr (J < 15) att_pv1<NEXT, SLOT, J + 1>(a);
}
DI float max3(float x, float y, float z) { return __builtin_fmaxf(__builtin_fmaxf(x, y), z); }
DI float att_rowmax(const Att& a) {
  float m0 = max3(a.s[0][0], a.s[0][1], a.s[0][2]), m1 = max3(a.s[1][0], a.s[1][1], a.s[1][2]);
#pragma unroll
  for (int r = 3; r < 15; r += 2) { m0 = max3(m0, a.s[0][r], a.s[0][r + 1]); m1 = max3(m1, a.s[1][r], a.s[1][r + 1]); }
  const float mx = __builtin_fmaxf(max3(m0, m1, a.s[0][15]), a.s[1][15]);
  auto rr = __builtin_amdgcn_permlane32_swap(__float_as_uint(mx), __float_as_uint(mx), false, false);
  return __builtin_fmaxf(__uint_as_float(rr[0]), __uint_as_float(rr[1]));
}
DI float att_exp16(const f32x16& v, bf16x8& p0, bf16x8& p1) {
  float s0 = 0.f, s1 = 0.f;
  u32x4 w[2];
#pragma unroll
  for (int h = 0; h < 2; ++h) {
    float e[8];
#pragma unroll
    for (int r = 0; r < 8; ++r) e[r] = __builtin_amdgcn_exp2f(v[8 * h + r]);
#pragma unroll
    for (int r = 0; r < 8; r += 2) {
      if (h == 0 && r == 0) { s0 = e[0]; s1 = e[1]; } else { s0 += e[r]; asm("" : "+v"(s0)); s1 += e[r + 1]; asm("" : "+v"(s1)); }
      w[h][r >> 1] = pk2(e[r], e[r + 1]);
    }
    asm volatile("" : "+v"(w[h]));
    SCHED();
  }
  p0 = __builtin_bit_cast(bf16x8, w[0]); p1 = __builtin_bit_cast(bf16x8, w[1]);
  return s0 + s1;
}
template <bool FIRST> DI void att_sm1(Att& a) {
  if (FIRST) {
    const float mx = att_rowmax(a);
#pragma unroll
    for (int r = 0; r < 16; ++r) { a.s[0][r] -= mx; a.s[1][r] -= mx; a.negm[r] = -mx; }
  }
  a.ts = att_exp16(a.s[0], a.pn[0], a.pn[1]);
  asm volatile("" : "+v"(a.pn[0]), "+v"(a.pn[1]), "+v"(a.ts), "+v"(a.s[1]), "+v"(a.negm));
}
template <bool FIRST> DI void att_sm2(Att& a) {
  a.ts += att_exp16(a.s[1], a.pf[2], a.pf[3]);
  if (!FIRST) {
    if (__builtin_expect(!__all(a.ts <= BIGSUM), 0)) {
      const float dl = fmaxf(att_rowmax(a), 0.f), fct = __builtin_amdgcn_exp2f(-dl);
#pragma unroll
      for (int db = 0; db < 4; ++db) a.o[db] *= fct;
      a.l *= fct;
      add_bcast3(a.s[0], a.s[1], a.negm, -dl);
      a.ts = att_exp16(a.s[0], a.pn[0], a.pn[1]) + att_exp16(a.s[1], a.pf[2], a.pf[3]);
    }
  }
  a.l += a.ts;
  a.pf[0] = a.pn[0]; a.pf[1] = a.pn[1];
  asm volatile("" : "+v"(a.pf[0]), "+v"(a.pf[1]), "+v"(a.pf[2]), "+v"(a.pf[3]), "+v"(a.l));
}

extern "C" __global__ __launch_bounds__(512, 2) __attribute__((amdgpu_num_vgpr(127)))
void rsk_main(const u16* __restrict__ x, char* __restrict__ ws, const u16* __restrict__ wq, const u16* __restrict__ kc, const u16* __restrict__ vc,
              const u16* __restrict__ w1, const u16* __restrict__ wo, const u16* __restrict__ w2, const u16* __restrict__ b1, const u16* __restrict__ b2,
              u16* __restrict__ out) {
  if (rfl(threadIdx.x) == 0u) touch8k(code_rsrc(), threadIdx.x * 128u + (threadIdx.x < 48u ? 0u : (blockIdx.x >> 3) * 2048u), 0u);
  extern __shared__ __attribute__((aligned(16))) char lds[];
  const unsigned L = (unsigned)(size_t)lds;
  const char* xn = ws + WS_XN; const char* hh = ws + WS_H;
  const unsigned tid = threadIdx.x, lane = tid & 63u, wid = rfl(tid >> 6), grp = wid >> 2, lw = wid & 3u;
  const unsigned bid = blockIdx.x, xcd = bid & 7u, idx = bid >> 3;
  const unsigned qt = (xcd & 1u) * 4u + (idx & 3u), kvh = xcd >> 1, head = kvh * 8u + (idx >> 2);
  const bool hasU = idx < 28u;
  const unsigned udt = (xcd & 1u) * 4u + (idx & 3u), uft = (xcd >> 1) * 7u + (idx >> 2);
  const unsigned ufc = (idx >> 2) * 4u + (xcd >> 1);
  const rsrc_t sK = mkrsrc(kc + ((size_t)kvh * SK + (SK - WIN)) * DH, WIN * DH * 2), sV = mkrsrc(vc + ((size_t)kvh * SK + (SK - WIN)) * DH, WIN * DH * 2);
  const rsrc_t sH = mkrsrc(hh, SEQ * DM * 2), sW1 = mkrsrcB(w1, SEQ * FF * 2);
  const unsigned voK0 = voffRow(lane, 256u, 0), voV = voffT(lane, 256u);
  auto dmaK2 = [&](unsigned slot, unsigned t, unsigned p0) {
    unsigned voK1; asm volatile("v_xor_b32 %0, 64, %1" : "=v"(voK1) : "v"(voK0));
    dma16(L + KBASE + slot + 1024u * p0, voK0, sK, t * 16384u + (p0 & 7u) * 2048u + (p0 >> 3) * 128u);
    dma16(L + KBASE + slot + 1024u * p0 + 1024u, voK1, sK, t * 16384u + (p0 & 7u) * 2048u + 2048u + (p0 >> 3) * 128u); };
  auto dmaV2 = [&](unsigned slot, unsigned t, unsigned p0) {
    dma16(L + VBASE + slot + 1024u * p0, voV, sV, t * 16384u + (p0 >> 1) * 2048u);
    dma16(L + VBASE + slot + 1024u * p0 + 1024u, voV, sV, t * 16384u + (p0 >> 1) * 2048u + 128u); };
  auto tileK = [&](unsigned t) {
#pragma unroll
    for (int p = 0; p < 16; p += 2) dmaK2(KSLOT[t % 3], t, p); };
  auto tileV = [&](unsigned t) {
#pragma unroll
    for (int p = 0; p < 16; p += 2) dmaV2(VSLOT[t % 3], t, p); };
  Att a;
  { const unsigned r32 = lane & 31u, hi = lane >> 5;
#pragma unroll
    for (int j = 0; j < 4; ++j) a.aK[j] = L + KBASE + r32 * 128u + 16u * ((2u * j + hi) ^ ((r32 >> 1) & 7u)); }

  ST_DECL; STAMP(0);
  u64* flags = (u64*)(ws + WS_FLAG);
  const rsrc_t sS = lw == 0 ? mkrsrcB(wq, DM * NH * DH * 2) : mkrsrcB(xn, SEQ * DM * 2);
  unsigned asrc = 4096u + (lw == 0 ? head * 256u : (qt * 256u + (lw - 1u) * 128u) * 2048u), adst = L + lw * 16384u;
  const unsigned avo0 = lw == 0 ? voffT(lane, 8192u) : voffRow(lane, 2048u, 0), avo1 = lw == 0 ? avo0 : avo0 ^ 64u;
  constexpr unsigned HT = 147456u;
  auto astep = [&](unsigned vo0, unsigned vo1) {
    const unsigned sp = lw == 0 ? 8u * 8192u : 16u * 2048u, so = (lw == 0 ? 128u : 8u * 2048u) - 1024u;
#pragma unroll
    for (int q = 0; q < 4; ++q) { const unsigned e = asrc + 2u * q * sp; dma64<true>(adst + 4096u * q, vo0, vo1, sS, e, e + so, e + sp - 2048u, e + sp - 2048u + so); }
    asrc += lw == 0 ? 64u * 8192u : 128u; adst = adst >= L + 2u * QSTG ? adst - 2u * QSTG : adst + QSTG;
  };
  u64 tag;
  {
    const unsigned row = (bid >> 2) * 32u + ((bid >> 1) & 1u) * 16u + (bid & 1u) * 4u + (wid >> 2) * 8u + (wid & 3u);
    u32x4 xa, xb; u32x2 ep;
    asm volatile("buffer_load_dwordx4 %0, %3, %4, %5 offen\n\tbuffer_load_dwordx4 %1, %3, %4, %5 offen offset:1024\n\tglobal_load_dwordx2 %2, %6, %7 sc1"
                 : "=&v"(xa), "=&v"(xb), "=&v"(ep) : "v"(lane * 16u), "s"(mkrsrc(x, SEQ * DM * 2)), "s"(row * 2048u), "v"(0u), "s"(flags) : "memory");
    __builtin_amdgcn_s_setprio(1);
    if (wid == 7) {
      touch8k(mkrsrc(wq, DM * NH * DH * 2), lane * 128u, bid * 8192u);
      waitv<1>();
    } else waitv<0>();
    asm volatile("" : "+v"(xa), "+v"(xb), "+v"(ep));
    STAMP(16);
    tag = (((u64)rfl(ep[1]) << 32) | rfl(ep[0])) * 0x9E3779B97F4A7C15ull + 0x632BE59BD9B4E019ull;
    float v[16];
#pragma unroll
    for (int i = 0; i < 4; ++i) { v[2 * i] = bflo(xa[i]); v[2 * i + 1] = bfhi(xa[i]); v[8 + 2 * i] = bflo(xb[i]); v[9 + 2 * i] = bfhi(xb[i]); }
    ln16(v);
    const rsrc_t sW = mkrsrc(ws, WS_O);
#pragma unroll
    for (int p = 0; p < 2; ++p) {
      const unsigned lo0 = pkx(v[8 * p], v[8 * p + 1]), lo1 = pkx(v[8 * p + 2], v[8 * p + 3]), hi0 = pkx(v[8 * p + 4], v[8 * p + 5]), hi1 = pkx(v[8 * p + 6], v[8 * p + 7]);
      const bool odd = lane & 1u;
      const unsigned s0 = __builtin_amdgcn_update_dpp(0, (int)(odd ? lo0 : hi0), 0xB1, 0xf, 0xf, false), s1 = __builtin_amdgcn_update_dpp(0, (int)(odd ? lo1 : hi1), 0xB1, 0xf, 0xf, false);
      __builtin_amdgcn_raw_buffer_store_b128(odd ? (u32x4){s0, s1, hi0, hi1} : (u32x4){lo0, lo1, s0, s1}, sW, WS_XN + row * 2048u + lane * 16u + 1024u * p, 0, 16);
    }
    STAMP(17);
    waitv<0>();
    if (lane == 0u) flag_set(flags + FL_X + bid * 8u + wid, tag);
    STAMP(18);
    if (wid == 4u) { astep(avo0, avo1); astep(avo0, avo1); }
    if (grp == 0) __builtin_amdgcn_s_setprio(0);
    ln16(v);
    *(LAS u32x4*)(size_t)(L + HT + wid * 2048u + lane * 16u) = (u32x4){pkx(v[0], v[1]), pkx(v[2], v[3]), pkx(v[4], v[5]), pkx(v[6], v[7])};
    *(LAS u32x4*)(size_t)(L + HT + wid * 2048u + 1024u + lane * 16u) = (u32x4){pkx(v[8], v[9]), pkx(v[10], v[11]), pkx(v[12], v[13]), pkx(v[14], v[15])};
    WAITL0();
    __builtin_amdgcn_s_setprio(0);
    STAMP(19);
  }
  auto hstore = [&](unsigned i) {
    const unsigned d0 = i * 64u, ad = L + HT + ((lane & 15u) >> 2) * 2048u + (d0 + (lane >> 4) * 16u + (lane & 3u) * 4u) * 2u;
    __builtin_amdgcn_raw_buffer_store_b128(__builtin_bit_cast(u32x4, cat(ldsT(ad), ldsT(ad + 8192u))), mkrsrc(ws, WS_O), WS_H + ((d0 >> 7) * 256u + bid) * 2048u + ((d0 & 64u) + lane) * 16u, 0, 16);
  };
  if (!hasU) {
    BAR();
    hstore(wid * 2u); hstore(wid * 2u + 1u);
    waitv<0>(); BAR();
    if (tid == 0u) flag_set(flags + FL_H + bid, tag);
    const unsigned w = ((idx - 28u) * 8u + xcd) * 8u + wid, vo = lane * 128u;
    const rsrc_t sWq = mkrsrc(wq, DM * NH * DH * 2);
#pragma unroll
    for (int j = 1; j < 4; ++j) touch8k(sWq, vo, (j * 256u + w) * 8192u);
    touch8k(mkrsrc((w & 64u) ? vc : kc, 4u * SK * DH * 2u), w < 128u ? vo : 0x80000000u, ((w >> 4) & 3u) * (SK * DH * 2u) + (SK - WIN) * DH * 2u + (w & 15u) * 8192u);
#pragma unroll
    for (int j = 0; j < NW1; ++j) { waitv<2>(); touch8k(sW1, vo, (j * 256u + w) * 8192u + 4096u); }
    waitv<2>();
    if (wid < 7u - NW1) touch8k(sW1, vo, ((NW1 + wid) * 256u + bid) * 8192u + 4096u);
    if (wid < 7u) touch8k(mkrsrc(w2, FF * SEQ * 2), vo, (wid * 256u + bid) * 8192u);
    if (wid >= 4u) touch8k(mkrsrc(wo, NH * DH * DM * 2), vo, ((wid - 4u) * 256u + bid) * 8192u);
    waitv<0>();
    STAMP(20);
  }
  if (wid == 7) {
    const unsigned vo = lane * 128u;
    BAR(); BAR();
#pragma nounroll
    for (unsigned i = 0; i < 4; ++i) {
      BAR();
      if (hasU) { hstore(i * 4u); hstore(i * 4u + 1u); hstore(i * 4u + 2u); hstore(i * 4u + 3u); }
    }
    BAR(); BAR();
    waitv<0>();
    if (lane == 0u) flag_set(flags + FL_H + bid, tag);
    BAR(); BAR(); BAR(); BAR();
    const u64 f0 = flag_get(flags + FL_H + lane), f1 = flag_get(flags + FL_H + 64u + lane), f2 = flag_get(flags + FL_H + 128u + lane), f3 = flag_get(flags + FL_H + 192u + lane);
    touch8k(code_rsrc(), lane < 16u ? vo : 0x80000000u, idx * 2048u);
    BAR(); BAR();
    if (__any(f0 != tag || f1 != tag || f2 != tag || f3 != tag)) {
#pragma nounroll
      for (unsigned j = 0; j < 256u; j += 64u) flag_wait(flags + FL_H + j + lane, tag, ~0ull);
    }
    if (bid == 0u && lane == 0u) flag_set(flags, tag);
    BAR(); BAR(); BAR();
#pragma unroll
    for (int j = NW1; j < 6; ++j) touch8k(sW1, vo, (j * 256u + bid) * 8192u + 4096u);
    att_loadK(a, KSLOT[0]);
  } else if (grp == 1) {
    const unsigned vo0 = avo0, vo1 = avo1;
    if (lw != 0) {
      const u64* fx = flags + FL_X + qt * 256u + (lw - 1u) * 128u + lane;
      const u64 t0 = __builtin_amdgcn_s_memrealtime();
      while (__any(((flag_get(fx) ^ tag) | (flag_get(fx + 64) ^ tag)) != 0ull)) {
        if (__builtin_amdgcn_s_memrealtime() - t0 > SPIN_LIMIT) break;
        __builtin_amdgcn_s_sleep(2);
      }
      asm volatile("" ::: "memory");
      STAMP(21);
      astep(vo0, vo1); astep(vo0, vo1);
      STAMP(22);
    }
    waitv<16>(); STAMP(23);
    BAR(); STAMP(1);
    astep(vo0, vo1);
#pragma nounroll
    for (unsigned t = 0; t < 13; ++t) { waitv<16>(); BAR(); astep(vo0, vo1); }
    waitv<16>(); BAR();
    if (lw == 0) tileK(0); else if (lw == 1) tileK(2); else tileV(0);
    waitv<16>(); BAR();
    if (lw == 1) tileK(1); else if (lw == 2) tileV(1);
    if (lw == 0) waitv<0>();
    BAR();
    att_loadK(a, KSLOT[0]);
  } else {
    Opnd<T1> P; Opnd<RROW> Q;
    P.init(L, 0u, lane); Q.init(L + 16384u, lw * 64u, lane);
    f32x4 acc[8][4] = {};
    gemm_wide<T1, RROW, 3, QSTG>(acc, P, Q, 16);
    STAMP(2);
    att_loadK(a, KSLOT[0]);
    const unsigned wa = L + (lane >> 5) * 4096u + (lw * 64u + (lane & 15u)) * 16u + ((lane >> 4) & 1u) * 8u;
#pragma unroll
    for (int j = 0; j < 16; ++j) {
      const f32x4 v = acc[j >> 1][j & 1] * QSCALE;
      *(LAS u32x2*)(size_t)(wa + 8192u * (j >> 1) + 256u * (j & 1)) = (u32x2){pk2(v[0], v[1]), pk2(v[2], v[3])};
    }
#pragma unroll
    for (int ks = 0; ks < 8; ++ks) a.qf[ks] = ldsR(L + (2u * ks + (lane >> 5)) * 4096u + (lw * 64u + (lane & 31u)) * 16u);
    SCHED();
#pragma unroll
    for (int j = 0; j < 16; ++j) {
      a.s[j & 1] = __builtin_amdgcn_mfma_f32_32x32x16_bf16(a.kv[j], a.qf[j >> 1], j < 2 ? (f32x16){} : a.s[j & 1], 0, 0, 0);
      float v[4];
#pragma unroll
      for (int c = 0; c < 4; ++c) { v[c] = acc[j >> 1][2 + (j & 1)][c] * QSCALE; asm("" : "+v"(v[c])); }
      *(LAS u32x2*)(size_t)(wa + 8192u * (j >> 1) + 256u * (2 + (j & 1))) = (u32x2){pk2(v[0], v[1]), pk2(v[2], v[3])};
      SCHED();
    }
    WAITL0();
  }
  STAMP(3);

  const rsrc_t sO = mkrsrc(ws + WS_O, SEQ * NH * DH * 2);
  {
    const unsigned l2 = lane_id(), hi = l2 >> 5, blk = (l2 >> 4) & 1u, q4 = (l2 & 15u) >> 2, p4 = l2 & 3u;
#pragma unroll
    for (int n = 0; n < 2; ++n) a.aV[n] = L + VBASE + 128u * (4u * hi + q4) + 32u * ((2u * n + blk) ^ (((q4 >> 1) << 1) | hi)) + 8u * p4;
#pragma unroll
    for (int db = 0; db < 4; ++db) a.o[db] = (f32x16){};
    a.l = 0.f;
    const rsrc_t sW1u = mkrsrc((const char*)w1 - 4096, hasU ? SEQ * FF * 2 + 4096u : 0u), sHu = mkrsrc(hh, hasU ? SEQ * DM * 2 : 0u);
    unsigned voU = 0;
    auto uP = [&](unsigned j) {
      const unsigned so = 4096u + (j * 64u + wid * 8u) * (FF * 2u) + ufc * 256u;
      asm volatile("s_mov_b32 m0, %0\n\ts_nop 0\n\tbuffer_load_dwordx4 %1, %2, %3 offen lds\n\tbuffer_load_dwordx4 %1, %2, %4 offen offset:1024 lds"
                   :: "s"(L + j * 32768u + wid * 2048u), "v"(voU), "s"(sW1u), "s"(so), "s"(so + 128u - 1024u) : "memory"); };
    auto uQ = [&](unsigned j) {
      asm volatile("s_mov_b32 m0, %0\n\ts_nop 0\n\tbuffer_load_dwordx4 %1, %2, %3 offen sc1 lds\n\tbuffer_load_dwordx4 %1, %2, %3 offen offset:1024 sc1 lds"
                   :: "s"(L + j * 32768u + 16384u + wid * 2048u), "v"(lane_id() * 16u), "s"(sHu), "s"((udt * 32u + j) * 16384u + wid * 2048u) : "memory"); };
    const bool pre = hasU && grp == 1;
    if (grp == 1) {
      if (wid != 7) waitv<0>();
      WAITL0(); BAR();
#pragma unroll
      for (int ks = 0; ks < 8; ++ks) a.qf[ks] = ldsR(L + (2u * ks + hi) * 4096u + (lw * 64u + 32u + (l2 & 31u)) * 16u);
      SCHED(); att_qk1<true, 0u, 0>(a);
    }
    STAMP(4);
    const rsrc_t sWo = mkrsrc(wo, NH * DH * DM * 2), sW2 = mkrsrc(w2, FF * SEQ * 2);
    const unsigned lk = bid * 8192u + (wid >> 1) * 2048u + (wid & 1u) * 128u;
#define LOOK(v) do { if ((v) < 1) touch8k(sW1, voV, lk + 6u * 0x200000u + 4096u); else if ((v) < 5) touch8k(sWo, voV, lk + ((v) - 1) * 0x200000u); \
                     else if ((v) < 12) touch8k(sW2, voV, lk + (((v) - 2) % 7) * 0x200000u); } while (0)
#define ATT_TILE(i, FIRST, WB, WD)                                                                                            \
    SCHED(); if (!(FIRST)) att_qk1<false, VSLOT[((i) + 2) % 3], 0>(a); BAR(); SCHED();                                        \
    if ((i) <= 4) dmaK2(KSLOT[(i) % 3], (i) + 3, wid * 2u);                                                                   \
    LOOK(2 * (i));                                                                                                            \
    if ((i) == 5) { voU = voffT(lane_id(), FF * 2u); uP(0); uQ(0); } else if ((i) == 6) { uQ(1); uP(2); } else if ((i) == 7) uP(3); \
    att_sm1<FIRST>(a);                                                                                                        \
    waitv<WB>();                                                                                                              \
    if (!(FIRST)) att_wait(a); SCHED(); BAR();                                                                                \
    if (FIRST) att_loadK(a, KSLOT[1]); else { SCHED(); att_pv1<((i) < 7 ? 1 : 2), ((i) < 7 ? KSLOT[((i) + 1) % 3] : VSLOT[7 % 3]), 0>(a); } \
    BAR(); SCHED();                                                                                                           \
    if ((i) <= 5) dmaV2(VSLOT[((i) + 2) % 3], (i) + 2, wid * 2u);                                                             \
    LOOK(2 * (i) + 1);                                                                                                        \
    if ((i) == 5) uP(1); else if ((i) == 6) uQ(2); else if ((i) == 7) uQ(3);                                                  \
    att_sm2<FIRST>(a);                                                                                                        \
    waitv<WD>();                                                                                                              \
    att_wait(a); SCHED(); BAR();
    ATT_TILE(0, true, 63, 63) ATT_TILE(1, false, 63, 10) ATT_TILE(2, false, 10, 10) ATT_TILE(3, false, 10, 10)
    ATT_TILE(4, false, 10, 10) ATT_TILE(5, false, 12, 14) ATT_TILE(6, false, 15, 9) ATT_TILE(7, false, 63, 63)
#undef ATT_TILE
#undef LOOK
    SCHED(); att_pv1<0, 0u, 0>(a); STAMP(5);
    if (grp == 0) { BAR(); waitv<0>(); }
    if (pre) { waitv<2>(); BAR(); STAMP(24); }
  }
  auto ost2 = [&](int db) {
    const unsigned l3 = lane_id(), row = qt * 256u + lw * 64u + grp * 32u + (l3 & 31u);
    const unsigned oo = (((row >> 7) * 64u + head * 2u) * 8u + (l3 >> 5)) * 2048u + (row & 127u) * 16u;
#pragma unroll
    for (int i = 0; i < 2; ++i) {
      float w[8];
#pragma unroll
      for (int c = 0; c < 8; ++c) { w[c] = a.o[db][8 * i + c] * a.l; asm("" : "+v"(w[c])); }
      __builtin_amdgcn_raw_buffer_store_b128((u32x4){pk2(w[0], w[1]), pk2(w[2], w[3]), pk2(w[4], w[5]), pk2(w[6], w[7])}, sO, oo + db * 8192u + i * 4096u, 0, 16);
    }
  };
  { auto rr = __builtin_amdgcn_permlane32_swap(__float_as_uint(a.l), __float_as_uint(a.l), false, false);
    a.l = __builtin_amdgcn_rcpf(__uint_as_float(rr[0]) + __uint_as_float(rr[1])); }
  if (!(hasU && grp == 1)) { ost2(0); ost2(1); ost2(2); ost2(3); STAMP(6); }
  const unsigned ln = lane_id();
  const bool isY = xcd >= 4u;
  const unsigned dt = udt, st = ((xcd >> 1) & 1u) * 8u + (idx >> 2), tile = st * 8u + dt;
  u64* fG = flags + FL_G + tile * 16u; u64* fO = flags + FL_O; u64* fU = flags + FL_U + dt * 32u;
  const rsrc_t sG = mkrsrc(ws + WS_G + (size_t)tile * 32768u, 32768u), sU = mkrsrc(ws + WS_U, DM * FF * 2);
  constexpr unsigned XO = 32768u, GO = 65536u;
  auto finish = [&]() {
    WAITL0(); BAR();
#pragma unroll
    for (int q = 0; q < 4; ++q) {
      const unsigned row = wid * 16u + q * 4u + (ln >> 4);
      const u32x4 v = *(const LAS u32x4*)(size_t)(L + XO + row * 256u + 16u * ((ln & 15u) ^ (row & 15u)));
      __builtin_amdgcn_raw_buffer_store_b128(v, mkrsrc(out, SEQ * DM * 2), (st * 128u + row) * 2048u + dt * 256u + (ln & 15u) * 16u, 0, 16);
    }
    STAMP(14); waitv<0>(); STAMP(15); ST_OUT(1);
  };
  if (!hasU) {
    waitv<0>(); BAR();
    if (tid == 256u) flag_set(fO + qt * 32u + head, tag);
    STAMP(7);
  }
  if (grp == 0) {
    const unsigned wp = lw >> 1, wq2 = lw & 1u, fr = ln & 15u, fq = ln >> 4;
    if (hasU) {
      Opnd<T1> P; Opnd<RCHK> Q;
      P.init(L, wp * 64u, ln); Q.init(L + 16384u, wq2 * 64u, ln);
      u32x2 bv[4];
      f32x4 acc[4][4] = {};
      gemm_full<T1, RCHK, 5, 32768u, 0, 0, 16>(acc, P, Q, 32, [&] {
#pragma unroll
        for (int p = 0; p < 4; ++p) bv[p] = *(const u32x2*)(b1 + ufc * 128u + wp * 64u + p * 16u + fq * 4u);
      });
      STAMP(8);
      const unsigned uo = ((udt * 56u + uft * 2u + wp) * 8u + fq) * 2048u + (wq2 * 64u + fr) * 16u;
#pragma unroll
      for (int pp = 0; pp < 2; ++pp) {
        const f32x2 b0 = bf2(bv[2 * pp][0]), b1v = bf2(bv[2 * pp][1]), b2v = bf2(bv[2 * pp + 1][0]), b3 = bf2(bv[2 * pp + 1][1]);
#pragma unroll
        for (int m = 0; m < 4; ++m)
          __builtin_amdgcn_raw_buffer_store_b128((u32x4){gelu2(lo2(acc[2 * pp][m]) + b0), gelu2(hi2(acc[2 * pp][m]) + b1v), gelu2(lo2(acc[2 * pp + 1][m]) + b2v), gelu2(hi2(acc[2 * pp + 1][m]) + b3)},
                                                 sU, uo + pp * 8192u + m * 256u, 0, 16);
      }
      STAMP(9);
      if (isY) { waitv<0>(); BAR(); }
    }
    if (isY || !hasU) BAR();
    f32x4 acc[4][4] = {};
    const unsigned t4 = (wid * 64u + ln) * 16u;
    if (isY) {
      float bias[4];
#pragma unroll
      for (int q = 0; q < 4; ++q) bias[q] = bflo(b2[st * 128u + wq2 * 64u + q * 16u + fr]);
      Opnd<RCHK> P; Opnd<T2> Q;
      P.init(L + 65536u, wp * 64u, ln); Q.init(L + 65536u + 16384u, wq2 * 64u, ln);
      gemm_full<RCHK, T2, 5, 32768u, 3, 2, 0>(acc, P, Q, 56, [] {});
      STAMP(12);
#pragma unroll
      for (int j = 0; j < 8; ++j) {
        const int q = 2 * (j & 1); const f32x4 y0 = acc[j >> 1][q] + bias[q], y1 = acc[j >> 1][q + 1] + bias[q + 1];
        __builtin_amdgcn_raw_buffer_store_b128((u32x4){silu2(lo2(y0)), silu2(hi2(y0)), silu2(lo2(y1)), silu2(hi2(y1))}, sG, t4 + j * 4096u, 0, 16);
      }
      waitv<0>(); BAR();
      if (tid == 0u) flag_set(fG, tag);
      STAMP(15); ST_OUT(1); return;
    }
    Opnd<T1> P; Opnd<RCHK> Q;
    P.init(L + 65536u, wp * 64u, ln); Q.init(L + 65536u + 16384u, wq2 * 64u, ln);
    gemm_full<T1, RCHK, 5, 32768u, 3, 2, 2>(acc, P, Q, 64, [] {});
    STAMP(12);
    const unsigned xa = L + XO + (wq2 * 64u + fr) * 256u + (fq & 1u) * 8u;
    unsigned xp[4];
    f32x4 xv[4][4];
#pragma unroll
    for (int p = 0; p < 4; ++p) {
      xp[p] = xa + 16u * ((wp * 8u + 2u * p + (fq >> 1)) ^ fr);
#pragma unroll
      for (int q = 0; q < 4; ++q) {
        const u32x2 w = *(const LAS u32x2*)(size_t)(xp[p] + q * 4096u);
        xv[p][q] = (f32x4){bflo(w[0]), bfhi(w[0]), bflo(w[1]), bfhi(w[1])};
        asm volatile("" : "+v"(xv[p][q]));
      }
    }
    BAR();
    STAMP(13);
    u32x4 gp[8];
#pragma unroll
    for (int j = 0; j < 8; ++j) gp[j] = *(const LAS u32x4*)(size_t)(L + GO + t4 + j * 4096u);
#pragma unroll
    for (int p = 0; p < 4; ++p)
#pragma unroll
      for (int q = 0; q < 4; ++q) {
        const unsigned g0 = gp[2 * p + (q >> 1)][2 * (q & 1)], g1 = gp[2 * p + (q >> 1)][2 * (q & 1) + 1];
        const f32x4 r = __builtin_elementwise_fma(acc[p][q], (f32x4){bflo(g0), bfhi(g0), bflo(g1), bfhi(g1)}, xv[p][q]);
        *(LAS u32x2*)(size_t)(xp[p] + q * 4096u) = (u32x2){pk2(r[0], r[1]), pk2(r[2], r[3])};
      }
  } else {
    const unsigned N = isY ? 56u : 64u;
    const rsrc_t sT = isY ? mkrsrcB(w2, FF * SEQ * 2) : mkrsrcB(wo, NH * DH * DM * 2);
    const rsrc_t sR = isY ? sU : sO;
    const unsigned ldT = isY ? 4096u : 2048u, voT = voffT(ln, ldT), voL = ln * 16u;
    const unsigned tcol = (isY ? st : dt) * 256u, rbase = (isY ? dt : st) * N * 16384u + lw * 4096u;
    const unsigned toff = (isY ? 16384u : 0u) + lw * 4096u, roff = (isY ? 0u : 16384u) + lw * 4096u, g0 = isY ? (xcd >> 1) * 14u : 0u;
    const u64* dep = (isY ? fU : fO + (st >> 1) * 32u) + (ln & 31u);
    auto peek = [&]() { asm volatile("global_load_dwordx2 v[254:255], %0, off sc1" :: "v"(dep) : "memory", "v254", "v255"); };
    auto await = [&](u64 mask, bool peeked) {
      if (peeked) { u64 v; asm volatile("v_mov_b64 %0, v[254:255]" : "=v"(v) :: "memory"); if ((__ballot(v != tag) & mask) == 0ull) return; }
      flag_wait(dep, tag, mask);
    };
    unsigned sb = L + 4u * 32768u;
    auto nexts = [&]() { sb = sb == L + 4u * 32768u ? L : sb + 32768u; };
    unsigned kT = g0;
    unsigned kW = isY ? (xcd >> 1) * 2u : 0u;
    const unsigned kj = isY ? 7u : 1u;
    auto dT = [&]() { T4 p; p.set(kW * 64u * ldT + tcol, lw, ldT); p.go(sb + toff, voT, sT); ++kT; kW += (kW & 1u) ? kj : 1u; nexts(); };
    if (hasU) {
      const unsigned voW1 = voffT(ln, FF * 2u), hw = (wid & 1u) * 2u;
      unsigned j = 4;
      auto ufill = [&](auto wait) {
        T4 p, q; p.set(j * 64u * (FF * 2u) + ufc * 256u, hw, FF * 2u); q.set(j * 64u * (FF * 2u) + ufc * 256u, hw + 1u, FF * 2u);
        unsigned d0 = sb + hw * 4096u, r0 = (udt * 32u + j) * 16384u + hw * 4096u; p.pin(); q.pin(); PINS(d0); PINS(r0);
        wait();
        p.go(d0, voW1, sW1); dma64d(d0 + 16384u, voL, sH, r0); q.go(d0 + 4096u, voW1, sW1); dma64d(d0 + 20480u, voL, sH, r0 + 4096u);
        ++j; nexts();
      };
      if (wid >= 6u) {
        ufill([] {});
#pragma nounroll
        for (unsigned t = 0; t < 12; ++t) ufill([] { waitv<48>(); BAR(); });
        waitv<48>(); BAR(); ost2(0); waitv<34>(); BAR(); ost2(1); waitv<20>(); BAR(); ost2(2); waitv<6>(); BAR(); ost2(3);
        touch8k(code_rsrc(), ln < 10u ? ln * 128u : 0x80000000u, (idx * 2u + (wid & 1u)) * 1280u);
        for (unsigned t = 16; t < 22; ++t) BAR();
        waitv<0>(); BAR();
        if (tid == 384u) flag_set(fO + qt * 32u + head, tag);
        BAR(); BAR(); BAR(); BAR();
      } else {
        BAR(); STAMP(25); BAR(); waitv<0>(); BAR();
        ost2(0); BAR(); ost2(1); BAR(); ost2(2); BAR(); ost2(3);
        for (unsigned t = 6; t < 12; ++t) BAR();
        STAMP(26);
        j = 17; sb = L + 2u * 32768u;
#pragma nounroll
        for (unsigned t = 12; t < 27; ++t) ufill([] { waitv<48>(); BAR(); });
        STAMP(28);
      }
      sb = L + 2u * 32768u;
      waitv<48>(); BAR(); dT(); waitv<36>(); BAR(); dT(); peek(); waitv<24>(); BAR(); dT(); waitv<12>(); BAR(); dT(); BAR(); dT();
      if (isY) {
        BAR();
        if (tid == 256u) flag_set(fU + uft, tag);
      } else waitv<12>();
      STAMP(8);
    } else {
      sb = L + 2u * 32768u;
#pragma nounroll
      for (unsigned j = 0; j < 5; ++j) dT();
    }
    if (hasU && !isY) await(0xffffffffull, true);
    else { if (wid == 4u) await(isY ? 0x7full << (g0 >> 1) : 0xffffffffull, false); BAR(); }
#pragma nounroll
    for (unsigned j = 0; j < 5; ++j) { dma64d(sb + roff, voL, sR, rbase + (g0 + j) * 16384u); nexts(); }
    auto dstep = [&](auto wait) {
      T4 p; p.set(kW * 64u * ldT + tcol, lw, ldT);
      unsigned d0 = sb + toff, d1 = sb + roff, r0 = rbase + kT * 16384u; p.pin(); PINS(d0); PINS(d1); PINS(r0);
      wait(); BAR();
      p.go(d0, voT, sT); dma64d(d1, voL, sR, r0);
      ++kT; kW += (kW & 1u) ? kj : 1u; nexts();
    };
    waitv<16>(); BAR(); STAMP(11);
    dstep([] { waitv<12>(); }); dstep([] { waitv<16>(); }); dstep([] { waitv<20>(); });
    if (!isY && hasU && tid == 256u) flag_set(fU + uft, tag);
#pragma nounroll
    for (unsigned seg = isY ? 0u : 5u; seg < 6u; ++seg) {
      const unsigned n = !isY ? 56u : seg == 0u ? 2u : seg == 2u ? 10u : seg >= 4u ? 14u : 4u;
#pragma nounroll
      for (unsigned i = 0; i < n; ++i) dstep([] { waitv<24>(); });
      if (seg == 0u || seg == 2u) peek();
      else if (seg < 4u) { waitv<32>(); await(seg == 1u ? 0x3fffull << 14 : 0x3fffull, true); kT = seg == 1u ? 70u - g0 : 0u; kW = seg == 1u ? 10u - (xcd >> 1) * 2u : 0u; }
      else kW = 2u;
    }
    waitv<24>(); BAR();
    if (isY) { waitv<0>(); BAR(); STAMP(12); ST_OUT(1); return; }
    {
      const rsrc_t sX = mkrsrc(x, SEQ * DM * 2);
#pragma unroll
      for (int q = 0; q < 8; ++q)
        dma16(L + XO + (lw * 8u + q) * 1024u, (ln >> 4) * 2048u + 16u * ((ln & 15u) ^ ((4u * q + (ln >> 4)) & 15u)), sX, (st * 128u + lw * 32u + q * 4u) * 2048u + dt * 256u);
    }
    waitv<8>(); BAR();
    waitv<0>(); BAR();
    STAMP(12);
    {
      flag_wait<6>(fG, tag, 1ull);
      u32x4 g[8];
#pragma unroll
      for (int q = 0; q < 8; ++q) g[q] = __builtin_amdgcn_raw_buffer_load_b128(sG, voL + (lw * 8u + q) * 1024u, 0, 16);
#pragma unroll
      for (int q = 0; q < 8; ++q) *(LAS u32x4*)(size_t)(L + GO + voL + (lw * 8u + q) * 1024u) = g[q];
    }
    waitv<0>(); WAITL0(); STAMP(13); BAR();
  }
  finish();
}

extern "C" __global__ void rsk_end() {}
}

extern "C" void kernel_launch(void* const* d_in, const int* in_sizes, int n_in, void* d_out, int out_size, void* d_ws, size_t ws_size, hipStream_t stream) {
  const u16* x = (const u16*)d_in[0]; const u16* kc = (const u16*)d_in[1]; const u16* vc = (const u16*)d_in[2];
  const u16* wq = (const u16*)d_in[3]; const u16* wo = (const u16*)d_in[4];
  const u16* w1 = (const u16*)d_in[6]; const u16* b1 = (const u16*)d_in[7]; const u16* w2 = (const u16*)d_in[8]; const u16* b2 = (const u16*)d_in[9];
  char* ws = (char*)d_ws;
  (void)hipFuncSetAttribute((const void*)rsk_main, hipFuncAttributeMaxDynamicSharedMemorySize, LDS_BYTES);
  rsk_main<<<256, 512, LDS_BYTES, stream>>>(x, ws, wq, kc, vc, w1, wo, w2, b1, b2, (u16*)d_out);
}
```

```cpp
#define PROBE 0
#define PB 0
#include <hip/hip_runtime.h>
#include <hip/hip_bf16.h>

namespace {
typedef short bf16x8 __attribute__((ext_vector_type(8)));
typedef short s16x4 __attribute__((ext_vector_type(4)));
typedef float f32x2 __attribute__((ext_vector_type(2)));
typedef float f32x4 __attribute__((ext_vector_type(4)));
typedef float f32x16 __attribute__((ext_vector_type(16)));
typedef unsigned u32x4 __attribute__((ext_vector_type(4)));
typedef unsigned u32x2 __attribute__((ext_vector_type(2)));
typedef __bf16 bf2_t __attribute__((ext_vector_type(2)));
typedef unsigned short u16;
typedef __amdgpu_buffer_rsrc_t rsrc_t;
#define LAS __attribute__((address_space(3)))
#define DI __device__ __forceinline__
#define BAR() asm volatile("s_barrier" ::: "memory")
#define WAITL0() do { asm volatile("" ::: "memory"); __builtin_amdgcn_s_waitcnt(0xC07F); asm volatile("" ::: "memory"); } while (0)
#define ALIGN8() asm volatile(".p2align 3")
#define SCHED() __builtin_amdgcn_sched_barrier(0)
#define SGB(mask, n) __builtin_amdgcn_sched_group_barrier(mask, n, 0)

constexpr int SEQ = 2048, DM = 1024, NH = 32, DH = 128, SK = 2048, WIN = 512, FF = 3584;
constexpr unsigned WS_XN = 0u, WS_H = 4u << 20, WS_O = 8u << 20, WS_U = 24u << 20, WS_G = 32u << 20, WS_FLAG = 36u << 20;
constexpr unsigned LDS_BYTES = 163840;
constexpr unsigned long long SPIN_LIMIT = 2000000ull;
constexpr int NW1 = 1;

#define ST_DECL unsigned long long pr0_ = __builtin_amdgcn_s_memrealtime(), pr1_ = pr0_
#define STAMP(i) do { if ((i) == PROBE) { SCHED(); asm volatile("s_memrealtime %0\n\ts_waitcnt lgkmcnt(0)" : "=s"(pr1_) :: "memory"); SCHED(); } } while (0)
#define ST_OUT(k) do { if (blockIdx.x == PB && rfl(threadIdx.x) == 0u) { while (__builtin_amdgcn_s_memrealtime() - pr0_ < 15000ull + (pr1_ - pr0_)) __builtin_amdgcn_s_sleep(1); } } while (0)
template <int N> DI void waitv() { asm volatile("s_waitcnt vmcnt(%0)" :: "i"(N) : "memory"); }
DI unsigned rfl(unsigned x) { return __builtin_amdgcn_readfirstlane(x); }
DI rsrc_t mkrsrc(const void* p, unsigned bytes) { return __builtin_amdgcn_make_buffer_rsrc((void*)p, (short)0, (int)bytes, 0x00020000); }
DI void dma16(unsigned m0v, unsigned voff, rsrc_t r, unsigned soff) {
  asm volatile("s_mov_b32 m0, %0\n\ts_nop 0\n\tbuffer_load_dwordx4 %1, %2, %3 offen lds" :: "s"(m0v), "v"(voff), "s"(r), "s"(soff) : "memory");
}
DI rsrc_t code_rsrc() {
  unsigned long long b, e;
  asm volatile("s_getpc_b64 s[40:41]\n\ts_add_u32 s40, s40, rsk_main@rel32@lo+4\n\ts_addc_u32 s41, s41, rsk_main@rel32@hi+12\n\ts_mov_b64 %0, s[40:41]\n\t"
               "s_getpc_b64 s[40:41]\n\ts_add_u32 s40, s40, rsk_end@rel32@lo+4\n\ts_addc_u32 s41, s41, rsk_end@rel32@hi+12\n\ts_mov_b64 %1, s[40:41]"
               : "=&s"(b), "=&s"(e) :: "s40", "s41", "scc");
  return mkrsrc((const void*)b, (e > b && e - b < 0x40000ull) ? (unsigned)(e - b) : 0u);
}
template <bool DEV>
DI void dma64(unsigned m0v, unsigned va, unsigned vb, rsrc_t r, unsigned s0, unsigned s1, unsigned s2, unsigned s3) {
  if constexpr (DEV)
    asm volatile("s_mov_b32 m0, %0\n\ts_nop 0\n\tbuffer_load_dwordx4 %1, %3, %4 offen sc1 lds\n\tbuffer_load_dwordx4 %2, %3, %5 offen offset:1024 sc1 lds\n\t"
                 "buffer_load_dwordx4 %1, %3, %6 offen offset:2048 sc1 lds\n\tbuffer_load_dwordx4 %2, %3, %7 offen offset:3072 sc1 lds"
                 :: "s"(m0v), "v"(va), "v"(vb), "s"(r), "s"(s0), "s"(s1), "s"(s2), "s"(s3) : "memory");
  else
    asm volatile("s_mov_b32 m0, %0\n\ts_nop 0\n\tbuffer_load_dwordx4 %1, %3, %4 offen lds\n\tbuffer_load_dwordx4 %2, %3, %5 offen offset:1024 lds\n\t"
                 "buffer_load_dwordx4 %1, %3, %6 offen offset:2048 lds\n\tbuffer_load_dwordx4 %2, %3, %7 offen offset:3072 lds"
                 :: "s"(m0v), "v"(va), "v"(vb), "s"(r), "s"(s0), "s"(s1), "s"(s2), "s"(s3) : "memory");
}
DI void dma64d(unsigned m0v, unsigned va, rsrc_t r, unsigned s0) {
  asm volatile("s_mov_b32 m0, %0\n\ts_nop 0\n\tbuffer_load_dwordx4 %1, %2, %3 offen sc1 lds\n\tbuffer_load_dwordx4 %1, %2, %3 offen offset:1024 sc1 lds\n\t"
               "buffer_load_dwordx4 %1, %2, %3 offen offset:2048 sc1 lds\n\tbuffer_load_dwordx4 %1, %2, %3 offen offset:3072 sc1 lds"
               :: "s"(m0v), "v"(va), "s"(r), "s"(s0) : "memory");
}
DI rsrc_t mkrsrcB(const void* p, unsigned bytes) { return mkrsrc((const char*)p - 4096, bytes + 4096u); }
#define PINS(x) asm volatile("" : "+s"(x))
DI bf16x8 ldsR(unsigned a) { return *(const LAS bf16x8*)(size_t)a; }
DI s16x4 ldsT(unsigned a) { return __builtin_amdgcn_ds_read_tr16_b64_v4i16((LAS s16x4*)(size_t)a); }
DI bf16x8 cat(s16x4 a, s16x4 b) { return __builtin_shufflevector(a, b, 0, 1, 2, 3, 4, 5, 6, 7); }
DI unsigned pk2(float lo, float hi) { f32x2 v = {lo, hi}; return __builtin_bit_cast(unsigned, __builtin_convertvector(v, bf2_t)); }
DI float bflo(unsigned w) { return __uint_as_float(w << 16); }
DI float bfhi(unsigned w) { return __uint_as_float(w & 0xffff0000u); }
DI float rbf(float x) { return bflo(pk2(x, x) & 0xffffu); }
template <int CTRL, int RM> DI float dpp_add(float v) {
  return v + __builtin_bit_cast(float, __builtin_amdgcn_update_dpp(0, __builtin_bit_cast(int, v), CTRL, RM, 0xf, false));
}
DI void wsum2(float& u, float& v) {
#define WS2(C, M) u = dpp_add<C, M>(u); v = dpp_add<C, M>(v);
  WS2(0x111, 0xf) WS2(0x112, 0xf) WS2(0x114, 0xf) WS2(0x118, 0xf) WS2(0x142, 0xa) WS2(0x143, 0xc)
#undef WS2
  u = __builtin_bit_cast(float, __builtin_amdgcn_readlane(__builtin_bit_cast(int, u), 63));
  v = __builtin_bit_cast(float, __builtin_amdgcn_readlane(__builtin_bit_cast(int, v), 63));
}

DI void ln16(float (&v)[16]) {
  const float x0 = __builtin_bit_cast(float, __builtin_amdgcn_readfirstlane(__builtin_bit_cast(int, v[0])));
  float s = 0.f, q = 0.f;
#pragma unroll
  for (int i = 0; i < 16; ++i) { const float d = v[i] - x0; s += d; q = fmaf(d, d, q); }
  wsum2(s, q);
  const float md = s * (1.0f / DM), var = rbf(fmaxf(fmaf(-md, md, q * (1.0f / DM)), 0.f));
  const float mb = rbf(x0 + md), inv = __builtin_amdgcn_rcpf(rbf(__builtin_amdgcn_sqrtf(rbf(var + 1e-5f))));
#pragma unroll
  for (int i = 0; i < 16; i += 2) {
    const unsigned d = pk2(v[i] - mb, v[i + 1] - mb), r = pk2(bflo(d) * inv, bfhi(d) * inv);
    v[i] = bflo(r); v[i + 1] = bfhi(r);
  }
}
DI unsigned pkx(float lo, float hi) { return (__float_as_uint(lo) >> 16) | (__float_as_uint(hi) & 0xffff0000u); }
enum { RROW = 0, RCHK = 1, T1 = 2, T2 = 3 };
template <int KIND> struct Opnd {
  static constexpr int NA = KIND == RROW ? 2 : KIND == RCHK ? 1 : 4;
  static constexpr int RPB = KIND >= T1 ? 2 : 1;
  unsigned a[NA];
  DI void init(unsigned base, unsigned off, unsigned lane) {
    const unsigned fr = lane & 15u, g = lane >> 4, q = fr >> 2, p = lane & 3u;
    if constexpr (KIND == RROW) {
#pragma unroll
      for (int kk = 0; kk < 2; ++kk) a[kk] = base + (off + fr) * 128u + 16u * ((4u * kk + g) ^ (fr >> 1));
    } else if constexpr (KIND == RCHK) {
      a[0] = base + g * 2048u + (off + fr) * 16u;
    } else {
      const unsigned x = ((q >> 1) << 1) | (g & 1u);
#pragma unroll
      for (int n = 0; n < 4; ++n)
        a[n] = base + (KIND == T1 ? 4096u : 2048u) * (g >> 1) + 1024u * (off >> 6) + 128u * (4u * (g & 1u) + q) + 32u * ((unsigned)n ^ x) + 8u * p;
    }
  }
  DI bf16x8 ld(int b, int kk) const {
    if constexpr (KIND == RROW) return ldsR(a[kk] + 2048u * b);
    else if constexpr (KIND == RCHK) return ldsR(a[0] + 8192u * kk + 256u * b);
    else { const unsigned ad = a[b & 3] + 1024u * (b >> 2) + 8192u * kk; return cat(ldsT(ad), ldsT(ad + (KIND == T1 ? 2048u : 4096u))); }
  }
  DI void adv(unsigned delta) {
#pragma unroll
    for (int i = 0; i < NA; ++i) { a[i] += delta; asm volatile("" : "+v"(a[i])); }
  }
};

template <int PK, int QK, int NS, unsigned STG>
DI void gemm_wide(f32x4 (&acc)[8][4], Opnd<PK>& P, Opnd<QK>& Q, const int N) {
  bf16x8 pf[2][4][2], qf[4][2];
  auto loadP = [&](int h) {
#pragma unroll
    for (int p = 0; p < 4; ++p) { pf[h][p][0] = P.ld(4 * h + p, 0); pf[h][p][1] = P.ld(4 * h + p, 1); }
  };
  auto mma = [&](int h, int q) {
#pragma unroll
    for (int p0 = 0; p0 < 4; ++p0) {
      const int p = (q & 1) ? 3 - p0 : p0;
      acc[4 * h + p][q] = __builtin_amdgcn_mfma_f32_16x16x32_bf16(pf[h][p][0], qf[q][0], acc[4 * h + p][q], 0, 0, 0);
      acc[4 * h + p][q] = __builtin_amdgcn_mfma_f32_16x16x32_bf16(pf[h][p][1], qf[q][1], acc[4 * h + p][q], 0, 0, 0);
    }
  };
  BAR();
  loadP(0);
#pragma unroll
  for (int q = 0; q < 3; ++q) { qf[q][0] = Q.ld(q, 0); qf[q][1] = Q.ld(q, 1); }
  int stg = 0;
#pragma nounroll
  for (int t = 0; t < N; ++t) {
    SCHED();
    qf[3][0] = Q.ld(3, 0); qf[3][1] = Q.ld(3, 1);
    loadP(1); mma(0, 0); mma(0, 1); mma(0, 2); mma(0, 3);
    stg = (stg == NS - 1) ? 0 : stg + 1;
    const unsigned delta = stg == 0 ? (unsigned)(-(int)((NS - 1) * STG)) : STG;
    P.adv(delta); Q.adv(delta);
#pragma unroll
    for (int i = 0; i < 18; ++i) { SGB(0x8, 1); SGB(0x100, 1); }
#pragma unroll
    for (int i = 0; i < 14; ++i) { SGB(0x8, 1); SGB(0x2, 1); }
    SCHED();
    WAITL0(); BAR();
    SCHED();
#pragma unroll
    for (int q = 0; q < 4; ++q) {
      if (q > 0) { qf[q - 1][0] = Q.ld(q - 1, 0); qf[q - 1][1] = Q.ld(q - 1, 1); }
      if (q == 0) { pf[0][0][0] = P.ld(0, 0); pf[0][0][1] = P.ld(0, 1); pf[0][1][0] = P.ld(1, 0); pf[0][1][1] = P.ld(1, 1); }
      else if (q < 3) { pf[0][q + 1][0] = P.ld(q + 1, 0); pf[0][q + 1][1] = P.ld(q + 1, 1); }
      mma(1, q);
#pragma unroll
      for (int i = 0; i < 8; ++i) { SGB(0x8, 1); SGB(0x100, 1); }
      SCHED();
    }
  }
  SCHED();
}

template <int PK, int QK, int NS, unsigned STG, int TAIL, int S0, int DRAIN, class F>
DI void gemm_full(f32x4 (&acc)[4][4], Opnd<PK>& P, Opnd<QK>& Q, const int N, F hook) {
  bf16x8 pf[2][2][4], qf[2][2][4];
  constexpr int NR = 8 * (Opnd<PK>::RPB + Opnd<QK>::RPB);
  int stg = S0;
  auto load = [&](int b) {
#pragma unroll
    for (int i = 0; i < 4; ++i)
#pragma unroll
      for (int kk = 0; kk < 2; ++kk) { pf[b][kk][i] = P.ld(i, kk); qf[b][kk][i] = Q.ld(i, kk); }
    stg = (stg == NS - 1) ? 0 : stg + 1;
    const unsigned delta = stg == 0 ? (unsigned)(-(int)((NS - 1) * STG)) : STG;
    P.adv(delta); Q.adv(delta);
  };
  auto step = [&](int b, bool sync) {
    if (sync) { WAITL0(); BAR(); }
    SCHED();
    load(b ^ 1);
#pragma unroll
    for (int p = 0; p < 4; ++p)
#pragma unroll
      for (int q0 = 0; q0 < 4; ++q0) {
        const int q = (p & 1) ? 3 - q0 : q0;
        acc[p][q] = __builtin_amdgcn_mfma_f32_16x16x32_bf16(pf[b][0][p], qf[b][0][q], acc[p][q], 0, 0, 0);
        acc[p][q] = __builtin_amdgcn_mfma_f32_16x16x32_bf16(pf[b][1][p], qf[b][1][q], acc[p][q], 0, 0, 0);
      }
#pragma unroll
    for (int i = 0; i < NR; ++i) { SGB(0x8, 1); SGB(0x100, 1); }
#pragma unroll
    for (int i = NR; i < 32; ++i) { SGB(0x8, 1); SGB(0x2, 1); }
    SCHED();
  };
  BAR();
  load(0);
  if (DRAIN > 0) {
#pragma nounroll
    for (int t = 0; t < DRAIN; t += 2) { step(0, true); step(1, true); }
    waitv<0>();
    hook();
  }
#pragma nounroll
  for (int t = DRAIN; t < N - (TAIL ? 4 : 0); t += 2) { step(0, true); step(1, true); }
  if (TAIL) {
#pragma nounroll
    for (int t = 0; t < 4; t += 2) { step(0, true); step(1, false); }
  }
}

DI unsigned voffRow(unsigned lane, unsigned ldb, unsigned par) { return (lane >> 3) * ldb + 16u * ((lane & 7u) ^ (4u * par + (lane >> 4))); }
DI unsigned voffT(unsigned lane, unsigned ldb) { return (lane >> 3) * ldb + 16u * ((lane & 7u) ^ (2u * ((((lane >> 4) & 1u) << 1) | (lane >> 5)))); }
struct T4 { unsigned s0, s1, s2, s3;
  DI void set(unsigned so, unsigned lw, unsigned ldb) { s0 = so + 4096u + lw * 16u * ldb; s1 = s0 + 128u - 1024u; s2 = s0 + 8u * ldb - 2048u; s3 = s2 + 128u - 1024u; }
  DI void pin() { PINS(s0); PINS(s1); PINS(s2); PINS(s3); }
  template <bool DEV = false> DI void go(unsigned dst, unsigned vo, rsrc_t r) const { dma64<DEV>(dst, vo, vo, r, s0, s1, s2, s3); } };

typedef unsigned long long u64;
constexpr unsigned FL_H = 64, FL_O = 320, FL_U = 576, FL_G = 832, FL_X = 3072;
DI void flag_set(u64* p, u64 tag) { __hip_atomic_store(p, tag, __ATOMIC_RELAXED, __HIP_MEMORY_SCOPE_AGENT); }
DI u64 flag_get(const u64* p) { return __hip_atomic_load(p, __ATOMIC_RELAXED, __HIP_MEMORY_SCOPE_AGENT); }
template <int SLOW = 16> DI void flag_wait(const u64* p, u64 tag, u64 mask) {
  const u64 t0 = __builtin_amdgcn_s_memrealtime();
  while ((__ballot(flag_get(p) != tag) & mask) != 0ull) {
    const u64 dt = __builtin_amdgcn_s_memrealtime() - t0;
    if (dt > SPIN_LIMIT) break;
    if (dt > 400ull) __builtin_amdgcn_s_sleep(SLOW); else __builtin_amdgcn_s_sleep(3);
  }
  asm volatile("" ::: "memory");
}

DI f32x2 sigm2(f32x2 v, f32x2 z) {
  f32x2 e = {__builtin_amdgcn_exp2f(z[0]), __builtin_amdgcn_exp2f(z[1])};
  e += 1.0f;
  return v * (f32x2){__builtin_amdgcn_rcpf(e[0]), __builtin_amdgcn_rcpf(e[1])};
}
DI unsigned gelu2(f32x2 v) {
  const f32x2 r = sigm2(v, v * __builtin_elementwise_fma(v * v, (f32x2)(-0.1029432397f), (f32x2)(-2.3022081986f)));
  return pk2(r[0], r[1]);
}
DI unsigned silu2(f32x2 v) { const f32x2 r = sigm2(v, v * -1.4426950408889634f); return pk2(r[0], r[1]); }
DI f32x2 lo2(f32x4 v) { return __builtin_shufflevector(v, v, 0, 1); }
DI f32x2 hi2(f32x4 v) { return __builtin_shufflevector(v, v, 2, 3); }
DI f32x2 bf2(unsigned w) { return (f32x2){bflo(w), bfhi(w)}; }

constexpr unsigned QSTG = 49152u;
constexpr unsigned KBASE = 65536u, VBASE = 114688u;
constexpr unsigned KSLOT[3] = {0u, 32768u, 16384u}, VSLOT[3] = {32768u, 16384u, 0u};
DI void touch8k(rsrc_t r, unsigned vo128, unsigned soff) {
  asm volatile("buffer_load_dword v255, %0, %1, %2 offen" :: "v"(vo128), "s"(r), "s"(soff) : "memory", "v255");
}
constexpr float QSCALE = 0.12751743082f;
#define BIGSUM 4096.0f

struct Att {
  bf16x8 qf[8], kv[16], pf[4], pn[2];
  f32x16 o[4], s[2], negm;
  float l, ts;
  unsigned aK[4], aV[2];
};
template <unsigned SLOT, int J> DI void att_k(Att& a) {
  asm volatile("ds_read_b128 %0, %1 offset:%2" : "+v"(a.kv[J]) : "v"(a.aK[(J >> 1) & 3]), "i"(SLOT + 8192u * (J >> 3) + 4096u * (J & 1)) : "memory");
}
template <unsigned SLOT, int J> DI void att_v(Att& a) {
  s16x4 lo = __builtin_shufflevector(a.kv[J], a.kv[J], 0, 1, 2, 3), hi = __builtin_shufflevector(a.kv[J], a.kv[J], 4, 5, 6, 7);
  constexpr unsigned off = SLOT + 4096u * (J >> 2) + 1024u * ((J >> 1) & 1);
  asm volatile("ds_read_b64_tr_b16 %0, %2 offset:%3\n\tds_read_b64_tr_b16 %1, %2 offset:%4" : "+v"(lo), "+v"(hi) : "v"(a.aV[J & 1]), "i"(off), "i"(off + 2048u) : "memory");
  a.kv[J] = cat(lo, hi);
}
DI void att_wait(Att& a) {
  asm volatile("s_waitcnt lgkmcnt(0)" : "+v"(a.kv[0]), "+v"(a.kv[1]), "+v"(a.kv[2]), "+v"(a.kv[3]), "+v"(a.kv[4]), "+v"(a.kv[5]), "+v"(a.kv[6]), "+v"(a.kv[7]),
               "+v"(a.kv[8]), "+v"(a.kv[9]), "+v"(a.kv[10]), "+v"(a.kv[11]), "+v"(a.kv[12]), "+v"(a.kv[13]), "+v"(a.kv[14]), "+v"(a.kv[15]) :: "memory");
}
DI void att_loadK(Att& a, unsigned slot) {
#pragma unroll
  for (int j = 0; j < 16; ++j) a.kv[j] = ldsR(a.aK[(j >> 1) & 3] + slot + 8192u * (j >> 3) + 4096u * (j & 1));
}
DI unsigned lane_id() { unsigned l; asm volatile("v_mbcnt_lo_u32_b32 %0, -1, 0\n\tv_mbcnt_hi_u32_b32 %0, -1, %0" : "=v"(l)); return l; }
DI void add_bcast3(f32x16& t0, f32x16& t1, f32x16& t2, float v) {
  const float one = lane_id() < 32u ? 1.0f : 0.0f;
  asm volatile("s_nop 7\n\ts_nop 7\n\tv_mfma_f32_32x32x2_f32 %0, %3, %4, %0\n\tv_mfma_f32_32x32x2_f32 %1, %3, %4, %1\n\tv_mfma_f32_32x32x2_f32 %2, %3, %4, %2\n\t"
               "s_nop 7\n\ts_nop 7\n\ts_nop 7" : "+v"(t0), "+v"(t1), "+v"(t2) : "v"(one), "v"(v));
}
template <bool FIRST, unsigned SLOT, int J> DI void att_qk1(Att& a) {
  a.s[J & 1] = __builtin_amdgcn_mfma_f32_32x32x16_bf16(a.kv[J], a.qf[J >> 1], J < 2 ? (FIRST ? (f32x16){} : a.negm) : a.s[J & 1], 0, 0, 0);
  SCHED();
  if constexpr (J == 0) ALIGN8();
  if constexpr (!FIRST) att_v<SLOT, J>(a);
  SCHED();
  if constexpr (J < 15) att_qk1<FIRST, SLOT, J + 1>(a);
}
template <int NEXT, unsigned SLOT, int J> DI void att_pv1(Att& a) {
  a.o[J & 3] = __builtin_amdgcn_mfma_f32_32x32x16_bf16(a.kv[J], a.pf[J >> 2], a.o[J & 3], 0, 0, 0);
  SCHED();
  if constexpr (J == 0) ALIGN8();
  if constexpr (NEXT == 1) att_k<SLOT, J>(a); else if constexpr (NEXT == 2) att_v<SLOT, J>(a);
  SCHED();
  if constexpr (J < 15) att_pv1<NEXT, SLOT, J + 1>(a);
}
DI float max3(float x, float y, float z) { return __builtin_fmaxf(__builtin_fmaxf(x, y), z); }
DI float att_rowmax(const Att& a) {
  float m0 = max3(a.s[0][0], a.s[0][1], a.s[0][2]), m1 = max3(a.s[1][0], a.s[1][1], a.s[1][2]);
#pragma unroll
  for (int r = 3; r < 15; r += 2) { m0 = max3(m0, a.s[0][r], a.s[0][r + 1]); m1 = max3(m1, a.s[1][r], a.s[1][r + 1]); }
  const float mx = __builtin_fmaxf(max3(m0, m1, a.s[0][15]), a.s[1][15]);
  auto rr = __builtin_amdgcn_permlane32_swap(__float_as_uint(mx), __float_as_uint(mx), false, false);
  return __builtin_fmaxf(__uint_as_float(rr[0]), __uint_as_float(rr[1]));
}
DI float att_exp16(const f32x16& v, bf16x8& p0, bf16x8& p1) {
  float s0 = 0.f, s1 = 0.f;
  u32x4 w[2];
#pragma unroll
  for (int h = 0; h < 2; ++h) {
    float e[8];
#pragma unroll
    for (int r = 0; r < 8; ++r) e[r] = __builtin_amdgcn_exp2f(v[8 * h + r]);
#pragma unroll
    for (int r = 0; r < 8; r += 2) {
      if (h == 0 && r == 0) { s0 = e[0]; s1 = e[1]; } else { s0 += e[r]; asm("" : "+v"(s0)); s1 += e[r + 1]; asm("" : "+v"(s1)); }
      w[h][r >> 1] = pk2(e[r], e[r + 1]);
    }
    asm volatile("" : "+v"(w[h]));
    SCHED();
  }
  p0 = __builtin_bit_cast(bf16x8, w[0]); p1 = __builtin_bit_cast(bf16x8, w[1]);
  return s0 + s1;
}
template <bool FIRST> DI void att_sm1(Att& a) {
  if (FIRST) {
    const float mx = att_rowmax(a);
#pragma unroll
    for (int r = 0; r < 16; ++r) { a.s[0][r] -= mx; a.s[1][r] -= mx; a.negm[r] = -mx; }
  }
  a.ts = att_exp16(a.s[0], a.pn[0], a.pn[1]);
  asm volatile("" : "+v"(a.pn[0]), "+v"(a.pn[1]), "+v"(a.ts), "+v"(a.s[1]), "+v"(a.negm));
}
template <bool FIRST> DI void att_sm2(Att& a) {
  a.ts += att_exp16(a.s[1], a.pf[2], a.pf[3]);
  if (!FIRST) {
    if (__builtin_expect(!__all(a.ts <= BIGSUM), 0)) {
      const float dl = fmaxf(att_rowmax(a), 0.f), fct = __builtin_amdgcn_exp2f(-dl);
#pragma unroll
      for (int db = 0; db < 4; ++db) a.o[db] *= fct;
      a.l *= fct;
      add_bcast3(a.s[0], a.s[1], a.negm, -dl);
      a.ts = att_exp16(a.s[0], a.pn[0], a.pn[1]) + att_exp16(a.s[1], a.pf[2], a.pf[3]);
    }
  }
  a.l += a.ts;
  a.pf[0] = a.pn[0]; a.pf[1] = a.pn[1];
  asm volatile("" : "+v"(a.pf[0]), "+v"(a.pf[1]), "+v"(a.pf[2]), "+v"(a.pf[3]), "+v"(a.l));
}

extern "C" __global__ __launch_bounds__(512, 2) __attribute__((amdgpu_num_vgpr(127)))
void rsk_main(const u16* __restrict__ x, char* __restrict__ ws, const u16* __restrict__ wq, const u16* __restrict__ kc, const u16* __restrict__ vc,
              const u16* __restrict__ w1, const u16* __restrict__ wo, const u16* __restrict__ w2, const u16* __restrict__ b1, const u16* __restrict__ b2,
              u16* __restrict__ out) {
  if (rfl(threadIdx.x) == 0u) touch8k(code_rsrc(), threadIdx.x * 128u + (threadIdx.x < 48u ? 0u : (blockIdx.x >> 3) * 2048u), 0u);
  extern __shared__ __attribute__((aligned(16))) char lds[];
  const unsigned L = (unsigned)(size_t)lds;
  const char* xn = ws + WS_XN; const char* hh = ws + WS_H;
  const unsigned tid = threadIdx.x, lane = tid & 63u, wid = rfl(tid >> 6), grp = wid >> 2, lw = wid & 3u;
  const unsigned bid = blockIdx.x, xcd = bid & 7u, idx = bid >> 3;
  const unsigned qt = (xcd & 1u) * 4u + (idx & 3u), kvh = xcd >> 1, head = kvh * 8u + (idx >> 2);
  const bool hasU = idx < 28u;
  const unsigned udt = (xcd & 1u) * 4u + (idx & 3u), uft = (xcd >> 1) * 7u + (idx >> 2);
  const unsigned ufc = (idx >> 2) * 4u + (xcd >> 1);
  const rsrc_t sK = mkrsrc(kc + ((size_t)kvh * SK + (SK - WIN)) * DH, WIN * DH * 2), sV = mkrsrc(vc + ((size_t)kvh * SK + (SK - WIN)) * DH, WIN * DH * 2);
  const rsrc_t sH = mkrsrc(hh, SEQ * DM * 2), sW1 = mkrsrcB(w1, SEQ * FF * 2);
  const unsigned voK0 = voffRow(lane, 256u, 0), voV = voffT(lane, 256u);
  auto dmaK2 = [&](unsigned slot, unsigned t, unsigned p0) {
    unsigned voK1; asm volatile("v_xor_b32 %0, 64, %1" : "=v"(voK1) : "v"(voK0));
    dma16(L + KBASE + slot + 1024u * p0, voK0, sK, t * 16384u + (p0 & 7u) * 2048u + (p0 >> 3) * 128u);
    dma16(L + KBASE + slot + 1024u * p0 + 1024u, voK1, sK, t * 16384u + (p0 & 7u) * 2048u + 2048u + (p0 >> 3) * 128u); };
  auto dmaV2 = [&](unsigned slot, unsigned t, unsigned p0) {
    dma16(L + VBASE + slot + 1024u * p0, voV, sV, t * 16384u + (p0 >> 1) * 2048u);
    dma16(L + VBASE + slot + 1024u * p0 + 1024u, voV, sV, t * 16384u + (p0 >> 1) * 2048u + 128u); };
  auto tileK = [&](unsigned t) {
#pragma unroll
    for (int p = 0; p < 16; p += 2) dmaK2(KSLOT[t % 3], t, p); };
  auto tileV = [&](unsigned t) {
#pragma unroll
    for (int p = 0; p < 16; p += 2) dmaV2(VSLOT[t % 3], t, p); };
  Att a;
  { const unsigned r32 = lane & 31u, hi = lane >> 5;
#pragma unroll
    for (int j = 0; j < 4; ++j) a.aK[j] = L + KBASE + r32 * 128u + 16u * ((2u * j + hi) ^ ((r32 >> 1) & 7u)); }

  ST_DECL; STAMP(0);
  u64* flags = (u64*)(ws + WS_FLAG);
  const rsrc_t sS = lw == 0 ? mkrsrcB(wq, DM * NH * DH * 2) : mkrsrcB(xn, SEQ * DM * 2);
  unsigned asrc = 4096u + (lw == 0 ? head * 256u : (qt * 256u + (lw - 1u) * 128u) * 2048u), adst = L + lw * 16384u;
  const unsigned avo0 = lw == 0 ? voffT(lane, 8192u) : voffRow(lane, 2048u, 0), avo1 = lw == 0 ? avo0 : avo0 ^ 64u;
  constexpr unsigned HT = 147456u;
  auto astep = [&](unsigned vo0, unsigned vo1) {
    const unsigned sp = lw == 0 ? 8u * 8192u : 16u * 2048u, so = (lw == 0 ? 128u : 8u * 2048u) - 1024u;
#pragma unroll
    for (int q = 0; q < 4; ++q) { const unsigned e = asrc + 2u * q * sp; dma64<true>(adst + 4096u * q, vo0, vo1, sS, e, e + so, e + sp - 2048u, e + sp - 2048u + so); }
    asrc += lw == 0 ? 64u * 8192u : 128u; adst = adst >= L + 2u * QSTG ? adst - 2u * QSTG : adst + QSTG;
  };
  u64 tag;
  {
    const unsigned row = (bid >> 2) * 32u + ((bid >> 1) & 1u) * 16u + (bid & 1u) * 4u + (wid >> 2) * 8u + (wid & 3u);
    u32x4 xa, xb; u32x2 ep;
    asm volatile("buffer_load_dwordx4 %0, %3, %4, %5 offen\n\tbuffer_load_dwordx4 %1, %3, %4, %5 offen offset:1024\n\tglobal_load_dwordx2 %2, %6, %7 sc1"
                 : "=&v"(xa), "=&v"(xb), "=&v"(ep) : "v"(lane * 16u), "s"(mkrsrc(x, SEQ * DM * 2)), "s"(row * 2048u), "v"(0u), "s"(flags) : "memory");
    __builtin_amdgcn_s_setprio(1);
    if (wid == 7) {
      touch8k(mkrsrc(wq, DM * NH * DH * 2), lane * 128u, bid * 8192u);
      waitv<1>();
    } else waitv<0>();
    asm volatile("" : "+v"(xa), "+v"(xb), "+v"(ep));
    STAMP(16);
    tag = (((u64)rfl(ep[1]) << 32) | rfl(ep[0])) * 0x9E3779B97F4A7C15ull + 0x632BE59BD9B4E019ull;
    float v[16];
#pragma unroll
    for (int i = 0; i < 4; ++i) { v[2 * i] = bflo(xa[i]); v[2 * i + 1] = bfhi(xa[i]); v[8 + 2 * i] = bflo(xb[i]); v[9 + 2 * i] = bfhi(xb[i]); }
    ln16(v);
    const rsrc_t sW = mkrsrc(ws, WS_O);
#pragma unroll
    for (int p = 0; p < 2; ++p) {
      const unsigned lo0 = pkx(v[8 * p], v[8 * p + 1]), lo1 = pkx(v[8 * p + 2], v[8 * p + 3]), hi0 = pkx(v[8 * p + 4], v[8 * p + 5]), hi1 = pkx(v[8 * p + 6], v[8 * p + 7]);
      const bool odd = lane & 1u;
      const unsigned s0 = __builtin_amdgcn_update_dpp(0, (int)(odd ? lo0 : hi0), 0xB1, 0xf, 0xf, false), s1 = __builtin_amdgcn_update_dpp(0, (int)(odd ? lo1 : hi1), 0xB1, 0xf, 0xf, false);
      __builtin_amdgcn_raw_buffer_store_b128(odd ? (u32x4){s0, s1, hi0, hi1} : (u32x4){lo0, lo1, s0, s1}, sW, WS_XN + row * 2048u + lane * 16u + 1024u * p, 0, 16);
    }
    STAMP(17);
    waitv<0>();
    if (lane == 0u) flag_set(flags + FL_X + bid * 8u + wid, tag);
    STAMP(18);
    if (wid == 4u) { astep(avo0, avo1); astep(avo0, avo1); }
    if (grp == 0) __builtin_amdgcn_s_setprio(0);
    ln16(v);
    *(LAS u32x4*)(size_t)(L + HT + wid * 2048u + lane * 16u) = (u32x4){pkx(v[0], v[1]), pkx(v[2], v[3]), pkx(v[4], v[5]), pkx(v[6], v[7])};
    *(LAS u32x4*)(size_t)(L + HT + wid * 2048u + 1024u + lane * 16u) = (u32x4){pkx(v[8], v[9]), pkx(v[10], v[11]), pkx(v[12], v[13]), pkx(v[14], v[15])};
    WAITL0();
    __builtin_amdgcn_s_setprio(0);
    STAMP(19);
  }
  auto hstore = [&](unsigned i) {
    const unsigned d0 = i * 64u, ad = L + HT + ((lane & 15u) >> 2) * 2048u + (d0 + (lane >> 4) * 16u + (lane & 3u) * 4u) * 2u;
    __builtin_amdgcn_raw_buffer_store_b128(__builtin_bit_cast(u32x4, cat(ldsT(ad), ldsT(ad + 8192u))), mkrsrc(ws, WS_O), WS_H + ((d0 >> 7) * 256u + bid) * 2048u + ((d0 & 64u) + lane) * 16u, 0, 16);
  };
  if (!hasU) {
    BAR();
    hstore(wid * 2u); hstore(wid * 2u + 1u);
    waitv<0>(); BAR();
    if (tid == 0u) flag_set(flags + FL_H + bid, tag);
    const unsigned w = ((idx - 28u) * 8u + xcd) * 8u + wid, vo = lane * 128u;
    const rsrc_t sWq = mkrsrc(wq, DM * NH * DH * 2);
#pragma unroll
    for (int j = 1; j < 4; ++j) touch8k(sWq, vo, (j * 256u + w) * 8192u);
    touch8k(mkrsrc((w & 64u) ? vc : kc, 4u * SK * DH * 2u), w < 128u ? vo : 0x80000000u, ((w >> 4) & 3u) * (SK * DH * 2u) + (SK - WIN) * DH * 2u + (w & 15u) * 8192u);
#pragma unroll
    for (int j = 0; j < NW1; ++j) { waitv<2>(); touch8k(sW1, vo, (j * 256u + w) * 8192u + 4096u); }
    waitv<2>();
    if (wid < 7u - NW1) touch8k(sW1, vo, ((NW1 + wid) * 256u + bid) * 8192u + 4096u);
    if (wid < 7u) touch8k(mkrsrc(w2, FF * SEQ * 2), vo, (wid * 256u + bid) * 8192u);
    if (wid >= 4u) touch8k(mkrsrc(wo, NH * DH * DM * 2), vo, ((wid - 4u) * 256u + bid) * 8192u);
    waitv<0>();
    STAMP(20);
  }
  if (wid == 7) {
    const unsigned vo = lane * 128u;
    BAR(); BAR();
#pragma nounroll
    for (unsigned i = 0; i < 4; ++i) {
      BAR();
      if (hasU) { hstore(i * 4u); hstore(i * 4u + 1u); hstore(i * 4u + 2u); hstore(i * 4u + 3u); }
    }
    BAR(); BAR();
    waitv<0>();
    if (lane == 0u) flag_set(flags + FL_H + bid, tag);
    BAR(); BAR(); BAR(); BAR();
    const u64 f0 = flag_get(flags + FL_H + lane), f1 = flag_get(flags + FL_H + 64u + lane), f2 = flag_get(flags + FL_H + 128u + lane), f3 = flag_get(flags + FL_H + 192u + lane);
    touch8k(code_rsrc(), lane < 16u ? vo : 0x80000000u, idx * 2048u);
    BAR(); BAR();
    if (__any(f0 != tag || f1 != tag || f2 != tag || f3 != tag)) {
#pragma nounroll
      for (unsigned j = 0; j < 256u; j += 64u) flag_wait(flags + FL_H + j + lane, tag, ~0ull);
    }
    if (bid == 0u && lane == 0u) flag_set(flags, tag);
    BAR(); BAR(); BAR();
#pragma unroll
    for (int j = NW1; j < 6; ++j) touch8k(sW1, vo, (j * 256u + bid) * 8192u + 4096u);
    att_loadK(a, KSLOT[0]);
  } else if (grp == 1) {
    const unsigned vo0 = avo0, vo1 = avo1;
    if (lw != 0) {
      const u64* fx = flags + FL_X + qt * 256u + (lw - 1u) * 128u + lane;
      const u64 t0 = __builtin_amdgcn_s_memrealtime();
      while (__any(((flag_get(fx) ^ tag) | (flag_get(fx + 64) ^ tag)) != 0ull)) {
        if (__builtin_amdgcn_s_memrealtime() - t0 > SPIN_LIMIT) break;
        __builtin_amdgcn_s_sleep(2);
      }
      asm volatile("" ::: "memory");
      STAMP(21);
      astep(vo0, vo1); astep(vo0, vo1);
      STAMP(22);
    }
    waitv<16>(); STAMP(23);
    BAR(); STAMP(1);
    astep(vo0, vo1);
#pragma nounroll
    for (unsigned t = 0; t < 13; ++t) { waitv<16>(); BAR(); astep(vo0, vo1); }
    waitv<16>(); BAR();
    if (lw == 0) tileK(0); else if (lw == 1) tileK(2); else tileV(0);
    waitv<16>(); BAR();
    if (lw == 1) tileK(1); else if (lw == 2) tileV(1);
    if (lw == 0) waitv<0>();
    BAR();
    att_loadK(a, KSLOT[0]);
  } else {
    Opnd<T1> P; Opnd<RROW> Q;
    P.init(L, 0u, lane); Q.init(L + 16384u, lw * 64u, lane);
    f32x4 acc[8][4] = {};
    gemm_wide<T1, RROW, 3, QSTG>(acc, P, Q, 16);
    STAMP(2);
    att_loadK(a, KSLOT[0]);
    const unsigned wa = L + (lane >> 5) * 4096u + (lw * 64u + (lane & 15u)) * 16u + ((lane >> 4) & 1u) * 8u;
#pragma unroll
    for (int j = 0; j < 16; ++j) {
      const f32x4 v = acc[j >> 1][j & 1] * QSCALE;
      *(LAS u32x2*)(size_t)(wa + 8192u * (j >> 1) + 256u * (j & 1)) = (u32x2){pk2(v[0], v[1]), pk2(v[2], v[3])};
    }
#pragma unroll
    for (int ks = 0; ks < 8; ++ks) a.qf[ks] = ldsR(L + (2u * ks + (lane >> 5)) * 4096u + (lw * 64u + (lane & 31u)) * 16u);
    SCHED();
#pragma unroll
    for (int j = 0; j < 16; ++j) {
      a.s[j & 1] = __builtin_amdgcn_mfma_f32_32x32x16_bf16(a.kv[j], a.qf[j >> 1], j < 2 ? (f32x16){} : a.s[j & 1], 0, 0, 0);
      float v[4];
#pragma unroll
      for (int c = 0; c < 4; ++c) { v[c] = acc[j >> 1][2 + (j & 1)][c] * QSCALE; asm("" : "+v"(v[c])); }
      *(LAS u32x2*)(size_t)(wa + 8192u * (j >> 1) + 256u * (2 + (j & 1))) = (u32x2){pk2(v[0], v[1]), pk2(v[2], v[3])};
      SCHED();
    }
    WAITL0();
  }
  STAMP(3);

  const rsrc_t sO = mkrsrc(ws + WS_O, SEQ * NH * DH * 2);
  {
    const unsigned l2 = lane_id(), hi = l2 >> 5, blk = (l2 >> 4) & 1u, q4 = (l2 & 15u) >> 2, p4 = l2 & 3u;
#pragma unroll
    for (int n = 0; n < 2; ++n) a.aV[n] = L + VBASE + 128u * (4u * hi + q4) + 32u * ((2u * n + blk) ^ (((q4 >> 1) << 1) | hi)) + 8u * p4;
#pragma unroll
    for (int db = 0; db < 4; ++db) a.o[db] = (f32x16){};
    a.l = 0.f;
    const rsrc_t sW1u = mkrsrc((const char*)w1 - 4096, hasU ? SEQ * FF * 2 + 4096u : 0u), sHu = mkrsrc(hh, hasU ? SEQ * DM * 2 : 0u);
    unsigned voU = 0;
    auto uP = [&](unsigned j) {
      const unsigned so = 4096u + (j * 64u + wid * 8u) * (FF * 2u) + ufc * 256u;
      asm volatile("s_mov_b32 m0, %0\n\ts_nop 0\n\tbuffer_load_dwordx4 %1, %2, %3 offen lds\n\tbuffer_load_dwordx4 %1, %2, %4 offen offset:1024 lds"
                   :: "s"(L + j * 32768u + wid * 2048u), "v"(voU), "s"(sW1u), "s"(so), "s"(so + 128u - 1024u) : "memory"); };
    auto uQ = [&](unsigned j) {
      asm volatile("s_mov_b32 m0, %0\n\ts_nop 0\n\tbuffer_load_dwordx4 %1, %2, %3 offen sc1 lds\n\tbuffer_load_dwordx4 %1, %2, %3 offen offset:1024 sc1 lds"
                   :: "s"(L + j * 32768u + 16384u + wid * 2048u), "v"(lane_id() * 16u), "s"(sHu), "s"((udt * 32u + j) * 16384u + wid * 2048u) : "memory"); };
    const bool pre = hasU && grp == 1;
    if (grp == 1) {
      if (wid != 7) waitv<0>();
      WAITL0(); BAR();
#pragma unroll
      for (int ks = 0; ks < 8; ++ks) a.qf[ks] = ldsR(L + (2u * ks + hi) * 4096u + (lw * 64u + 32u + (l2 & 31u)) * 16u);
      SCHED(); att_qk1<true, 0u, 0>(a);
    }
    STAMP(4);
    const rsrc_t sWo = mkrsrc(wo, NH * DH * DM * 2), sW2 = mkrsrc(w2, FF * SEQ * 2);
    const unsigned lk = bid * 8192u + (wid >> 1) * 2048u + (wid & 1u) * 128u;
#define LOOK(v) do { if ((v) < 1) touch8k(sW1, voV, lk + 6u * 0x200000u + 4096u); else if ((v) < 5) touch8k(sWo, voV, lk + ((v) - 1) * 0x200000u); \
                     else if ((v) < 12) touch8k(sW2, voV, lk + (((v) - 2) % 7) * 0x200000u); } while (0)
#define ATT_TILE(i, FIRST, WB, WD)                                                                                            \
    SCHED(); if (!(FIRST)) att_qk1<false, VSLOT[((i) + 2) % 3], 0>(a); BAR(); SCHED();                                        \
    if ((i) <= 4) dmaK2(KSLOT[(i) % 3], (i) + 3, wid * 2u);                                                                   \
    LOOK(2 * (i));                                                                                                            \
    if ((i) == 5) { voU = voffT(lane_id(), FF * 2u); uP(0); uQ(0); } else if ((i) == 6) { uQ(1); uP(2); } else if ((i) == 7) uP(3); \
    att_sm1<FIRST>(a);                                                                                                        \
    waitv<WB>();                                                                                                              \
    if (!(FIRST)) att_wait(a); SCHED(); BAR();                                                                                \
    if (FIRST) att_loadK(a, KSLOT[1]); else { SCHED(); att_pv1<((i) < 7 ? 1 : 2), ((i) < 7 ? KSLOT[((i) + 1) % 3] : VSLOT[7 % 3]), 0>(a); } \
    BAR(); SCHED();                                                                                                           \
    if ((i) <= 5) dmaV2(VSLOT[((i) + 2) % 3], (i) + 2, wid * 2u);                                                             \
    LOOK(2 * (i) + 1);                                                                                                        \
    if ((i) == 5) uP(1); else if ((i) == 6) uQ(2); else if ((i) == 7) uQ(3);                                                  \
    att_sm2<FIRST>(a);                                                                                                        \
    waitv<WD>();                                                                                                              \
    att_wait(a); SCHED(); BAR();
    ATT_TILE(0, true, 63, 63) ATT_TILE(1, false, 63, 10) ATT_TILE(2, false, 10, 10) ATT_TILE(3, false, 10, 10)
    ATT_TILE(4, false, 10, 10) ATT_TILE(5, false, 12, 14) ATT_TILE(6, false, 15, 9) ATT_TILE(7, false, 63, 63)
#undef ATT_TILE
#undef LOOK
    SCHED(); att_pv1<0, 0u, 0>(a); STAMP(5);
    if (grp == 0) { BAR(); waitv<0>(); }
    if (pre) { waitv<2>(); BAR(); STAMP(24); }
  }
  auto ost2 = [&](int db) {
    const unsigned l3 = lane_id(), row = qt * 256u + lw * 64u + grp * 32u + (l3 & 31u);
    const unsigned oo = (((row >> 7) * 64u + head * 2u) * 8u + (l3 >> 5)) * 2048u + (row & 127u) * 16u;
#pragma unroll
    for (int i = 0; i < 2; ++i) {
      float w[8];
#pragma unroll
      for (int c = 0; c < 8; ++c) { w[c] = a.o[db][8 * i + c] * a.l; asm("" : "+v"(w[c])); }
      __builtin_amdgcn_raw_buffer_store_b128((u32x4){pk2(w[0], w[1]), pk2(w[2], w[3]), pk2(w[4], w[5]), pk2(w[6], w[7])}, sO, oo + db * 8192u + i * 4096u, 0, 16);
    }
  };
  { auto rr = __builtin_amdgcn_permlane32_swap(__float_as_uint(a.l), __float_as_uint(a.l), false, false);
    a.l = __builtin_amdgcn_rcpf(__uint_as_float(rr[0]) + __uint_as_float(rr[1])); }
  if (!(hasU && grp == 1)) { ost2(0); ost2(1); ost2(2); ost2(3); STAMP(6); }
  const unsigned ln = lane_id();
  const bool isY = xcd >= 4u;
  const unsigned dt = udt, st = ((xcd >> 1) & 1u) * 8u + (idx >> 2), tile = st * 8u + dt;
  u64* fG = flags + FL_G + tile * 16u; u64* fO = flags + FL_O; u64* fU = flags + FL_U + dt * 32u;
  const rsrc_t sG = mkrsrc(ws + WS_G + (size_t)tile * 32768u, 32768u), sU = mkrsrc(ws + WS_U, DM * FF * 2);
  constexpr unsigned XO = 32768u, GO = 65536u;
  auto finish = [&]() {
    WAITL0(); BAR();
#pragma unroll
    for (int q = 0; q < 4; ++q) {
      const unsigned row = wid * 16u + q * 4u + (ln >> 4);
      const u32x4 v = *(const LAS u32x4*)(size_t)(L + XO + row * 256u + 16u * ((ln & 15u) ^ (row & 15u)));
      __builtin_amdgcn_raw_buffer_store_b128(v, mkrsrc(out, SEQ * DM * 2), (st * 128u + row) * 2048u + dt * 256u + (ln & 15u) * 16u, 0, 16);
    }
    STAMP(14); waitv<0>(); STAMP(15); ST_OUT(1);
  };
  if (!hasU) {
    waitv<0>(); BAR();
    if (tid == 256u) flag_set(fO + qt * 32u + head, tag);
    STAMP(7);
  }
  if (grp == 0) {
    const unsigned wp = lw >> 1, wq2 = lw & 1u, fr = ln & 15u, fq = ln >> 4;
    if (hasU) {
      Opnd<T1> P; Opnd<RCHK> Q;
      P.init(L, wp * 64u, ln); Q.init(L + 16384u, wq2 * 64u, ln);
      u32x2 bv[4];
      f32x4 acc[4][4] = {};
      gemm_full<T1, RCHK, 5, 32768u, 0, 0, 16>(acc, P, Q, 32, [&] {
#pragma unroll
        for (int p = 0; p < 4; ++p) bv[p] = *(const u32x2*)(b1 + ufc * 128u + wp * 64u + p * 16u + fq * 4u);
      });
      STAMP(8);
      const unsigned uo = ((udt * 56u + uft * 2u + wp) * 8u + fq) * 2048u + (wq2 * 64u + fr) * 16u;
#pragma unroll
      for (int pp = 0; pp < 2; ++pp) {
        const f32x2 b0 = bf2(bv[2 * pp][0]), b1v = bf2(bv[2 * pp][1]), b2v = bf2(bv[2 * pp + 1][0]), b3 = bf2(bv[2 * pp + 1][1]);
#pragma unroll
        for (int m = 0; m < 4; ++m)
          __builtin_amdgcn_raw_buffer_store_b128((u32x4){gelu2(lo2(acc[2 * pp][m]) + b0), gelu2(hi2(acc[2 * pp][m]) + b1v), gelu2(lo2(acc[2 * pp + 1][m]) + b2v), gelu2(hi2(acc[2 * pp + 1][m]) + b3)},
                                                 sU, uo + pp * 8192u + m * 256u, 0, 16);
      }
      STAMP(9);
      if (isY) { waitv<0>(); BAR(); }
    }
    if (isY || !hasU) BAR();
    f32x4 acc[4][4] = {};
    const unsigned t4 = (wid * 64u + ln) * 16u;
    if (isY) {
      float bias[4];
#pragma unroll
      for (int q = 0; q < 4; ++q) bias[q] = bflo(b2[st * 128u + wq2 * 64u + q * 16u + fr]);
      Opnd<RCHK> P; Opnd<T2> Q;
      P.init(L + 65536u, wp * 64u, ln); Q.init(L + 65536u + 16384u, wq2 * 64u, ln);
      gemm_full<RCHK, T2, 5, 32768u, 3, 2, 0>(acc, P, Q, 56, [] {});
      STAMP(12);
#pragma unroll
      for (int j = 0; j < 8; ++j) {
        const int q = 2 * (j & 1); const f32x4 y0 = acc[j >> 1][q] + bias[q], y1 = acc[j >> 1][q + 1] + bias[q + 1];
        __builtin_amdgcn_raw_buffer_store_b128((u32x4){silu2(lo2(y0)), silu2(hi2(y0)), silu2(lo2(y1)), silu2(hi2(y1))}, sG, t4 + j * 4096u, 0, 16);
      }
      waitv<0>(); BAR();
      if (tid == 0u) flag_set(fG, tag);
      STAMP(15); ST_OUT(1); return;
    }
    Opnd<T1> P; Opnd<RCHK> Q;
    P.init(L + 65536u, wp * 64u, ln); Q.init(L + 65536u + 16384u, wq2 * 64u, ln);
    gemm_full<T1, RCHK, 5, 32768u, 3, 2, 2>(acc, P, Q, 64, [] {});
    STAMP(12);
    const unsigned xa = L + XO + (wq2 * 64u + fr) * 256u + (fq & 1u) * 8u;
    unsigned xp[4];
    f32x4 xv[4][4];
#pragma unroll
    for (int p = 0; p < 4; ++p) {
      xp[p] = xa + 16u * ((wp * 8u + 2u * p + (fq >> 1)) ^ fr);
#pragma unroll
      for (int q = 0; q < 4; ++q) {
        const u32x2 w = *(const LAS u32x2*)(size_t)(xp[p] + q * 4096u);
        xv[p][q] = (f32x4){bflo(w[0]), bfhi(w[0]), bflo(w[1]), bfhi(w[1])};
        asm volatile("" : "+v"(xv[p][q]));
      }
    }
    BAR();
    STAMP(13);
    u32x4 gp[8];
#pragma unroll
    for (int j = 0; j < 8; ++j) gp[j] = *(const LAS u32x4*)(size_t)(L + GO + t4 + j * 4096u);
#pragma unroll
    for (int p = 0; p < 4; ++p)
#pragma unroll
      for (int q = 0; q < 4; ++q) {
        const unsigned g0 = gp[2 * p + (q >> 1)][2 * (q & 1)], g1 = gp[2 * p + (q >> 1)][2 * (q & 1) + 1];
        const f32x4 r = __builtin_elementwise_fma(acc[p][q], (f32x4){bflo(g0), bfhi(g0), bflo(g1), bfhi(g1)}, xv[p][q]);
        *(LAS u32x2*)(size_t)(xp[p] + q * 4096u) = (u32x2){pk2(r[0], r[1]), pk2(r[2], r[3])};
      }
  } else {
    const unsigned N = isY ? 56u : 64u;
    const rsrc_t sT = isY ? mkrsrcB(w2, FF * SEQ * 2) : mkrsrcB(wo, NH * DH * DM * 2);
    const rsrc_t sR = isY ? sU : sO;
    const unsigned ldT = isY ? 4096u : 2048u, voT = voffT(ln, ldT), voL = ln * 16u;
    const unsigned tcol = (isY ? st : dt) * 256u, rbase = (isY ? dt : st) * N * 16384u + lw * 4096u;
    const unsigned toff = (isY ? 16384u : 0u) + lw * 4096u, roff = (isY ? 0u : 16384u) + lw * 4096u, g0 = isY ? (xcd >> 1) * 14u : 0u;
    const u64* dep = (isY ? fU : fO + (st >> 1) * 32u) + (ln & 31u);
    auto peek = [&]() { asm volatile("global_load_dwordx2 v[254:255], %0, off sc1" :: "v"(dep) : "memory", "v254", "v255"); };
    auto await = [&](u64 mask, bool peeked) {
      if (peeked) { u64 v; asm volatile("v_mov_b64 %0, v[254:255]" : "=v"(v) :: "memory"); if ((__ballot(v != tag) & mask) == 0ull) return; }
      flag_wait(dep, tag, mask);
    };
    unsigned sb = L + 4u * 32768u;
    auto nexts = [&]() { sb = sb == L + 4u * 32768u ? L : sb + 32768u; };
    unsigned kT = g0;
    unsigned kW = isY ? (xcd >> 1) * 2u : 0u;
    const unsigned kj = isY ? 7u : 1u;
    auto dT = [&]() { T4 p; p.set(kW * 64u * ldT + tcol, lw, ldT); p.go(sb + toff, voT, sT); ++kT; kW += (kW & 1u) ? kj : 1u; nexts(); };
    if (hasU) {
      const unsigned voW1 = voffT(ln, FF * 2u), hw = (wid & 1u) * 2u;
      unsigned j = 4;
      auto ufill = [&](auto wait) {
        T4 p, q; p.set(j * 64u * (FF * 2u) + ufc * 256u, hw, FF * 2u); q.set(j * 64u * (FF * 2u) + ufc * 256u, hw + 1u, FF * 2u);
        unsigned d0 = sb + hw * 4096u, r0 = (udt * 32u + j) * 16384u + hw * 4096u; p.pin(); q.pin(); PINS(d0); PINS(r0);
        wait();
        p.go(d0, voW1, sW1); dma64d(d0 + 16384u, voL, sH, r0); q.go(d0 + 4096u, voW1, sW1); dma64d(d0 + 20480u, voL, sH, r0 + 4096u);
        ++j; nexts();
      };
      if (wid >= 6u) {
        ufill([] {});
#pragma nounroll
        for (unsigned t = 0; t < 12; ++t) ufill([] { waitv<48>(); BAR(); });
        waitv<48>(); BAR(); ost2(0); waitv<34>(); BAR(); ost2(1); waitv<20>(); BAR(); ost2(2); waitv<6>(); BAR(); ost2(3);
        touch8k(code_rsrc(), ln < 10u ? ln * 128u : 0x80000000u, (idx * 2u + (wid & 1u)) * 1280u);
        for (unsigned t = 16; t < 22; ++t) BAR();
        waitv<0>(); BAR();
        if (tid == 384u) flag_set(fO + qt * 32u + head, tag);
        BAR(); BAR(); BAR(); BAR();
      } else {
        BAR(); STAMP(25); BAR(); waitv<0>(); BAR();
        ost2(0); BAR(); ost2(1); BAR(); ost2(2); BAR(); ost2(3);
        for (unsigned t = 6; t < 12; ++t) BAR();
        STAMP(26);
        j = 17; sb = L + 2u * 32768u;
#pragma nounroll
        for (unsigned t = 12; t < 27; ++t) ufill([] { waitv<48>(); BAR(); });
        STAMP(28);
      }
      sb = L + 2u * 32768u;
      waitv<48>(); BAR(); dT(); waitv<36>(); BAR(); dT(); peek(); waitv<24>(); BAR(); dT(); waitv<12>(); BAR(); dT(); BAR(); dT();
      if (isY) {
        BAR();
        if (tid == 256u) flag_set(fU + uft, tag);
      } else waitv<12>();
      STAMP(8);
    } else {
      sb = L + 2u * 32768u;
#pragma nounroll
      for (unsigned j = 0; j < 5; ++j) dT();
    }
    if (hasU && !isY) await(0xffffffffull, true);
    else { if (wid == 4u) await(isY ? 0x7full << (g0 >> 1) : 0xffffffffull, false); BAR(); }
#pragma nounroll
    for (unsigned j = 0; j < 5; ++j) { dma64d(sb + roff, voL, sR, rbase + (g0 + j) * 16384u); nexts(); }
    auto dstep = [&](auto wait) {
      T4 p; p.set(kW * 64u * ldT + tcol, lw, ldT);
      unsigned d0 = sb + toff, d1 = sb + roff, r0 = rbase + kT * 16384u; p.pin(); PINS(d0); PINS(d1); PINS(r0);
      wait(); BAR();
      p.go(d0, voT, sT); dma64d(d1, voL, sR, r0);
      ++kT; kW += (kW & 1u) ? kj : 1u; nexts();
    };
    waitv<16>(); BAR(); STAMP(11);
    dstep([] { waitv<12>(); }); dstep([] { waitv<16>(); }); dstep([] { waitv<20>(); });
    if (!isY && hasU && tid == 256u) flag_set(fU + uft, tag);
#pragma nounroll
    for (unsigned seg = isY ? 0u : 5u; seg < 6u; ++seg) {
      const unsigned n = !isY ? 56u : seg == 0u ? 2u : seg == 2u ? 10u : seg >= 4u ? 14u : 4u;
#pragma nounroll
      for (unsigned i = 0; i < n; ++i) dstep([] { waitv<24>(); });
      if (seg == 0u || seg == 2u) peek();
      else if (seg < 4u) { waitv<32>(); await(seg == 1u ? 0x3fffull << 14 : 0x3fffull, true); kT = seg == 1u ? 70u - g0 : 0u; kW = seg == 1u ? 10u - (xcd >> 1) * 2u : 0u; }
      else kW = 2u;
    }
    waitv<24>(); BAR();
    if (isY) { waitv<0>(); BAR(); STAMP(12); ST_OUT(1); return; }
    {
      const rsrc_t sX = mkrsrc(x, SEQ * DM * 2);
#pragma unroll
      for (int q = 0; q < 8; ++q)
        dma16(L + XO + (lw * 8u + q) * 1024u, (ln >> 4) * 2048u + 16u * ((ln & 15u) ^ ((4u * q + (ln >> 4)) & 15u)), sX, (st * 128u + lw * 32u + q * 4u) * 2048u + dt * 256u);
    }
    waitv<8>(); BAR();
    waitv<0>(); BAR();
    STAMP(12);
    {
      flag_wait<6>(fG, tag, 1ull);
      u32x4 g[8];
#pragma unroll
      for (int q = 0; q < 8; ++q) g[q] = __builtin_amdgcn_raw_buffer_load_b128(sG, voL + (lw * 8u + q) * 1024u, 0, 16);
#pragma unroll
      for (int q = 0; q < 8; ++q) *(LAS u32x4*)(size_t)(L + GO + voL + (lw * 8u + q) * 1024u) = g[q];
    }
    waitv<0>(); WAITL0(); STAMP(13); BAR();
  }
  finish();
}

extern "C" __global__ void rsk_end() {}
}

extern "C" void kernel_launch(void* const* d_in, const int* in_sizes, int n_in, void* d_out, int out_size, void* d_ws, size_t ws_size, hipStream_t stream) {
  const u16* x = (const u16*)d_in[0]; const u16* kc = (const u16*)d_in[1]; const u16* vc = (const u16*)d_in[2];
  const u16* wq = (const u16*)d_in[3]; const u16* wo = (const u16*)d_in[4];
  const u16* w1 = (const u16*)d_in[6]; const u16* b1 = (const u16*)d_in[7]; const u16* w2 = (const u16*)d_in[8]; const u16* b2 = (const u16*)d_in[9];
  char* ws = (char*)d_ws;
  (void)hipFuncSetAttribute((const void*)rsk_main, hipFuncAttributeMaxDynamicSharedMemorySize, LDS_BYTES);
  rsk_main<<<256, 512, LDS_BYTES, stream>>>(x, ws, wq, kc, vc, w1, wo, w2, b1, b2, (u16*)d_out);
}
```

```cpp
#include <hip/hip_runtime.h>
#include <hip/hip_bf16.h>

namespace {
typedef short bf16x8 __attribute__((ext_vector_type(8)));
typedef short s16x4 __attribute__((ext_vector_type(4)));
typedef float f32x2 __attribute__((ext_vector_type(2)));
typedef float f32x4 __attribute__((ext_vector_type(4)));
typedef float f32x16 __attribute__((ext_vector_type(16)));
typedef unsigned u32x4 __attribute__((ext_vector_type(4)));
typedef unsigned u32x2 __attribute__((ext_vector_type(2)));
typedef __bf16 bf2_t __attribute__((ext_vector_type(2)));
typedef unsigned short u16;
typedef __amdgpu_buffer_rsrc_t rsrc_t;
#define LAS __attribute__((address_space(3)))
#define DI __device__ __forceinline__
#define BAR() asm volatile("s_barrier" ::: "memory")
#define WAITL0() do { asm volatile("" ::: "memory"); __builtin_amdgcn_s_waitcnt(0xC07F); asm volatile("" ::: "memory"); } while (0)
#define ALIGN8() asm volatile(".p2align 3")
#define SCHED() __builtin_amdgcn_sched_barrier(0)
#define SGB(mask, n) __builtin_amdgcn_sched_group_barrier(mask, n, 0)

constexpr int SEQ = 2048, DM = 1024, NH = 32, DH = 128, SK = 2048, WIN = 512, FF = 3584;
constexpr unsigned WS_XN = 0u, WS_H = 4u << 20, WS_O = 8u << 20, WS_U = 24u << 20, WS_G = 32u << 20, WS_FLAG = 36u << 20;
constexpr unsigned LDS_BYTES = 163840;
constexpr unsigned long long SPIN_LIMIT = 2000000ull;

template <int N> DI void waitv() { asm volatile("s_waitcnt vmcnt(%0)" :: "i"(N) : "memory"); }
DI unsigned rfl(unsigned x) { return __builtin_amdgcn_readfirstlane(x); }
DI rsrc_t mkrsrc(const void* p, unsigned bytes) { return __builtin_amdgcn_make_buffer_rsrc((void*)p, (short)0, (int)bytes, 0x00020000); }
DI void dma16(unsigned m0v, unsigned voff, rsrc_t r, unsigned soff) {
  asm volatile("s_mov_b32 m0, %0\n\ts_nop 0\n\tbuffer_load_dwordx4 %1, %2, %3 offen lds" :: "s"(m0v), "v"(voff), "s"(r), "s"(soff) : "memory");
}
DI rsrc_t code_rsrc() {
  unsigned long long b, e;
  asm volatile("s_getpc_b64 s[40:41]\n\ts_add_u32 s40, s40, rsk_main@rel32@lo+4\n\ts_addc_u32 s41, s41, rsk_main@rel32@hi+12\n\ts_mov_b64 %0, s[40:41]\n\t"
               "s_getpc_b64 s[40:41]\n\ts_add_u32 s40, s40, rsk_end@rel32@lo+4\n\ts_addc_u32 s41, s41, rsk_end@rel32@hi+12\n\ts_mov_b64 %1, s[40:41]"
               : "=&s"(b), "=&s"(e) :: "s40", "s41", "scc");
  const unsigned n = (unsigned)e - (unsigned)b;
  return mkrsrc((const void*)b, n < 0x40000u ? n : 0u);
}
template <bool DEV>
DI void dma64(unsigned m0v, unsigned va, unsigned vb, rsrc_t r, unsigned s0, unsigned s1, unsigned s2, unsigned s3) {
  if constexpr (DEV)
    asm volatile("s_mov_b32 m0, %0\n\ts_nop 0\n\tbuffer_load_dwordx4 %1, %3, %4 offen sc1 lds\n\tbuffer_load_dwordx4 %2, %3, %5 offen offset:1024 sc1 lds\n\t"
                 "buffer_load_dwordx4 %1, %3, %6 offen offset:2048 sc1 lds\n\tbuffer_load_dwordx4 %2, %3, %7 offen offset:3072 sc1 lds"
                 :: "s"(m0v), "v"(va), "v"(vb), "s"(r), "s"(s0), "s"(s1), "s"(s2), "s"(s3) : "memory");
  else
    asm volatile("s_mov_b32 m0, %0\n\ts_nop 0\n\tbuffer_load_dwordx4 %1, %3, %4 offen lds\n\tbuffer_load_dwordx4 %2, %3, %5 offen offset:1024 lds\n\t"
                 "buffer_load_dwordx4 %1, %3, %6 offen offset:2048 lds\n\tbuffer_load_dwordx4 %2, %3, %7 offen offset:3072 lds"
                 :: "s"(m0v), "v"(va), "v"(vb), "s"(r), "s"(s0), "s"(s1), "s"(s2), "s"(s3) : "memory");
}
DI void dma64d(unsigned m0v, unsigned va, rsrc_t r, unsigned s0) {
  asm volatile("s_mov_b32 m0, %0\n\ts_nop 0\n\tbuffer_load_dwordx4 %1, %2, %3 offen sc1 lds\n\tbuffer_load_dwordx4 %1, %2, %3 offen offset:1024 sc1 lds\n\t"
               "buffer_load_dwordx4 %1, %2, %3 offen offset:2048 sc1 lds\n\tbuffer_load_dwordx4 %1, %2, %3 offen offset:3072 sc1 lds"
               :: "s"(m0v), "v"(va), "s"(r), "s"(s0) : "memory");
}
DI rsrc_t mkrsrcB(const void* p, unsigned bytes) { return mkrsrc((const char*)p - 4096, bytes + 4096u); }
#define PINS(x) asm volatile("" : "+s"(x))
DI bf16x8 ldsR(unsigned a) { return *(const LAS bf16x8*)(size_t)a; }
DI s16x4 ldsT(unsigned a) { return __builtin_amdgcn_ds_read_tr16_b64_v4i16((LAS s16x4*)(size_t)a); }
DI bf16x8 cat(s16x4 a, s16x4 b) { return __builtin_shufflevector(a, b, 0, 1, 2, 3, 4, 5, 6, 7); }
DI unsigned pk2(float lo, float hi) { f32x2 v = {lo, hi}; return __builtin_bit_cast(unsigned, __builtin_convertvector(v, bf2_t)); }
DI float bflo(unsigned w) { return __uint_as_float(w << 16); }
DI float bfhi(unsigned w) { return __uint_as_float(w & 0xffff0000u); }
DI float rbf(float x) { return bflo(pk2(x, x) & 0xffffu); }
template <int CTRL, int RM> DI float dpp_add(float v) {
  return v + __builtin_bit_cast(float, __builtin_amdgcn_update_dpp(0, __builtin_bit_cast(int, v), CTRL, RM, 0xf, false));
}
DI void wsum2(float& u, float& v) {
#define WS2(C, M) u = dpp_add<C, M>(u); v = dpp_add<C, M>(v);
  WS2(0x111, 0xf) WS2(0x112, 0xf) WS2(0x114, 0xf) WS2(0x118, 0xf) WS2(0x142, 0xa) WS2(0x143, 0xc)
#undef WS2
  u = __builtin_bit_cast(float, __builtin_amdgcn_readlane(__builtin_bit_cast(int, u), 63));
  v = __builtin_bit_cast(float, __builtin_amdgcn_readlane(__builtin_bit_cast(int, v), 63));
}

DI void ln16(float (&v)[16]) {
  const float x0 = __builtin_bit_cast(float, __builtin_amdgcn_readfirstlane(__builtin_bit_cast(int, v[0])));
  float s = 0.f, q = 0.f;
#pragma unroll
  for (int i = 0; i < 16; ++i) { const float d = v[i] - x0; s += d; q = fmaf(d, d, q); }
  wsum2(s, q);
  const float md = s * (1.0f / DM), var = rbf(fmaxf(fmaf(-md, md, q * (1.0f / DM)), 0.f));
  const float mb = rbf(x0 + md), inv = __builtin_amdgcn_rcpf(rbf(__builtin_amdgcn_sqrtf(rbf(var + 1e-5f))));
#pragma unroll
  for (int i = 0; i < 16; i += 2) {
    const unsigned d = pk2(v[i] - mb, v[i + 1] - mb), r = pk2(bflo(d) * inv, bfhi(d) * inv);
    v[i] = bflo(r); v[i + 1] = bfhi(r);
  }
}
DI unsigned pkx(float lo, float hi) { return (__float_as_uint(lo) >> 16) | (__float_as_uint(hi) & 0xffff0000u); }
enum { RROW = 0, RCHK = 1, T1 = 2, T2 = 3 };
template <int KIND> struct Opnd {
  static constexpr int NA = KIND == RROW ? 2 : KIND == RCHK ? 1 : 4;
  static constexpr int RPB = KIND >= T1 ? 2 : 1;
  unsigned a[NA];
  DI void init(unsigned base, unsigned off, unsigned lane) {
    const unsigned fr = lane & 15u, g = lane >> 4, q = fr >> 2, p = lane & 3u;
    if constexpr (KIND == RROW) {
#pragma unroll
      for (int kk = 0; kk < 2; ++kk) a[kk] = base + (off + fr) * 128u + 16u * ((4u * kk + g) ^ (fr >> 1));
    } else if constexpr (KIND == RCHK) {
      a[0] = base + g * 2048u + (off + fr) * 16u;
    } else {
      const unsigned x = ((q >> 1) << 1) | (g & 1u);
#pragma unroll
      for (int n = 0; n < 4; ++n)
        a[n] = base + (KIND == T1 ? 4096u : 2048u) * (g >> 1) + 1024u * (off >> 6) + 128u * (4u * (g & 1u) + q) + 32u * ((unsigned)n ^ x) + 8u * p;
    }
  }
  DI bf16x8 ld(int b, int kk) const {
    if constexpr (KIND == RROW) return ldsR(a[kk] + 2048u * b);
    else if constexpr (KIND == RCHK) return ldsR(a[0] + 8192u * kk + 256u * b);
    else { const unsigned ad = a[b & 3] + 1024u * (b >> 2) + 8192u * kk; return cat(ldsT(ad), ldsT(ad + (KIND == T1 ? 2048u : 4096u))); }
  }
  DI void adv(unsigned delta) {
#pragma unroll
    for (int i = 0; i < NA; ++i) { a[i] += delta; asm volatile("" : "+v"(a[i])); }
  }
};

template <int PK, int QK, int NS, unsigned STG>
DI void gemm_wide(f32x4 (&acc)[8][4], Opnd<PK>& P, Opnd<QK>& Q, const int N) {
  bf16x8 pf[2][4][2], qf[4][2];
  auto loadP = [&](int h) {
#pragma unroll
    for (int p = 0; p < 4; ++p) { pf[h][p][0] = P.ld(4 * h + p, 0); pf[h][p][1] = P.ld(4 * h + p, 1); }
  };
  auto mma = [&](int h, int q) {
#pragma unroll
    for (int p0 = 0; p0 < 4; ++p0) {
      const int p = (q & 1) ? 3 - p0 : p0;
      acc[4 * h + p][q] = __builtin_amdgcn_mfma_f32_16x16x32_bf16(pf[h][p][0], qf[q][0], acc[4 * h + p][q], 0, 0, 0);
      acc[4 * h + p][q] = __builtin_amdgcn_mfma_f32_16x16x32_bf16(pf[h][p][1], qf[q][1], acc[4 * h + p][q], 0, 0, 0);
    }
  };
  BAR();
  loadP(0);
#pragma unroll
  for (int q = 0; q < 3; ++q) { qf[q][0] = Q.ld(q, 0); qf[q][1] = Q.ld(q, 1); }
  int stg = 0;
#pragma nounroll
  for (int t = 0; t < N; ++t) {
    SCHED();
    qf[3][0] = Q.ld(3, 0); qf[3][1] = Q.ld(3, 1);
    loadP(1); mma(0, 0); mma(0, 1); mma(0, 2); mma(0, 3);
    stg = (stg == NS - 1) ? 0 : stg + 1;
    const unsigned delta = stg == 0 ? (unsigned)(-(int)((NS - 1) * STG)) : STG;
    P.adv(delta); Q.adv(delta);
#pragma unroll
    for (int i = 0; i < 18; ++i) { SGB(0x8, 1); SGB(0x100, 1); }
#pragma unroll
    for (int i = 0; i < 14; ++i) { SGB(0x8, 1); SGB(0x2, 1); }
    SCHED();
    WAITL0(); BAR();
    SCHED();
#pragma unroll
    for (int q = 0; q < 4; ++q) {
      if (q > 0) { qf[q - 1][0] = Q.ld(q - 1, 0); qf[q - 1][1] = Q.ld(q - 1, 1); }
      if (q == 0) { pf[0][0][0] = P.ld(0, 0); pf[0][0][1] = P.ld(0, 1); pf[0][1][0] = P.ld(1, 0); pf[0][1][1] = P.ld(1, 1); }
      else if (q < 3) { pf[0][q + 1][0] = P.ld(q + 1, 0); pf[0][q + 1][1] = P.ld(q + 1, 1); }
      mma(1, q);
#pragma unroll
      for (int i = 0; i < 8; ++i) { SGB(0x8, 1); SGB(0x100, 1); }
      SCHED();
    }
  }
  SCHED();
}

template <int PK, int QK, int NS, unsigned STG, int TAIL, int S0, int DRAIN, class F>
DI void gemm_full(f32x4 (&acc)[4][4], Opnd<PK>& P, Opnd<QK>& Q, const int N, F hook) {
  bf16x8 pf[2][2][4], qf[2][2][4];
  constexpr int NR = 8 * (Opnd<PK>::RPB + Opnd<QK>::RPB);
  int stg = S0;
  auto load = [&](int b) {
#pragma unroll
    for (int i = 0; i < 4; ++i)
#pragma unroll
      for (int kk = 0; kk < 2; ++kk) { pf[b][kk][i] = P.ld(i, kk); qf[b][kk][i] = Q.ld(i, kk); }
    stg = (stg == NS - 1) ? 0 : stg + 1;
    const unsigned delta = stg == 0 ? (unsigned)(-(int)((NS - 1) * STG)) : STG;
    P.adv(delta); Q.adv(delta);
  };
  auto step = [&](int b, bool sync) {
    if (sync) { WAITL0(); BAR(); }
    SCHED();
    load(b ^ 1);
#pragma unroll
    for (int p = 0; p < 4; ++p)
#pragma unroll
      for (int q0 = 0; q0 < 4; ++q0) {
        const int q = (p & 1) ? 3 - q0 : q0;
        acc[p][q] = __builtin_amdgcn_mfma_f32_16x16x32_bf16(pf[b][0][p], qf[b][0][q], acc[p][q], 0, 0, 0);
        acc[p][q] = __builtin_amdgcn_mfma_f32_16x16x32_bf16(pf[b][1][p], qf[b][1][q], acc[p][q], 0, 0, 0);
      }
#pragma unroll
    for (int i = 0; i < NR; ++i) { SGB(0x8, 1); SGB(0x100, 1); }
#pragma unroll
    for (int i = NR; i < 32; ++i) { SGB(0x8, 1); SGB(0x2, 1); }
    SCHED();
  };
  BAR();
  load(0);
  if (DRAIN > 0) {
#pragma nounroll
    for (int t = 0; t < DRAIN; t += 2) { step(0, true); step(1, true); }
    waitv<0>();
    hook();
  }
#pragma nounroll
  for (int t = DRAIN; t < N - (TAIL ? 4 : 0); t += 2) { step(0, true); step(1, true); }
  if (TAIL) {
#pragma nounroll
    for (int t = 0; t < 4; t += 2) { step(0, true); step(1, false); }
  }
}

DI unsigned voffRow(unsigned lane, unsigned ldb, unsigned par) { return (lane >> 3) * ldb + 16u * ((lane & 7u) ^ (4u * par + (lane >> 4))); }
DI unsigned voffT(unsigned lane, unsigned ldb) { return (lane >> 3) * ldb + 16u * ((lane & 7u) ^ (2u * ((((lane >> 4) & 1u) << 1) | (lane >> 5)))); }
struct T4 { unsigned s0, s1, s2, s3;
  DI void set(unsigned so, unsigned lw, unsigned ldb) { s0 = so + 4096u + lw * 16u * ldb; s1 = s0 + 128u - 1024u; s2 = s0 + 8u * ldb - 2048u; s3 = s2 + 128u - 1024u; }
  DI void pin() { PINS(s0); PINS(s1); PINS(s2); PINS(s3); }
  template <bool DEV = false> DI void go(unsigned dst, unsigned vo, rsrc_t r) const { dma64<DEV>(dst, vo, vo, r, s0, s1, s2, s3); } };

typedef unsigned long long u64;
constexpr unsigned FL_H = 64, FL_O = 320, FL_U = 576, FL_G = 832, FL_X = 3072;
DI void flag_set(u64* p, u64 tag) { __hip_atomic_store(p, tag, __ATOMIC_RELAXED, __HIP_MEMORY_SCOPE_AGENT); }
DI u64 flag_get(const u64* p) { return __hip_atomic_load(p, __ATOMIC_RELAXED, __HIP_MEMORY_SCOPE_AGENT); }
template <int SLOW = 16> DI void flag_wait(const u64* p, u64 tag, u64 mask) {
  const u64 t0 = __builtin_amdgcn_s_memrealtime();
  while ((__ballot(flag_get(p) != tag) & mask) != 0ull) {
    const u64 dt = __builtin_amdgcn_s_memrealtime() - t0;
    if (dt > SPIN_LIMIT) break;
    if (dt > 400ull) __builtin_amdgcn_s_sleep(SLOW); else __builtin_amdgcn_s_sleep(3);
  }
  asm volatile("" ::: "memory");
}

DI f32x2 sigm2(f32x2 v, f32x2 z) {
  f32x2 e = {__builtin_amdgcn_exp2f(z[0]), __builtin_amdgcn_exp2f(z[1])};
  e += 1.0f;
  return v * (f32x2){__builtin_amdgcn_rcpf(e[0]), __builtin_amdgcn_rcpf(e[1])};
}
DI unsigned gelu2(f32x2 v) {
  const f32x2 r = sigm2(v, v * __builtin_elementwise_fma(v * v, (f32x2)(-0.1029432397f), (f32x2)(-2.3022081986f)));
  return pk2(r[0], r[1]);
}
DI unsigned silu2(f32x2 v) { const f32x2 r = sigm2(v, v * -1.4426950408889634f); return pk2(r[0], r[1]); }
DI f32x2 lo2(f32x4 v) { return __builtin_shufflevector(v, v, 0, 1); }
DI f32x2 hi2(f32x4 v) { return __builtin_shufflevector(v, v, 2, 3); }
DI f32x2 bf2(unsigned w) { return (f32x2){bflo(w), bfhi(w)}; }

constexpr unsigned QSTG = 49152u;
constexpr unsigned KBASE = 65536u, VBASE = 114688u;
constexpr unsigned KSLOT[3] = {0u, 32768u, 16384u}, VSLOT[3] = {32768u, 16384u, 0u};
DI void touch8k(rsrc_t r, unsigned vo128, unsigned soff) {
  asm volatile("buffer_load_dword v255, %0, %1, %2 offen" :: "v"(vo128), "s"(r), "s"(soff) : "memory", "v255");
}
constexpr float QSCALE = 0.12751743082f;
#define BIGSUM 4096.0f

struct Att {
  bf16x8 qf[8], kv[16], pf[4], pn[2];
  f32x16 o[4], s[2], negm;
  float l, ts;
  unsigned aK[4], aV[2];
};
template <unsigned SLOT, int J> DI void att_k(Att& a) {
  asm volatile("ds_read_b128 %0, %1 offset:%2" : "+v"(a.kv[J]) : "v"(a.aK[(J >> 1) & 3]), "i"(SLOT + 8192u * (J >> 3) + 4096u * (J & 1)) : "memory");
}
template <unsigned SLOT, int J> DI void att_v(Att& a) {
  s16x4 lo = __builtin_shufflevector(a.kv[J], a.kv[J], 0, 1, 2, 3), hi = __builtin_shufflevector(a.kv[J], a.kv[J], 4, 5, 6, 7);
  constexpr unsigned off = SLOT + 4096u * (J >> 2) + 1024u * ((J >> 1) & 1);
  asm volatile("ds_read_b64_tr_b16 %0, %2 offset:%3\n\tds_read_b64_tr_b16 %1, %2 offset:%4" : "+v"(lo), "+v"(hi) : "v"(a.aV[J & 1]), "i"(off), "i"(off + 2048u) : "memory");
  a.kv[J] = cat(lo, hi);
}
DI void att_wait(Att& a) {
  asm volatile("s_waitcnt lgkmcnt(0)" : "+v"(a.kv[0]), "+v"(a.kv[1]), "+v"(a.kv[2]), "+v"(a.kv[3]), "+v"(a.kv[4]), "+v"(a.kv[5]), "+v"(a.kv[6]), "+v"(a.kv[7]),
               "+v"(a.kv[8]), "+v"(a.kv[9]), "+v"(a.kv[10]), "+v"(a.kv[11]), "+v"(a.kv[12]), "+v"(a.kv[13]), "+v"(a.kv[14]), "+v"(a.kv[15]) :: "memory");
}
DI void att_loadK(Att& a, unsigned slot) {
#pragma unroll
  for (int j = 0; j < 16; ++j) a.kv[j] = ldsR(a.aK[(j >> 1) & 3] + slot + 8192u * (j >> 3) + 4096u * (j & 1));
}
DI unsigned lane_id() { unsigned l; asm volatile("v_mbcnt_lo_u32_b32 %0, -1, 0\n\tv_mbcnt_hi_u32_b32 %0, -1, %0" : "=v"(l)); return l; }
DI void add_bcast3(f32x16& t0, f32x16& t1, f32x16& t2, float v) {
  const float one = lane_id() < 32u ? 1.0f : 0.0f;
  asm volatile("s_nop 7\n\ts_nop 7\n\tv_mfma_f32_32x32x2_f32 %0, %3, %4, %0\n\tv_mfma_f32_32x32x2_f32 %1, %3, %4, %1\n\tv_mfma_f32_32x32x2_f32 %2, %3, %4, %2\n\t"
               "s_nop 7\n\ts_nop 7\n\ts_nop 7" : "+v"(t0), "+v"(t1), "+v"(t2) : "v"(one), "v"(v));
}
template <bool FIRST, unsigned SLOT, int J> DI void att_qk1(Att& a) {
  a.s[J & 1] = __builtin_amdgcn_mfma_f32_32x32x16_bf16(a.kv[J], a.qf[J >> 1], J < 2 ? (FIRST ? (f32x16){} : a.negm) : a.s[J & 1], 0, 0, 0);
  SCHED();
  if constexpr (J == 0) ALIGN8();
  if constexpr (!FIRST) att_v<SLOT, J>(a);
  SCHED();
  if constexpr (J < 15) att_qk1<FIRST, SLOT, J + 1>(a);
}
template <int NEXT, unsigned SLOT, int J> DI void att_pv1(Att& a) {
  a.o[J & 3] = __builtin_amdgcn_mfma_f32_32x32x16_bf16(a.kv[J], a.pf[J >> 2], a.o[J & 3], 0, 0, 0);
  SCHED();
  if constexpr (J == 0) ALIGN8();
  if constexpr (NEXT == 1) att_k<SLOT, J>(a); else if constexpr (NEXT == 2) att_v<SLOT, J>(a);
  SCHED();
  if constexpr (J < 15) att_pv1<NEXT, SLOT, J + 1>(a);
}
DI float max3(float x, float y, float z) { return __builtin_fmaxf(__builtin_fmaxf(x, y), z); }
DI float att_rowmax(const Att& a) {
  float m0 = max3(a.s[0][0], a.s[0][1], a.s[0][2]), m1 = max3(a.s[1][0], a.s[1][1], a.s[1][2]);
#pragma unroll
  for (int r = 3; r < 15; r += 2) { m0 = max3(m0, a.s[0][r], a.s[0][r + 1]); m1 = max3(m1, a.s[1][r], a.s[1][r + 1]); }
  const float mx = __builtin_fmaxf(max3(m0, m1, a.s[0][15]), a.s[1][15]);
  auto rr = __builtin_amdgcn_permlane32_swap(__float_as_uint(mx), __float_as_uint(mx), false, false);
  return __builtin_fmaxf(__uint_as_float(rr[0]), __uint_as_float(rr[1]));
}
DI float att_exp16(const f32x16& v, bf16x8& p0, bf16x8& p1) {
  float s0 = 0.f, s1 = 0.f;
  u32x4 w[2];
#pragma unroll
  for (int h = 0; h < 2; ++h) {
    float e[8];
#pragma unroll
    for (int r = 0; r < 8; ++r) e[r] = __builtin_amdgcn_exp2f(v[8 * h + r]);
#pragma unroll
    for (int r = 0; r < 8; r += 2) {
      if (h == 0 && r == 0) { s0 = e[0]; s1 = e[1]; } else { s0 += e[r]; asm("" : "+v"(s0)); s1 += e[r + 1]; asm("" : "+v"(s1)); }
      w[h][r >> 1] = pk2(e[r], e[r + 1]);
    }
    asm volatile("" : "+v"(w[h]));
    SCHED();
  }
  p0 = __builtin_bit_cast(bf16x8, w[0]); p1 = __builtin_bit_cast(bf16x8, w[1]);
  return s0 + s1;
}
template <bool FIRST> DI void att_sm1(Att& a) {
  if (FIRST) {
    const float mx = att_rowmax(a);
#pragma unroll
    for (int r = 0; r < 16; ++r) { a.s[0][r] -= mx; a.s[1][r] -= mx; a.negm[r] = -mx; }
  }
  a.ts = att_exp16(a.s[0], a.pn[0], a.pn[1]);
  asm volatile("" : "+v"(a.pn[0]), "+v"(a.pn[1]), "+v"(a.ts), "+v"(a.s[1]), "+v"(a.negm));
}
template <bool FIRST> DI void att_sm2(Att& a) {
  a.ts += att_exp16(a.s[1], a.pf[2], a.pf[3]);
  if (!FIRST) {
    if (__builtin_expect(!__all(a.ts <= BIGSUM), 0)) {
      const float dl = fmaxf(att_rowmax(a), 0.f), fct = __builtin_amdgcn_exp2f(-dl);
#pragma unroll
      for (int db = 0; db < 4; ++db) a.o[db] *= fct;
      a.l *= fct;
      add_bcast3(a.s[0], a.s[1], a.negm, -dl);
      a.ts = att_exp16(a.s[0], a.pn[0], a.pn[1]) + att_exp16(a.s[1], a.pf[2], a.pf[3]);
    }
  }
  a.l += a.ts;
  a.pf[0] = a.pn[0]; a.pf[1] = a.pn[1];
  asm volatile("" : "+v"(a.pf[0]), "+v"(a.pf[1]), "+v"(a.pf[2]), "+v"(a.pf[3]), "+v"(a.l));
}

extern "C" __global__ __launch_bounds__(512, 2) __attribute__((amdgpu_num_vgpr(127)))
void rsk_main(const u16* __restrict__ x, char* __restrict__ ws, const u16* __restrict__ wq, const u16* __restrict__ kc, const u16* __restrict__ vc,
              const u16* __restrict__ w1, const u16* __restrict__ wo, const u16* __restrict__ w2, const u16* __restrict__ b1, const u16* __restrict__ b2,
              u16* __restrict__ out) {
  const unsigned tid = threadIdx.x, lane = tid & 63u, wid = rfl(tid >> 6), grp = wid >> 2, lw = wid & 3u;
  const unsigned bid = blockIdx.x, xcd = bid & 7u, idx = bid >> 3;
  { unsigned o0 = wid * 768u, o1 = 5376u + idx * 2048u + wid * 256u; PINS(o0); PINS(o1);
    touch8k(code_rsrc(), lane * 128u + (lane < 6u ? o0 : lane < 8u ? o1 : 0x80000000u), 0u); }
  SCHED();
  u64* flags = (u64*)(ws + WS_FLAG);
  const unsigned lnrow = (bid >> 2) * 32u + ((bid >> 1) & 1u) * 16u + (bid & 1u) * 4u + grp * 8u + lw;
  u32x4 xa, xb; u32x2 ep;
  asm volatile("buffer_load_dwordx4 %0, %3, %4, %5 offen\n\tbuffer_load_dwordx4 %1, %3, %4, %5 offen offset:1024\n\tglobal_load_dwordx2 %2, %6, %7 sc1"
               : "=&v"(xa), "=&v"(xb), "=&v"(ep) : "v"(lane * 16u), "s"(mkrsrc(x, SEQ * DM * 2)), "s"(lnrow * 2048u), "v"(0u), "s"(flags) : "memory");
  SCHED();
  extern __shared__ __attribute__((aligned(16))) char lds[];
  const unsigned L = (unsigned)(size_t)lds;
  const char* xn = ws + WS_XN; const char* hh = ws + WS_H;
  const unsigned qt = (xcd & 1u) * 4u + (idx & 3u), kvh = xcd >> 1, head = kvh * 8u + (idx >> 2);
  const bool hasU = idx < 28u;
  const unsigned udt = (xcd & 1u) * 4u + (idx & 3u), uft = (xcd >> 1) * 7u + (idx >> 2);
  const unsigned ufc = (idx >> 2) * 4u + (xcd >> 1);
  const rsrc_t sK = mkrsrc(kc + ((size_t)kvh * SK + (SK - WIN)) * DH, WIN * DH * 2), sV = mkrsrc(vc + ((size_t)kvh * SK + (SK - WIN)) * DH, WIN * DH * 2);
  const rsrc_t sH = mkrsrc(hh, SEQ * DM * 2), sW1 = mkrsrcB(w1, SEQ * FF * 2);
  const unsigned voK0 = voffRow(lane, 256u, 0), voV = voffT(lane, 256u);
  auto dmaK2 = [&](unsigned slot, unsigned t, unsigned p0) {
    unsigned voK1; asm volatile("v_xor_b32 %0, 64, %1" : "=v"(voK1) : "v"(voK0));
    dma16(L + KBASE + slot + 1024u * p0, voK0, sK, t * 16384u + (p0 & 7u) * 2048u + (p0 >> 3) * 128u);
    dma16(L + KBASE + slot + 1024u * p0 + 1024u, voK1, sK, t * 16384u + (p0 & 7u) * 2048u + 2048u + (p0 >> 3) * 128u); };
  auto dmaV2 = [&](unsigned slot, unsigned t, unsigned p0) {
    dma16(L + VBASE + slot + 1024u * p0, voV, sV, t * 16384u + (p0 >> 1) * 2048u);
    dma16(L + VBASE + slot + 1024u * p0 + 1024u, voV, sV, t * 16384u + (p0 >> 1) * 2048u + 128u); };
  auto tileK = [&](unsigned t) {
#pragma unroll
    for (int p = 0; p < 16; p += 2) dmaK2(KSLOT[t % 3], t, p); };
  auto tileV = [&](unsigned t) {
#pragma unroll
    for (int p = 0; p < 16; p += 2) dmaV2(VSLOT[t % 3], t, p); };
  Att a;
  { const unsigned r32 = lane & 31u, hi = lane >> 5;
#pragma unroll
    for (int j = 0; j < 4; ++j) a.aK[j] = L + KBASE + r32 * 128u + 16u * ((2u * j + hi) ^ ((r32 >> 1) & 7u)); }

  const rsrc_t sS = lw == 0 ? mkrsrcB(wq, DM * NH * DH * 2) : mkrsrcB(xn, SEQ * DM * 2);
  unsigned asrc = 4096u + (lw == 0 ? head * 256u : (qt * 256u + (lw - 1u) * 128u) * 2048u), adst = L + lw * 16384u;
  const unsigned avo0 = lw == 0 ? voffT(lane, 8192u) : voffRow(lane, 2048u, 0), avo1 = lw == 0 ? avo0 : avo0 ^ 64u;
  constexpr unsigned HT = 147456u;
  auto astep = [&](unsigned vo0, unsigned vo1) {
    const unsigned sp = lw == 0 ? 8u * 8192u : 16u * 2048u, so = (lw == 0 ? 128u : 8u * 2048u) - 1024u;
#pragma unroll
    for (int q = 0; q < 4; ++q) { const unsigned e = asrc + 2u * q * sp; dma64<true>(adst + 4096u * q, vo0, vo1, sS, e, e + so, e + sp - 2048u, e + sp - 2048u + so); }
    asrc += lw == 0 ? 64u * 8192u : 128u; adst = adst >= L + 2u * QSTG ? adst - 2u * QSTG : adst + QSTG;
  };
  u64 tag;
  {
    const unsigned row = lnrow;
    __builtin_amdgcn_s_setprio(1);
    if (wid == 7) {
      touch8k(mkrsrc(wq, DM * NH * DH * 2), lane * 128u, bid * 8192u);
      waitv<1>();
    } else if (wid == 4u) { astep(avo0, avo1); waitv<16>(); } else waitv<0>();
    asm volatile("" : "+v"(xa), "+v"(xb), "+v"(ep));
    tag = (((u64)rfl(ep[1]) << 32) | rfl(ep[0])) * 0x9E3779B97F4A7C15ull + 0x632BE59BD9B4E019ull;
    float v[16];
#pragma unroll
    for (int i = 0; i < 4; ++i) { v[2 * i] = bflo(xa[i]); v[2 * i + 1] = bfhi(xa[i]); v[8 + 2 * i] = bflo(xb[i]); v[9 + 2 * i] = bfhi(xb[i]); }
    ln16(v);
    const rsrc_t sW = mkrsrc(ws, WS_O);
#pragma unroll
    for (int p = 0; p < 2; ++p) {
      const unsigned lo0 = pkx(v[8 * p], v[8 * p + 1]), lo1 = pkx(v[8 * p + 2], v[8 * p + 3]), hi0 = pkx(v[8 * p + 4], v[8 * p + 5]), hi1 = pkx(v[8 * p + 6], v[8 * p + 7]);
      const bool odd = lane & 1u;
      const unsigned s0 = __builtin_amdgcn_update_dpp(0, (int)(odd ? lo0 : hi0), 0xB1, 0xf, 0xf, false), s1 = __builtin_amdgcn_update_dpp(0, (int)(odd ? lo1 : hi1), 0xB1, 0xf, 0xf, false);
      __builtin_amdgcn_raw_buffer_store_b128(odd ? (u32x4){s0, s1, hi0, hi1} : (u32x4){lo0, lo1, s0, s1}, sW, WS_XN + row * 2048u + lane * 16u + 1024u * p, 0, 16);
    }
    waitv<0>();
    if (lane == 0u) flag_set(flags + FL_X + bid * 8u + wid, tag);
    if (grp == 0) __builtin_amdgcn_s_setprio(0);
    ln16(v);
    *(LAS u32x4*)(size_t)(L + HT + wid * 2048u + lane * 16u) = (u32x4){pkx(v[0], v[1]), pkx(v[2], v[3]), pkx(v[4], v[5]), pkx(v[6], v[7])};
    *(LAS u32x4*)(size_t)(L + HT + wid * 2048u + 1024u + lane * 16u) = (u32x4){pkx(v[8], v[9]), pkx(v[10], v[11]), pkx(v[12], v[13]), pkx(v[14], v[15])};
    WAITL0();
    __builtin_amdgcn_s_setprio(0);
  }
  auto hstore = [&](unsigned i) {
    const unsigned d0 = i * 64u, ad = L + HT + ((lane & 15u) >> 2) * 2048u + (d0 + (lane >> 4) * 16u + (lane & 3u) * 4u) * 2u;
    __builtin_amdgcn_raw_buffer_store_b128(__builtin_bit_cast(u32x4, cat(ldsT(ad), ldsT(ad + 8192u))), mkrsrc(ws, WS_O), WS_H + ((d0 >> 7) * 256u + bid) * 2048u + ((d0 & 64u) + lane) * 16u, 0, 16);
  };
  if (!hasU) {
    BAR();
    hstore(wid * 2u); hstore(wid * 2u + 1u);
    waitv<0>(); BAR();
    if (tid == 0u) flag_set(flags + FL_H + bid, tag);
    const unsigned w = ((idx - 28u) * 8u + xcd) * 8u + wid, vo = lane * 128u;
    const rsrc_t sWq = mkrsrc(wq, DM * NH * DH * 2);
#pragma unroll
    for (int j = 1; j < 4; ++j) touch8k(sWq, vo, (j * 256u + w) * 8192u);
    touch8k(mkrsrc((w & 64u) ? vc : kc, 4u * SK * DH * 2u), w < 128u ? vo : 0x80000000u, ((w >> 4) & 3u) * (SK * DH * 2u) + (SK - WIN) * DH * 2u + (w & 15u) * 8192u);
    touch8k(sW1, wid < 7u ? vo : 0x80000000u, (wid * 256u + bid) * 8192u + 4096u);
    waitv<0>();
  }
  if (wid == 7) {
    const unsigned vo = lane * 128u;
    BAR(); BAR();
#pragma nounroll
    for (unsigned i = 0; i < 4; ++i) {
      BAR();
      if (hasU) { hstore(i * 4u); hstore(i * 4u + 1u); hstore(i * 4u + 2u); hstore(i * 4u + 3u); }
    }
    BAR(); BAR();
    waitv<0>();
    if (lane == 0u) flag_set(flags + FL_H + bid, tag);
    touch8k(sW1, vo, bid * 8192u + 4096u);
    BAR(); BAR(); BAR(); BAR();
    const u64 f0 = flag_get(flags + FL_H + lane), f1 = flag_get(flags + FL_H + 64u + lane), f2 = flag_get(flags + FL_H + 128u + lane), f3 = flag_get(flags + FL_H + 192u + lane);
    touch8k(code_rsrc(), lane < 16u ? vo : 0x80000000u, idx * 2048u);
    BAR(); BAR();
    if (__any(f0 != tag || f1 != tag || f2 != tag || f3 != tag)) {
#pragma nounroll
      for (unsigned j = 0; j < 256u; j += 64u) flag_wait(flags + FL_H + j + lane, tag, ~0ull);
    }
    if (bid == 0u && lane == 0u) flag_set(flags, tag);
    touch8k(sW1, vo, (256u + bid) * 8192u + 4096u);
    BAR(); BAR(); BAR();
#pragma unroll
    for (int j = 2; j < 6; ++j) touch8k(sW1, vo, (j * 256u + bid) * 8192u + 4096u);
    att_loadK(a, KSLOT[0]);
  } else if (grp == 1) {
    const unsigned vo0 = avo0, vo1 = avo1;
    if (lw != 0) {
      const u64* fx = flags + FL_X + qt * 256u + (lw - 1u) * 128u + lane;
      const u64 t0 = __builtin_amdgcn_s_memrealtime();
      while (__any(((flag_get(fx) ^ tag) | (flag_get(fx + 64) ^ tag)) != 0ull)) {
        if (__builtin_amdgcn_s_memrealtime() - t0 > SPIN_LIMIT) break;
        __builtin_amdgcn_s_sleep(2);
      }
      asm volatile("" ::: "memory");
      astep(vo0, vo1);
    } else __builtin_amdgcn_s_sleep(16);
    astep(vo0, vo1);
    waitv<16>();
    BAR();
    astep(vo0, vo1);
#pragma nounroll
    for (unsigned t = 0; t < 13; ++t) { waitv<16>(); BAR(); astep(vo0, vo1); }
    waitv<16>(); BAR();
    if (lw == 0) tileK(0); else if (lw == 1) tileK(2); else tileV(0);
    waitv<16>(); BAR();
    if (lw == 1) tileK(1); else if (lw == 2) tileV(1);
    if (lw == 0) waitv<0>();
    BAR();
    att_loadK(a, KSLOT[0]);
  } else {
    Opnd<T1> P; Opnd<RROW> Q;
    P.init(L, 0u, lane); Q.init(L + 16384u, lw * 64u, lane);
    f32x4 acc[8][4] = {};
    gemm_wide<T1, RROW, 3, QSTG>(acc, P, Q, 16);
    att_loadK(a, KSLOT[0]);
    const unsigned wa = L + (lane >> 5) * 4096u + (lw * 64u + (lane & 15u)) * 16u + ((lane >> 4) & 1u) * 8u;
#pragma unroll
    for (int j = 0; j < 16; ++j) {
      const f32x4 v = acc[j >> 1][j & 1] * QSCALE;
      *(LAS u32x2*)(size_t)(wa + 8192u * (j >> 1) + 256u * (j & 1)) = (u32x2){pk2(v[0], v[1]), pk2(v[2], v[3])};
    }
#pragma unroll
    for (int ks = 0; ks < 8; ++ks) a.qf[ks] = ldsR(L + (2u * ks + (lane >> 5)) * 4096u + (lw * 64u + (lane & 31u)) * 16u);
    SCHED();
#pragma unroll
    for (int j = 0; j < 16; ++j) {
      a.s[j & 1] = __builtin_amdgcn_mfma_f32_32x32x16_bf16(a.kv[j], a.qf[j >> 1], j < 2 ? (f32x16){} : a.s[j & 1], 0, 0, 0);
      float v[4];
#pragma unroll
      for (int c = 0; c < 4; ++c) { v[c] = acc[j >> 1][2 + (j & 1)][c] * QSCALE; asm("" : "+v"(v[c])); }
      *(LAS u32x2*)(size_t)(wa + 8192u * (j >> 1) + 256u * (2 + (j & 1))) = (u32x2){pk2(v[0], v[1]), pk2(v[2], v[3])};
      SCHED();
    }
    WAITL0();
  }

  const rsrc_t sO = mkrsrc(ws + WS_O, SEQ * NH * DH * 2);
  {
    const unsigned l2 = lane_id(), hi = l2 >> 5, blk = (l2 >> 4) & 1u, q4 = (l2 & 15u) >> 2, p4 = l2 & 3u;
#pragma unroll
    for (int n = 0; n < 2; ++n) a.aV[n] = L + VBASE + 128u * (4u * hi + q4) + 32u * ((2u * n + blk) ^ (((q4 >> 1) << 1) | hi)) + 8u * p4;
#pragma unroll
    for (int db = 0; db < 4; ++db) a.o[db] = (f32x16){};
    a.l = 0.f;
    const rsrc_t sW1u = mkrsrc((const char*)w1 - 4096, hasU ? SEQ * FF * 2 + 4096u : 0u), sHu = mkrsrc(hh, hasU ? SEQ * DM * 2 : 0u);
    unsigned voU = 0;
    auto uP = [&](unsigned j) {
      const unsigned so = 4096u + (j * 64u + wid * 8u) * (FF * 2u) + ufc * 256u;
      asm volatile("s_mov_b32 m0, %0\n\ts_nop 0\n\tbuffer_load_dwordx4 %1, %2, %3 offen lds\n\tbuffer_load_dwordx4 %1, %2, %4 offen offset:1024 lds"
                   :: "s"(L + j * 32768u + wid * 2048u), "v"(voU), "s"(sW1u), "s"(so), "s"(so + 128u - 1024u) : "memory"); };
    auto uQ = [&](unsigned j) {
      asm volatile("s_mov_b32 m0, %0\n\ts_nop 0\n\tbuffer_load_dwordx4 %1, %2, %3 offen sc1 lds\n\tbuffer_load_dwordx4 %1, %2, %3 offen offset:1024 sc1 lds"
                   :: "s"(L + j * 32768u + 16384u + wid * 2048u), "v"(lane_id() * 16u), "s"(sHu), "s"((udt * 32u + j) * 16384u + wid * 2048u) : "memory"); };
    const bool pre = hasU && grp == 1;
    if (grp == 1) {
      if (wid != 7) waitv<0>();
      WAITL0(); BAR();
#pragma unroll
      for (int ks = 0; ks < 8; ++ks) a.qf[ks] = ldsR(L + (2u * ks + hi) * 4096u + (lw * 64u + 32u + (l2 & 31u)) * 16u);
      SCHED(); att_qk1<true, 0u, 0>(a);
    }
    const rsrc_t sWo = mkrsrc(wo, NH * DH * DM * 2), sW2 = mkrsrc(w2, FF * SEQ * 2);
    const unsigned lk = bid * 8192u + (wid >> 1) * 2048u + (wid & 1u) * 128u;
#define LOOK(v) do { if ((v) < 1) touch8k(sW1, voV, lk + 6u * 0x200000u + 4096u); else if ((v) < 5) touch8k(sWo, voV, lk + ((v) - 1) * 0x200000u); \
                     else if ((v) < 12) touch8k(sW2, voV, lk + (((v) - 2) % 7) * 0x200000u); } while (0)
#define ATT_TILE(i, FIRST, WB, WD)                                                                                            \
    SCHED(); if (!(FIRST)) att_qk1<false, VSLOT[((i) + 2) % 3], 0>(a); BAR(); SCHED();                                        \
    if ((i) <= 4) dmaK2(KSLOT[(i) % 3], (i) + 3, wid * 2u);                                                                   \
    LOOK(2 * (i));                                                                                                            \
    if ((i) == 5) { voU = voffT(lane_id(), FF * 2u); uP(0); uQ(0); } else if ((i) == 6) { uQ(1); uP(2); } else if ((i) == 7) uP(3); \
    att_sm1<FIRST>(a);                                                                                                        \
    waitv<WB>();                                                                                                              \
    if (!(FIRST)) att_wait(a); SCHED(); BAR();                                                                                \
    if (FIRST) att_loadK(a, KSLOT[1]); else { SCHED(); att_pv1<((i) < 7 ? 1 : 2), ((i) < 7 ? KSLOT[((i) + 1) % 3] : VSLOT[7 % 3]), 0>(a); } \
    BAR(); SCHED();                                                                                                           \
    if ((i) <= 5) dmaV2(VSLOT[((i) + 2) % 3], (i) + 2, wid * 2u);                                                             \
    LOOK(2 * (i) + 1);                                                                                                        \
    if ((i) == 5) uP(1); else if ((i) == 6) uQ(2); else if ((i) == 7) uQ(3);                                                  \
    att_sm2<FIRST>(a);                                                                                                        \
    waitv<WD>();                                                                                                              \
    att_wait(a); SCHED(); BAR();
    ATT_TILE(0, true, 63, 63) ATT_TILE(1, false, 63, 10) ATT_TILE(2, false, 10, 10) ATT_TILE(3, false, 10, 10)
    ATT_TILE(4, false, 10, 10) ATT_TILE(5, false, 12, 14) ATT_TILE(6, false, 15, 9) ATT_TILE(7, false, 63, 63)
#undef ATT_TILE
#undef LOOK
    SCHED(); att_pv1<0, 0u, 0>(a);
    if (grp == 0) { BAR(); waitv<0>(); }
    if (pre) { waitv<2>(); BAR(); }
  }
  auto ost2 = [&](int db) {
    const unsigned l3 = lane_id(), row = qt * 256u + lw * 64u + grp * 32u + (l3 & 31u);
    const unsigned oo = (((row >> 7) * 64u + head * 2u) * 8u + (l3 >> 5)) * 2048u + (row & 127u) * 16u;
#pragma unroll
    for (int i = 0; i < 2; ++i) {
      float w[8];
#pragma unroll
      for (int c = 0; c < 8; ++c) { w[c] = a.o[db][8 * i + c] * a.l; asm("" : "+v"(w[c])); }
      __builtin_amdgcn_raw_buffer_store_b128((u32x4){pk2(w[0], w[1]), pk2(w[2], w[3]), pk2(w[4], w[5]), pk2(w[6], w[7])}, sO, oo + db * 8192u + i * 4096u, 0, 16);
    }
  };
  { auto rr = __builtin_amdgcn_permlane32_swap(__float_as_uint(a.l), __float_as_uint(a.l), false, false);
    a.l = __builtin_amdgcn_rcpf(__uint_as_float(rr[0]) + __uint_as_float(rr[1])); }
  if (!(hasU && grp == 1)) { ost2(0); ost2(1); ost2(2); ost2(3); }
  const unsigned ln = lane_id();
  const bool isY = xcd >= 4u;
  const unsigned dt = udt, st = ((xcd >> 1) & 1u) * 8u + (idx >> 2), tile = st * 8u + dt;
  u64* fG = flags + FL_G + tile * 16u; u64* fO = flags + FL_O; u64* fU = flags + FL_U + dt * 32u;
  const rsrc_t sG = mkrsrc(ws + WS_G + (size_t)tile * 32768u, 32768u), sU = mkrsrc(ws + WS_U, DM * FF * 2);
  constexpr unsigned XO = 32768u, GO = 65536u;
  auto finish = [&]() {
    WAITL0(); BAR();
#pragma unroll
    for (int q = 0; q < 4; ++q) {
      const unsigned row = wid * 16u + q * 4u + (ln >> 4);
      const u32x4 v = *(const LAS u32x4*)(size_t)(L + XO + row * 256u + 16u * ((ln & 15u) ^ (row & 15u)));
      __builtin_amdgcn_raw_buffer_store_b128(v, mkrsrc(out, SEQ * DM * 2), (st * 128u + row) * 2048u + dt * 256u + (ln & 15u) * 16u, 0, 16);
    }
  };
  if (!hasU) {
    waitv<0>(); BAR();
    if (tid == 256u) flag_set(fO + qt * 32u + head, tag);
  }
  if (grp == 0) {
    const unsigned wp = lw >> 1, wq2 = lw & 1u, fr = ln & 15u, fq = ln >> 4;
    if (hasU) {
      Opnd<T1> P; Opnd<RCHK> Q;
      P.init(L, wp * 64u, ln); Q.init(L + 16384u, wq2 * 64u, ln);
      u32x2 bv[4];
      f32x4 acc[4][4] = {};
      gemm_full<T1, RCHK, 5, 32768u, 0, 0, 16>(acc, P, Q, 32, [&] {
#pragma unroll
        for (int p = 0; p < 4; ++p) bv[p] = *(const u32x2*)(b1 + ufc * 128u + wp * 64u + p * 16u + fq * 4u);
      });
      const unsigned uo = ((udt * 56u + uft * 2u + wp) * 8u + fq) * 2048u + (wq2 * 64u + fr) * 16u;
#pragma unroll
      for (int pp = 0; pp < 2; ++pp) {
        const f32x2 b0 = bf2(bv[2 * pp][0]), b1v = bf2(bv[2 * pp][1]), b2v = bf2(bv[2 * pp + 1][0]), b3 = bf2(bv[2 * pp + 1][1]);
#pragma unroll
        for (int m = 0; m < 4; ++m)
          __builtin_amdgcn_raw_buffer_store_b128((u32x4){gelu2(lo2(acc[2 * pp][m]) + b0), gelu2(hi2(acc[2 * pp][m]) + b1v), gelu2(lo2(acc[2 * pp + 1][m]) + b2v), gelu2(hi2(acc[2 * pp + 1][m]) + b3)},
                                                 sU, uo + pp * 8192u + m * 256u, 0, 16);
      }
      if (isY) { waitv<0>(); BAR(); }
    }
    if (isY || !hasU) BAR();
    f32x4 acc[4][4] = {};
    const unsigned t4 = (wid * 64u + ln) * 16u;
    if (isY) {
      float bias[4];
#pragma unroll
      for (int q = 0; q < 4; ++q) bias[q] = bflo(b2[st * 128u + wq2 * 64u + q * 16u + fr]);
      Opnd<RCHK> P; Opnd<T2> Q;
      P.init(L + 65536u, wp * 64u, ln); Q.init(L + 65536u + 16384u, wq2 * 64u, ln);
      gemm_full<RCHK, T2, 5, 32768u, 3, 2, 0>(acc, P, Q, 56, [] {});
#pragma unroll
      for (int j = 0; j < 8; ++j) {
        const int q = 2 * (j & 1); const f32x4 y0 = acc[j >> 1][q] + bias[q], y1 = acc[j >> 1][q + 1] + bias[q + 1];
        __builtin_amdgcn_raw_buffer_store_b128((u32x4){silu2(lo2(y0)), silu2(hi2(y0)), silu2(lo2(y1)), silu2(hi2(y1))}, sG, t4 + j * 4096u, 0, 16);
      }
      waitv<0>(); BAR();
      if (tid == 0u) flag_set(fG, tag);
      return;
    }
    Opnd<T1> P; Opnd<RCHK> Q;
    P.init(L + 65536u, wp * 64u, ln); Q.init(L + 65536u + 16384u, wq2 * 64u, ln);
    gemm_full<T1, RCHK, 5, 32768u, 3, 2, 2>(acc, P, Q, 64, [] {});
    const unsigned xa = L + XO + (wq2 * 64u + fr) * 256u + (fq & 1u) * 8u;
    unsigned xp[4];
    f32x4 xv[4][4];
#pragma unroll
    for (int p = 0; p < 4; ++p) {
      xp[p] = xa + 16u * ((wp * 8u + 2u * p + (fq >> 1)) ^ fr);
#pragma unroll
      for (int q = 0; q < 4; ++q) {
        const u32x2 w = *(const LAS u32x2*)(size_t)(xp[p] + q * 4096u);
        xv[p][q] = (f32x4){bflo(w[0]), bfhi(w[0]), bflo(w[1]), bfhi(w[1])};
        asm volatile("" : "+v"(xv[p][q]));
      }
    }
    BAR();
    u32x4 gp[8];
#pragma unroll
    for (int j = 0; j < 8; ++j) gp[j] = *(const LAS u32x4*)(size_t)(L + GO + t4 + j * 4096u);
#pragma unroll
    for (int p = 0; p < 4; ++p)
#pragma unroll
      for (int q = 0; q < 4; ++q) {
        const unsigned g0 = gp[2 * p + (q >> 1)][2 * (q & 1)], g1 = gp[2 * p + (q >> 1)][2 * (q & 1) + 1];
        const f32x4 r = __builtin_elementwise_fma(acc[p][q], (f32x4){bflo(g0), bfhi(g0), bflo(g1), bfhi(g1)}, xv[p][q]);
        *(LAS u32x2*)(size_t)(xp[p] + q * 4096u) = (u32x2){pk2(r[0], r[1]), pk2(r[2], r[3])};
      }
  } else {
    const unsigned N = isY ? 56u : 64u;
    const rsrc_t sT = isY ? mkrsrcB(w2, FF * SEQ * 2) : mkrsrcB(wo, NH * DH * DM * 2);
    const rsrc_t sR = isY ? sU : sO;
    const unsigned ldT = isY ? 4096u : 2048u, voT = voffT(ln, ldT), voL = ln * 16u;
    const unsigned tcol = (isY ? st : dt) * 256u, rbase = (isY ? dt : st) * N * 16384u + lw * 4096u;
    const unsigned toff = (isY ? 16384u : 0u) + lw * 4096u, roff = (isY ? 0u : 16384u) + lw * 4096u, g0 = isY ? (xcd >> 1) * 14u : 0u;
    const u64* dep = (isY ? fU : fO + (st >> 1) * 32u) + (ln & 31u);
    auto peek = [&]() { asm volatile("global_load_dwordx2 v[254:255], %0, off sc1" :: "v"(dep) : "memory", "v254", "v255"); };
    auto await = [&](u64 mask, bool peeked) {
      if (peeked) { u64 v; asm volatile("v_mov_b64 %0, v[254:255]" : "=v"(v) :: "memory"); if ((__ballot(v != tag) & mask) == 0ull) return; }
      flag_wait(dep, tag, mask);
    };
    unsigned sb = L + 4u * 32768u;
    auto nexts = [&]() { sb = sb == L + 4u * 32768u ? L : sb + 32768u; };
    unsigned kT = g0;
    unsigned kW = isY ? (xcd >> 1) * 2u : 0u;
    const unsigned kj = isY ? 7u : 1u;
    auto dT = [&]() { T4 p; p.set(kW * 64u * ldT + tcol, lw, ldT); p.go(sb + toff, voT, sT); ++kT; kW += (kW & 1u) ? kj : 1u; nexts(); };
    if (hasU) {
      const unsigned voW1 = voffT(ln, FF * 2u), hw = (wid & 1u) * 2u;
      unsigned j = 4;
      auto ufill = [&](auto wait) {
        T4 p, q; p.set(j * 64u * (FF * 2u) + ufc * 256u, hw, FF * 2u); q.set(j * 64u * (FF * 2u) + ufc * 256u, hw + 1u, FF * 2u);
        unsigned d0 = sb + hw * 4096u, r0 = (udt * 32u + j) * 16384u + hw * 4096u; p.pin(); q.pin(); PINS(d0); PINS(r0);
        wait();
        p.go(d0, voW1, sW1); dma64d(d0 + 16384u, voL, sH, r0); q.go(d0 + 4096u, voW1, sW1); dma64d(d0 + 20480u, voL, sH, r0 + 4096u);
        ++j; nexts();
      };
      if (wid >= 6u) {
        ufill([] {});
#pragma nounroll
        for (unsigned t = 0; t < 12; ++t) ufill([] { waitv<48>(); BAR(); });
        waitv<48>(); BAR(); ost2(0); waitv<34>(); BAR(); ost2(1); waitv<20>(); BAR(); ost2(2); waitv<6>(); BAR(); ost2(3);
        touch8k(code_rsrc(), ln < 10u ? ln * 128u : 0x80000000u, (idx * 2u + (wid & 1u)) * 1280u);
        for (unsigned t = 16; t < 22; ++t) BAR();
        waitv<0>(); BAR();
        if (tid == 384u) flag_set(fO + qt * 32u + head, tag);
        BAR(); BAR(); BAR(); BAR();
      } else {
        BAR(); BAR(); waitv<0>(); BAR();
        ost2(0); BAR(); ost2(1); BAR(); ost2(2); BAR(); ost2(3);
        for (unsigned t = 6; t < 12; ++t) BAR();
        j = 17; sb = L + 2u * 32768u;
#pragma nounroll
        for (unsigned t = 12; t < 27; ++t) ufill([] { waitv<48>(); BAR(); });
      }
      sb = L + 2u * 32768u;
      waitv<48>(); BAR(); dT(); waitv<36>(); BAR(); dT(); peek(); waitv<24>(); BAR(); dT(); waitv<12>(); BAR(); dT(); BAR(); dT();
      if (isY) {
        BAR();
        if (tid == 256u) flag_set(fU + uft, tag);
      } else waitv<12>();
    } else {
      sb = L + 2u * 32768u;
#pragma nounroll
      for (unsigned j = 0; j < 5; ++j) dT();
    }
    if (hasU && !isY) await(0xffffffffull, true);
    else { if (wid == 4u) await(isY ? 0x7full << (g0 >> 1) : 0xffffffffull, false); BAR(); }
#pragma nounroll
    for (unsigned j = 0; j < 5; ++j) { dma64d(sb + roff, voL, sR, rbase + (g0 + j) * 16384u); nexts(); }
    auto dstep = [&](auto wait) {
      T4 p; p.set(kW * 64u * ldT + tcol, lw, ldT);
      unsigned d0 = sb + toff, d1 = sb + roff, r0 = rbase + kT * 16384u; p.pin(); PINS(d0); PINS(d1); PINS(r0);
      wait(); BAR();
      p.go(d0, voT, sT); dma64d(d1, voL, sR, r0);
      ++kT; kW += (kW & 1u) ? kj : 1u; nexts();
    };
    waitv<16>(); BAR();
    dstep([] { waitv<12>(); }); dstep([] { waitv<16>(); }); dstep([] { waitv<20>(); });
    if (!isY && hasU && tid == 256u) flag_set(fU + uft, tag);
#pragma nounroll
    for (unsigned seg = isY ? 0u : 5u; seg < 6u; ++seg) {
      const unsigned n = !isY ? 56u : seg == 0u ? 2u : seg == 2u ? 10u : seg >= 4u ? 14u : 4u;
#pragma nounroll
      for (unsigned i = 0; i < n; ++i) dstep([] { waitv<24>(); });
      if (seg == 0u || seg == 2u) peek();
      else if (seg < 4u) { waitv<32>(); await(seg == 1u ? 0x3fffull << 14 : 0x3fffull, true); kT = seg == 1u ? 70u - g0 : 0u; kW = seg == 1u ? 10u - (xcd >> 1) * 2u : 0u; }
      else kW = 2u;
    }
    waitv<24>(); BAR();
    if (isY) { waitv<0>(); BAR(); return; }
    {
      const rsrc_t sX = mkrsrc(x, SEQ * DM * 2);
#pragma unroll
      for (int q = 0; q < 8; ++q)
        dma16(L + XO + (lw * 8u + q) * 1024u, (ln >> 4) * 2048u + 16u * ((ln & 15u) ^ ((4u * q + (ln >> 4)) & 15u)), sX, (st * 128u + lw * 32u + q * 4u) * 2048u + dt * 256u);
    }
    waitv<8>(); BAR();
    waitv<0>(); BAR();
    {
      flag_wait<6>(fG, tag, 1ull);
      u32x4 g[8];
#pragma unroll
      for (int q = 0; q < 8; ++q) g[q] = __builtin_amdgcn_raw_buffer_load_b128(sG, voL + (lw * 8u + q) * 1024u, 0, 16);
#pragma unroll
      for (int q = 0; q < 8; ++q) *(LAS u32x4*)(size_t)(L + GO + voL + (lw * 8u + q) * 1024u) = g[q];
    }
    waitv<0>(); WAITL0(); BAR();
  }
  finish();
}

extern "C" __global__ void rsk_end() {}
}

extern "C" void kernel_launch(void* const* d_in, const int* in_sizes, int n_in, void* d_out, int out_size, void* d_ws, size_t ws_size, hipStream_t stream) {
  const u16* x = (const u16*)d_in[0]; const u16* kc = (const u16*)d_in[1]; const u16* vc = (const u16*)d_in[2];
  const u16* wq = (const u16*)d_in[3]; const u16* wo = (const u16*)d_in[4];
  const u16* w1 = (const u16*)d_in[6]; const u16* b1 = (const u16*)d_in[7]; const u16* w2 = (const u16*)d_in[8]; const u16* b2 = (const u16*)d_in[9];
  char* ws = (char*)d_ws;
  (void)hipFuncSetAttribute((const void*)rsk_main, hipFuncAttributeMaxDynamicSharedMemorySize, LDS_BYTES);
  rsk_main<<<256, 512, LDS_BYTES, stream>>>(x, ws, wq, kc, vc, w1, wo, w2, b1, b2, (u16*)d_out);
}
```
